```python
import torch

def _softmax(x, axis=-1):
    return torch.softmax(x, dim=axis)

def _gelu(x):
    return torch.nn.functional.gelu(x, approximate='tanh')

def _silu(x):
    return torch.nn.functional.silu(x)

def _sigmoid(x):
    return torch.sigmoid(x)

def _layernorm(x, axis=-1, eps=1e-05):
    m = x.mean(dim=axis, keepdim=True)
    v = x.var(dim=axis, keepdim=True, unbiased=False)
    return (x - m) / torch.sqrt(v + eps)

def _scatter_add(tgt, idx, src, axis=0):
    return tgt.clone().index_add_(axis, idx, src)

def _segment_sum(src, seg, n):
    out = torch.zeros((n,) + src.shape[1:], dtype=src.dtype)
    return out.index_add_(0, seg, src)

def _pad(x, pw):
    flat = tuple((v for pair in reversed(pw) for v in pair))
    return torch.nn.functional.pad(x, flat)
kernel_name = 'concat_heads__ssm_conv_s5__memory_aug_recurrent_mem_tr__prompt'

def setup_inputs():

    _w = lambda a: (lambda t: t.to(torch.bfloat16) if t.is_floating_point() else t)(torch.as_tensor(a))

    def _A_setup():
        B, S, D = (1, 2048, 512)
        P = D
        Lambda_re = -0.5 * (lambda _x: torch.exp(torch.as_tensor(_x)))((lambda *s: torch.randn(*s, dtype=torch.bfloat16) * 0.8)(P))
        Lambda_im = (lambda *s: torch.randn(*s, dtype=torch.bfloat16) * 0.8)(P) * 1.0
        B_re = (lambda *s: torch.randn(*s, dtype=torch.bfloat16) * 0.8)(P, D) * D ** (-0.5)
        B_im = (lambda *s: torch.randn(*s, dtype=torch.bfloat16) * 0.8)(P, D) * D ** (-0.5)
        C_re = (lambda *s: torch.randn(*s, dtype=torch.bfloat16) * 0.8)(D, P) * P ** (-0.5)
        C_im = (lambda *s: torch.randn(*s, dtype=torch.bfloat16) * 0.8)(D, P) * P ** (-0.5)
        log_dt = (lambda *s: torch.randn(*s, dtype=torch.bfloat16) * 0.8)(P) * 0.3 - 2.0
        Dskip = (lambda *s: torch.randn(*s, dtype=torch.bfloat16) * 0.8)(D)
        return {'x': _w((lambda *s: torch.randn(*s, dtype=torch.bfloat16) * 0.05)(B, S, D)), 'Lambda_re': _w(Lambda_re), 'Lambda_im': _w(Lambda_im), 'B_re': _w(B_re), 'B_im': _w(B_im), 'C_re': _w(C_re), 'C_im': _w(C_im), 'log_dt': _w(log_dt), 'Dskip': _w(Dskip)}
    _A_in = _A_setup()

    def _B_setup():
        B, S, Dm = (1, 2048, 512)
        H, Dh = (32, 128)
        M, seg = (8, 32)
        seg = min(seg, S)
        S = S // seg * seg
        return {'x': _w((lambda *s: torch.randn(*s, dtype=torch.bfloat16) * 0.05)(B, S, Dm)), 'mem0': _w((lambda *s: torch.randn(*s, dtype=torch.bfloat16) * 0.8)(B, M, Dm)), 'Wq': _w((lambda *s: torch.randn(*s, dtype=torch.bfloat16) * 1.6)(Dm, H, Dh) * Dm ** (-0.5)), 'Wk': _w((lambda *s: torch.randn(*s, dtype=torch.bfloat16) * 1.6)(Dm, H, Dh) * Dm ** (-0.5)), 'Wv': _w((lambda *s: torch.randn(*s, dtype=torch.bfloat16) * 0.8)(Dm, H, Dh) * Dm ** (-0.5)), 'Wo': _w((lambda *s: torch.randn(*s, dtype=torch.bfloat16) * 0.8)(H, Dh, Dm) * (H * Dh) ** (-0.5)), 'seg_len': seg}
    _B_in = _B_setup()
    return {'A_x': _A_in['x'], 'A_Lambda_re': _A_in['Lambda_re'], 'A_Lambda_im': _A_in['Lambda_im'], 'A_B_re': _A_in['B_re'], 'A_B_im': _A_in['B_im'], 'A_C_re': _A_in['C_re'], 'A_C_im': _A_in['C_im'], 'A_log_dt': _A_in['log_dt'], 'A_Dskip': _A_in['Dskip'], 'B_x': _B_in['x'], 'B_mem0': _B_in['mem0'], 'B_Wq': _B_in['Wq'], 'B_Wk': _B_in['Wk'], 'B_Wv': _B_in['Wv'], 'B_Wo': _B_in['Wo'], 'B_seg_len': _B_in['seg_len']}

def reference(A_x, A_Lambda_re, A_Lambda_im, A_B_re, A_B_im, A_C_re, A_C_im, A_log_dt, A_Dskip, B_x, B_mem0, B_Wq, B_Wk, B_Wv, B_Wo, B_seg_len):

    def _A_ref(x, Lambda_re, Lambda_im, B_re, B_im, C_re, C_im, log_dt, Dskip):
        Bsz, L, H = x.shape
        Lam = Lambda_re + 1j * Lambda_im
        Bmat = B_re + 1j * B_im
        Cmat = C_re + 1j * C_im
        dt = torch.exp(log_dt)
        Lam_bar = torch.exp(dt * Lam)
        B_bar = ((Lam_bar - 1.0) / Lam)[:, None] * Bmat
        Bu = torch.einsum('bld,pd->blp', x + 0j, B_bar)
        h = torch.zeros_like(Bu[:, 0, :])
        hs = []
        for t in range(L):
            h = Lam_bar[None, :] * h + Bu[:, t, :]
            hs.append(h)
        hseq = torch.stack(hs, 1)
        y = 2.0 * torch.einsum('blp,dp->bld', hseq, Cmat).real + x * Dskip[None, None, :]
        return y

    def _B_ref(x, mem0, Wq, Wk, Wv, Wo, seg_len):
        B, S, Dm = x.shape
        H, Dh = (Wq.shape[1], Wq.shape[2])
        M = mem0.shape[1]
        scale = Dh ** (-0.5)
        n_seg = S // seg_len
        L = M + seg_len
        idx = torch.arange(L)
        mask = (idx[:, None] < M) | (idx[:, None] >= idx[None, :])
        mem = mem0
        outs = []
        for s in range(n_seg):
            xs = x[:, s * seg_len:(s + 1) * seg_len, :].clone()
            h = torch.concatenate([mem, xs], axis=1)
            q = torch.einsum('btd,dhe->bhte', h, Wq)
            k = torch.einsum('btd,dhe->bhte', h, Wk)
            v = torch.einsum('btd,dhe->bhte', h, Wv)
            scores = torch.einsum('bhqe,bhke->bhqk', q, k) * scale
            scores = torch.where(mask[None, None], scores, -1000000000.0)
            p = _softmax(scores, axis=-1)
            o = torch.einsum('bhqk,bhke->bhqe', p, v)
            o = torch.einsum('bhte,hed->btd', o, Wo)
            mem = o[:, :M, :].clone()
            outs.append(o[:, M:, :])
        return torch.concatenate(outs, axis=1)
    a = _A_ref(A_x, A_Lambda_re, A_Lambda_im, A_B_re, A_B_im, A_C_re, A_C_im, A_log_dt, A_Dskip)
    b = _B_ref(B_x, B_mem0, B_Wq, B_Wk, B_Wv, B_Wo, B_seg_len)
    return torch.concatenate([a, b], axis=-1)
```

```cpp
#include <hip/hip_runtime.h>
#include <hip/hip_bf16.h>
#include <stdint.h>

typedef __attribute__((ext_vector_type(2))) unsigned u2;
typedef __attribute__((ext_vector_type(4))) unsigned u4;
typedef __attribute__((ext_vector_type(8))) unsigned u8;
typedef __attribute__((ext_vector_type(2))) float f2;
typedef __attribute__((ext_vector_type(4))) float f4;
typedef __attribute__((ext_vector_type(2))) __bf16 bf2;
typedef __attribute__((ext_vector_type(8))) __bf16 bf8;
typedef __attribute__((ext_vector_type(16))) __bf16 bf16v;
typedef __amdgpu_buffer_rsrc_t rsrc_t;
#define DEV __device__ __forceinline__

struct Params {
  const void *ax, *lre, *lim, *bre, *bim, *cre, *cim, *ldt, *dsk, *bx, *mem0, *wq, *wk, *wv, *wo;
  float* out;
  char* ws;
};

constexpr int NSEG = 64;
constexpr unsigned SPIN_CAP = 400000u;
constexpr unsigned DYN_LDS = 156u * 1024u;

constexpr unsigned F_INIT = 0, F_KX = 512, F_KM = 768, F_PX = 1024, F_M = 1536, F_U = 2560, F_A = 3584;
constexpr unsigned FLAG_BYTES = 4096;
constexpr unsigned O_AE = 5u << 20, O_DIAG = 6u << 20;
constexpr unsigned O_KX = 8u << 20, O_KM = 24u << 20, O_PX = 28u << 20, O_PM = 32u << 20, O_M2 = 34u << 20,
                   O_U = 50u << 20, WS_BYTES = 114u << 20;
constexpr unsigned B_MEM = 0, B_P = 63, B_M = 67, B_END = 99, B_PROBE = 100;

DEV rsrc_t mk(const void* p, unsigned bytes) {
  return __builtin_amdgcn_make_buffer_rsrc((void*)p, 0, bytes, 0x00020000);
}
DEV unsigned rfl(unsigned v) { return __builtin_amdgcn_readfirstlane(v); }
DEV u4 ld16(rsrc_t r, unsigned v, unsigned s) { return __builtin_amdgcn_raw_buffer_load_b128(r, v, s, 0); }
DEV u2 ld8(rsrc_t r, unsigned v, unsigned s) { return __builtin_amdgcn_raw_buffer_load_b64(r, v, s, 0); }
DEV unsigned ld4(rsrc_t r, unsigned v, unsigned s) { return __builtin_amdgcn_raw_buffer_load_b32(r, v, s, 0); }
DEV unsigned ld2(rsrc_t r, unsigned v, unsigned s) { return __builtin_amdgcn_raw_buffer_load_b16(r, v, s, 0); }
DEV u4 ld16c(rsrc_t r, unsigned v, unsigned s) { return __builtin_amdgcn_raw_buffer_load_b128(r, v, s, 16); }
DEV u2 ld8c(rsrc_t r, unsigned v, unsigned s) { return __builtin_amdgcn_raw_buffer_load_b64(r, v, s, 16); }
DEV unsigned ld4c(rsrc_t r, unsigned v, unsigned s) { return __builtin_amdgcn_raw_buffer_load_b32(r, v, s, 16); }
DEV void st16c(u4 d, rsrc_t r, unsigned v, unsigned s) { __builtin_amdgcn_raw_buffer_store_b128(d, r, v + s, 0, 16); }
DEV void st8c(u2 d, rsrc_t r, unsigned v, unsigned s) { __builtin_amdgcn_raw_buffer_store_b64(d, r, v, s, 16); }
DEV void st4c(unsigned d, rsrc_t r, unsigned v, unsigned s) { __builtin_amdgcn_raw_buffer_store_b32(d, r, v, s, 16); }
DEV void fence() { asm volatile("" ::: "memory"); }
DEV void drain() { asm volatile("s_waitcnt vmcnt(0)" ::: "memory"); }
DEV void bar() { asm volatile("s_waitcnt lgkmcnt(0)\n\ts_barrier" ::: "memory"); }
DEV bool all_lanes(bool ok) { return __builtin_amdgcn_ballot_w64(!ok) == 0ull; }

DEV float bf2f(unsigned h) { return __uint_as_float(h << 16); }
DEV unsigned pk(float a, float b) {
  f2 v = {a, b};
  return __builtin_bit_cast(unsigned, __builtin_convertvector(v, bf2));
}
DEV float rbf(float a) { return bf2f(pk(a, a) & 0xffffu); }
DEV float smul(float a, float b) {
  float r;
  asm("v_mul_f32 %0, %1, %2" : "=v"(r) : "v"(a), "v"(b));
  return r;
}
DEV float sadd(float a, float b) {
  float r;
  asm("v_add_f32 %0, %1, %2" : "=v"(r) : "v"(a), "v"(b));
  return r;
}
DEV f4 mma(u4 a, u4 b, f4 c) {
  return __builtin_amdgcn_mfma_f32_16x16x32_bf16(__builtin_bit_cast(bf8, a), __builtin_bit_cast(bf8, b), c, 0, 0, 0);
}
DEV f4 smma(u4 a, u8 b, f4 c, unsigned idx) {
  return __builtin_amdgcn_smfmac_f32_16x16x64_bf16(__builtin_bit_cast(bf8, a), __builtin_bit_cast(bf16v, b), c,
                                                   (int)idx, 0, 0);
}
DEV float swap32_add(float a, float b) {
  auto r = __builtin_amdgcn_permlane32_swap(__float_as_uint(a), __float_as_uint(b), false, false);
  return __uint_as_float(r[0]) + __uint_as_float(r[1]);
}
DEV float swap16_add(float a, float b) {
  auto r = __builtin_amdgcn_permlane16_swap(__float_as_uint(a), __float_as_uint(b), false, false);
  return __uint_as_float(r[0]) + __uint_as_float(r[1]);
}
DEV float group_max(float m) {
  auto r = __builtin_amdgcn_permlane16_swap(__float_as_uint(m), __float_as_uint(m), false, false);
  m = fmaxf(__uint_as_float(r[0]), __uint_as_float(r[1]));
  auto q = __builtin_amdgcn_permlane32_swap(__float_as_uint(m), __float_as_uint(m), false, false);
  return fmaxf(__uint_as_float(q[0]), __uint_as_float(q[1]));
}
DEV float group_sum(float m) {
  const float t = swap16_add(m, m);
  return swap32_add(t, t);
}
DEV u4& lds16(char* sm, unsigned off) { return *reinterpret_cast<u4*>(sm + off); }
DEV void lds_put2(char* sm, unsigned off, unsigned v) { *reinterpret_cast<unsigned short*>(sm + off) = (unsigned short)v; }

#define STAMP(slot, idx) do { } while (0)

DEV unsigned wait_flags(rsrc_t ws, unsigned off, unsigned need, bool& alive) {
  unsigned v = 0;
  for (unsigned it = 0; alive; ++it) {
    fence();
    v = ld4c(ws, off, 0);
    if (all_lanes(v >= need)) break;
    if (it > SPIN_CAP) alive = false;
    __builtin_amdgcn_s_sleep(1);
  }
  fence();
  return v;
}
DEV unsigned crit_base(const Params& p) {
  const unsigned lo = (unsigned)(uintptr_t)p.ws;
  return ((lo + 65536u + 0x1fffffu) & ~0x1fffffu) - lo;
}
DEV unsigned wait_init(const Params& p, rsrc_t ws, bool& alive) {
  const unsigned v = wait_flags(ws, F_INIT + (threadIdx.x & 63) * 4u, 1u, alive);
  return crit_base(p) + (rfl(v) == 2u ? 8192u : 0u);
}

DEV void load_pair(rsrc_t r, unsigned voff, unsigned soff, unsigned rstride, u4& a, u4& b) {
  unsigned d[8];
#pragma unroll
  for (int j = 0; j < 8; ++j) d[j] = ld4(r, voff, soff + j * rstride);
#pragma unroll
  for (int j = 0; j < 4; ++j) {
    a[j] = __builtin_amdgcn_perm(d[2 * j + 1], d[2 * j], 0x05040100u);
    b[j] = __builtin_amdgcn_perm(d[2 * j + 1], d[2 * j], 0x07060302u);
  }
}
DEV u4 load_single(rsrc_t r, unsigned voff, unsigned soff, unsigned rstride) {
  unsigned d[8];
  u4 a;
#pragma unroll
  for (int j = 0; j < 8; ++j) d[j] = ld2(r, voff, soff + j * rstride);
#pragma unroll
  for (int j = 0; j < 4; ++j) a[j] = __builtin_amdgcn_perm(d[2 * j + 1], d[2 * j], 0x05040100u);
  return a;
}
DEV u8 cat(u4 a, u4 b) { return __builtin_shufflevector(a, b, 0, 1, 2, 3, 4, 5, 6, 7); }

DEV u4 load_mem0(const Params& p, unsigned w, unsigned c, unsigned g) {
  const rsrc_t r = mk(p.mem0, 8192);
  const unsigned cg = 4 * w + g;
  const unsigned voff = ((c & 7) * 512u + 32u * (cg >> 1) + (cg & 1) + 4u * (c >> 3)) * 2u;
  u4 a;
#pragma unroll
  for (int i = 0; i < 4; ++i) a[i] = ld2(r, voff, 16 * i) | (ld2(r, voff, 16 * i + 4) << 16);
  return a;
}
DEV u4 poll_mem(rsrc_t ws, unsigned voff, unsigned soff, bool& alive) {
  u4 a = {0, 0, 0, 0};
  for (unsigned it = 0; alive; ++it) {
    fence();
    a = ld16c(ws, voff, soff);
    const unsigned m = max(max(a[0], a[1]), max(a[2], a[3]));
    if (all_lanes(m != 0xffffffffu)) break;
    if (it > SPIN_CAP) alive = false;
  }
  return a;
}

constexpr float SCALE_LOG2E = 0.08838834764831845f * 1.4426950408889634f;

DEV void role_s1(const Params& p, unsigned h, char* sm) {
  constexpr unsigned L_MEM = 0, L_Q = 8192, L_K = 10240, L_KX = 12288;
  const unsigned l = threadIdx.x & 63, w = rfl(threadIdx.x >> 6), c = l & 15, g = l >> 4;
  const rsrc_t ws = mk(p.ws, WS_BYTES);
  bool alive = true;
  STAMP(15, w);

  u8 wa[8], wb[8];
  {
    const rsrc_t wr = mk(w < 4 ? p.wq : p.wk, 4u << 20);
    const unsigned voff = ((16u * (g & 1) + (g >> 1)) * 4096u + h * 128u + (w & 3) * 32u + 2u * c) * 2u;
#pragma unroll
    for (int i = 0; i < 8; ++i) {
      const unsigned t = (w + i) & 7;
      u4 a0, b0, a1, b1;
      load_pair(wr, voff, (64u * t) * 8192u, 16384u, a0, b0);
      load_pair(wr, voff, (64u * t + 32u) * 8192u, 16384u, a1, b1);
      wa[i] = cat(a0, a1);
      wb[i] = cat(b0, b1);
    }
  }
  u4 a0 = load_mem0(p, w, c, g);
  const unsigned cb = wait_init(p, ws, alive);
  STAMP(15, 8 + w);

  const unsigned idx = c < 8 ? 0x44444444u : 0xeeeeeeeeu;
  const unsigned mem_off = (4u * w + g) * 256u + ((c & 7) >> 2) * 128u + (c >> 3) * 64u + (c & 3) * 16u;
  const unsigned tok = ((c >> 1) & 1) * 16u + (c >> 3) * 8u + (w >> 2) * 4u + ((c >> 2) & 1) * 2u + (c & 1);
  const unsigned kx_off = O_KX + h * 8192u + (((w & 3) * 2u + (tok >> 4)) * 64u + g * 16u + (tok & 15)) * 16u;
  const unsigned tr_off = (w < 4 ? L_Q : L_K) + (((w & 3) * 4u + (c >> 2)) * 8u + 4u * (g & 1)) * 16u +
                          (4u * (l >> 5) + (c & 3)) * 2u;
  const unsigned q_off = L_Q + (g * 8u + (c & 7)) * 16u;
  const unsigned km_off = L_K + (g * 8u + ((c >> 2) & 1) * 4u + (c >> 3) * 2u + (c & 1)) * 16u;
  const unsigned piece = ((c & 7) >> 2) * 4u + (h >> 3), hl = h & 7;
  const unsigned p_base = (B_P + (piece >> 1)) * 16384u + (piece & 1) * 4096u;
  const unsigned px_off = p_base + (hl >> 1) * 512u + (2u * (hl & 1) + (g >> 1)) * 128u + ((g & 1) * 4u + (c & 3)) * 16u;
  const unsigned pm_off = p_base + 2048u + (hl >> 1) * 128u + ((g >> 1) * 4u + (c & 3)) * 16u + (hl & 1) * 8u;

  unsigned kx_seen = rfl(wait_flags(ws, F_KX + h * 4u, 1u, alive));
  lds16(sm, L_KX + w * 1024u + l * 16u) = ld16c(ws, kx_off, 0);
  STAMP(15, 16 + w);

  for (int s = 0; s < NSEG; ++s) {
    if (s > 0) a0 = poll_mem(ws, mem_off, cb + (B_MEM + s - 1) * 16384u, alive);
    if (w == 0) STAMP(0, s);
    lds16(sm, L_MEM + w * 1024u + l * 16u) = a0;
    f4 ca = {0, 0, 0, 0}, cbv = {0, 0, 0, 0};
    ca = smma(a0, wa[0], ca, idx);
    cbv = smma(a0, wb[0], cbv, idx);
    bar();
#pragma unroll
    for (int i = 1; i < 8; ++i) {
      const u4 a = lds16(sm, L_MEM + ((w + i) & 7) * 1024u + l * 16u);
      ca = smma(a, wa[i], ca, idx);
      cbv = smma(a, wb[i], cbv, idx);
    }
    {
      const unsigned d01 = pk(swap32_add(ca[0], cbv[0]), swap32_add(ca[1], cbv[1]));
      const unsigned d23 = pk(swap32_add(ca[2], cbv[2]), swap32_add(ca[3], cbv[3]));
      lds_put2(sm, tr_off, d01);
      lds_put2(sm, tr_off + 16, d01 >> 16);
      lds_put2(sm, tr_off + 32, d23);
      lds_put2(sm, tr_off + 48, d23 >> 16);
    }
    bar();
    if (w == 0 && s < NSEG - 1) {
      STAMP(1, s);
      f4 sm_ = {0, 0, 0, 0}, s0 = {0, 0, 0, 0}, s1 = {0, 0, 0, 0};
      const unsigned kxb = L_KX + (s & 1) * 8192u + l * 16u;
#pragma unroll
      for (int m = 0; m < 4; ++m) {
        const u4 q = lds16(sm, q_off + m * 512u);
        s0 = mma(lds16(sm, kxb + m * 1024u), q, s0);
        s1 = mma(lds16(sm, kxb + 4096u + m * 1024u), q, s1);
        sm_ = mma(lds16(sm, km_off + m * 512u), q, sm_);
      }
      float mx = fmaxf(fmaxf(fmaxf(s0[0], s0[1]), fmaxf(s0[2], s0[3])), fmaxf(fmaxf(s1[0], s1[1]), fmaxf(s1[2], s1[3])));
      mx = group_max(fmaxf(mx, fmaxf(sm_[0], sm_[1])));
      const float mc = -mx * SCALE_LOG2E;
      float e[10];
#pragma unroll
      for (int r = 0; r < 4; ++r) {
        e[r] = __builtin_amdgcn_exp2f(fmaf(s0[r], SCALE_LOG2E, mc));
        e[4 + r] = __builtin_amdgcn_exp2f(fmaf(s1[r], SCALE_LOG2E, mc));
      }
      e[8] = __builtin_amdgcn_exp2f(fmaf(sm_[0], SCALE_LOG2E, mc));
      e[9] = __builtin_amdgcn_exp2f(fmaf(sm_[1], SCALE_LOG2E, mc));
      const float sum = group_sum(((e[0] + e[1]) + (e[2] + e[3])) + ((e[4] + e[5]) + (e[6] + e[7])) + (e[8] + e[9]));
      float rs = __builtin_amdgcn_rcpf(sum);
      rs = (s & 1) ? -rs : rs;
      const u4 px = {pk(e[0] * rs, e[1] * rs), pk(e[2] * rs, e[3] * rs), pk(e[4] * rs, e[5] * rs), pk(e[6] * rs, e[7] * rs)};
      if (c < 8) st16c(px, ws, px_off, cb);
      const unsigned dm = pk(e[8] * rs, e[9] * rs);
      auto r2 = __builtin_amdgcn_permlane16_swap(dm, dm, false, false);
      const u2 pm = {r2[0], r2[1]};
      if (c < 8 && !(g & 1)) st8c(pm, ws, pm_off, cb);
      STAMP(2, s);
    }
    if (w == 4) {
      const unsigned so = O_KM + (s * 32u + h) * 2048u;
      st16c(lds16(sm, L_K + l * 16u), ws, l * 16u, so);
      st16c(lds16(sm, L_K + 1024u + l * 16u), ws, 1024u + l * 16u, so);
      drain();
      if (l == 0) st4c(s + 1, ws, F_KM + h * 4u, 0);
    }
    if (s < NSEG - 2) {
      if (kx_seen < (unsigned)s + 2u) kx_seen = rfl(wait_flags(ws, F_KX + h * 4u, s + 2, alive));
      lds16(sm, L_KX + ((s + 1) & 1) * 8192u + w * 1024u + l * 16u) = ld16c(ws, kx_off, (s + 1) * 262144u);
    }
  }
}

DEV void load_wo(const Params& p, unsigned h, unsigned w, unsigned c, unsigned g, u4 (&wo)[4][4]) {
  const rsrc_t r = mk(p.wo, 4u << 20);
  const unsigned voff = ((h * 128u + 8u * g) * 512u + 64u * w + 2u * c) * 2u;
#pragma unroll
  for (int b = 0; b < 2; ++b)
#pragma unroll
    for (int f = 0; f < 4; ++f) load_pair(r, voff, f * 32768u + b * 64u, 1024u, wo[2 * b][f], wo[2 * b + 1][f]);
}

DEV void role_vm(const Params& p, unsigned h, char* sm) {
  constexpr unsigned L_MEM = 0, L_V = 8192;
  const unsigned l = threadIdx.x & 63, w = rfl(threadIdx.x >> 6), c = l & 15, g = l >> 4;
  const rsrc_t ws = mk(p.ws, WS_BYTES);
  bool alive = true;

  u8 wv[8], wo[4][2];
  {
    const rsrc_t wr = mk(p.wv, 4u << 20);
    const unsigned voff = ((16u * (g & 1) + (g >> 1)) * 4096u + h * 128u + w * 16u + c) * 2u;
#pragma unroll
    for (int i = 0; i < 8; ++i) {
      const unsigned t = (w + i) & 7;
      wv[i] = cat(load_single(wr, voff, (64u * t) * 8192u, 16384u), load_single(wr, voff, (64u * t + 32u) * 8192u, 16384u));
    }
    u4 t[4][4];
    load_wo(p, h, w, c, g, t);
#pragma unroll
    for (int i = 0; i < 4; ++i) {
      wo[i][0] = cat(t[i][0], t[i][1]);
      wo[i][1] = cat(t[i][2], t[i][3]);
    }
  }
  u4 a0 = load_mem0(p, w, c, g);
  const unsigned cb = wait_init(p, ws, alive);
  STAMP(15, 8 + w);

  const unsigned idx = c < 8 ? 0x44444444u : 0xeeeeeeeeu;
  const unsigned mem_off = (4u * w + g) * 256u + ((c & 7) >> 2) * 128u + (c >> 3) * 64u + (c & 3) * 16u;
  const unsigned tr_off = L_V + (w * 16u + ((c >> 1) & 1) * 8u + 4u * (g & 1) + 2u * (l >> 5)) * 16u +
                          (2u * (c >> 2) + (c & 1)) * 2u;
  const unsigned cg = 4u * w + ((g & 1) * 2u + (g >> 1));
  const unsigned ring_off = (B_M + cg) * 16384u + h * 256u + c * 16u;
  const unsigned copy_off = O_M2 + cg * 8192u + h * 256u + c * 16u;

  for (int s = 0; s < NSEG; ++s) {
    if (s > 0) a0 = poll_mem(ws, mem_off, cb + (B_MEM + s - 1) * 16384u, alive);
    if (w == 0) STAMP(0, s);
    lds16(sm, L_MEM + w * 1024u + l * 16u) = a0;
    f4 cv = {0, 0, 0, 0};
    cv = smma(a0, wv[0], cv, idx);
    bar();
#pragma unroll
    for (int i = 1; i < 8; ++i) cv = smma(lds16(sm, L_MEM + ((w + i) & 7) * 1024u + l * 16u), wv[i], cv, idx);
    {
      const unsigned d = pk(swap32_add(cv[0], cv[2]), swap32_add(cv[1], cv[3]));
      lds_put2(sm, tr_off, d);
      lds_put2(sm, tr_off + 16, d >> 16);
    }
    bar();
    const u4 v0 = lds16(sm, L_V + l * 16u), v1 = lds16(sm, L_V + 1024u + l * 16u);
    f4 m[4];
#pragma unroll
    for (int i = 0; i < 4; ++i) m[i] = smma(v0, wo[i][0], f4{0, 0, 0, 0}, idx);
#pragma unroll
    for (int i = 0; i < 4; ++i) m[i] = smma(v1, wo[i][1], m[i], idx);
    const unsigned x0 = pk(swap32_add(m[0][0], m[1][0]), swap32_add(m[0][1], m[1][1]));
    const unsigned x1 = pk(swap32_add(m[0][2], m[1][2]), swap32_add(m[0][3], m[1][3]));
    const unsigned y0 = pk(swap32_add(m[2][0], m[3][0]), swap32_add(m[2][1], m[3][1]));
    const unsigned y1 = pk(swap32_add(m[2][2], m[3][2]), swap32_add(m[2][3], m[3][3]));
    auto r0 = __builtin_amdgcn_permlane16_swap(x0, y0, false, false);
    auto r1 = __builtin_amdgcn_permlane16_swap(x1, y1, false, false);
    const u4 o = {r0[0], r1[0], r0[1], r1[1]};
    if (s < NSEG - 1) {
      const unsigned ph = s & 1;
      const u4 t = {(o[0] & ~1u) | ph, (o[1] & ~1u) | ph, (o[2] & ~1u) | ph, (o[3] & ~1u) | ph};
      st16c(t, ws, ring_off, cb);
    }
    if (w == 0) STAMP(2, s);
    st16c(o, ws, copy_off, s * 262144u);
    drain();
    if (l == 0) st4c(s + 1, ws, F_M + (w * 32u + h) * 4u, 0);
  }
}

DEV void role_v1(const Params& p, unsigned j, char* sm) {
  const unsigned l = threadIdx.x & 63, w = rfl(threadIdx.x >> 6), c = l & 15, g = l >> 4;
  const unsigned cg = j >> 1, rg = j & 1;
  const rsrc_t ws = mk(p.ws, WS_BYTES);
  bool alive = true;
  {
    const unsigned cb0 = crit_base(p);
    const u4 ones = {~0u, ~0u, ~0u, ~0u};
    for (unsigned o = (j * 512u + threadIdx.x) * 16u; o < B_END * 16384u; o += 64u * 512u * 16u) st16c(ones, ws, o, cb0);
    drain();
    unsigned sel = 1;
    if (j == 0 && w == 0) {
      unsigned best[2] = {~0u, ~0u};
      for (int k = 0; k < 8; ++k) {
        const unsigned off = B_PROBE * 16384u + (k & 1) * 8192u + (l & 7) * 16u + (k >> 1) * 1024u;
        const unsigned long long t0 = __builtin_amdgcn_s_memtime();
        asm volatile("s_waitcnt lgkmcnt(0)" ::: "memory");
        st16c(ones, ws, off, cb0);
        drain();
        const unsigned long long t1 = __builtin_amdgcn_s_memtime();
        asm volatile("s_waitcnt lgkmcnt(0)" ::: "memory");
        const unsigned dt = (unsigned)(t1 - t0);
        if (k & 1) best[1] = min(best[1], dt); else best[0] = min(best[0], dt);
      }
      sel = best[1] < best[0] ? 2u : 1u;
    }
    bar();
    if (threadIdx.x == 0) st4c(sel, ws, F_INIT + j * 4u, 0);
  }
  if (w >= 4) return;
  const unsigned cb = wait_init(p, ws, alive);
  STAMP(15, 8 + w);

  const unsigned idx = (c & 4) ? 0xeeeeeeeeu : 0x44444444u;
  const bool act = c < 8;
  const unsigned piece = rg * 4u + w;
  const unsigned p_off = (B_P + (piece >> 1)) * 16384u + (piece & 1) * 4096u + g * 128u + (c & 7) * 16u;
  const unsigned m_off = (B_M + cg) * 16384u + w * 2048u + l * 16u;
  const unsigned u_off = O_U + (cg * 32u + 8u * w) * 1024u + l * 16u;
  const unsigned uf_off = F_U + ((cg >> 2) * 32u + 8u * w + (l & 7)) * 4u;
  const unsigned red_w = ((((c >> 1) & 1) * 4u + g) * 8u + 2u * (c >> 2) + (c & 1)) * 16u + w * 4u;
  const unsigned out_off = cg * 256u + rg * 128u + (l >> 3) * 16u;
  unsigned u_seen = 0;

  for (int s = 0; s < NSEG - 1; ++s) {
    if (!all_lanes(u_seen > (unsigned)s)) u_seen = wait_flags(ws, uf_off, s + 1, alive);
    u8 ub[4];
#pragma unroll
    for (int t = 0; t < 4; ++t)
      ub[t] = cat(ld16c(ws, u_off + t * 2048u, s * 1048576u), ld16c(ws, u_off + t * 2048u + 1024u, s * 1048576u));
    const unsigned ph = (s & 1) ? ~0u : 0u;
    u4 m0 = {0, 0, 0, 0}, m1 = {0, 0, 0, 0};
    for (unsigned it = 0; alive; ++it) {
      fence();
      m0 = ld16c(ws, m_off, cb);
      m1 = ld16c(ws, m_off + 1024u, cb);
      const unsigned bad = ((m0[0] ^ ph) | (m0[1] ^ ph) | (m0[2] ^ ph) | (m0[3] ^ ph) | (m1[0] ^ ph) | (m1[1] ^ ph) |
                            (m1[2] ^ ph) | (m1[3] ^ ph)) & 1u;
      if (all_lanes(bad == 0)) break;
      if (it > SPIN_CAP) alive = false;
    }
    if (w == 0) STAMP(0, s);
    u4 a[5];
#pragma unroll
    for (int t = 0; t < 5; ++t) a[t] = u4{ph, ph, ph, ph};
    for (unsigned it = 0; alive; ++it) {
      fence();
      if (act) {
#pragma unroll
        for (int t = 0; t < 5; ++t) a[t] = ld16c(ws, p_off + t * 512u, cb);
      }
      unsigned bad = 0;
#pragma unroll
      for (int t = 0; t < 5; ++t) bad |= (a[t][0] ^ ph) | (a[t][1] ^ ph) | (a[t][2] ^ ph) | (a[t][3] ^ ph);
      if (all_lanes((bad & 0x80008000u) == 0)) break;
      if (it > SPIN_CAP) alive = false;
    }
    if (w == 0) STAMP(1, s);
    f4 acc = {0, 0, 0, 0};
#pragma unroll
    for (int t = 0; t < 4; ++t) acc = smma(a[t], ub[t], acc, idx);
    acc = smma(a[4], cat(m0, m1), acc, idx);
    {
      const float s01 = swap16_add(acc[0], acc[1]), s23 = swap16_add(acc[2], acc[3]);
      auto r = __builtin_amdgcn_permlane32_swap(__float_as_uint(s01), __float_as_uint(s23), false, false);
      *reinterpret_cast<unsigned*>(sm + (s & 1) * 1024u + red_w) = r[0];
    }
    bar();
    if (w == 0) {
      const f4 q = __builtin_bit_cast(f4, lds16(sm, (s & 1) * 1024u + l * 16u));
      float v = sadd(sadd(q[0], q[1]), sadd(q[2], q[3]));
      v = __uint_as_float(__float_as_uint(v) ^ (ph & 0x80000000u));
      const float vn = __uint_as_float(__builtin_amdgcn_update_dpp(0u, __float_as_uint(v), 0xb1, 0xf, 0xf, true));
      const unsigned d = pk(v, vn);
      const u4 o = {d, (unsigned)__builtin_amdgcn_update_dpp(0u, d, 0x102, 0xf, 0xf, true),
                    (unsigned)__builtin_amdgcn_update_dpp(0u, d, 0x104, 0xf, 0xf, true),
                    (unsigned)__builtin_amdgcn_update_dpp(0u, d, 0x106, 0xf, 0xf, true)};
      if ((l & 7) == 0) st16c(o, ws, out_off, cb + (B_MEM + s) * 16384u);
      STAMP(2, s);
    }
  }
}

DEV void load_x(rsrc_t xr, unsigned w, unsigned c, unsigned g, int s, u4 (&xp)[4]) {
  const unsigned voff = ((16u * (w >> 2) + c) * 512u + 128u * (w & 3) + 8u * g) * 2u;
#pragma unroll
  for (int i = 0; i < 4; ++i) xp[i] = ld16(xr, voff + i * 64u, s * 32768u);
}
DEV void put_x(char* sm, unsigned base, unsigned w, unsigned l, const u4 (&xp)[4]) {
#pragma unroll
  for (int i = 0; i < 4; ++i) lds16(sm, base + (4u * w + i) * 1024u + l * 16u) = xp[i];
}

DEV void role_s2(const Params& p, unsigned h, char* sm) {
  constexpr int LAG = 2, R = LAG + 1;
  constexpr unsigned L_X = 0, L_QK = 65536;
  const unsigned l = threadIdx.x & 63, w = rfl(threadIdx.x >> 6), c = l & 15, g = l >> 4;
  const rsrc_t ws = mk(p.ws, WS_BYTES), xr = mk(p.bx, 2u << 20);
  bool alive = true;

  u4 xp[4];
  load_x(xr, w, c, g, 0, xp);
  u4 wa[16], wb[16];
  {
    const rsrc_t wr = mk(w < 4 ? p.wq : p.wk, 4u << 20);
    const unsigned voff = (8u * g * 4096u + h * 128u + (w & 3) * 32u + 2u * c) * 2u;
#pragma unroll
    for (int f = 0; f < 16; ++f) load_pair(wr, voff, 32u * f * 8192u, 8192u, wa[f], wb[f]);
  }
  put_x(sm, L_X, w, l, xp);
  bar();
  STAMP(15, 8 + w);

  const unsigned km_off = O_KM + h * 2048u + (g * 8u + 2u * (c >> 2) + (c & 1)) * 16u;
  int slot = 0;
  for (int it = 0; it < NSEG + LAG; ++it) {
    if (it < NSEG) {
      if (it + 1 < NSEG) load_x(xr, w, c, g, it + 1, xp);
      f4 acc[2][2] = {};
      const unsigned xb = L_X + (it & 1) * 32768u + l * 16u;
#pragma unroll
      for (int ks = 0; ks < 16; ++ks)
#pragma unroll
        for (int t = 0; t < 2; ++t) {
          const u4 x = lds16(sm, xb + (t * 16 + ks) * 1024u);
          acc[0][t] = mma(wa[ks], x, acc[0][t]);
          acc[1][t] = mma(wb[ks], x, acc[1][t]);
        }
#pragma unroll
      for (int t = 0; t < 2; ++t) {
        const u4 o = {pk(acc[0][t][0], acc[0][t][1]), pk(acc[0][t][2], acc[0][t][3]), pk(acc[1][t][0], acc[1][t][1]),
                      pk(acc[1][t][2], acc[1][t][3])};
        lds16(sm, L_QK + slot * 16384u + (w >> 2) * 8192u + (t * 4u + (w & 3)) * 1024u + l * 16u) = o;
        if (w >= 4) st16c(o, ws, O_KX + h * 8192u + ((w & 3) * 2u + t) * 1024u + l * 16u, it * 262144u);
      }
      if (it + 1 < NSEG) put_x(sm, L_X + ((it + 1) & 1) * 32768u, w, l, xp);
      drain();
    }
    bar();
    if (it < NSEG && w == 7 && l == 0) st4c(it + 1, ws, F_KX + h * 4u, 0);
    slot = slot == R - 1 ? 0 : slot + 1;
    const int s = it - LAG;
    if (s >= 0 && w < 2) {
      const unsigned qb = L_QK + slot * 16384u + l * 16u;
      u4 q[4];
      f4 s0 = {0, 0, 0, 0}, s1 = {0, 0, 0, 0}, sm_ = {0, 0, 0, 0};
#pragma unroll
      for (int m = 0; m < 4; ++m) {
        q[m] = lds16(sm, qb + (w * 4u + m) * 1024u);
        s0 = mma(lds16(sm, qb + 8192u + m * 1024u), q[m], s0);
        s1 = mma(lds16(sm, qb + 12288u + m * 1024u), q[m], s1);
      }
      wait_flags(ws, F_KM + h * 4u, s + 1, alive);
#pragma unroll
      for (int m = 0; m < 4; ++m) sm_ = mma(ld16c(ws, km_off + m * 512u, s * 65536u), q[m], sm_);
      const unsigned tq = 16u * w + c;
      const float ninf = -__builtin_inff();
#pragma unroll
      for (int r = 0; r < 4; ++r) {
        s0[r] = 4u * g + r <= tq ? s0[r] : ninf;
        s1[r] = 16u + 4u * g + r <= tq ? s1[r] : ninf;
      }
      float mx = fmaxf(fmaxf(fmaxf(s0[0], s0[1]), fmaxf(s0[2], s0[3])), fmaxf(fmaxf(s1[0], s1[1]), fmaxf(s1[2], s1[3])));
      mx = group_max(fmaxf(mx, fmaxf(sm_[0], sm_[1])));
      const float mc = -mx * SCALE_LOG2E;
      float e[10];
#pragma unroll
      for (int r = 0; r < 4; ++r) {
        e[r] = __builtin_amdgcn_exp2f(fmaf(s0[r], SCALE_LOG2E, mc));
        e[4 + r] = __builtin_amdgcn_exp2f(fmaf(s1[r], SCALE_LOG2E, mc));
      }
      e[8] = __builtin_amdgcn_exp2f(fmaf(sm_[0], SCALE_LOG2E, mc));
      e[9] = __builtin_amdgcn_exp2f(fmaf(sm_[1], SCALE_LOG2E, mc));
      const float sum = group_sum(((e[0] + e[1]) + (e[2] + e[3])) + ((e[4] + e[5]) + (e[6] + e[7])) + (e[8] + e[9]));
      const float rs = __builtin_amdgcn_rcpf(sum);
      const u4 px = {pk(e[0] * rs, e[1] * rs), pk(e[2] * rs, e[3] * rs), pk(e[4] * rs, e[5] * rs), pk(e[6] * rs, e[7] * rs)};
      st16c(px, ws, O_PX + (w * 32u + h) * 1024u + l * 16u, s * 65536u);
      const unsigned dm = pk(e[8] * rs, e[9] * rs);
      auto r16 = __builtin_amdgcn_permlane16_swap(dm, dm, false, false);
      auto ra = __builtin_amdgcn_permlane32_swap(r16[0], r16[0], false, false);
      auto rb = __builtin_amdgcn_permlane32_swap(r16[1], r16[1], false, false);
      const u4 pm = {ra[0], rb[0], ra[1], rb[1]};
      if (g == 0) st16c(pm, ws, O_PM + (w * 8u + (h >> 2)) * 1024u + ((h & 3) * 16u + c) * 16u, s * 16384u);
      drain();
      if (l == 0) st4c(s + 1, ws, F_PX + (w * 32u + h) * 4u, 0);
      if (w == 0) STAMP(2, s);
    }
  }
}

DEV void role_vu(const Params& p, unsigned h, char* sm) {
  constexpr unsigned L_X = 0, L_V = 65536;
  const unsigned l = threadIdx.x & 63, w = rfl(threadIdx.x >> 6), c = l & 15, g = l >> 4;
  const rsrc_t ws = mk(p.ws, WS_BYTES), xr = mk(p.bx, 2u << 20);

  u4 xp[4];
  load_x(xr, w, c, g, 0, xp);
  u4 wv[16], wo[4][4];
  {
    const rsrc_t wr = mk(p.wv, 4u << 20);
    const unsigned voff = (8u * g * 4096u + h * 128u + w * 16u + c) * 2u;
#pragma unroll
    for (int f = 0; f < 16; ++f) wv[f] = load_single(wr, voff, 32u * f * 8192u, 8192u);
    load_wo(p, h, w, c, g, wo);
  }
  put_x(sm, L_X, w, l, xp);
  bar();
  STAMP(15, 8 + w);

  const unsigned v_off = L_V + ((w >> 1) * 64u + (2u * (w & 1) + (g >> 1)) * 16u + c) * 16u + (g & 1) * 8u;
  for (int s = 0; s < NSEG; ++s) {
    if (s + 1 < NSEG) load_x(xr, w, c, g, s + 1, xp);
    f4 acc[2] = {};
    const unsigned xb = L_X + (s & 1) * 32768u + l * 16u;
#pragma unroll
    for (int ks = 0; ks < 16; ++ks)
#pragma unroll
      for (int t = 0; t < 2; ++t) acc[t] = mma(wv[ks], lds16(sm, xb + (t * 16 + ks) * 1024u), acc[t]);
#pragma unroll
    for (int t = 0; t < 2; ++t)
      *reinterpret_cast<u2*>(sm + v_off + (s & 1) * 8192u + t * 4096u) = u2{pk(acc[t][0], acc[t][1]), pk(acc[t][2], acc[t][3])};
    if (s + 1 < NSEG) put_x(sm, L_X + ((s + 1) & 1) * 32768u, w, l, xp);
    bar();
    f4 u[4][2] = {};
#pragma unroll
    for (int m = 0; m < 4; ++m)
#pragma unroll
      for (int t = 0; t < 2; ++t) {
        const u4 v = lds16(sm, L_V + (s & 1) * 8192u + (t * 4 + m) * 1024u + l * 16u);
#pragma unroll
        for (int i = 0; i < 4; ++i) u[i][t] = mma(v, wo[i][m], u[i][t]);
      }
#pragma unroll
    for (int i = 0; i < 4; ++i) {
      const u4 o = {pk(u[i][0][0], u[i][0][1]), pk(u[i][0][2], u[i][0][3]), pk(u[i][1][0], u[i][1][1]),
                    pk(u[i][1][2], u[i][1][3])};
      st16c(o, ws, O_U + ((4u * w + i) * 32u + h) * 1024u + l * 16u, s * 1048576u);
    }
    drain();
    if (l == 0) st4c(s + 1, ws, F_U + (w * 32u + h) * 4u, 0);
    if (w == 0) STAMP(2, s);
  }
}

DEV void role_v2(const Params& p, unsigned j, char* sm) {
  constexpr unsigned ROW = 144, PART = 16 * ROW, BUF = 8 * PART;
  const unsigned l = threadIdx.x & 63, w = rfl(threadIdx.x >> 6), c = l & 15, g = l >> 4;
  const unsigned b = j >> 1, qt = j & 1;
  const rsrc_t ws = mk(p.ws, WS_BYTES), outr = mk(p.out, 8u << 20);
  bool alive = true;

  const unsigned hq = 4u * w + (l & 3);
  const unsigned uf_off = F_U + ((b >> 1) * 32u + hq) * 4u;
  const unsigned pf_off = (l & 4) ? F_M + ((b >> 1) * 32u + hq) * 4u : F_PX + (qt * 32u + hq) * 4u;
  const unsigned u_off = O_U + (2u * b * 32u + 4u * w) * 1024u + l * 16u;
  const unsigned m_off = O_M2 + 2u * b * 8192u + w * 1024u + l * 16u;
  const unsigned px_off = O_PX + (qt * 32u + 4u * w) * 1024u + l * 16u;
  const unsigned pm_off = O_PM + (qt * 8u + w) * 1024u + l * 16u;
  const unsigned out_off = ((16u * qt + (threadIdx.x >> 5)) * 1024u + 512u + 32u * b + (threadIdx.x & 31)) * 4u;
  unsigned u_seen = 0, p_seen = 0;

  for (int s = 0; s < NSEG; ++s) {
    if (!all_lanes(u_seen > (unsigned)s)) u_seen = wait_flags(ws, uf_off, s + 1, alive);
    u4 ua[2][4];
#pragma unroll
    for (int par = 0; par < 2; ++par)
#pragma unroll
      for (int i = 0; i < 4; ++i) ua[par][i] = ld16c(ws, u_off + par * 32768u + i * 1024u, s * 1048576u);
    if (!all_lanes(p_seen > (unsigned)s)) p_seen = wait_flags(ws, pf_off, s + 1, alive);
    u4 px[4];
#pragma unroll
    for (int i = 0; i < 4; ++i) px[i] = ld16c(ws, px_off + i * 1024u, s * 65536u);
    const u4 pm = ld16c(ws, pm_off, s * 16384u);
    const u4 m0 = ld16c(ws, m_off, s * 262144u), m1 = ld16c(ws, m_off + 8192u, s * 262144u);
    f4 a0 = {0, 0, 0, 0}, a1 = {0, 0, 0, 0};
#pragma unroll
    for (int i = 0; i < 4; ++i) {
      a0 = mma(ua[0][i], px[i], a0);
      a1 = mma(ua[1][i], px[i], a1);
    }
    a0 = mma(m0, pm, a0);
    a1 = mma(m1, pm, a1);
    const unsigned rb = (s & 1) * BUF;
    *reinterpret_cast<f4*>(sm + rb + w * PART + c * ROW + g * 32u) = f4{a0[0], a1[0], a0[1], a1[1]};
    *reinterpret_cast<f4*>(sm + rb + w * PART + c * ROW + g * 32u + 16u) = f4{a0[2], a1[2], a0[3], a1[3]};
    bar();
    float v = 0.f;
#pragma unroll
    for (int k = 0; k < 8; ++k)
      v += *reinterpret_cast<const float*>(sm + rb + k * PART + (threadIdx.x >> 5) * ROW + (threadIdx.x & 31) * 4u);
    __builtin_amdgcn_raw_buffer_store_b32(__float_as_uint(rbf(v)), outr, out_off, s * 131072u, 0);
    if (w == 0) STAMP(2, s);
  }
}

DEV f2 cmul(f2 a, f2 b) {
  float re, im;
  asm("v_mul_f32 %0, %3, %5\n\tv_mul_f32 %1, %3, %4\n\tv_fma_f32 %0, %2, %4, -%0\n\tv_fma_f32 %1, %2, %5, %1"
      : "=&v"(re), "=&v"(im) : "v"(a[0]), "v"(a[1]), "v"(b[0]), "v"(b[1]));
  return f2{re, im};
}
DEV f2 cfma(f2 a, f2 b, f2 c) {
  float re, im;
  asm("v_fma_f32 %0, -%3, %5, %6\n\tv_fma_f32 %1, %3, %4, %7\n\tv_fma_f32 %0, %2, %4, %0\n\tv_fma_f32 %1, %2, %5, %1"
      : "=&v"(re), "=&v"(im) : "v"(a[0]), "v"(a[1]), "v"(b[0]), "v"(b[1]), "v"(c[0]), "v"(c[1]));
  return f2{re, im};
}
DEV f2 from_group(float re, float im, unsigned c, int k) {
  return f2{__shfl(re, (int)c + 16 * k, 64), __shfl(im, (int)c + 16 * k, 64)};
}

DEV void role_a(const Params& p, unsigned ci, char* sm) {
  constexpr unsigned GS = 272, KS = 4 * GS, TS = 32 * KS + 32, L_TAB = 4 * TS;
  const unsigned tid = threadIdx.x, l = tid & 63, w = rfl(tid >> 6), c = l & 15, g = l >> 4;
  const rsrc_t ws = mk(p.ws, WS_BYTES), xr = mk(p.ax, 2u << 20), outr = mk(p.out, 8u << 20);
  bool alive = true;

  {
    const unsigned voff = ((64u * ci + 16u * (c >> 2) + 4u * (w >> 1) + (c & 3)) * 512u + 256u * (w & 1) + 8u * g) * 2u;
    u4 xp[8];
#pragma unroll
    for (int i = 0; i < 8; ++i) xp[i] = ld16(xr, voff + i * 64u, 0);
#pragma unroll
    for (int i = 0; i < 8; ++i) lds16(sm, (8u * w + i) * 1024u + l * 16u) = xp[i];
  }
  f2 lam64;
  {
    const float lr = bf2f(((const unsigned short*)p.lre)[tid]), li = bf2f(((const unsigned short*)p.lim)[tid]);
    const float dt = rbf(expf(bf2f(((const unsigned short*)p.ldt)[tid])));
    const float er = expf(dt * lr);
    float sn, cs;
    sincosf(dt * li, &sn, &cs);
    f2 lam = {er * cs, er * sn};
    asm volatile("s_nop 1" : "+v"(lam));
    const float nr = lam[0] - 1.0f, ni = lam[1];
    f2 coef;
    if (fabsf(lr) >= fabsf(li)) {
      const float t = li / lr, d = 1.0f / (lr + li * t);
      coef = f2{smul(nr + smul(ni, t), d), smul(ni - smul(nr, t), d)};
    } else {
      const float t = lr / li, d = 1.0f / (lr * t + li);
      coef = f2{smul(smul(nr, t) + ni, d), smul(smul(ni, t) - nr, d)};
    }
    f2 l16 = cmul(lam, lam);
    l16 = cmul(l16, l16);
    l16 = cmul(l16, l16);
    l16 = cmul(l16, l16);
    lam64 = cmul(l16, l16);
    lam64 = cmul(lam64, lam64);
    *reinterpret_cast<f4*>(sm + L_TAB + tid * 32u) = f4{lam[0], lam[1], coef[0], coef[1]};
    *reinterpret_cast<f2*>(sm + L_TAB + tid * 32u + 16u) = l16;
  }
  bar();

  f4 hr[4][4] = {}, hi[4][4] = {};
  {
    const rsrc_t br = mk(p.bre, 1u << 19), bi = mk(p.bim, 1u << 19);
    const unsigned voff = ((64u * w + c) * 512u + 8u * g) * 2u;
    u4 fr[4], fi[4];
#pragma unroll
    for (int k = 0; k < 4; ++k) {
      fr[k] = ld16(br, voff + k * 64u, 0);
      fi[k] = ld16(bi, voff + k * 64u, 0);
    }
#pragma unroll
    for (int n = 0; n < 64; ++n) {
      const int ct = n >> 4, ks = n & 15;
      const u4 r = fr[n & 3], i = fi[n & 3];
      fence();
      if (n + 4 < 64) {
        fr[n & 3] = ld16(br, voff + ((n + 4) & 15) * 64u, ((n + 4) >> 4) * 16384u);
        fi[n & 3] = ld16(bi, voff + ((n + 4) & 15) * 64u, ((n + 4) >> 4) * 16384u);
      }
#pragma unroll
      for (int t = 0; t < 4; ++t) {
        const u4 x = lds16(sm, (t * 16 + ks) * 1024u + l * 16u);
        hr[ct][t] = mma(x, r, hr[ct][t]);
        hi[ct][t] = mma(x, i, hi[ct][t]);
      }
    }
  }
  __builtin_amdgcn_sched_barrier(0);
  asm volatile("s_nop 7\n\ts_nop 7" ::: "memory");
  __builtin_amdgcn_sched_barrier(0);
#pragma unroll
  for (int ct = 0; ct < 4; ++ct) {
    const unsigned ch = 64u * w + 16u * ct + c;
    const f4 tb = *reinterpret_cast<const f4*>(sm + L_TAB + ch * 32u);
    const f2 l16 = *reinterpret_cast<const f2*>(sm + L_TAB + ch * 32u + 16u);
    const f2 lam = {tb[0], tb[1]}, coef = {tb[2], tb[3]};
    f2 hh = {0.f, 0.f};
#pragma unroll
    for (int i = 0; i < 16; ++i) {
      hh = cfma(lam, hh, cmul(coef, f2{hr[ct][i >> 2][i & 3], hi[ct][i >> 2][i & 3]}));
      hr[ct][i >> 2][i & 3] = hh[0];
      hi[ct][i >> 2][i & 3] = hh[1];
    }
    f2 e = from_group(hh[0], hh[1], c, 0);
#pragma unroll
    for (int k = 1; k < 4; ++k) e = cfma(l16, e, from_group(hh[0], hh[1], c, k));
    if (g == 0) st8c(__builtin_bit_cast(u2, e), ws, O_AE + ci * 4096u + ch * 8u, 0);
  }
  drain();
  bar();
  if (tid == 0) st4c(1u, ws, F_A + ci * 4u, 0);
  wait_flags(ws, F_A + (l & 31) * 4u, (l & 31) < ci ? 1u : 0u, alive);
  {
    f2 hin = {0.f, 0.f};
#pragma unroll 8
    for (unsigned k = 0; k < ci; ++k) hin = cfma(lam64, hin, __builtin_bit_cast(f2, ld8c(ws, O_AE + tid * 8u, k * 4096u)));
    *reinterpret_cast<f2*>(sm + L_TAB + tid * 32u + 24u) = hin;
  }
  bar();
#pragma unroll
  for (int ct = 0; ct < 4; ++ct) {
    const unsigned ch = 64u * w + 16u * ct + c;
    const f4 t0 = *reinterpret_cast<const f4*>(sm + L_TAB + ch * 32u);
    const f4 t1 = *reinterpret_cast<const f4*>(sm + L_TAB + ch * 32u + 16u);
    const f2 lam = {t0[0], t0[1]}, l16 = {t1[0], t1[1]};
    f2 z = {t1[2], t1[3]};
#pragma unroll
    for (int k = 0; k < 3; ++k) {
      const f2 n = cfma(l16, z, from_group(hr[ct][3][3], hi[ct][3][3], c, k));
      z = (unsigned)k < g ? n : z;
    }
    const unsigned hb = g * TS + (ch >> 5) * KS + ((ch >> 3) & 3) * GS + (ch & 7) * 2u;
#pragma unroll
    for (int i = 0; i < 16; ++i) {
      z = cmul(lam, z);
      const unsigned d = pk(2.0f * (hr[ct][i >> 2][i & 3] + z[0]), -2.0f * (hi[ct][i >> 2][i & 3] + z[1]));
      lds_put2(sm, hb + i * 16u, d);
      lds_put2(sm, hb + 16u * KS + i * 16u, d >> 16);
    }
  }
  bar();
  f4 y[4][4] = {};
  {
    const rsrc_t cr = mk(p.cre, 1u << 19), cm = mk(p.cim, 1u << 19);
    const unsigned voff = ((64u * w + c) * 512u + 8u * g) * 2u;
    u4 fa[2][4];
#pragma unroll
    for (int k = 0; k < 2; ++k)
#pragma unroll
      for (int d = 0; d < 4; ++d) fa[k][d] = ld16(cr, voff + k * 64u, d * 16384u);
#pragma unroll
    for (int ks = 0; ks < 32; ++ks) {
      u4 a[4];
      fence();
#pragma unroll
      for (int d = 0; d < 4; ++d) {
        a[d] = fa[ks & 1][d];
        if (ks + 2 < 32) fa[ks & 1][d] = ld16(ks + 2 < 16 ? cr : cm, voff + ((ks + 2) & 15) * 64u, d * 16384u);
      }
#pragma unroll
      for (int t = 0; t < 4; ++t) {
        const u4 hb = lds16(sm, t * TS + ks * KS + g * GS + c * 16u);
#pragma unroll
        for (int d = 0; d < 4; ++d) y[d][t] = mma(a[d], hb, y[d][t]);
      }
    }
  }
  const rsrc_t dr = mk(p.dsk, 1024);
#pragma unroll
  for (int d = 0; d < 4; ++d) {
    const unsigned col = 64u * w + 16u * d + 4u * g;
    const u2 dk = ld8(dr, col * 2u, 0);
#pragma unroll
    for (int t = 0; t < 4; ++t) {
      const unsigned row = 64u * ci + 16u * t + c;
      const u2 xv = ld8(xr, (row * 512u + col) * 2u, 0);
      const f4 o = {y[d][t][0] + rbf(smul(bf2f(xv[0] & 0xffffu), bf2f(dk[0] & 0xffffu))),
                    y[d][t][1] + rbf(smul(bf2f(xv[0] >> 16), bf2f(dk[0] >> 16))),
                    y[d][t][2] + rbf(smul(bf2f(xv[1] & 0xffffu), bf2f(dk[1] & 0xffffu))),
                    y[d][t][3] + rbf(smul(bf2f(xv[1] >> 16), bf2f(dk[1] >> 16)))};
      __builtin_amdgcn_raw_buffer_store_b128(__builtin_bit_cast(u4, o), outr, (row * 1024u + col) * 4u, 0, 0);
    }
  }
  if (w == 0) STAMP(2, 0);
}

__global__ void __launch_bounds__(512, 2) fused_kernel(Params p) {
  extern __shared__ __attribute__((aligned(16))) char smem[];
  const unsigned x = blockIdx.x & 7, r = blockIdx.x >> 3;
  if (x == 1 || x == 2) {
    role_v1(p, (x - 1) * 32u + r, smem);
  } else if (x < 4) {
    const unsigned h = (x ? 16u : 0u) + (r >> 1);
    if (r & 1) role_vm(p, h, smem); else role_s1(p, h, smem);
  } else {
    const unsigned i = (x - 4) * 8u + (r >> 2);
    switch (r & 3) {
      case 0: role_s2(p, i, smem); break;
      case 1: role_vu(p, i, smem); break;
      case 2: role_v2(p, i, smem); break;
      default: role_a(p, i, smem); break;
    }
  }
}

extern "C" void kernel_launch(void* const* d_in, const int* in_sizes, int n_in, void* d_out, int out_size, void* d_ws,
                              size_t ws_size, hipStream_t stream) {
  Params p;
  p.ax = d_in[0]; p.lre = d_in[1]; p.lim = d_in[2]; p.bre = d_in[3]; p.bim = d_in[4]; p.cre = d_in[5];
  p.cim = d_in[6]; p.ldt = d_in[7]; p.dsk = d_in[8]; p.bx = d_in[9]; p.mem0 = d_in[10]; p.wq = d_in[11];
  p.wk = d_in[12]; p.wv = d_in[13]; p.wo = d_in[14];
  p.out = (float*)d_out;
  p.ws = (char*)d_ws;
  (void)hipFuncSetAttribute((const void*)fused_kernel, hipFuncAttributeMaxDynamicSharedMemorySize, DYN_LDS);
  (void)hipMemsetAsync(d_ws, 0, FLAG_BYTES, stream);
  fused_kernel<<<256, 512, DYN_LDS, stream>>>(p);
}
```

```cpp
#include <hip/hip_runtime.h>
#include <hip/hip_bf16.h>
#include <stdint.h>

typedef __attribute__((ext_vector_type(2))) unsigned u2;
typedef __attribute__((ext_vector_type(4))) unsigned u4;
typedef __attribute__((ext_vector_type(8))) unsigned u8;
typedef __attribute__((ext_vector_type(16))) unsigned u16v;
typedef __attribute__((ext_vector_type(32))) unsigned u32v;
typedef __attribute__((ext_vector_type(2))) float f2;
typedef __attribute__((ext_vector_type(4))) float f4;
typedef __attribute__((ext_vector_type(2))) __bf16 bf2;
typedef __attribute__((ext_vector_type(8))) __bf16 bf8;
typedef __attribute__((ext_vector_type(16))) __bf16 bf16v;
typedef __amdgpu_buffer_rsrc_t rsrc_t;
#define DEV __device__ __forceinline__

struct Params {
  const void *ax, *lre, *lim, *bre, *bim, *cre, *cim, *ldt, *dsk, *bx, *mem0, *wq, *wk, *wv, *wo;
  float* out;
  char* ws;
};

#define WSLEEP 4
constexpr int NSEG = 64;
constexpr unsigned SPIN_CAP = 400000u;
constexpr unsigned DYN_LDS = 156u * 1024u;

constexpr unsigned FL = 128, F_INIT = 0, F_KM = 64 * FL, F_KX = 96 * FL, F_PX = 128 * FL, F_M = 192 * FL, F_U = 224 * FL,
                   F_A = 256 * FL, FLAG_BYTES = 288 * FL;
constexpr unsigned O_AE = 5u << 20, O_DIAG = 6u << 20;
constexpr unsigned O_KX = 8u << 20, O_KM = 24u << 20, O_PX = 28u << 20, O_PM = 32u << 20, O_M2 = 34u << 20,
                   O_U = 50u << 20, WS_BYTES = 114u << 20;
constexpr unsigned OOB = 1u << 30;
constexpr unsigned B_MEM = 0, B_P = 63, B_M = 67, B_END = 99, B_PROBE = 100;

DEV rsrc_t mk(const void* p, unsigned bytes) {
  return __builtin_amdgcn_make_buffer_rsrc((void*)p, 0, bytes, 0x00020000);
}
DEV unsigned rfl(unsigned v) { return __builtin_amdgcn_readfirstlane(v); }
DEV u4 ld16(rsrc_t r, unsigned v, unsigned s) { return __builtin_amdgcn_raw_buffer_load_b128(r, v, s, 0); }
DEV u2 ld8(rsrc_t r, unsigned v, unsigned s) { return __builtin_amdgcn_raw_buffer_load_b64(r, v, s, 0); }
DEV unsigned ld4(rsrc_t r, unsigned v, unsigned s) { return __builtin_amdgcn_raw_buffer_load_b32(r, v, s, 0); }
DEV unsigned ld2(rsrc_t r, unsigned v, unsigned s) { return __builtin_amdgcn_raw_buffer_load_b16(r, v, s, 0); }
DEV u4 ld16c(rsrc_t r, unsigned v, unsigned s) { return __builtin_amdgcn_raw_buffer_load_b128(r, v, s, 16); }
DEV u2 ld8c(rsrc_t r, unsigned v, unsigned s) { return __builtin_amdgcn_raw_buffer_load_b64(r, v, s, 16); }
DEV unsigned ld4c(rsrc_t r, unsigned v, unsigned s) { return __builtin_amdgcn_raw_buffer_load_b32(r, v, s, 16); }
DEV void st16c(u4 d, rsrc_t r, unsigned v, unsigned s) { __builtin_amdgcn_raw_buffer_store_b128(d, r, v + s, 0, 16); }
DEV void st8c(u2 d, rsrc_t r, unsigned v, unsigned s) { __builtin_amdgcn_raw_buffer_store_b64(d, r, v, s, 16); }
DEV void st4c(unsigned d, rsrc_t r, unsigned v, unsigned s) { __builtin_amdgcn_raw_buffer_store_b32(d, r, v, s, 16); }
DEV void fence() { asm volatile("" ::: "memory"); }
DEV void drain() { asm volatile("s_waitcnt vmcnt(0)" ::: "memory"); }
DEV void bar() { asm volatile("s_waitcnt lgkmcnt(0)\n\ts_barrier" ::: "memory"); }
DEV bool all_lanes(bool ok) { return __builtin_amdgcn_ballot_w64(!ok) == 0ull; }

DEV float bf2f(unsigned h) { return __uint_as_float(h << 16); }
DEV unsigned pk(float a, float b) {
  f2 v = {a, b};
  return __builtin_bit_cast(unsigned, __builtin_convertvector(v, bf2));
}
DEV float rbf(float a) { return bf2f(pk(a, a) & 0xffffu); }
DEV float smul(float a, float b) {
  float r;
  asm("v_mul_f32 %0, %1, %2" : "=v"(r) : "v"(a), "v"(b));
  return r;
}
DEV float sadd(float a, float b) {
  float r;
  asm("v_add_f32 %0, %1, %2" : "=v"(r) : "v"(a), "v"(b));
  return r;
}
DEV f4 mma(u4 a, u4 b, f4 c) {
  return __builtin_amdgcn_mfma_f32_16x16x32_bf16(__builtin_bit_cast(bf8, a), __builtin_bit_cast(bf8, b), c, 0, 0, 0);
}
DEV f4 smma(u4 a, u8 b, f4 c, unsigned idx) {
  return __builtin_amdgcn_smfmac_f32_16x16x64_bf16(__builtin_bit_cast(bf8, a), __builtin_bit_cast(bf16v, b), c,
                                                   (int)idx, 0, 0);
}
DEV float swap32_add(float a, float b) {
  auto r = __builtin_amdgcn_permlane32_swap(__float_as_uint(a), __float_as_uint(b), false, false);
  return __uint_as_float(r[0]) + __uint_as_float(r[1]);
}
DEV float swap16_add(float a, float b) {
  auto r = __builtin_amdgcn_permlane16_swap(__float_as_uint(a), __float_as_uint(b), false, false);
  return __uint_as_float(r[0]) + __uint_as_float(r[1]);
}
DEV float group_max(float m) {
  auto r = __builtin_amdgcn_permlane16_swap(__float_as_uint(m), __float_as_uint(m), false, false);
  m = fmaxf(__uint_as_float(r[0]), __uint_as_float(r[1]));
  auto q = __builtin_amdgcn_permlane32_swap(__float_as_uint(m), __float_as_uint(m), false, false);
  return fmaxf(__uint_as_float(q[0]), __uint_as_float(q[1]));
}
DEV float group_sum(float m) {
  const float t = swap16_add(m, m);
  return swap32_add(t, t);
}
DEV u4& lds16(char* sm, unsigned off) { return *reinterpret_cast<u4*>(sm + off); }
DEV void lds_put2(char* sm, unsigned off, unsigned v) { *reinterpret_cast<unsigned short*>(sm + off) = (unsigned short)v; }

#define STAMP(slot, idx) do { } while (0)

#define TSTAMP(idx) do { } while (0)

DEV unsigned wait_flags(rsrc_t ws, unsigned off, unsigned need, bool& alive) {
  unsigned v = 0;
  for (unsigned it = 0; alive; ++it) {
    fence();
    v = ld4c(ws, off, 0);
    if (all_lanes(v >= need)) break;
    if (it > SPIN_CAP) alive = false;
    __builtin_amdgcn_s_sleep(WSLEEP);
  }
  fence();
  return v;
}
DEV unsigned crit_base(const Params& p) {
  const unsigned lo = (unsigned)(uintptr_t)p.ws;
  return ((lo + 65536u + 0x1fffffu) & ~0x1fffffu) - lo;
}
DEV unsigned wait_init(const Params& p, rsrc_t ws, unsigned n, bool& alive) {
  const unsigned l = threadIdx.x & 63, w = threadIdx.x >> 6;
  const unsigned v = wait_flags(ws, F_INIT + (l ? (l & (64u / n - 1u)) + w * (64u / n) : 0u) * FL, 1u, alive);
  return crit_base(p) + (rfl(v) == 2u ? 8192u : 0u);
}

DEV void load_pair(rsrc_t r, unsigned voff, unsigned soff, unsigned rstride, u4& a, u4& b) {
  unsigned d[8];
#pragma unroll
  for (int j = 0; j < 8; ++j) d[j] = ld4(r, voff, soff + j * rstride);
#pragma unroll
  for (int j = 0; j < 4; ++j) {
    a[j] = __builtin_amdgcn_perm(d[2 * j + 1], d[2 * j], 0x05040100u);
    b[j] = __builtin_amdgcn_perm(d[2 * j + 1], d[2 * j], 0x07060302u);
  }
}
DEV u4 load_single(rsrc_t r, unsigned voff, unsigned soff, unsigned rstride) {
  unsigned d[8];
  u4 a;
#pragma unroll
  for (int j = 0; j < 8; ++j) d[j] = ld2(r, voff, soff + j * rstride);
#pragma unroll
  for (int j = 0; j < 4; ++j) a[j] = __builtin_amdgcn_perm(d[2 * j + 1], d[2 * j], 0x05040100u);
  return a;
}
DEV u8 cat(u4 a, u4 b) { return __builtin_shufflevector(a, b, 0, 1, 2, 3, 4, 5, 6, 7); }
DEV u16v cat(u8 a, u8 b) { return __builtin_shufflevector(a, b, 0, 1, 2, 3, 4, 5, 6, 7, 8, 9, 10, 11, 12, 13, 14, 15); }
DEV u32v cat(u8 a, u8 b, u8 c, u8 d) {
  return __builtin_shufflevector(cat(a, b), cat(c, d), 0, 1, 2, 3, 4, 5, 6, 7, 8, 9, 10, 11, 12, 13, 14, 15, 16, 17, 18,
                                 19, 20, 21, 22, 23, 24, 25, 26, 27, 28, 29, 30, 31);
}
DEV u4 srd(const void* p, unsigned bytes) {
  return u4{(unsigned)(uintptr_t)p, (unsigned)((uintptr_t)p >> 32) & 0xffffu, bytes, 0x00020000u};
}
DEV unsigned lds_base(char* sm) { return (unsigned)(uintptr_t)(__attribute__((address_space(3))) char*)sm; }
DEV unsigned swz(unsigned g) { return (g & 1) | ((g & 2) << 1); }
DEV void sched() { __builtin_amdgcn_sched_barrier(0); }

DEV u4 load_mem0(const Params& p, unsigned w, unsigned c, unsigned g) {
  const rsrc_t r = mk(p.mem0, 8192);
  const unsigned cg = 4 * w + g;
  const unsigned voff = ((c >> 1) * 512u + 32u * (cg >> 1) + (cg & 1) + 4u * (c & 1)) * 2u;
  u4 a;
#pragma unroll
  for (int i = 0; i < 4; ++i) a[i] = ld2(r, voff, 16 * i) | (ld2(r, voff, 16 * i + 4) << 16);
  return a;
}
constexpr float SCALE_LOG2E = 0.08838834764831845f * 1.4426950408889634f;

#define A_STAMP(r)
#define A_SAVE(r, slot)
#define A_NEXT
#define A_KXWAIT "s_waitcnt vmcnt(2)\n"
#define A_DIAG_OUT
#define A_DIAG_CLOB
#define A_CLK(r)
#define A_SAVECLK
#define A_DIAG_DECL
#define A_SCLOB "s80", "s81", "s82", "s83", "s84", "s85", "s86", "s87", "s88", "s89", "s90", "s91", "vcc", "scc", "memory"
#define SMF(d, a, b, i) "v_smfmac_f32_16x16x64_bf16 v[" d "], v[" a "], v[" b "], " i "\n"
#define MFM(d, a, b, c) "v_mfma_f32_16x16x32_bf16 v[" d "], v[" a "], v[" b "], " c "\n"
#define DPP_ALL " row_mask:0xf bank_mask:0xf\n"
#define A_POLL_MEM \
  ".Lpoll%=:\n" \
  "buffer_load_dwordx4 v[148:151], v129, %[ws], %[mem] offen sc1\n" \
  "s_sub_u32 %[cap], %[cap], 1\n" \
  "s_cbranch_scc1 .Lexit%=\n" \
  "s_waitcnt vmcnt(0)\n" \
  "ds_write_b128 v130, v[148:151]\n" \
  "v_max3_u32 v152, v148, v149, v150\n" \
  "v_max_u32 v152, v152, v151\n" \
  "v_cmp_eq_u32 vcc, -1, v152\n" \
  "s_cbranch_vccnz .Lpoll%=\n"
#define A_READ_MEM \
  "s_waitcnt lgkmcnt(0)\n" \
  "s_barrier\n" \
  A_CLK("s[72:73]") \
  "ds_read_b128 v[152:155], v131\n" \
  "ds_read_b128 v[156:159], v132\n" \
  "ds_read_b128 v[160:163], v133\n" \
  "ds_read_b128 v[164:167], v134\n" \
  "ds_read_b128 v[168:171], v135\n" \
  "ds_read_b128 v[172:175], v136\n" \
  "ds_read_b128 v[176:179], v137\n"
#define D_S1 0
#define D_S0 0
#define D_VM 0
#define D_V1 0
#define STR_(x) #x
#define SLEEP(x) "s_sleep " STR_(x) "\n"
#define WL(n) "s_waitcnt lgkmcnt(" #n ")\n"
#define WV(n) "s_waitcnt vmcnt(" #n ")\n"

DEV void role_s1(const Params& p, unsigned h, char* sm) {
  constexpr unsigned L_MEM = 0, L_Q = 8192, L_K = 10240;
  const unsigned l = threadIdx.x & 63, w = rfl(threadIdx.x >> 6), c = l & 15, g = l >> 4;
  const rsrc_t ws = mk(p.ws, WS_BYTES);
  bool alive = true;
  STAMP(15, w);

  u8 wa[8], wb[8];
  {
    const rsrc_t wr = mk(w < 4 ? p.wq : p.wk, 4u << 20);
    const unsigned voff = ((16u * (g & 1) + (g >> 1)) * 4096u + h * 128u + (w & 3) * 32u + 2u * c) * 2u;
#pragma unroll
    for (int i = 0; i < 8; ++i) {
      const unsigned t = (w + i) & 7;
      u4 a0, b0, a1, b1;
      load_pair(wr, voff, (64u * t) * 8192u, 16384u, a0, b0);
      load_pair(wr, voff, (64u * t + 32u) * 8192u, 16384u, a1, b1);
      wa[i] = cat(a0, a1);
      wb[i] = cat(b0, b1);
    }
  }
  u4 a0 = load_mem0(p, w, c, g);
  const unsigned cb = wait_init(p, ws, 8, alive);
  STAMP(15, 8 + w);

  const unsigned lb = lds_base(sm);
  u16v k0;
  u8 k1;
  k0[0] = (c & 1) ? 0xeeeeeeeeu : 0x44444444u;
  k0[1] = w * 1024u + l * 16u;
#pragma unroll
  for (int i = 0; i < 8; ++i) k0[2 + i] = lb + L_MEM + ((w + i) & 7) * 1024u + l * 16u;
#pragma unroll
  for (int r = 0; r < 2; ++r)
    k0[10 + r] = lb + (w < 4 ? L_Q : L_K) + (((w & 3) * 4u + (c >> 2)) * 8u + ((2u * g + r) ^ swz(c >> 2))) * 16u + (c & 3) * 4u;
  k0[12] = k0[13] = 0;
  k0[14] = lb + L_Q + (g * 8u + ((c & 7) ^ swz(g))) * 16u;
  k0[15] = lb + L_K + (g * 8u + ((((c >> 2) & 1) * 4u + (c >> 3) * 2u + (c & 1)) ^ swz(g))) * 16u;
  const unsigned piece = ((c & 7) >> 2) * 4u + (h >> 3), hl = h & 7;
  const unsigned p_base = cb + (B_P + (piece >> 1)) * 16384u + (piece & 1) * 4096u;
  const unsigned tok = ((c >> 1) & 1) * 16u + (c >> 3) * 8u + ((c >> 2) & 1) * 2u + (c & 1);
  k1[0] = p_base + (hl >> 1) * 512u + (2u * (hl & 1) + (g >> 1)) * 128u + ((g & 1) * 4u + (c & 3)) * 16u;
  k1[1] = p_base + 2048u + (hl >> 1) * 128u + ((g >> 1) * 4u + (c & 3)) * 16u + (hl & 1) * 8u;
  k1[2] = O_KX + h * 8192u + ((tok >> 4) * 64u + g * 16u + (tok & 15)) * 16u;
  k1[3] = k1[2] + 4096u;
  k1[4] = w == 0 ? F_KX + h * FL : OOB;
  k1[5] = lb + L_K + l * 16u;
  k1[6] = O_KM + h * 2048u + l * 16u;
  k1[7] = F_KM + h * FL;

  u32v kx = {};
  unsigned seen;
  if (w == 0) {
    wait_flags(ws, k1[4], 1u, alive);
    u4 f[8];
#pragma unroll
    for (int i = 0; i < 8; ++i) f[i] = ld16c(ws, k1[2] + (i & 3) * 2048u + (i >> 2) * 64u, 0);
    kx = cat(cat(f[0], f[1]), cat(f[2], f[3]), cat(f[4], f[5]), cat(f[6], f[7]));
  }
  STAMP(15, 16 + w);
  if (w == 0) TSTAMP(1);

  unsigned s = 0, mem = rfl(cb + B_MEM * 16384u - 16384u), cap = rfl(alive ? SPIN_CAP : 0u), kxo = 262144u, kmo = 0;
  u32v t0;
  u8 t1;
  u16v t2;
  A_DIAG_DECL;
  asm volatile(
      "s_mov_b32 s80, 0xff00ff00\n"
      "s_mov_b32 s81, 0xff00ff00\n"
      "s_mov_b32 s82, 0x3e0293ee\n"
      "s_mov_b32 s83, 0\n"
      "s_cmp_eq_u32 %[w], 0\n"
      "s_cselect_b32 s90, 63, 0\n"
      "v_mov_b32 v184, 0\nv_mov_b32 v185, 0\nv_mov_b32 v186, 0\nv_mov_b32 v187, 0\n"
      "v_mov_b32 v188, 0\nv_mov_b32 v189, 0\nv_mov_b32 v190, 0\nv_mov_b32 v191, 0\n"
      "ds_write_b128 v130, v[148:151]\n"
      "s_branch .Lhave%=\n"
      A_POLL_MEM
      ".Lhave%=:\n"
      A_STAMP("s[84:85]") A_CLK("s[70:71]")
      SMF("184:187", "148:151", "0:7", "v128") SMF("188:191", "148:151", "8:15", "v128")
      "buffer_load_dword v248, v244, %[ws], 0 offen sc1\n"
      A_READ_MEM
      WL(6) SMF("184:187", "152:155", "16:23", "v128") SMF("188:191", "152:155", "24:31", "v128")
      WL(5) SMF("184:187", "156:159", "32:39", "v128") SMF("188:191", "156:159", "40:47", "v128")
      WL(4) SMF("184:187", "160:163", "48:55", "v128") SMF("188:191", "160:163", "56:63", "v128")
      WL(3) SMF("184:187", "164:167", "64:71", "v128") SMF("188:191", "164:167", "72:79", "v128")
      WL(2) SMF("184:187", "168:171", "80:87", "v128") SMF("188:191", "168:171", "88:95", "v128")
      WL(1) SMF("184:187", "172:175", "96:103", "v128") SMF("188:191", "172:175", "104:111", "v128")
      WL(0) SMF("184:187", "176:179", "112:119", "v128") SMF("188:191", "176:179", "120:127", "v128")
      A_CLK("s[74:75]")
      "s_cmp_lt_u32 %[s], s90\n"
      "s_nop 5\n"
      "v_add_f32 v152, v184, v185\n"
      "v_add_f32 v154, v186, v187\n"
      "v_add_f32 v153, v188, v189\n"
      "v_add_f32 v155, v190, v191\n"
      "v_cvt_pk_bf16_f32 v152, v152, v153\n"
      "v_cvt_pk_bf16_f32 v154, v154, v155\n"
      "ds_write_b32 v138, v152\n"
      "ds_write_b32 v139, v154\n"
      A_STAMP("s[86:87]") A_CLK("s[76:77]")
      "v_mov_b32 v184, 0\nv_mov_b32 v185, 0\nv_mov_b32 v186, 0\nv_mov_b32 v187, 0\n"
      "v_mov_b32 v188, 0\nv_mov_b32 v189, 0\nv_mov_b32 v190, 0\nv_mov_b32 v191, 0\n"
      "s_waitcnt lgkmcnt(0)\n"
      "s_barrier\n"
      "s_cbranch_scc0 .Lother%=\n"
      A_CLK("s[78:79]")
      "ds_read_b128 v[152:155], v142\n"
      "ds_read_b128 v[168:171], v143\n"
      "ds_read_b128 v[156:159], v142 offset:512\n"
      "ds_read_b128 v[172:175], v143 offset:512\n"
      "ds_read_b128 v[160:163], v142 offset:1024\n"
      "ds_read_b128 v[176:179], v143 offset:1024\n"
      "ds_read_b128 v[164:167], v142 offset:1536\n"
      "ds_read_b128 v[180:183], v143 offset:1536\n"
      WL(7) MFM("224:227", "192:195", "152:155", "0") MFM("228:231", "208:211", "152:155", "0")
      WL(6) MFM("232:235", "168:171", "152:155", "0")
      WL(5) MFM("224:227", "196:199", "156:159", "v[224:227]") MFM("228:231", "212:215", "156:159", "v[228:231]")
      WL(4) MFM("232:235", "172:175", "156:159", "v[232:235]")
      WL(3) MFM("224:227", "200:203", "160:163", "v[224:227]") MFM("228:231", "216:219", "160:163", "v[228:231]")
      WL(2) MFM("232:235", "176:179", "160:163", "v[232:235]")
      WL(1) MFM("224:227", "204:207", "164:167", "v[224:227]") MFM("228:231", "220:223", "164:167", "v[228:231]")
      WL(0) MFM("232:235", "180:183", "164:167", "v[232:235]")
      A_CLK("s[92:93]")
      "s_nop 5\n"
      "v_max3_f32 v152, v224, v225, v226\n"
      "v_max3_f32 v153, v227, v228, v229\n"
      "v_max3_f32 v154, v230, v231, v232\n"
      "v_max3_f32 v152, v152, v153, v233\n"
      "v_max_f32 v153, v152, v154\n"
      "v_max_f32 v152, v152, v154\n"
      "v_cndmask_b32 v156, v224, v228, s[80:81]\n"
      "v_cndmask_b32 v157, v225, v229, s[80:81]\n"
      "v_permlane16_swap_b32 v152, v153\n"
      "v_max_f32 v154, v152, v153\n"
      "v_max_f32 v155, v152, v153\n"
      "v_cndmask_b32 v158, v226, v230, s[80:81]\n"
      "v_cndmask_b32 v159, v227, v231, s[80:81]\n"
      "v_permlane32_swap_b32 v154, v155\n"
      "v_cndmask_b32 v160, v232, v233, s[80:81]\n"
      "v_max_f32 v152, v154, v155\n"
      "v_mul_f32 v152, 0xbe0293ee, v152\n"
      "v_fma_f32 v156, v156, s82, v152\n"
      "v_fma_f32 v157, v157, s82, v152\n"
      "v_fma_f32 v158, v158, s82, v152\n"
      "v_fma_f32 v159, v159, s82, v152\n"
      "v_fma_f32 v160, v160, s82, v152\n"
      "v_exp_f32 v156, v156\n"
      "v_exp_f32 v157, v157\n"
      "v_exp_f32 v158, v158\n"
      "v_exp_f32 v159, v159\n"
      "v_exp_f32 v160, v160\n"
      "v_add_f32 v152, v156, v157\n"
      "v_add_f32 v153, v158, v159\n"
      "v_add_f32 v152, v152, v160\n"
      "v_add_f32 v152, v152, v153\n"
      "v_xor_b32 v156, s83, v156\n"
      "v_xor_b32 v157, s83, v157\n"
      "v_add_f32_dpp v154, v152, v152 row_ror:8" DPP_ALL
      "v_add_f32_dpp v155, v152, v152 row_ror:8" DPP_ALL
      "v_xor_b32 v158, s83, v158\n"
      "v_xor_b32 v159, s83, v159\n"
      "v_permlane16_swap_b32 v154, v155\n"
      "v_add_f32 v152, v154, v155\n"
      "v_add_f32 v153, v154, v155\n"
      "v_xor_b32 v160, s83, v160\n"
      "s_xor_b32 s83, s83, 0x80000000\n"
      "v_permlane32_swap_b32 v152, v153\n"
      "v_add_f32 v152, v152, v153\n"
      "v_rcp_f32 v152, v152\n"
      A_CLK("s[94:95]")
      "s_add_u32 s91, %[s], 2\n"
      "v_mul_f32 v156, v156, v152\n"
      "v_mul_f32 v157, v157, v152\n"
      "v_mul_f32 v158, v158, v152\n"
      "v_mul_f32 v159, v159, v152\n"
      "v_mul_f32 v160, v160, v152\n"
      "v_cvt_pk_bf16_f32 v164, v156, v157\n"
      "v_cvt_pk_bf16_f32 v165, v158, v159\n"
      "v_mov_b32_dpp v161, v160 row_ror:8" DPP_ALL
      "v_mov_b32_dpp v166, v164 row_ror:8" DPP_ALL
      "v_mov_b32_dpp v167, v165 row_ror:8" DPP_ALL
      "v_cvt_pk_bf16_f32 v162, v160, v161\n"
      "v_cvt_pk_bf16_f32 v163, v160, v161\n"
      "s_not_b64 exec, s[80:81]\n"
      "buffer_store_dwordx4 v[164:167], v240, %[ws], 0 offen sc1\n"
      "s_mov_b64 exec, -1\n"
      "v_permlane16_swap_b32 v162, v163\n"
      "s_mov_b32 exec_lo, 0xff\n"
      "s_mov_b32 exec_hi, 0xff\n"
      "buffer_store_dwordx2 v[162:163], v241, %[ws], 0 offen sc1\n"
      "s_mov_b64 exec, -1\n"
      A_STAMP("s[88:89]") A_CLK("s[96:97]") A_SAVE("s84", 0) A_SAVE("s86", 1) A_SAVE("s88", 2) A_SAVECLK
      "s_cmp_ge_u32 %[s], 62\n"
      "s_cbranch_scc1 .Lnext%=\n"
      A_KXWAIT
      ".Lkx%=:\n"
      "v_cmp_gt_u32 vcc, s91, v248\n"
      "s_cbranch_vccz .Lkxok%=\n"
      "buffer_load_dword v248, v244, %[ws], 0 offen sc1\n"
      "s_sub_u32 %[cap], %[cap], 1\n"
      "s_cbranch_scc1 .Lexit%=\n"
      "s_waitcnt vmcnt(0)\n"
      "s_branch .Lkx%=\n"
      ".Lkxok%=:\n"
      "buffer_load_dwordx4 v[192:195], v242, %[ws], %[kxo] offen sc1\n"
      "buffer_load_dwordx4 v[208:211], v242, %[ws], %[kxo] offen offset:64 sc1\n"
      "buffer_load_dwordx4 v[196:199], v242, %[ws], %[kxo] offen offset:2048 sc1\n"
      "buffer_load_dwordx4 v[212:215], v242, %[ws], %[kxo] offen offset:2112 sc1\n"
      "buffer_load_dwordx4 v[200:203], v243, %[ws], %[kxo] offen sc1\n"
      "buffer_load_dwordx4 v[216:219], v243, %[ws], %[kxo] offen offset:64 sc1\n"
      "buffer_load_dwordx4 v[204:207], v243, %[ws], %[kxo] offen offset:2048 sc1\n"
      "buffer_load_dwordx4 v[220:223], v243, %[ws], %[kxo] offen offset:2112 sc1\n"
      "s_add_u32 %[kxo], %[kxo], 0x40000\n"
      SLEEP(D_S0)
      "s_branch .Lnext%=\n"
      ".Lidle%=:\n"
      SLEEP(D_S1)
      "s_branch .Lnext%=\n"
      ".Lother%=:\n"
      "s_cmp_lg_u32 %[w], 6\n"
      "s_cbranch_scc1 .Lidle%=\n"
      "ds_read_b128 v[152:155], v245\n"
      "ds_read_b128 v[156:159], v245 offset:1024\n"
      "s_add_u32 s91, %[s], 1\n"
      "v_mov_b32 v160, s91\n"
      WL(1) "buffer_store_dwordx4 v[152:155], v246, %[ws], %[kmo] offen sc1\n"
      WL(0) "buffer_store_dwordx4 v[156:159], v246, %[ws], %[kmo] offen offset:1024 sc1\n"
      "s_add_u32 %[kmo], %[kmo], 0x10000\n"
      "s_waitcnt vmcnt(0)\n"
      "s_mov_b64 exec, 1\n"
      "buffer_store_dword v160, v247, %[ws], 0 offen sc1\n"
      "s_mov_b64 exec, -1\n"
      SLEEP(D_S0)
      ".Lnext%=:\n"
      A_NEXT
      "s_add_u32 %[mem], %[mem], 0x4000\n"
      "s_add_u32 %[s], %[s], 1\n"
      "s_cmp_lt_u32 %[s], 64\n"
      "s_cbranch_scc1 .Lpoll%=\n"
      ".Lexit%=:\n"
      : [s] "+s"(s), [mem] "+s"(mem), [cap] "+s"(cap), [kxo] "+s"(kxo), [kmo] "+s"(kmo), "+{v[148:151]}"(a0),
        "=&{v[152:183]}"(t0), "=&{v[184:191]}"(t1), "+{v[192:223]}"(kx), "=&{v[224:239]}"(t2), "=&{v248}"(seen) A_DIAG_OUT
      : [ws] "s"(srd(p.ws, WS_BYTES)), [w] "s"(w), "{v[0:31]}"(cat(wa[0], wb[0], wa[1], wb[1])),
        "{v[32:63]}"(cat(wa[2], wb[2], wa[3], wb[3])), "{v[64:95]}"(cat(wa[4], wb[4], wa[5], wb[5])),
        "{v[96:127]}"(cat(wa[6], wb[6], wa[7], wb[7])), "{v[128:143]}"(k0), "{v[240:247]}"(k1)
      : A_SCLOB A_DIAG_CLOB);
}

DEV void load_wo(const Params& p, unsigned h, unsigned w, unsigned c, unsigned g, u4 (&wo)[4][4]) {
  const rsrc_t r = mk(p.wo, 4u << 20);
  const unsigned voff = ((h * 128u + 8u * g) * 512u + 64u * w + 2u * c) * 2u;
#pragma unroll
  for (int b = 0; b < 2; ++b)
#pragma unroll
    for (int f = 0; f < 4; ++f) load_pair(r, voff, f * 32768u + b * 64u, 1024u, wo[2 * b][f], wo[2 * b + 1][f]);
}

DEV void role_vm(const Params& p, unsigned h, char* sm) {
  constexpr unsigned L_MEM = 0, L_V = 8192;
  const unsigned l = threadIdx.x & 63, w = rfl(threadIdx.x >> 6), c = l & 15, g = l >> 4;
  const rsrc_t ws = mk(p.ws, WS_BYTES);
  bool alive = true;

  u8 wv[8], wo[4][2];
  {
    const rsrc_t wr = mk(p.wv, 4u << 20);
    const unsigned voff = ((16u * (g & 1) + (g >> 1)) * 4096u + h * 128u + w * 16u + c) * 2u;
#pragma unroll
    for (int i = 0; i < 8; ++i) {
      const unsigned t = (w + i) & 7;
      wv[i] = cat(load_single(wr, voff, (64u * t) * 8192u, 16384u), load_single(wr, voff, (64u * t + 32u) * 8192u, 16384u));
    }
    u4 t[4][4];
    load_wo(p, h, w, c, g, t);
#pragma unroll
    for (int i = 0; i < 4; ++i) {
      wo[i][0] = cat(t[i][0], t[i][1]);
      wo[i][1] = cat(t[i][2], t[i][3]);
    }
  }
  u4 a0 = load_mem0(p, w, c, g);
  const unsigned cb = wait_init(p, ws, 8, alive);
  STAMP(15, 8 + w);

  const unsigned lb = lds_base(sm);
  u16v k0;
  k0[0] = (c & 1) ? 0xeeeeeeeeu : 0x44444444u;
  k0[1] = w * 1024u + l * 16u;
#pragma unroll
  for (int i = 0; i < 8; ++i) k0[2 + i] = lb + L_MEM + ((w + i) & 7) * 1024u + l * 16u;
  k0[10] = lb + L_V + (w * 16u + 4u * g + ((c >> 1) & 1)) * 16u + (2u * (c >> 2) + (c & 1)) * 2u;
  k0[11] = lb + L_V + l * 16u;
  const unsigned cg = 4u * w + g;
  k0[12] = cb + (B_M + cg) * 16384u + h * 256u + c * 16u;
  k0[13] = O_M2 + cg * 8192u + h * 256u + c * 16u;
  k0[14] = threadIdx.x == 448 ? F_M + h * FL : OOB;
  k0[15] = 0;

  unsigned s = 0, mem = rfl(cb + B_MEM * 16384u - 16384u), cap = rfl(alive ? SPIN_CAP : 0u), m2o = 0;
  u32v t0;
  u16v t1, t2;
  A_DIAG_DECL;
#define VM_ZERO(a) "v_mov_b32 v" #a ", 0\n"
#define VM_ZERO_M \
      VM_ZERO(192) VM_ZERO(193) VM_ZERO(194) VM_ZERO(195) VM_ZERO(196) VM_ZERO(197) VM_ZERO(198) VM_ZERO(199) \
      VM_ZERO(200) VM_ZERO(201) VM_ZERO(202) VM_ZERO(203) VM_ZERO(204) VM_ZERO(205) VM_ZERO(206) VM_ZERO(207)
  asm volatile(
      "s_mov_b32 s83, 0\n"
      VM_ZERO(180) VM_ZERO(184) VM_ZERO(185) VM_ZERO(186) VM_ZERO(187) VM_ZERO_M
      "ds_write_b128 v130, v[148:151]\n"
      "s_branch .Lhave%=\n"
      A_POLL_MEM
      ".Lhave%=:\n"
      A_STAMP("s[84:85]") A_CLK("s[70:71]")
      SMF("184:187", "148:151", "0:7", "v128")
      A_READ_MEM
      WL(6) SMF("184:187", "152:155", "8:15", "v128")
      WL(5) SMF("184:187", "156:159", "16:23", "v128")
      WL(4) SMF("184:187", "160:163", "24:31", "v128")
      WL(3) SMF("184:187", "164:167", "32:39", "v128")
      WL(2) SMF("184:187", "168:171", "40:47", "v128")
      WL(1) SMF("184:187", "172:175", "48:55", "v128")
      WL(0) SMF("184:187", "176:179", "56:63", "v128")
      A_CLK("s[74:75]")
      "s_nop 7\n"
      "v_add_f32 v152, v184, v185\n"
      "v_add_f32 v153, v186, v187\n"
      "v_cvt_pk_bf16_f32 v152, v152, v153\n"
      "ds_write_b16 v138, v152\n"
      "ds_write_b16_d16_hi v138, v152 offset:32\n"
      A_STAMP("s[86:87]") A_CLK("s[76:77]")
      VM_ZERO(184) VM_ZERO(185) VM_ZERO(186) VM_ZERO(187)
      "s_waitcnt lgkmcnt(0)\n"
      "s_barrier\n"
      A_CLK("s[78:79]")
      "ds_read_b128 v[152:155], v139\n"
      "ds_read_b128 v[156:159], v139 offset:1024\n"
      "buffer_store_dword v180, v142, %[ws], 0 offen sc1\n"
      WL(1) SMF("192:195", "152:155", "64:71", "v128") SMF("200:203", "152:155", "96:103", "v128")
      WL(0) SMF("192:195", "156:159", "72:79", "v128") SMF("200:203", "156:159", "104:111", "v128")
      SMF("196:199", "152:155", "80:87", "v128") SMF("204:207", "152:155", "112:119", "v128")
      SMF("196:199", "156:159", "88:95", "v128") SMF("204:207", "156:159", "120:127", "v128")
      A_CLK("s[92:93]")
      "s_nop 2\n"
      "v_add_f32 v192, v192, v193\n"
      "v_add_f32 v200, v200, v201\n"
      "v_add_f32 v194, v194, v195\n"
      "v_add_f32 v202, v202, v203\n"
      "v_cvt_pk_bf16_f32 v160, v192, v194\n"
      "v_cvt_pk_bf16_f32 v162, v200, v202\n"
      "v_bfi_b32 v160, -2, v160, s83\n"
      "v_bfi_b32 v162, -2, v162, s83\n"
      "v_add_f32 v196, v196, v197\n"
      "v_add_f32 v204, v204, v205\n"
      "v_add_f32 v198, v198, v199\n"
      "v_add_f32 v206, v206, v207\n"
      "v_cvt_pk_bf16_f32 v161, v196, v198\n"
      "v_cvt_pk_bf16_f32 v163, v204, v206\n"
      "v_bfi_b32 v161, -2, v161, s83\n"
      "v_bfi_b32 v163, -2, v163, s83\n"
      "v_permlane32_swap_b32 v160, v162\n"
      "s_add_u32 %[s], %[s], 1\n"
      "v_permlane32_swap_b32 v161, v163\n"
      "s_nop 1\n"
      "v_permlane16_swap_b32 v160, v161\n"
      "v_permlane16_swap_b32 v162, v163\n"
      "buffer_store_dwordx4 v[160:163], v140, %[ws], 0 offen sc1\n"
      A_STAMP("s[88:89]") A_CLK("s[94:95]") A_CLK("s[96:97]")
      "buffer_store_dwordx4 v[160:163], v141, %[ws], %[m2o] offen sc1\n"
      "v_mov_b32 v180, %[s]\n"
      VM_ZERO_M
      "s_xor_b32 s83, s83, 1\n"
      "s_add_u32 %[m2o], %[m2o], 0x40000\n"
      "s_add_u32 %[mem], %[mem], 0x4000\n"
      A_NEXT
      SLEEP(D_VM)
      "s_cmp_lt_u32 %[s], 64\n"
      "s_cbranch_scc1 .Lpoll%=\n"
      "s_waitcnt vmcnt(0)\n"
      "s_barrier\n"
      "buffer_store_dword v180, v142, %[ws], 0 offen sc1\n"
      ".Lexit%=:\n"
      : [s] "+s"(s), [mem] "+s"(mem), [cap] "+s"(cap), [m2o] "+s"(m2o), "+{v[148:151]}"(a0), "=&{v[152:183]}"(t0),
        "=&{v[184:199]}"(t1), "=&{v[200:215]}"(t2) A_DIAG_OUT
      : [ws] "s"(srd(p.ws, WS_BYTES)), [w] "s"(w), "{v[0:31]}"(cat(wv[0], wv[1], wv[2], wv[3])),
        "{v[32:63]}"(cat(wv[4], wv[5], wv[6], wv[7])), "{v[64:95]}"(cat(wo[0][0], wo[0][1], wo[1][0], wo[1][1])),
        "{v[96:127]}"(cat(wo[2][0], wo[2][1], wo[3][0], wo[3][1])), "{v[128:143]}"(k0)
      : A_SCLOB A_DIAG_CLOB);
}

DEV void role_v1(const Params& p, unsigned j, char* sm) {
  const unsigned l = threadIdx.x & 63, w = rfl(threadIdx.x >> 6), c = l & 15, g = l >> 4;
  const unsigned cg = j >> 1, rg = j & 1;
  const rsrc_t ws = mk(p.ws, WS_BYTES);
  bool alive = true;
  {
    const unsigned cb0 = crit_base(p);
    const u4 ones = {~0u, ~0u, ~0u, ~0u};
    for (unsigned o = (j * 512u + threadIdx.x) * 16u; o < B_END * 16384u; o += 64u * 512u * 16u) st16c(ones, ws, o, cb0);
    drain();
    unsigned sel = 1;
    if (j == 0 && w == 0) {
      unsigned best[2] = {~0u, ~0u};
      for (int k = 0; k < 8; ++k) {
        const unsigned off = B_PROBE * 16384u + (k & 1) * 8192u + (l & 7) * 16u + (k >> 1) * 1024u;
        const unsigned long long t0 = __builtin_amdgcn_s_memtime();
        asm volatile("s_waitcnt lgkmcnt(0)" ::: "memory");
        st16c(ones, ws, off, cb0);
        drain();
        const unsigned long long t1 = __builtin_amdgcn_s_memtime();
        asm volatile("s_waitcnt lgkmcnt(0)" ::: "memory");
        const unsigned dt = (unsigned)(t1 - t0);
        if (k & 1) best[1] = min(best[1], dt); else best[0] = min(best[0], dt);
      }
      sel = best[1] < best[0] ? 2u : 1u;
    }
    bar();
    if (threadIdx.x == 0) st4c(sel, ws, F_INIT + j * FL, 0);
  }
  if (w >= 4) return;
  const unsigned cb = wait_init(p, ws, 4, alive);
  bar();
  STAMP(15, 8 + w);

  const unsigned lb = lds_base(sm);
  const unsigned piece = rg * 4u + w;
  u16v k0 = {};
  k0[0] = (c & 4) ? 0xeeeeeeeeu : 0x44444444u;
  k0[1] = cb + (B_P + (piece >> 1)) * 16384u + (piece & 1) * 4096u + g * 128u + (c & 7) * 16u + 2048u;
  k0[2] = k0[1] - 2048u + (c >> 3) * 512u;
  k0[3] = cb + (B_M + cg) * 16384u + w * 2048u + l * 16u;
  k0[4] = O_U + (cg * 32u + 8u * w) * 1024u + l * 16u;
  k0[5] = k0[4] + 4096u;
  k0[6] = F_U + (8u * w + (l & 7)) * FL;
  k0[7] = lb + ((g * 2u + ((c >> 1) & 1)) * 8u + 2u * (c >> 2) + (c & 1)) * 16u + w * 4u;
  k0[8] = lb + l * 16u;
  k0[9] = cg * 256u + rg * 128u + (l >> 3) * 16u;

  unsigned s = 0, mem = rfl(cb + B_MEM * 16384u), cap = rfl(alive ? SPIN_CAP : 0u), uo = 0, seen = 0;
  u32v t0, t1;
  A_DIAG_DECL;
#define V1_TAGS(d, a, b, c, OP) "v_bitop3_b32 v" d ", v" a ", v" b ", v" c " bitop3:" OP "\n"
#define V1_ZERO "v_mov_b32 v52, 0\nv_mov_b32 v53, 0\nv_mov_b32 v54, 0\nv_mov_b32 v55, 0\n" \
                "v_mov_b32 v56, 0\nv_mov_b32 v57, 0\nv_mov_b32 v58, 0\nv_mov_b32 v59, 0\n"
#define V1_LOAD_P \
      "buffer_load_dwordx4 v[40:43], v66, %[ws], 0 offen sc1\n" \
      "buffer_load_dwordx4 v[44:47], v66, %[ws], 0 offen offset:1024 sc1\n" \
      "buffer_load_dwordx4 v[48:51], v65, %[ws], 0 offen sc1\n"
#define V1_STEP(P, OP, MWANT, PWANT, NEG) \
      ".Ltop" P "%=:\n" \
      "v_cmp_ge_u32 vcc, %[s], v80\n" \
      "s_cbranch_vccnz .Luw" P "%=\n" \
      "buffer_load_dwordx4 v[0:3], v68, %[ws], %[uo] offen sc1\n" \
      "buffer_load_dwordx4 v[4:7], v68, %[ws], %[uo] offen offset:1024 sc1\n" \
      "buffer_load_dwordx4 v[8:11], v68, %[ws], %[uo] offen offset:2048 sc1\n" \
      "buffer_load_dwordx4 v[12:15], v68, %[ws], %[uo] offen offset:3072 sc1\n" \
      "buffer_load_dwordx4 v[16:19], v69, %[ws], %[uo] offen sc1\n" \
      "buffer_load_dwordx4 v[20:23], v69, %[ws], %[uo] offen offset:1024 sc1\n" \
      "buffer_load_dwordx4 v[24:27], v69, %[ws], %[uo] offen offset:2048 sc1\n" \
      "buffer_load_dwordx4 v[28:31], v69, %[ws], %[uo] offen offset:3072 sc1\n" \
      "buffer_load_dword v80, v70, %[ws], 0 offen sc1\n" \
      "s_add_u32 %[uo], %[uo], 0x100000\n" \
      V1_ZERO \
      SLEEP(D_V1) \
      ".Lboth" P "%=:\n" \
      "buffer_load_dwordx4 v[32:35], v67, %[ws], 0 offen sc1\n" \
      "buffer_load_dwordx4 v[36:39], v67, %[ws], 0 offen offset:1024 sc1\n" \
      V1_LOAD_P \
      WV(3) \
      V1_TAGS("60", "32", "33", "34", OP) V1_TAGS("61", "35", "36", "37", OP) V1_TAGS("60", "60", "38", "39", OP) \
      V1_TAGS("60", "60", "61", "61", OP) \
      "v_and_b32 v60, 1, v60\n" \
      "v_cmp_ne_u32 s[80:81], " MWANT ", v60\n" \
      ".Lp" P "%=:\n" \
      WV(2) SMF("52:55", "40:43", "0:7", "v64") SMF("56:59", "40:43", "8:15", "v64") \
      V1_TAGS("60", "40", "41", "42", OP) \
      WV(1) SMF("52:55", "44:47", "16:23", "v64") SMF("56:59", "44:47", "24:31", "v64") \
      V1_TAGS("60", "60", "43", "44", OP) V1_TAGS("61", "45", "46", "47", OP) \
      WV(0) SMF("52:55", "48:51", "32:39", "v64") \
      V1_TAGS("60", "60", "48", "49", OP) V1_TAGS("61", "61", "50", "51", OP) V1_TAGS("60", "60", "61", "61", OP) \
      "v_and_b32 v60, s82, v60\n" \
      "v_cmp_ne_u32 vcc, " PWANT ", v60\n" \
      "s_or_b64 vcc, vcc, s[80:81]\n" \
      "s_cbranch_scc1 .Lre" P "%=\n" \
      A_STAMP("s[86:87]") A_CLK("s[70:71]") \
      "s_nop 0\n" \
      "v_permlane32_swap_b32 v56, v54\n" \
      "v_permlane32_swap_b32 v57, v55\n" \
      "v_add_f32 v60, v52, v54\n" \
      "v_add_f32 v62, v58, v56\n" \
      "v_add_f32 v61, v53, v55\n" \
      "v_add_f32 v63, v59, v57\n" \
      "v_cndmask_b32 v60, v60, v62, s[84:85]\n" \
      "v_cndmask_b32 v61, v61, v63, s[84:85]\n" \
      "s_cmp_lg_u32 %[w], 0\n" \
      "s_nop 0\n" \
      "v_permlane16_swap_b32 v60, v61\n" \
      "v_add_f32 v60, v60, v61\n" \
      "ds_write_b32 v71, v60 offset:" P "*1024\n" \
      A_CLK("s[72:73]") \
      "s_waitcnt lgkmcnt(0)\n" \
      "s_barrier\n" \
      "s_cbranch_scc1 .Lend" P "%=\n" \
      A_CLK("s[74:75]") \
      "ds_read_b128 v[52:55], v72 offset:" P "*1024\n" \
      "s_waitcnt lgkmcnt(0)\n" \
      A_CLK("s[76:77]") \
      "v_add_f32 v52, v52, v53\n" \
      "v_add_f32 v54, v54, v55\n" \
      "v_add_f32_e64 v52, " NEG "v52, " NEG "v54\n" \
      "s_nop 1\n" \
      "v_mov_b32_dpp v53, v52 quad_perm:[1,0,3,2]" DPP_ALL \
      "v_cvt_pk_bf16_f32 v56, v52, v53\n" \
      "s_nop 1\n" \
      "v_mov_b32_dpp v57, v56 row_shl:2" DPP_ALL \
      "v_mov_b32_dpp v58, v56 row_shl:4" DPP_ALL \
      "v_mov_b32_dpp v59, v56 row_shl:6" DPP_ALL \
      "s_mov_b64 exec, s[90:91]\n" \
      "buffer_store_dwordx4 v[56:59], v73, %[ws], %[mem] offen sc1\n" \
      "s_mov_b64 exec, -1\n" \
      A_STAMP("s[88:89]") A_CLK("s[78:79]") A_SAVE("s86", 1) A_SAVE("s88", 2) A_SAVECLK \
      ".Lend" P "%=:\n" \
      "s_barrier\n" \
      A_NEXT \
      "s_add_u32 %[mem], %[mem], 0x4000\n" \
      "s_add_u32 %[s], %[s], 1\n"
#define V1_SIDE(P) \
      ".Luw" P "%=:\n" \
      "buffer_load_dword v80, v70, %[ws], 0 offen sc1\n" \
      "s_sub_u32 %[cap], %[cap], 1\n" \
      "s_cbranch_scc1 .Lexit%=\n" \
      "s_waitcnt vmcnt(0)\n" \
      "s_branch .Ltop" P "%=\n" \
      ".Lre" P "%=:\n" \
      "s_sub_u32 %[cap], %[cap], 1\n" \
      "s_cbranch_scc1 .Lexit%=\n" \
      "s_cmp_lg_u64 s[80:81], 0\n" \
      V1_ZERO \
      "s_cbranch_scc1 .Lboth" P "%=\n" \
      V1_LOAD_P \
      "s_branch .Lp" P "%=\n"
  asm volatile(
      "s_mov_b32 s82, 0x80008000\n"
      "s_mov_b32 s84, 0\n"
      "s_mov_b32 s85, -1\n"
      "s_mov_b32 s90, 0x01010101\n"
      "s_mov_b32 s91, 0x01010101\n"
      V1_STEP("0", "0xfe", "0", "0", "")
      "s_cmp_ge_u32 %[s], 63\n"
      "s_cbranch_scc1 .Lexit%=\n"
      V1_STEP("1", "0x80", "1", "s82", "-")
      "s_branch .Ltop0%=\n"
      V1_SIDE("0") V1_SIDE("1")
      ".Lexit%=:\n"
      : [s] "+s"(s), [mem] "+s"(mem), [cap] "+s"(cap), [uo] "+s"(uo), "=&{v[0:31]}"(t0), "=&{v[32:63]}"(t1),
        "+{v80}"(seen) A_DIAG_OUT
      : [ws] "s"(srd(p.ws, WS_BYTES)), [w] "s"(w), "{v[64:79]}"(k0)
      : A_SCLOB A_DIAG_CLOB);
}

DEV void load_x(rsrc_t xr, unsigned w, unsigned c, unsigned g, int s, u4 (&xp)[4]) {
  const unsigned voff = ((16u * (w >> 2) + c) * 512u + 128u * (w & 3) + 8u * g) * 2u;
#pragma unroll
  for (int i = 0; i < 4; ++i) xp[i] = ld16(xr, voff + i * 64u, s * 32768u);
}
DEV void put_x(char* sm, unsigned base, unsigned w, unsigned l, const u4 (&xp)[4]) {
#pragma unroll
  for (int i = 0; i < 4; ++i) lds16(sm, base + (4u * w + i) * 1024u + l * 16u) = xp[i];
}

DEV void token_softmax(rsrc_t ws, char* sm, unsigned qb, const u4 (&km)[4], unsigned h, unsigned w, unsigned l, int s) {
  const unsigned c = l & 15, g = l >> 4;
  u4 q[4], k0[4], k1[4];
#pragma unroll
  for (int m = 0; m < 4; ++m) {
    q[m] = lds16(sm, qb + (w * 4u + m) * 1024u);
    k0[m] = lds16(sm, qb + 8192u + m * 1024u);
    k1[m] = lds16(sm, qb + 12288u + m * 1024u);
  }
  sched();
  f4 s0 = {0, 0, 0, 0}, s1 = {0, 0, 0, 0}, sm_ = {0, 0, 0, 0};
#pragma unroll
  for (int m = 0; m < 4; ++m) {
    s0 = mma(k0[m], q[m], s0);
    s1 = mma(k1[m], q[m], s1);
  }
#pragma unroll
  for (int m = 0; m < 4; ++m) sm_ = mma(km[m], q[m], sm_);
  const unsigned tq = 16u * w + c;
  const float ninf = -__builtin_inff();
#pragma unroll
  for (int r = 0; r < 4; ++r) {
    s0[r] = 4u * g + r <= tq ? s0[r] : ninf;
    s1[r] = 16u + 4u * g + r <= tq ? s1[r] : ninf;
  }
  float mx = fmaxf(fmaxf(fmaxf(s0[0], s0[1]), fmaxf(s0[2], s0[3])), fmaxf(fmaxf(s1[0], s1[1]), fmaxf(s1[2], s1[3])));
  mx = group_max(fmaxf(mx, fmaxf(sm_[0], sm_[1])));
  const float mc = -mx * SCALE_LOG2E;
  float e[10];
#pragma unroll
  for (int r = 0; r < 4; ++r) {
    e[r] = __builtin_amdgcn_exp2f(fmaf(s0[r], SCALE_LOG2E, mc));
    e[4 + r] = __builtin_amdgcn_exp2f(fmaf(s1[r], SCALE_LOG2E, mc));
  }
  e[8] = __builtin_amdgcn_exp2f(fmaf(sm_[0], SCALE_LOG2E, mc));
  e[9] = __builtin_amdgcn_exp2f(fmaf(sm_[1], SCALE_LOG2E, mc));
  const float sum = group_sum(((e[0] + e[1]) + (e[2] + e[3])) + ((e[4] + e[5]) + (e[6] + e[7])) + (e[8] + e[9]));
  const float rs = __builtin_amdgcn_rcpf(sum);
  const u4 px = {pk(e[0] * rs, e[1] * rs), pk(e[2] * rs, e[3] * rs), pk(e[4] * rs, e[5] * rs), pk(e[6] * rs, e[7] * rs)};
  st16c(px, ws, O_PX + (w * 32u + h) * 1024u + l * 16u, s * 65536u);
  const unsigned dm = pk(e[8] * rs, e[9] * rs);
  auto r16 = __builtin_amdgcn_permlane16_swap(dm, dm, false, false);
  auto ra = __builtin_amdgcn_permlane32_swap(r16[0], r16[0], false, false);
  auto rb = __builtin_amdgcn_permlane32_swap(r16[1], r16[1], false, false);
  const u4 pm = {ra[0], rb[0], ra[1], rb[1]};
  st16c(pm, ws, g == 0 ? O_PM + (w * 8u + (h >> 2)) * 1024u + ((h & 3) * 16u + c) * 16u : OOB, s * 16384u);
}

DEV void role_s2(const Params& p, unsigned h, char* sm) {
  constexpr int LAG = 4, R = LAG + 1;
  constexpr unsigned L_X = 0, L_QK = 65536;
  const unsigned l = threadIdx.x & 63, w = rfl(threadIdx.x >> 6), c = l & 15, g = l >> 4;
  const rsrc_t ws = mk(p.ws, WS_BYTES), xr = mk(p.bx, 2u << 20);
  bool alive = true;

  u4 xp[4];
  load_x(xr, w, c, g, 0, xp);
  u4 wa[16], wb[16];
  {
    const rsrc_t wr = mk(w < 4 ? p.wq : p.wk, 4u << 20);
    const unsigned voff = (8u * g * 4096u + h * 128u + (w & 3) * 32u + 2u * c) * 2u;
#pragma unroll
    for (int f = 0; f < 16; ++f) load_pair(wr, voff, 32u * f * 8192u, 8192u, wa[f], wb[f]);
  }
  put_x(sm, L_X, w, l, xp);
  load_x(xr, w, c, g, 1, xp);
  bar();
  STAMP(15, 8 + w);
  if (w < 4) __builtin_amdgcn_s_setprio(1);

  const unsigned km_off = w < 2 ? O_KM + h * 2048u + (g * 8u + ((2u * (c >> 2) + (c & 1)) ^ swz(g))) * 16u : OOB;
  const unsigned km_flag = w < 2 ? F_KM + h * FL : OOB;
  const unsigned px_flag = w < 2 && l == 0 ? F_PX + (w * 32u + h) * FL : OOB;
  const unsigned kx_flag = threadIdx.x == 448 ? F_KX + h * FL : OOB;
  const unsigned kx_off = w >= 4 ? O_KX + h * 8192u + (w & 3) * 2048u + l * 16u : OOB;
  unsigned km_seen = 0;
  int slot = 0;
  for (int it = 0; it < NSEG; ++it) {
    const int s = max(it - LAG, 0);
    const bool soft = it >= LAG && w < 2;
    if (soft && !all_lanes(km_seen > (unsigned)s)) wait_flags(ws, km_flag, s + 1, alive);
    if (w == 0 || w == 4) STAMP(3 + (w >> 2) * 4, it);
    u4 km[4];
#pragma unroll
    for (int m = 0; m < 4; ++m) km[m] = ld16c(ws, km_off + m * 512u, s * 65536u);
    km_seen = ld4c(ws, km_flag, 0);
    f4 acc[2][2] = {};
    const unsigned xb = L_X + (it & 1) * 32768u + l * 16u;
    u4 x[3][2];
#pragma unroll
    for (int k = 0; k < 2; ++k)
#pragma unroll
      for (int t = 0; t < 2; ++t) x[k][t] = lds16(sm, xb + (t * 16 + k) * 1024u);
#pragma unroll
    for (int ks = 0; ks < 16; ++ks) {
      sched();
      if (ks + 2 < 16) {
#pragma unroll
        for (int t = 0; t < 2; ++t) x[(ks + 2) % 3][t] = lds16(sm, xb + (t * 16 + ks + 2) * 1024u);
      }
      sched();
#pragma unroll
      for (int t = 0; t < 2; ++t) {
        acc[0][t] = mma(wa[ks], x[ks % 3][t], acc[0][t]);
        acc[1][t] = mma(wb[ks], x[ks % 3][t], acc[1][t]);
      }
    }
    sched();
    asm volatile("s_waitcnt vmcnt(6)" ::: "memory");
    if (w == 0 || w == 4) STAMP(4 + (w >> 2) * 4, it);
    st4c(s, ws, px_flag, 0);
    put_x(sm, L_X + ((it + 1) & 1) * 32768u, w, l, xp);
#pragma unroll
    for (int t = 0; t < 2; ++t) {
      const u4 o = {pk(acc[0][t][0], acc[1][t][0]), pk(acc[0][t][1], acc[1][t][1]), pk(acc[0][t][2], acc[1][t][2]),
                    pk(acc[0][t][3], acc[1][t][3])};
      lds16(sm, L_QK + slot * 16384u + (w >> 2) * 8192u + (t * 4u + (w & 3)) * 1024u + l * 16u) = o;
      st16c(o, ws, kx_off + t * 1024u, it * 262144u);
    }
    load_x(xr, w, c, g, it + 2, xp);
    if (w == 0 || w == 4) STAMP(5 + (w >> 2) * 4, it);
    slot = slot == R - 1 ? 0 : slot + 1;
    if (soft) {
      token_softmax(ws, sm, L_QK + slot * 16384u + l * 16u, km, h, w, l, s);
      if (w == 0) STAMP(2, s);
    }
    if (it < LAG) drain();
    bar();
    st4c(it < LAG ? it + 1 : it, ws, kx_flag, 0);
  }
  if (w >= 2) return;
  for (int s = NSEG - LAG; s < NSEG; ++s) {
    wait_flags(ws, km_flag, s + 1, alive);
    u4 km[4];
#pragma unroll
    for (int m = 0; m < 4; ++m) km[m] = ld16c(ws, km_off + m * 512u, s * 65536u);
    st4c(s, ws, px_flag, 0);
    slot = slot == R - 1 ? 0 : slot + 1;
    token_softmax(ws, sm, L_QK + slot * 16384u + l * 16u, km, h, w, l, s);
    if (w == 0) STAMP(2, s);
  }
  drain();
  st4c(NSEG, ws, px_flag, 0);
}

DEV void role_vu(const Params& p, unsigned h, char* sm) {
  constexpr unsigned L_X = 0, L_V = 65536;
  constexpr int EAGER = 3;
  const unsigned l = threadIdx.x & 63, w = rfl(threadIdx.x >> 6), c = l & 15, g = l >> 4;
  const rsrc_t ws = mk(p.ws, WS_BYTES), xr = mk(p.bx, 2u << 20);

  u4 xp[4];
  load_x(xr, w, c, g, 0, xp);
  u4 wv[16], wo[4][4];
  {
    const rsrc_t wr = mk(p.wv, 4u << 20);
    const unsigned voff = (8u * g * 4096u + h * 128u + w * 16u + c) * 2u;
#pragma unroll
    for (int f = 0; f < 16; ++f) wv[f] = load_single(wr, voff, 32u * f * 8192u, 8192u);
    load_wo(p, h, w, c, g, wo);
  }
  put_x(sm, L_X, w, l, xp);
  load_x(xr, w, c, g, 1, xp);
  bar();
  STAMP(15, 8 + w);

  const unsigned v_off = L_V + ((w >> 1) * 64u + (2u * (w & 1) + (g >> 1)) * 16u + c) * 16u + (g & 1) * 8u;
  const unsigned flag = threadIdx.x == 448 ? F_U + h * FL : OOB;
  for (int s = 0; s < NSEG; ++s) {
    f4 acc[2] = {};
    const unsigned xb = L_X + (s & 1) * 32768u + l * 16u;
    u4 x[4][2];
#pragma unroll
    for (int k = 0; k < 3; ++k)
#pragma unroll
      for (int t = 0; t < 2; ++t) x[k][t] = lds16(sm, xb + (t * 16 + k) * 1024u);
#pragma unroll
    for (int ks = 0; ks < 16; ++ks) {
      sched();
      if (ks + 3 < 16) {
#pragma unroll
        for (int t = 0; t < 2; ++t) x[(ks + 3) & 3][t] = lds16(sm, xb + (t * 16 + ks + 3) * 1024u);
      }
      sched();
#pragma unroll
      for (int t = 0; t < 2; ++t) acc[t] = mma(wv[ks], x[ks & 3][t], acc[t]);
    }
    sched();
#pragma unroll
    for (int t = 0; t < 2; ++t)
      *reinterpret_cast<u2*>(sm + v_off + (s & 1) * 8192u + t * 4096u) = u2{pk(acc[t][0], acc[t][1]), pk(acc[t][2], acc[t][3])};
    if (w == 0) STAMP(3, s);
    asm volatile("s_waitcnt vmcnt(5)" ::: "memory");
    if (w == 0) STAMP(4, s);
    put_x(sm, L_X + ((s + 1) & 1) * 32768u, w, l, xp);
    load_x(xr, w, c, g, s + 2, xp);
    bar();
    st4c(max(s - 1, min(s, EAGER)), ws, flag, 0);
    if (w == 0) STAMP(5, s);
    u4 v[8];
#pragma unroll
    for (int k = 0; k < 8; ++k) v[k] = lds16(sm, L_V + (s & 1) * 8192u + ((k & 1) * 4 + (k >> 1)) * 1024u + l * 16u);
    sched();
    f4 u[4][2] = {};
#pragma unroll
    for (int k = 0; k < 8; ++k)
#pragma unroll
      for (int i = 0; i < 4; ++i) u[i][k & 1] = mma(v[k], wo[i][k >> 1], u[i][k & 1]);
#pragma unroll
    for (int i = 0; i < 4; ++i) {
      const u4 o = {pk(u[i][0][0], u[i][0][1]), pk(u[i][0][2], u[i][0][3]), pk(u[i][1][0], u[i][1][1]),
                    pk(u[i][1][2], u[i][1][3])};
      st16c(o, ws, O_U + ((4u * w + i) * 32u + h) * 1024u + l * 16u, s * 1048576u);
    }
    if (s < EAGER || s == NSEG - 1) {
      drain();
      bar();
      st4c(s + 1, ws, flag, 0);
    }
    if (w == 0) STAMP(2, s);
  }
}

DEV void role_v2(const Params& p, unsigned j, char* sm) {
  constexpr unsigned ROW = 144, PART = 16 * ROW, BUF = 8 * PART;
  const unsigned l = threadIdx.x & 63, w = rfl(threadIdx.x >> 6), c = l & 15, g = l >> 4;
  const unsigned b = j >> 1, qt = j & 1;
  const rsrc_t ws = mk(p.ws, WS_BYTES), outr = mk(p.out, 8u << 20);
  bool alive = true;

  const unsigned hq = 4u * w + (l & 3);
  const unsigned uf_off = F_U + hq * FL;
  const unsigned pf_off = (l & 4) ? F_M + hq * FL : F_PX + (qt * 32u + hq) * FL;
  const unsigned u_off = O_U + (2u * b * 32u + 4u * w) * 1024u + l * 16u;
  const unsigned m_off = O_M2 + 2u * b * 8192u + w * 1024u + l * 16u;
  const unsigned px_off = O_PX + (qt * 32u + 4u * w) * 1024u + l * 16u;
  const unsigned pm_off = O_PM + (qt * 8u + w) * 1024u + l * 16u;
  const unsigned out_off = ((16u * qt + (threadIdx.x >> 5)) * 1024u + 512u + 32u * b + (threadIdx.x & 31)) * 4u;
  unsigned u_seen = wait_flags(ws, uf_off, 1u, alive), p_seen = 0;
  u4 ua[2][4];
#pragma unroll
  for (int k = 0; k < 8; ++k) ua[k >> 2][k & 3] = ld16c(ws, u_off + (k >> 2) * 32768u + (k & 3) * 1024u, 0);

  for (int s = 0; s < NSEG; ++s) {
    if (!all_lanes(p_seen > (unsigned)s)) p_seen = wait_flags(ws, pf_off, s + 1, alive);
    u4 px[4];
#pragma unroll
    for (int i = 0; i < 4; ++i) px[i] = ld16c(ws, px_off + i * 1024u, s * 65536u);
    const u4 pm = ld16c(ws, pm_off, s * 16384u);
    const u4 m0 = ld16c(ws, m_off, s * 262144u), m1 = ld16c(ws, m_off + 8192u, s * 262144u);
    f4 a0 = {0, 0, 0, 0}, a1 = {0, 0, 0, 0};
#pragma unroll
    for (int i = 0; i < 4; ++i) {
      a0 = mma(ua[0][i], px[i], a0);
      a1 = mma(ua[1][i], px[i], a1);
    }
    a0 = mma(m0, pm, a0);
    a1 = mma(m1, pm, a1);
    sched();
    if (s + 1 < NSEG) {
      if (!all_lanes(u_seen > (unsigned)s + 1u)) u_seen = wait_flags(ws, uf_off, s + 2, alive);
#pragma unroll
      for (int k = 0; k < 8; ++k)
        ua[k >> 2][k & 3] = ld16c(ws, u_off + (k >> 2) * 32768u + (k & 3) * 1024u, (s + 1) * 1048576u);
    }
    sched();
    const unsigned rb = (s & 1) * BUF;
    *reinterpret_cast<f4*>(sm + rb + w * PART + c * ROW + g * 32u) = f4{a0[0], a1[0], a0[1], a1[1]};
    *reinterpret_cast<f4*>(sm + rb + w * PART + c * ROW + g * 32u + 16u) = f4{a0[2], a1[2], a0[3], a1[3]};
    bar();
    float v = 0.f;
#pragma unroll
    for (int k = 0; k < 8; ++k)
      v += *reinterpret_cast<const float*>(sm + rb + k * PART + (threadIdx.x >> 5) * ROW + (threadIdx.x & 31) * 4u);
    __builtin_amdgcn_raw_buffer_store_b32(__float_as_uint(rbf(v)), outr, out_off, s * 131072u, 0);
    if (w == 0) STAMP(2, s);
  }
}

DEV f2 cmul(f2 a, f2 b) {
  float re, im;
  asm("v_mul_f32 %0, %3, %5\n\tv_mul_f32 %1, %3, %4\n\tv_fma_f32 %0, %2, %4, -%0\n\tv_fma_f32 %1, %2, %5, %1"
      : "=&v"(re), "=&v"(im) : "v"(a[0]), "v"(a[1]), "v"(b[0]), "v"(b[1]));
  return f2{re, im};
}
DEV f2 cfma(f2 a, f2 b, f2 c) {
  float re, im;
  asm("v_fma_f32 %0, -%3, %5, %6\n\tv_fma_f32 %1, %3, %4, %7\n\tv_fma_f32 %0, %2, %4, %0\n\tv_fma_f32 %1, %2, %5, %1"
      : "=&v"(re), "=&v"(im) : "v"(a[0]), "v"(a[1]), "v"(b[0]), "v"(b[1]), "v"(c[0]), "v"(c[1]));
  return f2{re, im};
}
DEV f2 from_group(float re, float im, unsigned c, int k) {
  return f2{__shfl(re, (int)c + 16 * k, 64), __shfl(im, (int)c + 16 * k, 64)};
}

DEV void role_a(const Params& p, unsigned ci, char* sm) {
  constexpr unsigned GS = 272, KS = 4 * GS, TS = 32 * KS + 32, L_TAB = 4 * TS;
  const unsigned tid = threadIdx.x, l = tid & 63, w = rfl(tid >> 6), c = l & 15, g = l >> 4;
  const rsrc_t ws = mk(p.ws, WS_BYTES), xr = mk(p.ax, 2u << 20), outr = mk(p.out, 8u << 20);
  bool alive = true;

  {
    const unsigned voff = ((64u * ci + 16u * (c >> 2) + 4u * (w >> 1) + (c & 3)) * 512u + 256u * (w & 1) + 8u * g) * 2u;
    u4 xp[8];
#pragma unroll
    for (int i = 0; i < 8; ++i) xp[i] = ld16(xr, voff + i * 64u, 0);
#pragma unroll
    for (int i = 0; i < 8; ++i) lds16(sm, (8u * w + i) * 1024u + l * 16u) = xp[i];
  }
  f2 lam64;
  {
    const float lr = bf2f(((const unsigned short*)p.lre)[tid]), li = bf2f(((const unsigned short*)p.lim)[tid]);
    const float dt = rbf(expf(bf2f(((const unsigned short*)p.ldt)[tid])));
    const float er = expf(dt * lr);
    float sn, cs;
    sincosf(dt * li, &sn, &cs);
    f2 lam = {er * cs, er * sn};
    asm volatile("s_nop 1" : "+v"(lam));
    const float nr = lam[0] - 1.0f, ni = lam[1];
    f2 coef;
    if (fabsf(lr) >= fabsf(li)) {
      const float t = li / lr, d = 1.0f / (lr + li * t);
      coef = f2{smul(nr + smul(ni, t), d), smul(ni - smul(nr, t), d)};
    } else {
      const float t = lr / li, d = 1.0f / (lr * t + li);
      coef = f2{smul(smul(nr, t) + ni, d), smul(smul(ni, t) - nr, d)};
    }
    f2 l16 = cmul(lam, lam);
    l16 = cmul(l16, l16);
    l16 = cmul(l16, l16);
    l16 = cmul(l16, l16);
    lam64 = cmul(l16, l16);
    lam64 = cmul(lam64, lam64);
    *reinterpret_cast<f4*>(sm + L_TAB + tid * 32u) = f4{lam[0], lam[1], coef[0], coef[1]};
    *reinterpret_cast<f2*>(sm + L_TAB + tid * 32u + 16u) = l16;
  }
  bar();

  f4 hr[4][4] = {}, hi[4][4] = {};
  {
    const rsrc_t br = mk(p.bre, 1u << 19), bi = mk(p.bim, 1u << 19);
    const unsigned voff = ((64u * w + c) * 512u + 8u * g) * 2u;
    u4 fr[4], fi[4];
#pragma unroll
    for (int k = 0; k < 4; ++k) {
      fr[k] = ld16(br, voff + k * 64u, 0);
      fi[k] = ld16(bi, voff + k * 64u, 0);
    }
#pragma unroll
    for (int n = 0; n < 64; ++n) {
      const int ct = n >> 4, ks = n & 15;
      const u4 r = fr[n & 3], i = fi[n & 3];
      fence();
      if (n + 4 < 64) {
        fr[n & 3] = ld16(br, voff + ((n + 4) & 15) * 64u, ((n + 4) >> 4) * 16384u);
        fi[n & 3] = ld16(bi, voff + ((n + 4) & 15) * 64u, ((n + 4) >> 4) * 16384u);
      }
#pragma unroll
      for (int t = 0; t < 4; ++t) {
        const u4 x = lds16(sm, (t * 16 + ks) * 1024u + l * 16u);
        hr[ct][t] = mma(x, r, hr[ct][t]);
        hi[ct][t] = mma(x, i, hi[ct][t]);
      }
    }
  }
  __builtin_amdgcn_sched_barrier(0);
  asm volatile("s_nop 7\n\ts_nop 7" ::: "memory");
  __builtin_amdgcn_sched_barrier(0);
#pragma unroll
  for (int ct = 0; ct < 4; ++ct) {
    const unsigned ch = 64u * w + 16u * ct + c;
    const f4 tb = *reinterpret_cast<const f4*>(sm + L_TAB + ch * 32u);
    const f2 l16 = *reinterpret_cast<const f2*>(sm + L_TAB + ch * 32u + 16u);
    const f2 lam = {tb[0], tb[1]}, coef = {tb[2], tb[3]};
    f2 hh = {0.f, 0.f};
#pragma unroll
    for (int i = 0; i < 16; ++i) {
      hh = cfma(lam, hh, cmul(coef, f2{hr[ct][i >> 2][i & 3], hi[ct][i >> 2][i & 3]}));
      hr[ct][i >> 2][i & 3] = hh[0];
      hi[ct][i >> 2][i & 3] = hh[1];
    }
    f2 e = from_group(hh[0], hh[1], c, 0);
#pragma unroll
    for (int k = 1; k < 4; ++k) e = cfma(l16, e, from_group(hh[0], hh[1], c, k));
    if (g == 0) st8c(__builtin_bit_cast(u2, e), ws, O_AE + ci * 4096u + ch * 8u, 0);
  }
  drain();
  bar();
  if (tid == 0) st4c(1u, ws, F_A + ci * FL, 0);
  wait_flags(ws, F_A + (l & 31) * FL, (l & 31) < ci ? 1u : 0u, alive);
  {
    f2 hin = {0.f, 0.f};
#pragma unroll 8
    for (unsigned k = 0; k < ci; ++k) hin = cfma(lam64, hin, __builtin_bit_cast(f2, ld8c(ws, O_AE + tid * 8u, k * 4096u)));
    *reinterpret_cast<f2*>(sm + L_TAB + tid * 32u + 24u) = hin;
  }
  bar();
#pragma unroll
  for (int ct = 0; ct < 4; ++ct) {
    const unsigned ch = 64u * w + 16u * ct + c;
    const f4 t0 = *reinterpret_cast<const f4*>(sm + L_TAB + ch * 32u);
    const f4 t1 = *reinterpret_cast<const f4*>(sm + L_TAB + ch * 32u + 16u);
    const f2 lam = {t0[0], t0[1]}, l16 = {t1[0], t1[1]};
    f2 z = {t1[2], t1[3]};
#pragma unroll
    for (int k = 0; k < 3; ++k) {
      const f2 n = cfma(l16, z, from_group(hr[ct][3][3], hi[ct][3][3], c, k));
      z = (unsigned)k < g ? n : z;
    }
    const unsigned hb = g * TS + (ch >> 5) * KS + ((ch >> 3) & 3) * GS + (ch & 7) * 2u;
#pragma unroll
    for (int i = 0; i < 16; ++i) {
      z = cmul(lam, z);
      const unsigned d = pk(2.0f * (hr[ct][i >> 2][i & 3] + z[0]), -2.0f * (hi[ct][i >> 2][i & 3] + z[1]));
      lds_put2(sm, hb + i * 16u, d);
      lds_put2(sm, hb + 16u * KS + i * 16u, d >> 16);
    }
  }
  bar();
  f4 y[4][4] = {};
  {
    const rsrc_t cr = mk(p.cre, 1u << 19), cm = mk(p.cim, 1u << 19);
    const unsigned voff = ((64u * w + c) * 512u + 8u * g) * 2u;
    u4 fa[2][4];
#pragma unroll
    for (int k = 0; k < 2; ++k)
#pragma unroll
      for (int d = 0; d < 4; ++d) fa[k][d] = ld16(cr, voff + k * 64u, d * 16384u);
#pragma unroll
    for (int ks = 0; ks < 32; ++ks) {
      u4 a[4];
      fence();
#pragma unroll
      for (int d = 0; d < 4; ++d) {
        a[d] = fa[ks & 1][d];
        if (ks + 2 < 32) fa[ks & 1][d] = ld16(ks + 2 < 16 ? cr : cm, voff + ((ks + 2) & 15) * 64u, d * 16384u);
      }
#pragma unroll
      for (int t = 0; t < 4; ++t) {
        const u4 hb = lds16(sm, t * TS + ks * KS + g * GS + c * 16u);
#pragma unroll
        for (int d = 0; d < 4; ++d) y[d][t] = mma(a[d], hb, y[d][t]);
      }
    }
  }
  const rsrc_t dr = mk(p.dsk, 1024);
#pragma unroll
  for (int d = 0; d < 4; ++d) {
    const unsigned col = 64u * w + 16u * d + 4u * g;
    const u2 dk = ld8(dr, col * 2u, 0);
#pragma unroll
    for (int t = 0; t < 4; ++t) {
      const unsigned row = 64u * ci + 16u * t + c;
      const u2 xv = ld8(xr, (row * 512u + col) * 2u, 0);
      const f4 o = {y[d][t][0] + rbf(smul(bf2f(xv[0] & 0xffffu), bf2f(dk[0] & 0xffffu))),
                    y[d][t][1] + rbf(smul(bf2f(xv[0] >> 16), bf2f(dk[0] >> 16))),
                    y[d][t][2] + rbf(smul(bf2f(xv[1] & 0xffffu), bf2f(dk[1] & 0xffffu))),
                    y[d][t][3] + rbf(smul(bf2f(xv[1] >> 16), bf2f(dk[1] >> 16)))};
      __builtin_amdgcn_raw_buffer_store_b128(__builtin_bit_cast(u4, o), outr, (row * 1024u + col) * 4u, 0, 0);
    }
  }
  if (w == 0) STAMP(2, 0);
}

__global__ void __launch_bounds__(512, 2) fused_kernel(Params p) {
  extern __shared__ __attribute__((aligned(16))) char smem[];
  const unsigned x = blockIdx.x & 7, r = blockIdx.x >> 3;
  if (threadIdx.x < 64) TSTAMP(0);
  if (x == 1 || x == 2) {
    role_v1(p, (x - 1) * 32u + r, smem);
  } else if (x < 4) {
    const unsigned h = (x ? 16u : 0u) + (r >> 1);
    if (r & 1) role_vm(p, h, smem); else role_s1(p, h, smem);
  } else {
    const unsigned i = (x - 4) * 8u + (r >> 2);
    switch (r & 3) {
      case 0: role_s2(p, i, smem); break;
      case 1: role_vu(p, i, smem); break;
      case 2: role_v2(p, i, smem); break;
      case 3: role_a(p, i, smem); break;
    }
  }
  if (threadIdx.x < 64) TSTAMP(2);
}

extern "C" void kernel_launch(void* const* d_in, const int* in_sizes, int n_in, void* d_out, int out_size, void* d_ws,
                              size_t ws_size, hipStream_t stream) {
  Params p;
  p.ax = d_in[0]; p.lre = d_in[1]; p.lim = d_in[2]; p.bre = d_in[3]; p.bim = d_in[4]; p.cre = d_in[5];
  p.cim = d_in[6]; p.ldt = d_in[7]; p.dsk = d_in[8]; p.bx = d_in[9]; p.mem0 = d_in[10]; p.wq = d_in[11];
  p.wk = d_in[12]; p.wv = d_in[13]; p.wo = d_in[14];
  p.out = (float*)d_out;
  p.ws = (char*)d_ws;
  (void)hipFuncSetAttribute((const void*)fused_kernel, hipFuncAttributeMaxDynamicSharedMemorySize, DYN_LDS);
  (void)hipMemsetAsync(d_ws, 0, FLAG_BYTES, stream);
  fused_kernel<<<256, 512, DYN_LDS, stream>>>(p);
}
```

```cpp
#include <hip/hip_runtime.h>
#include <hip/hip_bf16.h>
#include <stdint.h>

typedef __attribute__((ext_vector_type(2))) unsigned u2;
typedef __attribute__((ext_vector_type(4))) unsigned u4;
typedef __attribute__((ext_vector_type(8))) unsigned u8;
typedef __attribute__((ext_vector_type(16))) unsigned u16v;
typedef __attribute__((ext_vector_type(32))) unsigned u32v;
typedef __attribute__((ext_vector_type(2))) float f2;
typedef __attribute__((ext_vector_type(4))) float f4;
typedef __attribute__((ext_vector_type(2))) __bf16 bf2;
typedef __attribute__((ext_vector_type(8))) __bf16 bf8;
typedef __attribute__((ext_vector_type(16))) __bf16 bf16v;
typedef __amdgpu_buffer_rsrc_t rsrc_t;
#define DEV __device__ __forceinline__

struct Params {
  const void *ax, *lre, *lim, *bre, *bim, *cre, *cim, *ldt, *dsk, *bx, *mem0, *wq, *wk, *wv, *wo;
  float* out;
  char* ws;
};

#define WSLEEP 4
constexpr int NSEG = 64;
constexpr unsigned SPIN_CAP = 400000u;
constexpr unsigned DYN_LDS = 156u * 1024u;

constexpr unsigned FL = 128, F_INIT = 0, F_KM = 64 * FL, F_KX = 96 * FL, F_PX = 128 * FL, F_M = 192 * FL, F_U = 224 * FL,
                   F_A = 256 * FL, FLAG_BYTES = 288 * FL;
constexpr unsigned O_AE = 5u << 20, O_DIAG = 6u << 20;
constexpr unsigned O_KX = 8u << 20, O_KM = 24u << 20, O_PX = 28u << 20, O_PM = 32u << 20, O_M2 = 34u << 20,
                   O_U = 50u << 20, WS_BYTES = 114u << 20;
constexpr unsigned OOB = 1u << 30;
constexpr unsigned B_MEM = 0, B_P = 63, B_M = 67, B_END = 99, B_PROBE = 100;

DEV rsrc_t mk(const void* p, unsigned bytes) {
  return __builtin_amdgcn_make_buffer_rsrc((void*)p, 0, bytes, 0x00020000);
}
DEV unsigned rfl(unsigned v) { return __builtin_amdgcn_readfirstlane(v); }
DEV u4 ld16(rsrc_t r, unsigned v, unsigned s) { return __builtin_amdgcn_raw_buffer_load_b128(r, v, s, 0); }
DEV u2 ld8(rsrc_t r, unsigned v, unsigned s) { return __builtin_amdgcn_raw_buffer_load_b64(r, v, s, 0); }
DEV unsigned ld4(rsrc_t r, unsigned v, unsigned s) { return __builtin_amdgcn_raw_buffer_load_b32(r, v, s, 0); }
DEV unsigned ld2(rsrc_t r, unsigned v, unsigned s) { return __builtin_amdgcn_raw_buffer_load_b16(r, v, s, 0); }
DEV u4 ld16c(rsrc_t r, unsigned v, unsigned s) { return __builtin_amdgcn_raw_buffer_load_b128(r, v, s, 16); }
DEV u2 ld8c(rsrc_t r, unsigned v, unsigned s) { return __builtin_amdgcn_raw_buffer_load_b64(r, v, s, 16); }
DEV unsigned ld4c(rsrc_t r, unsigned v, unsigned s) { return __builtin_amdgcn_raw_buffer_load_b32(r, v, s, 16); }
DEV void st16c(u4 d, rsrc_t r, unsigned v, unsigned s) { __builtin_amdgcn_raw_buffer_store_b128(d, r, v + s, 0, 16); }
DEV void st8c(u2 d, rsrc_t r, unsigned v, unsigned s) { __builtin_amdgcn_raw_buffer_store_b64(d, r, v, s, 16); }
DEV void st4c(unsigned d, rsrc_t r, unsigned v, unsigned s) { __builtin_amdgcn_raw_buffer_store_b32(d, r, v, s, 16); }
DEV void fence() { asm volatile("" ::: "memory"); }
DEV void drain() { asm volatile("s_waitcnt vmcnt(0)" ::: "memory"); }
DEV void bar() { asm volatile("s_waitcnt lgkmcnt(0)\n\ts_barrier" ::: "memory"); }
DEV bool all_lanes(bool ok) { return __builtin_amdgcn_ballot_w64(!ok) == 0ull; }

DEV float bf2f(unsigned h) { return __uint_as_float(h << 16); }
DEV unsigned pk(float a, float b) {
  f2 v = {a, b};
  return __builtin_bit_cast(unsigned, __builtin_convertvector(v, bf2));
}
DEV float rbf(float a) { return bf2f(pk(a, a) & 0xffffu); }
DEV float smul(float a, float b) {
  float r;
  asm("v_mul_f32 %0, %1, %2" : "=v"(r) : "v"(a), "v"(b));
  return r;
}
DEV float sadd(float a, float b) {
  float r;
  asm("v_add_f32 %0, %1, %2" : "=v"(r) : "v"(a), "v"(b));
  return r;
}
DEV f4 mma(u4 a, u4 b, f4 c) {
  return __builtin_amdgcn_mfma_f32_16x16x32_bf16(__builtin_bit_cast(bf8, a), __builtin_bit_cast(bf8, b), c, 0, 0, 0);
}
DEV f4 smma(u4 a, u8 b, f4 c, unsigned idx) {
  return __builtin_amdgcn_smfmac_f32_16x16x64_bf16(__builtin_bit_cast(bf8, a), __builtin_bit_cast(bf16v, b), c,
                                                   (int)idx, 0, 0);
}
DEV float swap32_add(float a, float b) {
  auto r = __builtin_amdgcn_permlane32_swap(__float_as_uint(a), __float_as_uint(b), false, false);
  return __uint_as_float(r[0]) + __uint_as_float(r[1]);
}
DEV float swap16_add(float a, float b) {
  auto r = __builtin_amdgcn_permlane16_swap(__float_as_uint(a), __float_as_uint(b), false, false);
  return __uint_as_float(r[0]) + __uint_as_float(r[1]);
}
DEV float group_max(float m) {
  auto r = __builtin_amdgcn_permlane16_swap(__float_as_uint(m), __float_as_uint(m), false, false);
  m = fmaxf(__uint_as_float(r[0]), __uint_as_float(r[1]));
  auto q = __builtin_amdgcn_permlane32_swap(__float_as_uint(m), __float_as_uint(m), false, false);
  return fmaxf(__uint_as_float(q[0]), __uint_as_float(q[1]));
}
DEV float group_sum(float m) {
  const float t = swap16_add(m, m);
  return swap32_add(t, t);
}
DEV u4& lds16(char* sm, unsigned off) { return *reinterpret_cast<u4*>(sm + off); }
DEV void lds_put2(char* sm, unsigned off, unsigned v) { *reinterpret_cast<unsigned short*>(sm + off) = (unsigned short)v; }

#define STAMP(slot, idx) do { } while (0)

#define TSTAMP(idx) do { } while (0)

DEV unsigned wait_flags(rsrc_t ws, unsigned off, unsigned need, bool& alive) {
  unsigned v = 0;
  for (unsigned it = 0; alive; ++it) {
    fence();
    v = ld4c(ws, off, 0);
    if (all_lanes(v >= need)) break;
    if (it > SPIN_CAP) alive = false;
    __builtin_amdgcn_s_sleep(WSLEEP);
  }
  fence();
  return v;
}
DEV unsigned crit_base(const Params& p) {
  const unsigned lo = (unsigned)(uintptr_t)p.ws;
  return ((lo + 65536u + 0x1fffffu) & ~0x1fffffu) - lo;
}
DEV unsigned wait_init(const Params& p, rsrc_t ws, unsigned n, bool& alive) {
  const unsigned l = threadIdx.x & 63, w = threadIdx.x >> 6;
  const unsigned v = wait_flags(ws, F_INIT + (l ? (l & (64u / n - 1u)) + w * (64u / n) : 0u) * FL, 1u, alive);
  return crit_base(p) + (rfl(v) == 2u ? 8192u : 0u);
}

DEV void load_pair(rsrc_t r, unsigned voff, unsigned soff, unsigned rstride, u4& a, u4& b) {
  unsigned d[8];
#pragma unroll
  for (int j = 0; j < 8; ++j) d[j] = ld4(r, voff, soff + j * rstride);
#pragma unroll
  for (int j = 0; j < 4; ++j) {
    a[j] = __builtin_amdgcn_perm(d[2 * j + 1], d[2 * j], 0x05040100u);
    b[j] = __builtin_amdgcn_perm(d[2 * j + 1], d[2 * j], 0x07060302u);
  }
}
DEV u4 load_single(rsrc_t r, unsigned voff, unsigned soff, unsigned rstride) {
  unsigned d[8];
  u4 a;
#pragma unroll
  for (int j = 0; j < 8; ++j) d[j] = ld2(r, voff, soff + j * rstride);
#pragma unroll
  for (int j = 0; j < 4; ++j) a[j] = __builtin_amdgcn_perm(d[2 * j + 1], d[2 * j], 0x05040100u);
  return a;
}
DEV u8 cat(u4 a, u4 b) { return __builtin_shufflevector(a, b, 0, 1, 2, 3, 4, 5, 6, 7); }
DEV u16v cat(u8 a, u8 b) { return __builtin_shufflevector(a, b, 0, 1, 2, 3, 4, 5, 6, 7, 8, 9, 10, 11, 12, 13, 14, 15); }
DEV u32v cat(u8 a, u8 b, u8 c, u8 d) {
  return __builtin_shufflevector(cat(a, b), cat(c, d), 0, 1, 2, 3, 4, 5, 6, 7, 8, 9, 10, 11, 12, 13, 14, 15, 16, 17, 18,
                                 19, 20, 21, 22, 23, 24, 25, 26, 27, 28, 29, 30, 31);
}
DEV u4 srd(const void* p, unsigned bytes) {
  return u4{(unsigned)(uintptr_t)p, (unsigned)((uintptr_t)p >> 32) & 0xffffu, bytes, 0x00020000u};
}
DEV unsigned lds_base(char* sm) { return (unsigned)(uintptr_t)(__attribute__((address_space(3))) char*)sm; }
DEV unsigned swz(unsigned g) { return (g & 1) | ((g & 2) << 1); }
DEV void sched() { __builtin_amdgcn_sched_barrier(0); }

DEV u4 load_mem0(const Params& p, unsigned w, unsigned c, unsigned g) {
  const rsrc_t r = mk(p.mem0, 8192);
  const unsigned cg = 4 * w + g;
  const unsigned voff = ((c >> 1) * 512u + 32u * (cg >> 1) + (cg & 1) + 4u * (c & 1)) * 2u;
  u4 a;
#pragma unroll
  for (int i = 0; i < 4; ++i) a[i] = ld2(r, voff, 16 * i) | (ld2(r, voff, 16 * i + 4) << 16);
  return a;
}
constexpr float SCALE_LOG2E = 0.08838834764831845f * 1.4426950408889634f;

#define A_STAMP(r)
#define A_SAVE(r, slot)
#define A_NEXT
#define A_KXWAIT "s_waitcnt vmcnt(2)\n"
#define A_DIAG_OUT
#define A_DIAG_CLOB
#define A_CLK(r)
#define A_SAVECLK
#define A_DIAG_DECL
#define A_SCLOB "s80", "s81", "s82", "s83", "s84", "s85", "s86", "s87", "s88", "s89", "s90", "s91", "vcc", "scc", "memory"
#define SMF(d, a, b, i) "v_smfmac_f32_16x16x64_bf16 v[" d "], v[" a "], v[" b "], " i "\n"
#define MFM(d, a, b, c) "v_mfma_f32_16x16x32_bf16 v[" d "], v[" a "], v[" b "], " c "\n"
#define DPP_ALL " row_mask:0xf bank_mask:0xf\n"
#define A_POLL_MEM \
  ".Lpoll%=:\n" \
  "buffer_load_dwordx4 v[148:151], v129, %[ws], %[mem] offen sc1\n" \
  "s_sub_u32 %[cap], %[cap], 1\n" \
  "s_cbranch_scc1 .Lexit%=\n" \
  "s_waitcnt vmcnt(0)\n" \
  "ds_write_b128 v130, v[148:151]\n" \
  "v_max3_u32 v152, v148, v149, v150\n" \
  "v_max_u32 v152, v152, v151\n" \
  "v_cmp_eq_u32 vcc, -1, v152\n" \
  "s_cbranch_vccnz .Lpoll%=\n"
#define A_READ_MEM \
  "s_waitcnt lgkmcnt(0)\n" \
  "s_barrier\n" \
  A_CLK("s[72:73]") \
  "ds_read_b128 v[152:155], v131\n" \
  "ds_read_b128 v[156:159], v132\n" \
  "ds_read_b128 v[160:163], v133\n" \
  "ds_read_b128 v[164:167], v134\n" \
  "ds_read_b128 v[168:171], v135\n" \
  "ds_read_b128 v[172:175], v136\n" \
  "ds_read_b128 v[176:179], v137\n"
#define D_S1 48
#define D_S0 38
#define D_VM 48
#define D_V1 50
#define STR_(x) #x
#define SLEEP(x) "s_sleep " STR_(x) "\n"
#define WL(n) "s_waitcnt lgkmcnt(" #n ")\n"
#define WV(n) "s_waitcnt vmcnt(" #n ")\n"

DEV void role_s1(const Params& p, unsigned h, char* sm) {
  constexpr unsigned L_MEM = 0, L_Q = 8192, L_K = 10240;
  const unsigned l = threadIdx.x & 63, w = rfl(threadIdx.x >> 6), c = l & 15, g = l >> 4;
  const rsrc_t ws = mk(p.ws, WS_BYTES);
  bool alive = true;
  STAMP(15, w);

  u8 wa[8], wb[8];
  {
    const rsrc_t wr = mk(w < 4 ? p.wq : p.wk, 4u << 20);
    const unsigned voff = ((16u * (g & 1) + (g >> 1)) * 4096u + h * 128u + (w & 3) * 32u + 2u * c) * 2u;
#pragma unroll
    for (int i = 0; i < 8; ++i) {
      const unsigned t = (w + i) & 7;
      u4 a0, b0, a1, b1;
      load_pair(wr, voff, (64u * t) * 8192u, 16384u, a0, b0);
      load_pair(wr, voff, (64u * t + 32u) * 8192u, 16384u, a1, b1);
      wa[i] = cat(a0, a1);
      wb[i] = cat(b0, b1);
    }
  }
  u4 a0 = load_mem0(p, w, c, g);
  const unsigned cb = wait_init(p, ws, 8, alive);
  STAMP(15, 8 + w);

  const unsigned lb = lds_base(sm);
  u16v k0;
  u8 k1;
  k0[0] = (c & 1) ? 0xeeeeeeeeu : 0x44444444u;
  k0[1] = w * 1024u + l * 16u;
#pragma unroll
  for (int i = 0; i < 8; ++i) k0[2 + i] = lb + L_MEM + ((w + i) & 7) * 1024u + l * 16u;
#pragma unroll
  for (int r = 0; r < 2; ++r)
    k0[10 + r] = lb + (w < 4 ? L_Q : L_K) + (((w & 3) * 4u + (c >> 2)) * 8u + ((2u * g + r) ^ swz(c >> 2))) * 16u + (c & 3) * 4u;
  k0[12] = k0[13] = 0;
  k0[14] = lb + L_Q + (g * 8u + ((c & 7) ^ swz(g))) * 16u;
  k0[15] = lb + L_K + (g * 8u + ((((c >> 2) & 1) * 4u + (c >> 3) * 2u + (c & 1)) ^ swz(g))) * 16u;
  const unsigned piece = ((c & 7) >> 2) * 4u + (h >> 3), hl = h & 7;
  const unsigned p_base = cb + (B_P + (piece >> 1)) * 16384u + (piece & 1) * 4096u;
  const unsigned tok = ((c >> 1) & 1) * 16u + (c >> 3) * 8u + ((c >> 2) & 1) * 2u + (c & 1);
  k1[0] = p_base + (hl >> 1) * 512u + (2u * (hl & 1) + (g >> 1)) * 128u + ((g & 1) * 4u + (c & 3)) * 16u;
  k1[1] = p_base + 2048u + (hl >> 1) * 128u + ((g >> 1) * 4u + (c & 3)) * 16u + (hl & 1) * 8u;
  k1[2] = O_KX + h * 8192u + ((tok >> 4) * 64u + g * 16u + (tok & 15)) * 16u;
  k1[3] = k1[2] + 4096u;
  k1[4] = w == 0 ? F_KX + h * FL : OOB;
  k1[5] = lb + L_K + l * 16u;
  k1[6] = O_KM + h * 2048u + l * 16u;
  k1[7] = F_KM + h * FL;

  u32v kx = {};
  unsigned seen;
  if (w == 0) {
    wait_flags(ws, k1[4], 1u, alive);
    u4 f[8];
#pragma unroll
    for (int i = 0; i < 8; ++i) f[i] = ld16c(ws, k1[2] + (i & 3) * 2048u + (i >> 2) * 64u, 0);
    kx = cat(cat(f[0], f[1]), cat(f[2], f[3]), cat(f[4], f[5]), cat(f[6], f[7]));
  }
  STAMP(15, 16 + w);
  if (w == 0) TSTAMP(1);

  unsigned s = 0, mem = rfl(cb + B_MEM * 16384u - 16384u), cap = rfl(alive ? SPIN_CAP : 0u), kxo = 262144u, kmo = 0;
  u32v t0;
  u8 t1;
  u16v t2;
  A_DIAG_DECL;
  asm volatile(
      "s_mov_b32 s80, 0xff00ff00\n"
      "s_mov_b32 s81, 0xff00ff00\n"
      "s_mov_b32 s82, 0x3e0293ee\n"
      "s_mov_b32 s83, 0\n"
      "s_cmp_eq_u32 %[w], 0\n"
      "s_cselect_b32 s90, 63, 0\n"
      "v_mov_b32 v184, 0\nv_mov_b32 v185, 0\nv_mov_b32 v186, 0\nv_mov_b32 v187, 0\n"
      "v_mov_b32 v188, 0\nv_mov_b32 v189, 0\nv_mov_b32 v190, 0\nv_mov_b32 v191, 0\n"
      "ds_write_b128 v130, v[148:151]\n"
      "s_branch .Lhave%=\n"
      A_POLL_MEM
      ".Lhave%=:\n"
      A_STAMP("s[84:85]") A_CLK("s[70:71]")
      SMF("184:187", "148:151", "0:7", "v128") SMF("188:191", "148:151", "8:15", "v128")
      "buffer_load_dword v248, v244, %[ws], 0 offen sc1\n"
      A_READ_MEM
      WL(6) SMF("184:187", "152:155", "16:23", "v128") SMF("188:191", "152:155", "24:31", "v128")
      WL(5) SMF("184:187", "156:159", "32:39", "v128") SMF("188:191", "156:159", "40:47", "v128")
      WL(4) SMF("184:187", "160:163", "48:55", "v128") SMF("188:191", "160:163", "56:63", "v128")
      WL(3) SMF("184:187", "164:167", "64:71", "v128") SMF("188:191", "164:167", "72:79", "v128")
      WL(2) SMF("184:187", "168:171", "80:87", "v128") SMF("188:191", "168:171", "88:95", "v128")
      WL(1) SMF("184:187", "172:175", "96:103", "v128") SMF("188:191", "172:175", "104:111", "v128")
      WL(0) SMF("184:187", "176:179", "112:119", "v128") SMF("188:191", "176:179", "120:127", "v128")
      A_CLK("s[74:75]")
      "s_cmp_lt_u32 %[s], s90\n"
      "s_nop 5\n"
      "v_add_f32 v152, v184, v185\n"
      "v_add_f32 v154, v186, v187\n"
      "v_add_f32 v153, v188, v189\n"
      "v_add_f32 v155, v190, v191\n"
      "v_cvt_pk_bf16_f32 v152, v152, v153\n"
      "v_cvt_pk_bf16_f32 v154, v154, v155\n"
      "ds_write_b32 v138, v152\n"
      "ds_write_b32 v139, v154\n"
      A_STAMP("s[86:87]") A_CLK("s[76:77]")
      "v_mov_b32 v184, 0\nv_mov_b32 v185, 0\nv_mov_b32 v186, 0\nv_mov_b32 v187, 0\n"
      "v_mov_b32 v188, 0\nv_mov_b32 v189, 0\nv_mov_b32 v190, 0\nv_mov_b32 v191, 0\n"
      "s_waitcnt lgkmcnt(0)\n"
      "s_barrier\n"
      "s_cbranch_scc0 .Lother%=\n"
      A_CLK("s[78:79]")
      "ds_read_b128 v[152:155], v142\n"
      "ds_read_b128 v[168:171], v143\n"
      "ds_read_b128 v[156:159], v142 offset:512\n"
      "ds_read_b128 v[172:175], v143 offset:512\n"
      "ds_read_b128 v[160:163], v142 offset:1024\n"
      "ds_read_b128 v[176:179], v143 offset:1024\n"
      "ds_read_b128 v[164:167], v142 offset:1536\n"
      "ds_read_b128 v[180:183], v143 offset:1536\n"
      WL(7) MFM("224:227", "192:195", "152:155", "0") MFM("228:231", "208:211", "152:155", "0")
      WL(6) MFM("232:235", "168:171", "152:155", "0")
      WL(5) MFM("224:227", "196:199", "156:159", "v[224:227]") MFM("228:231", "212:215", "156:159", "v[228:231]")
      WL(4) MFM("232:235", "172:175", "156:159", "v[232:235]")
      WL(3) MFM("224:227", "200:203", "160:163", "v[224:227]") MFM("228:231", "216:219", "160:163", "v[228:231]")
      WL(2) MFM("232:235", "176:179", "160:163", "v[232:235]")
      WL(1) MFM("224:227", "204:207", "164:167", "v[224:227]") MFM("228:231", "220:223", "164:167", "v[228:231]")
      WL(0) MFM("232:235", "180:183", "164:167", "v[232:235]")
      A_CLK("s[92:93]")
      "s_nop 5\n"
      "v_max3_f32 v152, v224, v225, v226\n"
      "v_max3_f32 v153, v227, v228, v229\n"
      "v_max3_f32 v154, v230, v231, v232\n"
      "v_max3_f32 v152, v152, v153, v233\n"
      "v_max_f32 v153, v152, v154\n"
      "v_max_f32 v152, v152, v154\n"
      "v_cndmask_b32 v156, v224, v228, s[80:81]\n"
      "v_cndmask_b32 v157, v225, v229, s[80:81]\n"
      "v_permlane16_swap_b32 v152, v153\n"
      "v_max_f32 v154, v152, v153\n"
      "v_max_f32 v155, v152, v153\n"
      "v_cndmask_b32 v158, v226, v230, s[80:81]\n"
      "v_cndmask_b32 v159, v227, v231, s[80:81]\n"
      "v_permlane32_swap_b32 v154, v155\n"
      "v_cndmask_b32 v160, v232, v233, s[80:81]\n"
      "v_max_f32 v152, v154, v155\n"
      "v_mul_f32 v152, 0xbe0293ee, v152\n"
      "v_fma_f32 v156, v156, s82, v152\n"
      "v_fma_f32 v157, v157, s82, v152\n"
      "v_fma_f32 v158, v158, s82, v152\n"
      "v_fma_f32 v159, v159, s82, v152\n"
      "v_fma_f32 v160, v160, s82, v152\n"
      "v_exp_f32 v156, v156\n"
      "v_exp_f32 v157, v157\n"
      "v_exp_f32 v158, v158\n"
      "v_exp_f32 v159, v159\n"
      "v_exp_f32 v160, v160\n"
      "v_add_f32 v152, v156, v157\n"
      "v_add_f32 v153, v158, v159\n"
      "v_add_f32 v152, v152, v160\n"
      "v_add_f32 v152, v152, v153\n"
      "v_xor_b32 v156, s83, v156\n"
      "v_xor_b32 v157, s83, v157\n"
      "v_add_f32_dpp v154, v152, v152 row_ror:8" DPP_ALL
      "v_add_f32_dpp v155, v152, v152 row_ror:8" DPP_ALL
      "v_xor_b32 v158, s83, v158\n"
      "v_xor_b32 v159, s83, v159\n"
      "v_permlane16_swap_b32 v154, v155\n"
      "v_add_f32 v152, v154, v155\n"
      "v_add_f32 v153, v154, v155\n"
      "v_xor_b32 v160, s83, v160\n"
      "s_xor_b32 s83, s83, 0x80000000\n"
      "v_permlane32_swap_b32 v152, v153\n"
      "v_add_f32 v152, v152, v153\n"
      "v_rcp_f32 v152, v152\n"
      A_CLK("s[94:95]")
      "s_add_u32 s91, %[s], 2\n"
      "v_mul_f32 v156, v156, v152\n"
      "v_mul_f32 v157, v157, v152\n"
      "v_mul_f32 v158, v158, v152\n"
      "v_mul_f32 v159, v159, v152\n"
      "v_mul_f32 v160, v160, v152\n"
      "v_cvt_pk_bf16_f32 v164, v156, v157\n"
      "v_cvt_pk_bf16_f32 v165, v158, v159\n"
      "v_mov_b32_dpp v161, v160 row_ror:8" DPP_ALL
      "v_mov_b32_dpp v166, v164 row_ror:8" DPP_ALL
      "v_mov_b32_dpp v167, v165 row_ror:8" DPP_ALL
      "v_cvt_pk_bf16_f32 v162, v160, v161\n"
      "v_cvt_pk_bf16_f32 v163, v160, v161\n"
      "s_not_b64 exec, s[80:81]\n"
      "buffer_store_dwordx4 v[164:167], v240, %[ws], 0 offen sc1\n"
      "s_mov_b64 exec, -1\n"
      "v_permlane16_swap_b32 v162, v163\n"
      "s_mov_b32 exec_lo, 0xff\n"
      "s_mov_b32 exec_hi, 0xff\n"
      "buffer_store_dwordx2 v[162:163], v241, %[ws], 0 offen sc1\n"
      "s_mov_b64 exec, -1\n"
      A_STAMP("s[88:89]") A_CLK("s[96:97]") A_SAVE("s84", 0) A_SAVE("s86", 1) A_SAVE("s88", 2) A_SAVECLK
      "s_cmp_ge_u32 %[s], 62\n"
      "s_cbranch_scc1 .Lnext%=\n"
      A_KXWAIT
      ".Lkx%=:\n"
      "v_cmp_gt_u32 vcc, s91, v248\n"
      "s_cbranch_vccz .Lkxok%=\n"
      "buffer_load_dword v248, v244, %[ws], 0 offen sc1\n"
      "s_sub_u32 %[cap], %[cap], 1\n"
      "s_cbranch_scc1 .Lexit%=\n"
      "s_waitcnt vmcnt(0)\n"
      "s_branch .Lkx%=\n"
      ".Lkxok%=:\n"
      "buffer_load_dwordx4 v[192:195], v242, %[ws], %[kxo] offen sc1\n"
      "buffer_load_dwordx4 v[208:211], v242, %[ws], %[kxo] offen offset:64 sc1\n"
      "buffer_load_dwordx4 v[196:199], v242, %[ws], %[kxo] offen offset:2048 sc1\n"
      "buffer_load_dwordx4 v[212:215], v242, %[ws], %[kxo] offen offset:2112 sc1\n"
      "buffer_load_dwordx4 v[200:203], v243, %[ws], %[kxo] offen sc1\n"
      "buffer_load_dwordx4 v[216:219], v243, %[ws], %[kxo] offen offset:64 sc1\n"
      "buffer_load_dwordx4 v[204:207], v243, %[ws], %[kxo] offen offset:2048 sc1\n"
      "buffer_load_dwordx4 v[220:223], v243, %[ws], %[kxo] offen offset:2112 sc1\n"
      "s_add_u32 %[kxo], %[kxo], 0x40000\n"
      SLEEP(D_S0)
      "s_branch .Lnext%=\n"
      ".Lidle%=:\n"
      SLEEP(D_S1)
      "s_branch .Lnext%=\n"
      ".Lother%=:\n"
      "s_cmp_lg_u32 %[w], 6\n"
      "s_cbranch_scc1 .Lidle%=\n"
      "ds_read_b128 v[152:155], v245\n"
      "ds_read_b128 v[156:159], v245 offset:1024\n"
      "s_add_u32 s91, %[s], 1\n"
      "v_mov_b32 v160, s91\n"
      WL(1) "buffer_store_dwordx4 v[152:155], v246, %[ws], %[kmo] offen sc1\n"
      WL(0) "buffer_store_dwordx4 v[156:159], v246, %[ws], %[kmo] offen offset:1024 sc1\n"
      "s_add_u32 %[kmo], %[kmo], 0x10000\n"
      "s_waitcnt vmcnt(0)\n"
      "s_mov_b64 exec, 1\n"
      "buffer_store_dword v160, v247, %[ws], 0 offen sc1\n"
      "s_mov_b64 exec, -1\n"
      SLEEP(D_S0)
      ".Lnext%=:\n"
      A_NEXT
      "s_add_u32 %[mem], %[mem], 0x4000\n"
      "s_add_u32 %[s], %[s], 1\n"
      "s_cmp_lt_u32 %[s], 64\n"
      "s_cbranch_scc1 .Lpoll%=\n"
      ".Lexit%=:\n"
      : [s] "+s"(s), [mem] "+s"(mem), [cap] "+s"(cap), [kxo] "+s"(kxo), [kmo] "+s"(kmo), "+{v[148:151]}"(a0),
        "=&{v[152:183]}"(t0), "=&{v[184:191]}"(t1), "+{v[192:223]}"(kx), "=&{v[224:239]}"(t2), "=&{v248}"(seen) A_DIAG_OUT
      : [ws] "s"(srd(p.ws, WS_BYTES)), [w] "s"(w), "{v[0:31]}"(cat(wa[0], wb[0], wa[1], wb[1])),
        "{v[32:63]}"(cat(wa[2], wb[2], wa[3], wb[3])), "{v[64:95]}"(cat(wa[4], wb[4], wa[5], wb[5])),
        "{v[96:127]}"(cat(wa[6], wb[6], wa[7], wb[7])), "{v[128:143]}"(k0), "{v[240:247]}"(k1)
      : A_SCLOB A_DIAG_CLOB);
}

DEV void load_wo(const Params& p, unsigned h, unsigned w, unsigned c, unsigned g, u4 (&wo)[4][4]) {
  const rsrc_t r = mk(p.wo, 4u << 20);
  const unsigned voff = ((h * 128u + 8u * g) * 512u + 64u * w + 2u * c) * 2u;
#pragma unroll
  for (int b = 0; b < 2; ++b)
#pragma unroll
    for (int f = 0; f < 4; ++f) load_pair(r, voff, f * 32768u + b * 64u, 1024u, wo[2 * b][f], wo[2 * b + 1][f]);
}

DEV void role_vm(const Params& p, unsigned h, char* sm) {
  constexpr unsigned L_MEM = 0, L_V = 8192;
  const unsigned l = threadIdx.x & 63, w = rfl(threadIdx.x >> 6), c = l & 15, g = l >> 4;
  const rsrc_t ws = mk(p.ws, WS_BYTES);
  bool alive = true;

  u8 wv[8], wo[4][2];
  {
    const rsrc_t wr = mk(p.wv, 4u << 20);
    const unsigned voff = ((16u * (g & 1) + (g >> 1)) * 4096u + h * 128u + w * 16u + c) * 2u;
#pragma unroll
    for (int i = 0; i < 8; ++i) {
      const unsigned t = (w + i) & 7;
      wv[i] = cat(load_single(wr, voff, (64u * t) * 8192u, 16384u), load_single(wr, voff, (64u * t + 32u) * 8192u, 16384u));
    }
    u4 t[4][4];
    load_wo(p, h, w, c, g, t);
#pragma unroll
    for (int i = 0; i < 4; ++i) {
      wo[i][0] = cat(t[i][0], t[i][1]);
      wo[i][1] = cat(t[i][2], t[i][3]);
    }
  }
  u4 a0 = load_mem0(p, w, c, g);
  const unsigned cb = wait_init(p, ws, 8, alive);
  STAMP(15, 8 + w);

  const unsigned lb = lds_base(sm);
  u16v k0;
  k0[0] = (c & 1) ? 0xeeeeeeeeu : 0x44444444u;
  k0[1] = w * 1024u + l * 16u;
#pragma unroll
  for (int i = 0; i < 8; ++i) k0[2 + i] = lb + L_MEM + ((w + i) & 7) * 1024u + l * 16u;
  k0[10] = lb + L_V + (w * 16u + 4u * g + ((c >> 1) & 1)) * 16u + (2u * (c >> 2) + (c & 1)) * 2u;
  k0[11] = lb + L_V + l * 16u;
  const unsigned cg = 4u * w + g;
  k0[12] = cb + (B_M + cg) * 16384u + h * 256u + c * 16u;
  k0[13] = O_M2 + cg * 8192u + h * 256u + c * 16u;
  k0[14] = threadIdx.x == 448 ? F_M + h * FL : OOB;
  k0[15] = 0;

  unsigned s = 0, mem = rfl(cb + B_MEM * 16384u - 16384u), cap = rfl(alive ? SPIN_CAP : 0u), m2o = 0;
  u32v t0;
  u16v t1, t2;
  A_DIAG_DECL;
#define VM_ZERO(a) "v_mov_b32 v" #a ", 0\n"
#define VM_ZERO_M \
      VM_ZERO(192) VM_ZERO(193) VM_ZERO(194) VM_ZERO(195) VM_ZERO(196) VM_ZERO(197) VM_ZERO(198) VM_ZERO(199) \
      VM_ZERO(200) VM_ZERO(201) VM_ZERO(202) VM_ZERO(203) VM_ZERO(204) VM_ZERO(205) VM_ZERO(206) VM_ZERO(207)
  asm volatile(
      "s_mov_b32 s83, 0\n"
      VM_ZERO(180) VM_ZERO(184) VM_ZERO(185) VM_ZERO(186) VM_ZERO(187) VM_ZERO_M
      "ds_write_b128 v130, v[148:151]\n"
      "s_branch .Lhave%=\n"
      A_POLL_MEM
      ".Lhave%=:\n"
      A_STAMP("s[84:85]") A_CLK("s[70:71]")
      SMF("184:187", "148:151", "0:7", "v128")
      A_READ_MEM
      WL(6) SMF("184:187", "152:155", "8:15", "v128")
      WL(5) SMF("184:187", "156:159", "16:23", "v128")
      WL(4) SMF("184:187", "160:163", "24:31", "v128")
      WL(3) SMF("184:187", "164:167", "32:39", "v128")
      WL(2) SMF("184:187", "168:171", "40:47", "v128")
      WL(1) SMF("184:187", "172:175", "48:55", "v128")
      WL(0) SMF("184:187", "176:179", "56:63", "v128")
      A_CLK("s[74:75]")
      "s_nop 7\n"
      "v_add_f32 v152, v184, v185\n"
      "v_add_f32 v153, v186, v187\n"
      "v_cvt_pk_bf16_f32 v152, v152, v153\n"
      "ds_write_b16 v138, v152\n"
      "ds_write_b16_d16_hi v138, v152 offset:32\n"
      A_STAMP("s[86:87]") A_CLK("s[76:77]")
      VM_ZERO(184) VM_ZERO(185) VM_ZERO(186) VM_ZERO(187)
      "s_waitcnt lgkmcnt(0)\n"
      "s_barrier\n"
      A_CLK("s[78:79]")
      "ds_read_b128 v[152:155], v139\n"
      "ds_read_b128 v[156:159], v139 offset:1024\n"
      "buffer_store_dword v180, v142, %[ws], 0 offen sc1\n"
      WL(1) SMF("192:195", "152:155", "64:71", "v128") SMF("200:203", "152:155", "96:103", "v128")
      WL(0) SMF("192:195", "156:159", "72:79", "v128") SMF("200:203", "156:159", "104:111", "v128")
      SMF("196:199", "152:155", "80:87", "v128") SMF("204:207", "152:155", "112:119", "v128")
      SMF("196:199", "156:159", "88:95", "v128") SMF("204:207", "156:159", "120:127", "v128")
      A_CLK("s[92:93]")
      "s_nop 2\n"
      "v_add_f32 v192, v192, v193\n"
      "v_add_f32 v200, v200, v201\n"
      "v_add_f32 v194, v194, v195\n"
      "v_add_f32 v202, v202, v203\n"
      "v_cvt_pk_bf16_f32 v160, v192, v194\n"
      "v_cvt_pk_bf16_f32 v162, v200, v202\n"
      "v_bfi_b32 v160, -2, v160, s83\n"
      "v_bfi_b32 v162, -2, v162, s83\n"
      "v_add_f32 v196, v196, v197\n"
      "v_add_f32 v204, v204, v205\n"
      "v_add_f32 v198, v198, v199\n"
      "v_add_f32 v206, v206, v207\n"
      "v_cvt_pk_bf16_f32 v161, v196, v198\n"
      "v_cvt_pk_bf16_f32 v163, v204, v206\n"
      "v_bfi_b32 v161, -2, v161, s83\n"
      "v_bfi_b32 v163, -2, v163, s83\n"
      "v_permlane32_swap_b32 v160, v162\n"
      "s_add_u32 %[s], %[s], 1\n"
      "v_permlane32_swap_b32 v161, v163\n"
      "s_nop 1\n"
      "v_permlane16_swap_b32 v160, v161\n"
      "v_permlane16_swap_b32 v162, v163\n"
      "buffer_store_dwordx4 v[160:163], v140, %[ws], 0 offen sc1\n"
      A_STAMP("s[88:89]") A_CLK("s[94:95]") A_CLK("s[96:97]")
      "buffer_store_dwordx4 v[160:163], v141, %[ws], %[m2o] offen sc1\n"
      "v_mov_b32 v180, %[s]\n"
      VM_ZERO_M
      "s_xor_b32 s83, s83, 1\n"
      "s_add_u32 %[m2o], %[m2o], 0x40000\n"
      "s_add_u32 %[mem], %[mem], 0x4000\n"
      A_NEXT
      SLEEP(D_VM)
      "s_cmp_lt_u32 %[s], 64\n"
      "s_cbranch_scc1 .Lpoll%=\n"
      "s_waitcnt vmcnt(0)\n"
      "s_barrier\n"
      "buffer_store_dword v180, v142, %[ws], 0 offen sc1\n"
      ".Lexit%=:\n"
      : [s] "+s"(s), [mem] "+s"(mem), [cap] "+s"(cap), [m2o] "+s"(m2o), "+{v[148:151]}"(a0), "=&{v[152:183]}"(t0),
        "=&{v[184:199]}"(t1), "=&{v[200:215]}"(t2) A_DIAG_OUT
      : [ws] "s"(srd(p.ws, WS_BYTES)), [w] "s"(w), "{v[0:31]}"(cat(wv[0], wv[1], wv[2], wv[3])),
        "{v[32:63]}"(cat(wv[4], wv[5], wv[6], wv[7])), "{v[64:95]}"(cat(wo[0][0], wo[0][1], wo[1][0], wo[1][1])),
        "{v[96:127]}"(cat(wo[2][0], wo[2][1], wo[3][0], wo[3][1])), "{v[128:143]}"(k0)
      : A_SCLOB A_DIAG_CLOB);
}

DEV void role_v1(const Params& p, unsigned j, char* sm) {
  const unsigned l = threadIdx.x & 63, w = rfl(threadIdx.x >> 6), c = l & 15, g = l >> 4;
  const unsigned cg = j >> 1, rg = j & 1;
  const rsrc_t ws = mk(p.ws, WS_BYTES);
  bool alive = true;
  {
    const unsigned cb0 = crit_base(p);
    const u4 ones = {~0u, ~0u, ~0u, ~0u};
    for (unsigned o = (j * 512u + threadIdx.x) * 16u; o < B_END * 16384u; o += 64u * 512u * 16u) st16c(ones, ws, o, cb0);
    drain();
    unsigned sel = 1;
    if (j == 0 && w == 0) {
      unsigned best[2] = {~0u, ~0u};
      for (int k = 0; k < 8; ++k) {
        const unsigned off = B_PROBE * 16384u + (k & 1) * 8192u + (l & 7) * 16u + (k >> 1) * 1024u;
        const unsigned long long t0 = __builtin_amdgcn_s_memtime();
        asm volatile("s_waitcnt lgkmcnt(0)" ::: "memory");
        st16c(ones, ws, off, cb0);
        drain();
        const unsigned long long t1 = __builtin_amdgcn_s_memtime();
        asm volatile("s_waitcnt lgkmcnt(0)" ::: "memory");
        const unsigned dt = (unsigned)(t1 - t0);
        if (k & 1) best[1] = min(best[1], dt); else best[0] = min(best[0], dt);
      }
      sel = best[1] < best[0] ? 2u : 1u;
    }
    bar();
    if (threadIdx.x == 0) st4c(sel, ws, F_INIT + j * FL, 0);
  }
  if (w >= 4) return;
  const unsigned cb = wait_init(p, ws, 4, alive);
  bar();
  STAMP(15, 8 + w);

  const unsigned lb = lds_base(sm);
  const unsigned piece = rg * 4u + w;
  u16v k0 = {};
  k0[0] = (c & 4) ? 0xeeeeeeeeu : 0x44444444u;
  k0[1] = cb + (B_P + (piece >> 1)) * 16384u + (piece & 1) * 4096u + g * 128u + (c & 7) * 16u + 2048u;
  k0[2] = k0[1] - 2048u + (c >> 3) * 512u;
  k0[3] = cb + (B_M + cg) * 16384u + w * 2048u + l * 16u;
  k0[4] = O_U + (cg * 32u + 8u * w) * 1024u + l * 16u;
  k0[5] = k0[4] + 4096u;
  k0[6] = F_U + (8u * w + (l & 7)) * FL;
  k0[7] = lb + ((g * 2u + ((c >> 1) & 1)) * 8u + 2u * (c >> 2) + (c & 1)) * 16u + w * 4u;
  k0[8] = lb + l * 16u;
  k0[9] = cg * 256u + rg * 128u + (l >> 3) * 16u;

  unsigned s = 0, mem = rfl(cb + B_MEM * 16384u), cap = rfl(alive ? SPIN_CAP : 0u), uo = 0, seen = 0;
  u32v t0, t1;
  A_DIAG_DECL;
#define V1_TAGS(d, a, b, c, OP) "v_bitop3_b32 v" d ", v" a ", v" b ", v" c " bitop3:" OP "\n"
#define V1_ZERO "v_mov_b32 v52, 0\nv_mov_b32 v53, 0\nv_mov_b32 v54, 0\nv_mov_b32 v55, 0\n" \
                "v_mov_b32 v56, 0\nv_mov_b32 v57, 0\nv_mov_b32 v58, 0\nv_mov_b32 v59, 0\n"
#define V1_LOAD_P \
      "buffer_load_dwordx4 v[40:43], v66, %[ws], 0 offen sc1\n" \
      "buffer_load_dwordx4 v[44:47], v66, %[ws], 0 offen offset:1024 sc1\n" \
      "buffer_load_dwordx4 v[48:51], v65, %[ws], 0 offen sc1\n"
#define V1_STEP(P, OP, MWANT, PWANT, NEG) \
      ".Ltop" P "%=:\n" \
      "v_cmp_ge_u32 vcc, %[s], v80\n" \
      "s_cbranch_vccnz .Luw" P "%=\n" \
      "buffer_load_dwordx4 v[0:3], v68, %[ws], %[uo] offen sc1\n" \
      "buffer_load_dwordx4 v[4:7], v68, %[ws], %[uo] offen offset:1024 sc1\n" \
      "buffer_load_dwordx4 v[8:11], v68, %[ws], %[uo] offen offset:2048 sc1\n" \
      "buffer_load_dwordx4 v[12:15], v68, %[ws], %[uo] offen offset:3072 sc1\n" \
      "buffer_load_dwordx4 v[16:19], v69, %[ws], %[uo] offen sc1\n" \
      "buffer_load_dwordx4 v[20:23], v69, %[ws], %[uo] offen offset:1024 sc1\n" \
      "buffer_load_dwordx4 v[24:27], v69, %[ws], %[uo] offen offset:2048 sc1\n" \
      "buffer_load_dwordx4 v[28:31], v69, %[ws], %[uo] offen offset:3072 sc1\n" \
      "buffer_load_dword v80, v70, %[ws], 0 offen sc1\n" \
      "s_add_u32 %[uo], %[uo], 0x100000\n" \
      V1_ZERO \
      SLEEP(D_V1) \
      ".Lboth" P "%=:\n" \
      "buffer_load_dwordx4 v[32:35], v67, %[ws], 0 offen sc1\n" \
      "buffer_load_dwordx4 v[36:39], v67, %[ws], 0 offen offset:1024 sc1\n" \
      V1_LOAD_P \
      WV(3) \
      V1_TAGS("60", "32", "33", "34", OP) V1_TAGS("61", "35", "36", "37", OP) V1_TAGS("60", "60", "38", "39", OP) \
      V1_TAGS("60", "60", "61", "61", OP) \
      "v_and_b32 v60, 1, v60\n" \
      "v_cmp_ne_u32 s[80:81], " MWANT ", v60\n" \
      ".Lp" P "%=:\n" \
      WV(2) SMF("52:55", "40:43", "0:7", "v64") SMF("56:59", "40:43", "8:15", "v64") \
      V1_TAGS("60", "40", "41", "42", OP) \
      WV(1) SMF("52:55", "44:47", "16:23", "v64") SMF("56:59", "44:47", "24:31", "v64") \
      V1_TAGS("60", "60", "43", "44", OP) V1_TAGS("61", "45", "46", "47", OP) \
      WV(0) SMF("52:55", "48:51", "32:39", "v64") \
      V1_TAGS("60", "60", "48", "49", OP) V1_TAGS("61", "61", "50", "51", OP) V1_TAGS("60", "60", "61", "61", OP) \
      "v_and_b32 v60, s82, v60\n" \
      "v_cmp_ne_u32 vcc, " PWANT ", v60\n" \
      "s_or_b64 vcc, vcc, s[80:81]\n" \
      "s_cbranch_scc1 .Lre" P "%=\n" \
      A_STAMP("s[86:87]") A_CLK("s[70:71]") \
      "s_nop 0\n" \
      "v_permlane32_swap_b32 v56, v54\n" \
      "v_permlane32_swap_b32 v57, v55\n" \
      "v_add_f32 v60, v52, v54\n" \
      "v_add_f32 v62, v58, v56\n" \
      "v_add_f32 v61, v53, v55\n" \
      "v_add_f32 v63, v59, v57\n" \
      "v_cndmask_b32 v60, v60, v62, s[84:85]\n" \
      "v_cndmask_b32 v61, v61, v63, s[84:85]\n" \
      "s_cmp_lg_u32 %[w], 0\n" \
      "s_nop 0\n" \
      "v_permlane16_swap_b32 v60, v61\n" \
      "v_add_f32 v60, v60, v61\n" \
      "ds_write_b32 v71, v60 offset:" P "*1024\n" \
      A_CLK("s[72:73]") \
      "s_waitcnt lgkmcnt(0)\n" \
      "s_barrier\n" \
      "s_cbranch_scc1 .Lend" P "%=\n" \
      A_CLK("s[74:75]") \
      "ds_read_b128 v[52:55], v72 offset:" P "*1024\n" \
      "s_waitcnt lgkmcnt(0)\n" \
      A_CLK("s[76:77]") \
      "v_add_f32 v52, v52, v53\n" \
      "v_add_f32 v54, v54, v55\n" \
      "v_add_f32_e64 v52, " NEG "v52, " NEG "v54\n" \
      "s_nop 1\n" \
      "v_mov_b32_dpp v53, v52 quad_perm:[1,0,3,2]" DPP_ALL \
      "v_cvt_pk_bf16_f32 v56, v52, v53\n" \
      "s_nop 1\n" \
      "v_mov_b32_dpp v57, v56 row_shl:2" DPP_ALL \
      "v_mov_b32_dpp v58, v56 row_shl:4" DPP_ALL \
      "v_mov_b32_dpp v59, v56 row_shl:6" DPP_ALL \
      "s_mov_b64 exec, s[90:91]\n" \
      "buffer_store_dwordx4 v[56:59], v73, %[ws], %[mem] offen sc1\n" \
      "s_mov_b64 exec, -1\n" \
      A_STAMP("s[88:89]") A_CLK("s[78:79]") A_SAVE("s86", 1) A_SAVE("s88", 2) A_SAVECLK \
      ".Lend" P "%=:\n" \
      "s_barrier\n" \
      A_NEXT \
      "s_add_u32 %[mem], %[mem], 0x4000\n" \
      "s_add_u32 %[s], %[s], 1\n"
#define V1_SIDE(P) \
      ".Luw" P "%=:\n" \
      "buffer_load_dword v80, v70, %[ws], 0 offen sc1\n" \
      "s_sub_u32 %[cap], %[cap], 1\n" \
      "s_cbranch_scc1 .Lexit%=\n" \
      "s_waitcnt vmcnt(0)\n" \
      "s_branch .Ltop" P "%=\n" \
      ".Lre" P "%=:\n" \
      "s_sub_u32 %[cap], %[cap], 1\n" \
      "s_cbranch_scc1 .Lexit%=\n" \
      "s_cmp_lg_u64 s[80:81], 0\n" \
      V1_ZERO \
      "s_cbranch_scc1 .Lboth" P "%=\n" \
      V1_LOAD_P \
      "s_branch .Lp" P "%=\n"
  asm volatile(
      "s_mov_b32 s82, 0x80008000\n"
      "s_mov_b32 s84, 0\n"
      "s_mov_b32 s85, -1\n"
      "s_mov_b32 s90, 0x01010101\n"
      "s_mov_b32 s91, 0x01010101\n"
      V1_STEP("0", "0xfe", "0", "0", "")
      "s_cmp_ge_u32 %[s], 63\n"
      "s_cbranch_scc1 .Lexit%=\n"
      V1_STEP("1", "0x80", "1", "s82", "-")
      "s_branch .Ltop0%=\n"
      V1_SIDE("0") V1_SIDE("1")
      ".Lexit%=:\n"
      : [s] "+s"(s), [mem] "+s"(mem), [cap] "+s"(cap), [uo] "+s"(uo), "=&{v[0:31]}"(t0), "=&{v[32:63]}"(t1),
        "+{v80}"(seen) A_DIAG_OUT
      : [ws] "s"(srd(p.ws, WS_BYTES)), [w] "s"(w), "{v[64:79]}"(k0)
      : A_SCLOB A_DIAG_CLOB);
}

DEV void load_x(rsrc_t xr, unsigned w, unsigned c, unsigned g, int s, u4 (&xp)[4]) {
  const unsigned voff = ((16u * (w >> 2) + c) * 512u + 128u * (w & 3) + 8u * g) * 2u;
#pragma unroll
  for (int i = 0; i < 4; ++i) xp[i] = ld16(xr, voff + i * 64u, s * 32768u);
}
DEV void put_x(char* sm, unsigned base, unsigned w, unsigned l, const u4 (&xp)[4]) {
#pragma unroll
  for (int i = 0; i < 4; ++i) lds16(sm, base + (4u * w + i) * 1024u + l * 16u) = xp[i];
}

DEV void token_softmax(rsrc_t ws, char* sm, unsigned qb, const u4 (&km)[4], unsigned h, unsigned w, unsigned l, int s) {
  const unsigned c = l & 15, g = l >> 4;
  u4 q[4], k0[4], k1[4];
#pragma unroll
  for (int m = 0; m < 4; ++m) {
    q[m] = lds16(sm, qb + (w * 4u + m) * 1024u);
    k0[m] = lds16(sm, qb + 8192u + m * 1024u);
    k1[m] = lds16(sm, qb + 12288u + m * 1024u);
  }
  sched();
  f4 s0 = {0, 0, 0, 0}, s1 = {0, 0, 0, 0}, sm_ = {0, 0, 0, 0};
#pragma unroll
  for (int m = 0; m < 4; ++m) {
    s0 = mma(k0[m], q[m], s0);
    s1 = mma(k1[m], q[m], s1);
  }
#pragma unroll
  for (int m = 0; m < 4; ++m) sm_ = mma(km[m], q[m], sm_);
  const unsigned tq = 16u * w + c;
  const float ninf = -__builtin_inff();
#pragma unroll
  for (int r = 0; r < 4; ++r) {
    s0[r] = 4u * g + r <= tq ? s0[r] : ninf;
    s1[r] = 16u + 4u * g + r <= tq ? s1[r] : ninf;
  }
  float mx = fmaxf(fmaxf(fmaxf(s0[0], s0[1]), fmaxf(s0[2], s0[3])), fmaxf(fmaxf(s1[0], s1[1]), fmaxf(s1[2], s1[3])));
  mx = group_max(fmaxf(mx, fmaxf(sm_[0], sm_[1])));
  const float mc = -mx * SCALE_LOG2E;
  float e[10];
#pragma unroll
  for (int r = 0; r < 4; ++r) {
    e[r] = __builtin_amdgcn_exp2f(fmaf(s0[r], SCALE_LOG2E, mc));
    e[4 + r] = __builtin_amdgcn_exp2f(fmaf(s1[r], SCALE_LOG2E, mc));
  }
  e[8] = __builtin_amdgcn_exp2f(fmaf(sm_[0], SCALE_LOG2E, mc));
  e[9] = __builtin_amdgcn_exp2f(fmaf(sm_[1], SCALE_LOG2E, mc));
  const float sum = group_sum(((e[0] + e[1]) + (e[2] + e[3])) + ((e[4] + e[5]) + (e[6] + e[7])) + (e[8] + e[9]));
  const float rs = __builtin_amdgcn_rcpf(sum);
  const u4 px = {pk(e[0] * rs, e[1] * rs), pk(e[2] * rs, e[3] * rs), pk(e[4] * rs, e[5] * rs), pk(e[6] * rs, e[7] * rs)};
  st16c(px, ws, O_PX + (w * 32u + h) * 1024u + l * 16u, s * 65536u);
  const unsigned dm = pk(e[8] * rs, e[9] * rs);
  auto r16 = __builtin_amdgcn_permlane16_swap(dm, dm, false, false);
  auto ra = __builtin_amdgcn_permlane32_swap(r16[0], r16[0], false, false);
  auto rb = __builtin_amdgcn_permlane32_swap(r16[1], r16[1], false, false);
  const u4 pm = {ra[0], rb[0], ra[1], rb[1]};
  st16c(pm, ws, g == 0 ? O_PM + (w * 8u + (h >> 2)) * 1024u + ((h & 3) * 16u + c) * 16u : OOB, s * 16384u);
}

DEV void role_s2(const Params& p, unsigned h, char* sm) {
  constexpr int LAG = 4, R = LAG + 1;
  constexpr unsigned L_X = 0, L_QK = 65536;
  const unsigned l = threadIdx.x & 63, w = rfl(threadIdx.x >> 6), c = l & 15, g = l >> 4;
  const rsrc_t ws = mk(p.ws, WS_BYTES), xr = mk(p.bx, 2u << 20);
  bool alive = true;

  u4 xp[4];
  load_x(xr, w, c, g, 0, xp);
  u4 wa[16], wb[16];
  {
    const rsrc_t wr = mk(w < 4 ? p.wq : p.wk, 4u << 20);
    const unsigned voff = (8u * g * 4096u + h * 128u + (w & 3) * 32u + 2u * c) * 2u;
#pragma unroll
    for (int f = 0; f < 16; ++f) load_pair(wr, voff, 32u * f * 8192u, 8192u, wa[f], wb[f]);
  }
  put_x(sm, L_X, w, l, xp);
  load_x(xr, w, c, g, 1, xp);
  bar();
  STAMP(15, 8 + w);
  if (w < 4) __builtin_amdgcn_s_setprio(1);

  const unsigned km_off = w < 2 ? O_KM + h * 2048u + (g * 8u + ((2u * (c >> 2) + (c & 1)) ^ swz(g))) * 16u : OOB;
  const unsigned km_flag = w < 2 ? F_KM + h * FL : OOB;
  const unsigned px_flag = w < 2 && l == 0 ? F_PX + (w * 32u + h) * FL : OOB;
  const unsigned kx_flag = threadIdx.x == 448 ? F_KX + h * FL : OOB;
  const unsigned kx_off = w >= 4 ? O_KX + h * 8192u + (w & 3) * 2048u + l * 16u : OOB;
  unsigned km_seen = 0;
  int slot = 0;
  for (int it = 0; it < NSEG; ++it) {
    const int s = max(it - LAG, 0);
    const bool soft = it >= LAG && w < 2;
    if (soft && !all_lanes(km_seen > (unsigned)s)) wait_flags(ws, km_flag, s + 1, alive);
    if (w == 0 || w == 4) STAMP(3 + (w >> 2) * 4, it);
    u4 km[4];
#pragma unroll
    for (int m = 0; m < 4; ++m) km[m] = ld16c(ws, km_off + m * 512u, s * 65536u);
    km_seen = ld4c(ws, km_flag, 0);
    f4 acc[2][2] = {};
    const unsigned xb = L_X + (it & 1) * 32768u + l * 16u;
    u4 x[3][2];
#pragma unroll
    for (int k = 0; k < 2; ++k)
#pragma unroll
      for (int t = 0; t < 2; ++t) x[k][t] = lds16(sm, xb + (t * 16 + k) * 1024u);
#pragma unroll
    for (int ks = 0; ks < 16; ++ks) {
      sched();
      if (ks + 2 < 16) {
#pragma unroll
        for (int t = 0; t < 2; ++t) x[(ks + 2) % 3][t] = lds16(sm, xb + (t * 16 + ks + 2) * 1024u);
      }
      sched();
#pragma unroll
      for (int t = 0; t < 2; ++t) {
        acc[0][t] = mma(wa[ks], x[ks % 3][t], acc[0][t]);
        acc[1][t] = mma(wb[ks], x[ks % 3][t], acc[1][t]);
      }
    }
    sched();
    asm volatile("s_waitcnt vmcnt(6)" ::: "memory");
    if (w == 0 || w == 4) STAMP(4 + (w >> 2) * 4, it);
    st4c(s, ws, px_flag, 0);
    put_x(sm, L_X + ((it + 1) & 1) * 32768u, w, l, xp);
#pragma unroll
    for (int t = 0; t < 2; ++t) {
      const u4 o = {pk(acc[0][t][0], acc[1][t][0]), pk(acc[0][t][1], acc[1][t][1]), pk(acc[0][t][2], acc[1][t][2]),
                    pk(acc[0][t][3], acc[1][t][3])};
      lds16(sm, L_QK + slot * 16384u + (w >> 2) * 8192u + (t * 4u + (w & 3)) * 1024u + l * 16u) = o;
      st16c(o, ws, kx_off + t * 1024u, it * 262144u);
    }
    load_x(xr, w, c, g, it + 2, xp);
    if (w == 0 || w == 4) STAMP(5 + (w >> 2) * 4, it);
    slot = slot == R - 1 ? 0 : slot + 1;
    if (soft) {
      token_softmax(ws, sm, L_QK + slot * 16384u + l * 16u, km, h, w, l, s);
      if (w == 0) STAMP(2, s);
    }
    if (it < LAG) drain();
    bar();
    st4c(it < LAG ? it + 1 : it, ws, kx_flag, 0);
  }
  if (w >= 2) return;
  for (int s = NSEG - LAG; s < NSEG; ++s) {
    wait_flags(ws, km_flag, s + 1, alive);
    u4 km[4];
#pragma unroll
    for (int m = 0; m < 4; ++m) km[m] = ld16c(ws, km_off + m * 512u, s * 65536u);
    st4c(s, ws, px_flag, 0);
    slot = slot == R - 1 ? 0 : slot + 1;
    token_softmax(ws, sm, L_QK + slot * 16384u + l * 16u, km, h, w, l, s);
    if (w == 0) STAMP(2, s);
  }
  drain();
  st4c(NSEG, ws, px_flag, 0);
}

DEV void role_vu(const Params& p, unsigned h, char* sm) {
  constexpr unsigned L_X = 0, L_V = 65536;
  constexpr int EAGER = 3;
  const unsigned l = threadIdx.x & 63, w = rfl(threadIdx.x >> 6), c = l & 15, g = l >> 4;
  const rsrc_t ws = mk(p.ws, WS_BYTES), xr = mk(p.bx, 2u << 20);

  u4 xp[4];
  load_x(xr, w, c, g, 0, xp);
  u4 wv[16], wo[4][4];
  {
    const rsrc_t wr = mk(p.wv, 4u << 20);
    const unsigned voff = (8u * g * 4096u + h * 128u + w * 16u + c) * 2u;
#pragma unroll
    for (int f = 0; f < 16; ++f) wv[f] = load_single(wr, voff, 32u * f * 8192u, 8192u);
    load_wo(p, h, w, c, g, wo);
  }
  put_x(sm, L_X, w, l, xp);
  load_x(xr, w, c, g, 1, xp);
  bar();
  STAMP(15, 8 + w);

  const unsigned v_off = L_V + ((w >> 1) * 64u + (2u * (w & 1) + (g >> 1)) * 16u + c) * 16u + (g & 1) * 8u;
  const unsigned flag = threadIdx.x == 448 ? F_U + h * FL : OOB;
  for (int s = 0; s < NSEG; ++s) {
    f4 acc[2] = {};
    const unsigned xb = L_X + (s & 1) * 32768u + l * 16u;
    u4 x[4][2];
#pragma unroll
    for (int k = 0; k < 3; ++k)
#pragma unroll
      for (int t = 0; t < 2; ++t) x[k][t] = lds16(sm, xb + (t * 16 + k) * 1024u);
#pragma unroll
    for (int ks = 0; ks < 16; ++ks) {
      sched();
      if (ks + 3 < 16) {
#pragma unroll
        for (int t = 0; t < 2; ++t) x[(ks + 3) & 3][t] = lds16(sm, xb + (t * 16 + ks + 3) * 1024u);
      }
      sched();
#pragma unroll
      for (int t = 0; t < 2; ++t) acc[t] = mma(wv[ks], x[ks & 3][t], acc[t]);
    }
    sched();
#pragma unroll
    for (int t = 0; t < 2; ++t)
      *reinterpret_cast<u2*>(sm + v_off + (s & 1) * 8192u + t * 4096u) = u2{pk(acc[t][0], acc[t][1]), pk(acc[t][2], acc[t][3])};
    if (w == 0) STAMP(3, s);
    asm volatile("s_waitcnt vmcnt(5)" ::: "memory");
    if (w == 0) STAMP(4, s);
    put_x(sm, L_X + ((s + 1) & 1) * 32768u, w, l, xp);
    load_x(xr, w, c, g, s + 2, xp);
    bar();
    st4c(max(s - 1, min(s, EAGER)), ws, flag, 0);
    if (w == 0) STAMP(5, s);
    u4 v[8];
#pragma unroll
    for (int k = 0; k < 8; ++k) v[k] = lds16(sm, L_V + (s & 1) * 8192u + ((k & 1) * 4 + (k >> 1)) * 1024u + l * 16u);
    sched();
    f4 u[4][2] = {};
#pragma unroll
    for (int k = 0; k < 8; ++k)
#pragma unroll
      for (int i = 0; i < 4; ++i) u[i][k & 1] = mma(v[k], wo[i][k >> 1], u[i][k & 1]);
#pragma unroll
    for (int i = 0; i < 4; ++i) {
      const u4 o = {pk(u[i][0][0], u[i][0][1]), pk(u[i][0][2], u[i][0][3]), pk(u[i][1][0], u[i][1][1]),
                    pk(u[i][1][2], u[i][1][3])};
      st16c(o, ws, O_U + ((4u * w + i) * 32u + h) * 1024u + l * 16u, s * 1048576u);
    }
    if (s < EAGER || s == NSEG - 1) {
      drain();
      bar();
      st4c(s + 1, ws, flag, 0);
    }
    if (w == 0) STAMP(2, s);
  }
}

DEV void role_v2(const Params& p, unsigned j, char* sm) {
  constexpr unsigned ROW = 144, PART = 16 * ROW, BUF = 8 * PART;
  const unsigned l = threadIdx.x & 63, w = rfl(threadIdx.x >> 6), c = l & 15, g = l >> 4;
  const unsigned b = j >> 1, qt = j & 1;
  const rsrc_t ws = mk(p.ws, WS_BYTES), outr = mk(p.out, 8u << 20);
  bool alive = true;

  const unsigned hq = 4u * w + (l & 3);
  const unsigned uf_off = F_U + hq * FL;
  const unsigned pf_off = (l & 4) ? F_M + hq * FL : F_PX + (qt * 32u + hq) * FL;
  const unsigned u_off = O_U + (2u * b * 32u + 4u * w) * 1024u + l * 16u;
  const unsigned m_off = O_M2 + 2u * b * 8192u + w * 1024u + l * 16u;
  const unsigned px_off = O_PX + (qt * 32u + 4u * w) * 1024u + l * 16u;
  const unsigned pm_off = O_PM + (qt * 8u + w) * 1024u + l * 16u;
  const unsigned out_off = ((16u * qt + (threadIdx.x >> 5)) * 1024u + 512u + 32u * b + (threadIdx.x & 31)) * 4u;
  unsigned u_seen = wait_flags(ws, uf_off, 1u, alive), p_seen = 0;
  u4 ua[2][4];
#pragma unroll
  for (int k = 0; k < 8; ++k) ua[k >> 2][k & 3] = ld16c(ws, u_off + (k >> 2) * 32768u + (k & 3) * 1024u, 0);

  for (int s = 0; s < NSEG; ++s) {
    if (!all_lanes(p_seen > (unsigned)s)) p_seen = wait_flags(ws, pf_off, s + 1, alive);
    u4 px[4];
#pragma unroll
    for (int i = 0; i < 4; ++i) px[i] = ld16c(ws, px_off + i * 1024u, s * 65536u);
    const u4 pm = ld16c(ws, pm_off, s * 16384u);
    const u4 m0 = ld16c(ws, m_off, s * 262144u), m1 = ld16c(ws, m_off + 8192u, s * 262144u);
    f4 a0 = {0, 0, 0, 0}, a1 = {0, 0, 0, 0};
#pragma unroll
    for (int i = 0; i < 4; ++i) {
      a0 = mma(ua[0][i], px[i], a0);
      a1 = mma(ua[1][i], px[i], a1);
    }
    a0 = mma(m0, pm, a0);
    a1 = mma(m1, pm, a1);
    sched();
    if (s + 1 < NSEG) {
      if (!all_lanes(u_seen > (unsigned)s + 1u)) u_seen = wait_flags(ws, uf_off, s + 2, alive);
#pragma unroll
      for (int k = 0; k < 8; ++k)
        ua[k >> 2][k & 3] = ld16c(ws, u_off + (k >> 2) * 32768u + (k & 3) * 1024u, (s + 1) * 1048576u);
    }
    sched();
    const unsigned rb = (s & 1) * BUF;
    *reinterpret_cast<f4*>(sm + rb + w * PART + c * ROW + g * 32u) = f4{a0[0], a1[0], a0[1], a1[1]};
    *reinterpret_cast<f4*>(sm + rb + w * PART + c * ROW + g * 32u + 16u) = f4{a0[2], a1[2], a0[3], a1[3]};
    bar();
    float v = 0.f;
#pragma unroll
    for (int k = 0; k < 8; ++k)
      v += *reinterpret_cast<const float*>(sm + rb + k * PART + (threadIdx.x >> 5) * ROW + (threadIdx.x & 31) * 4u);
    __builtin_amdgcn_raw_buffer_store_b32(__float_as_uint(rbf(v)), outr, out_off, s * 131072u, 0);
    if (w == 0) STAMP(2, s);
  }
}

DEV f2 cmul(f2 a, f2 b) {
  float re, im;
  asm("v_mul_f32 %0, %3, %5\n\tv_mul_f32 %1, %3, %4\n\tv_fma_f32 %0, %2, %4, -%0\n\tv_fma_f32 %1, %2, %5, %1"
      : "=&v"(re), "=&v"(im) : "v"(a[0]), "v"(a[1]), "v"(b[0]), "v"(b[1]));
  return f2{re, im};
}
DEV f2 cfma(f2 a, f2 b, f2 c) {
  float re, im;
  asm("v_fma_f32 %0, -%3, %5, %6\n\tv_fma_f32 %1, %3, %4, %7\n\tv_fma_f32 %0, %2, %4, %0\n\tv_fma_f32 %1, %2, %5, %1"
      : "=&v"(re), "=&v"(im) : "v"(a[0]), "v"(a[1]), "v"(b[0]), "v"(b[1]), "v"(c[0]), "v"(c[1]));
  return f2{re, im};
}
DEV f2 from_group(float re, float im, unsigned c, int k) {
  return f2{__shfl(re, (int)c + 16 * k, 64), __shfl(im, (int)c + 16 * k, 64)};
}

DEV void role_a(const Params& p, unsigned ci, char* sm) {
  constexpr unsigned GS = 272, KS = 4 * GS, TS = 32 * KS + 32, L_TAB = 4 * TS;
  const unsigned tid = threadIdx.x, l = tid & 63, w = rfl(tid >> 6), c = l & 15, g = l >> 4;
  const rsrc_t ws = mk(p.ws, WS_BYTES), xr = mk(p.ax, 2u << 20), outr = mk(p.out, 8u << 20);
  bool alive = true;

  {
    const unsigned voff = ((64u * ci + 16u * (c >> 2) + 4u * (w >> 1) + (c & 3)) * 512u + 256u * (w & 1) + 8u * g) * 2u;
    u4 xp[8];
#pragma unroll
    for (int i = 0; i < 8; ++i) xp[i] = ld16(xr, voff + i * 64u, 0);
#pragma unroll
    for (int i = 0; i < 8; ++i) lds16(sm, (8u * w + i) * 1024u + l * 16u) = xp[i];
  }
  f2 lam64;
  {
    const float lr = bf2f(((const unsigned short*)p.lre)[tid]), li = bf2f(((const unsigned short*)p.lim)[tid]);
    const float dt = rbf(expf(bf2f(((const unsigned short*)p.ldt)[tid])));
    const float er = expf(dt * lr);
    float sn, cs;
    sincosf(dt * li, &sn, &cs);
    f2 lam = {er * cs, er * sn};
    asm volatile("s_nop 1" : "+v"(lam));
    const float nr = lam[0] - 1.0f, ni = lam[1];
    f2 coef;
    if (fabsf(lr) >= fabsf(li)) {
      const float t = li / lr, d = 1.0f / (lr + li * t);
      coef = f2{smul(nr + smul(ni, t), d), smul(ni - smul(nr, t), d)};
    } else {
      const float t = lr / li, d = 1.0f / (lr * t + li);
      coef = f2{smul(smul(nr, t) + ni, d), smul(smul(ni, t) - nr, d)};
    }
    f2 l16 = cmul(lam, lam);
    l16 = cmul(l16, l16);
    l16 = cmul(l16, l16);
    l16 = cmul(l16, l16);
    lam64 = cmul(l16, l16);
    lam64 = cmul(lam64, lam64);
    *reinterpret_cast<f4*>(sm + L_TAB + tid * 32u) = f4{lam[0], lam[1], coef[0], coef[1]};
    *reinterpret_cast<f2*>(sm + L_TAB + tid * 32u + 16u) = l16;
  }
  bar();

  f4 hr[4][4] = {}, hi[4][4] = {};
  {
    const rsrc_t br = mk(p.bre, 1u << 19), bi = mk(p.bim, 1u << 19);
    const unsigned voff = ((64u * w + c) * 512u + 8u * g) * 2u;
    u4 fr[4], fi[4];
#pragma unroll
    for (int k = 0; k < 4; ++k) {
      fr[k] = ld16(br, voff + k * 64u, 0);
      fi[k] = ld16(bi, voff + k * 64u, 0);
    }
#pragma unroll
    for (int n = 0; n < 64; ++n) {
      const int ct = n >> 4, ks = n & 15;
      const u4 r = fr[n & 3], i = fi[n & 3];
      fence();
      if (n + 4 < 64) {
        fr[n & 3] = ld16(br, voff + ((n + 4) & 15) * 64u, ((n + 4) >> 4) * 16384u);
        fi[n & 3] = ld16(bi, voff + ((n + 4) & 15) * 64u, ((n + 4) >> 4) * 16384u);
      }
#pragma unroll
      for (int t = 0; t < 4; ++t) {
        const u4 x = lds16(sm, (t * 16 + ks) * 1024u + l * 16u);
        hr[ct][t] = mma(x, r, hr[ct][t]);
        hi[ct][t] = mma(x, i, hi[ct][t]);
      }
    }
  }
  __builtin_amdgcn_sched_barrier(0);
  asm volatile("s_nop 7\n\ts_nop 7" ::: "memory");
  __builtin_amdgcn_sched_barrier(0);
#pragma unroll
  for (int ct = 0; ct < 4; ++ct) {
    const unsigned ch = 64u * w + 16u * ct + c;
    const f4 tb = *reinterpret_cast<const f4*>(sm + L_TAB + ch * 32u);
    const f2 l16 = *reinterpret_cast<const f2*>(sm + L_TAB + ch * 32u + 16u);
    const f2 lam = {tb[0], tb[1]}, coef = {tb[2], tb[3]};
    f2 hh = {0.f, 0.f};
#pragma unroll
    for (int i = 0; i < 16; ++i) {
      hh = cfma(lam, hh, cmul(coef, f2{hr[ct][i >> 2][i & 3], hi[ct][i >> 2][i & 3]}));
      hr[ct][i >> 2][i & 3] = hh[0];
      hi[ct][i >> 2][i & 3] = hh[1];
    }
    f2 e = from_group(hh[0], hh[1], c, 0);
#pragma unroll
    for (int k = 1; k < 4; ++k) e = cfma(l16, e, from_group(hh[0], hh[1], c, k));
    if (g == 0) st8c(__builtin_bit_cast(u2, e), ws, O_AE + ci * 4096u + ch * 8u, 0);
  }
  drain();
  bar();
  if (tid == 0) st4c(1u, ws, F_A + ci * FL, 0);
  wait_flags(ws, F_A + (l & 31) * FL, (l & 31) < ci ? 1u : 0u, alive);
  {
    f2 hin = {0.f, 0.f};
#pragma unroll 8
    for (unsigned k = 0; k < ci; ++k) hin = cfma(lam64, hin, __builtin_bit_cast(f2, ld8c(ws, O_AE + tid * 8u, k * 4096u)));
    *reinterpret_cast<f2*>(sm + L_TAB + tid * 32u + 24u) = hin;
  }
  bar();
#pragma unroll
  for (int ct = 0; ct < 4; ++ct) {
    const unsigned ch = 64u * w + 16u * ct + c;
    const f4 t0 = *reinterpret_cast<const f4*>(sm + L_TAB + ch * 32u);
    const f4 t1 = *reinterpret_cast<const f4*>(sm + L_TAB + ch * 32u + 16u);
    const f2 lam = {t0[0], t0[1]}, l16 = {t1[0], t1[1]};
    f2 z = {t1[2], t1[3]};
#pragma unroll
    for (int k = 0; k < 3; ++k) {
      const f2 n = cfma(l16, z, from_group(hr[ct][3][3], hi[ct][3][3], c, k));
      z = (unsigned)k < g ? n : z;
    }
    const unsigned hb = g * TS + (ch >> 5) * KS + ((ch >> 3) & 3) * GS + (ch & 7) * 2u;
#pragma unroll
    for (int i = 0; i < 16; ++i) {
      z = cmul(lam, z);
      const unsigned d = pk(2.0f * (hr[ct][i >> 2][i & 3] + z[0]), -2.0f * (hi[ct][i >> 2][i & 3] + z[1]));
      lds_put2(sm, hb + i * 16u, d);
      lds_put2(sm, hb + 16u * KS + i * 16u, d >> 16);
    }
  }
  bar();
  f4 y[4][4] = {};
  {
    const rsrc_t cr = mk(p.cre, 1u << 19), cm = mk(p.cim, 1u << 19);
    const unsigned voff = ((64u * w + c) * 512u + 8u * g) * 2u;
    u4 fa[2][4];
#pragma unroll
    for (int k = 0; k < 2; ++k)
#pragma unroll
      for (int d = 0; d < 4; ++d) fa[k][d] = ld16(cr, voff + k * 64u, d * 16384u);
#pragma unroll
    for (int ks = 0; ks < 32; ++ks) {
      u4 a[4];
      fence();
#pragma unroll
      for (int d = 0; d < 4; ++d) {
        a[d] = fa[ks & 1][d];
        if (ks + 2 < 32) fa[ks & 1][d] = ld16(ks + 2 < 16 ? cr : cm, voff + ((ks + 2) & 15) * 64u, d * 16384u);
      }
#pragma unroll
      for (int t = 0; t < 4; ++t) {
        const u4 hb = lds16(sm, t * TS + ks * KS + g * GS + c * 16u);
#pragma unroll
        for (int d = 0; d < 4; ++d) y[d][t] = mma(a[d], hb, y[d][t]);
      }
    }
  }
  const rsrc_t dr = mk(p.dsk, 1024);
#pragma unroll
  for (int d = 0; d < 4; ++d) {
    const unsigned col = 64u * w + 16u * d + 4u * g;
    const u2 dk = ld8(dr, col * 2u, 0);
#pragma unroll
    for (int t = 0; t < 4; ++t) {
      const unsigned row = 64u * ci + 16u * t + c;
      const u2 xv = ld8(xr, (row * 512u + col) * 2u, 0);
      const f4 o = {y[d][t][0] + rbf(smul(bf2f(xv[0] & 0xffffu), bf2f(dk[0] & 0xffffu))),
                    y[d][t][1] + rbf(smul(bf2f(xv[0] >> 16), bf2f(dk[0] >> 16))),
                    y[d][t][2] + rbf(smul(bf2f(xv[1] & 0xffffu), bf2f(dk[1] & 0xffffu))),
                    y[d][t][3] + rbf(smul(bf2f(xv[1] >> 16), bf2f(dk[1] >> 16)))};
      __builtin_amdgcn_raw_buffer_store_b128(__builtin_bit_cast(u4, o), outr, (row * 1024u + col) * 4u, 0, 0);
    }
  }
  if (w == 0) STAMP(2, 0);
}

__global__ void __launch_bounds__(512, 2) fused_kernel(Params p) {
  extern __shared__ __attribute__((aligned(16))) char smem[];
  const unsigned x = blockIdx.x & 7, r = blockIdx.x >> 3;
  if (threadIdx.x < 64) TSTAMP(0);
  if (x == 1 || x == 2) {
    role_v1(p, (x - 1) * 32u + r, smem);
  } else if (x < 4) {
    const unsigned h = (x ? 16u : 0u) + (r >> 1);
    if (r & 1) role_vm(p, h, smem); else role_s1(p, h, smem);
  } else {
    const unsigned i = (x - 4) * 8u + (r >> 2);
    switch (r & 3) {
      case 0: role_s2(p, i, smem); break;
      case 1: role_vu(p, i, smem); break;
      case 2: role_v2(p, i, smem); break;
      case 3: role_a(p, i, smem); break;
    }
  }
  if (threadIdx.x < 64) TSTAMP(2);
}

extern "C" void kernel_launch(void* const* d_in, const int* in_sizes, int n_in, void* d_out, int out_size, void* d_ws,
                              size_t ws_size, hipStream_t stream) {
  Params p;
  p.ax = d_in[0]; p.lre = d_in[1]; p.lim = d_in[2]; p.bre = d_in[3]; p.bim = d_in[4]; p.cre = d_in[5];
  p.cim = d_in[6]; p.ldt = d_in[7]; p.dsk = d_in[8]; p.bx = d_in[9]; p.mem0 = d_in[10]; p.wq = d_in[11];
  p.wk = d_in[12]; p.wv = d_in[13]; p.wo = d_in[14];
  p.out = (float*)d_out;
  p.ws = (char*)d_ws;
  (void)hipFuncSetAttribute((const void*)fused_kernel, hipFuncAttributeMaxDynamicSharedMemorySize, DYN_LDS);
  (void)hipMemsetAsync(d_ws, 0, FLAG_BYTES, stream);
  fused_kernel<<<256, 512, DYN_LDS, stream>>>(p);
}
```

```cpp
#define TIMER
#include <cstdio>
#include <hip/hip_runtime.h>
#include <hip/hip_bf16.h>
#include <stdint.h>

typedef __attribute__((ext_vector_type(2))) unsigned u2;
typedef __attribute__((ext_vector_type(4))) unsigned u4;
typedef __attribute__((ext_vector_type(8))) unsigned u8;
typedef __attribute__((ext_vector_type(16))) unsigned u16v;
typedef __attribute__((ext_vector_type(32))) unsigned u32v;
typedef __attribute__((ext_vector_type(2))) float f2;
typedef __attribute__((ext_vector_type(4))) float f4;
typedef __attribute__((ext_vector_type(2))) __bf16 bf2;
typedef __attribute__((ext_vector_type(8))) __bf16 bf8;
typedef __attribute__((ext_vector_type(16))) __bf16 bf16v;
typedef __amdgpu_buffer_rsrc_t rsrc_t;
#define DEV __device__ __forceinline__

struct Params {
  const void *ax, *lre, *lim, *bre, *bim, *cre, *cim, *ldt, *dsk, *bx, *mem0, *wq, *wk, *wv, *wo;
  float* out;
  char* ws;
};

#define WSLEEP 4
constexpr int NSEG = 64;
constexpr unsigned SPIN_CAP = 400000u;
constexpr unsigned DYN_LDS = 156u * 1024u;

constexpr unsigned FL = 128, F_INIT = 0, F_KM = 64 * FL, F_KX = 96 * FL, F_PX = 128 * FL, F_M = 192 * FL, F_U = 224 * FL,
                   F_A = 256 * FL, FLAG_BYTES = 288 * FL;
constexpr unsigned O_AE = 5u << 20, O_DIAG = 6u << 20;
constexpr unsigned O_KX = 8u << 20, O_KM = 24u << 20, O_PX = 28u << 20, O_PM = 32u << 20, O_M2 = 34u << 20,
                   O_U = 50u << 20, WS_BYTES = 114u << 20;
constexpr unsigned OOB = 1u << 30;
constexpr unsigned B_MEM = 0, B_P = 63, B_M = 67, B_END = 99, B_PROBE = 100;

DEV rsrc_t mk(const void* p, unsigned bytes) {
  return __builtin_amdgcn_make_buffer_rsrc((void*)p, 0, bytes, 0x00020000);
}
DEV unsigned rfl(unsigned v) { return __builtin_amdgcn_readfirstlane(v); }
DEV u4 ld16(rsrc_t r, unsigned v, unsigned s) { return __builtin_amdgcn_raw_buffer_load_b128(r, v, s, 0); }
DEV u2 ld8(rsrc_t r, unsigned v, unsigned s) { return __builtin_amdgcn_raw_buffer_load_b64(r, v, s, 0); }
DEV unsigned ld4(rsrc_t r, unsigned v, unsigned s) { return __builtin_amdgcn_raw_buffer_load_b32(r, v, s, 0); }
DEV unsigned ld2(rsrc_t r, unsigned v, unsigned s) { return __builtin_amdgcn_raw_buffer_load_b16(r, v, s, 0); }
DEV u4 ld16c(rsrc_t r, unsigned v, unsigned s) { return __builtin_amdgcn_raw_buffer_load_b128(r, v, s, 16); }
DEV u2 ld8c(rsrc_t r, unsigned v, unsigned s) { return __builtin_amdgcn_raw_buffer_load_b64(r, v, s, 16); }
DEV unsigned ld4c(rsrc_t r, unsigned v, unsigned s) { return __builtin_amdgcn_raw_buffer_load_b32(r, v, s, 16); }
DEV void st16c(u4 d, rsrc_t r, unsigned v, unsigned s) { __builtin_amdgcn_raw_buffer_store_b128(d, r, v + s, 0, 16); }
DEV void st8c(u2 d, rsrc_t r, unsigned v, unsigned s) { __builtin_amdgcn_raw_buffer_store_b64(d, r, v, s, 16); }
DEV void st4c(unsigned d, rsrc_t r, unsigned v, unsigned s) { __builtin_amdgcn_raw_buffer_store_b32(d, r, v, s, 16); }
DEV void fence() { asm volatile("" ::: "memory"); }
DEV void drain() { asm volatile("s_waitcnt vmcnt(0)" ::: "memory"); }
DEV void bar() { asm volatile("s_waitcnt lgkmcnt(0)\n\ts_barrier" ::: "memory"); }
DEV bool all_lanes(bool ok) { return __builtin_amdgcn_ballot_w64(!ok) == 0ull; }

DEV float bf2f(unsigned h) { return __uint_as_float(h << 16); }
DEV unsigned pk(float a, float b) {
  f2 v = {a, b};
  return __builtin_bit_cast(unsigned, __builtin_convertvector(v, bf2));
}
DEV float rbf(float a) { return bf2f(pk(a, a) & 0xffffu); }
DEV float smul(float a, float b) {
  float r;
  asm("v_mul_f32 %0, %1, %2" : "=v"(r) : "v"(a), "v"(b));
  return r;
}
DEV float sadd(float a, float b) {
  float r;
  asm("v_add_f32 %0, %1, %2" : "=v"(r) : "v"(a), "v"(b));
  return r;
}
DEV f4 mma(u4 a, u4 b, f4 c) {
  return __builtin_amdgcn_mfma_f32_16x16x32_bf16(__builtin_bit_cast(bf8, a), __builtin_bit_cast(bf8, b), c, 0, 0, 0);
}
DEV f4 smma(u4 a, u8 b, f4 c, unsigned idx) {
  return __builtin_amdgcn_smfmac_f32_16x16x64_bf16(__builtin_bit_cast(bf8, a), __builtin_bit_cast(bf16v, b), c,
                                                   (int)idx, 0, 0);
}
DEV float swap32_add(float a, float b) {
  auto r = __builtin_amdgcn_permlane32_swap(__float_as_uint(a), __float_as_uint(b), false, false);
  return __uint_as_float(r[0]) + __uint_as_float(r[1]);
}
DEV float swap16_add(float a, float b) {
  auto r = __builtin_amdgcn_permlane16_swap(__float_as_uint(a), __float_as_uint(b), false, false);
  return __uint_as_float(r[0]) + __uint_as_float(r[1]);
}
DEV float group_max(float m) {
  auto r = __builtin_amdgcn_permlane16_swap(__float_as_uint(m), __float_as_uint(m), false, false);
  m = fmaxf(__uint_as_float(r[0]), __uint_as_float(r[1]));
  auto q = __builtin_amdgcn_permlane32_swap(__float_as_uint(m), __float_as_uint(m), false, false);
  return fmaxf(__uint_as_float(q[0]), __uint_as_float(q[1]));
}
DEV float group_sum(float m) {
  const float t = swap16_add(m, m);
  return swap32_add(t, t);
}
DEV u4& lds16(char* sm, unsigned off) { return *reinterpret_cast<u4*>(sm + off); }
DEV void lds_put2(char* sm, unsigned off, unsigned v) { *reinterpret_cast<unsigned short*>(sm + off) = (unsigned short)v; }

#define STAMP(slot, idx) do { } while (0)

#define TSTAMP(idx) do { \
  __builtin_amdgcn_raw_buffer_store_b32((unsigned)__builtin_amdgcn_s_memrealtime(), mk(p.ws, WS_BYTES), O_DIAG + (blockIdx.x * 1024u + 14u * 64u + (idx)) * 4u, 0, 0); } while (0)

DEV unsigned wait_flags(rsrc_t ws, unsigned off, unsigned need, bool& alive) {
  unsigned v = 0;
  for (unsigned it = 0; alive; ++it) {
    fence();
    v = ld4c(ws, off, 0);
    if (all_lanes(v >= need)) break;
    if (it > SPIN_CAP) alive = false;
    __builtin_amdgcn_s_sleep(WSLEEP);
  }
  fence();
  return v;
}
DEV unsigned crit_base(const Params& p) {
  const unsigned lo = (unsigned)(uintptr_t)p.ws;
  return ((lo + 65536u + 0x1fffffu) & ~0x1fffffu) - lo;
}
DEV unsigned wait_init(const Params& p, rsrc_t ws, unsigned n, bool& alive) {
  const unsigned l = threadIdx.x & 63, w = threadIdx.x >> 6;
  const unsigned v = wait_flags(ws, F_INIT + (l ? (l & (64u / n - 1u)) + w * (64u / n) : 0u) * FL, 1u, alive);
  return crit_base(p) + (rfl(v) == 2u ? 8192u : 0u);
}

DEV void load_pair(rsrc_t r, unsigned voff, unsigned soff, unsigned rstride, u4& a, u4& b) {
  unsigned d[8];
#pragma unroll
  for (int j = 0; j < 8; ++j) d[j] = ld4(r, voff, soff + j * rstride);
#pragma unroll
  for (int j = 0; j < 4; ++j) {
    a[j] = __builtin_amdgcn_perm(d[2 * j + 1], d[2 * j], 0x05040100u);
    b[j] = __builtin_amdgcn_perm(d[2 * j + 1], d[2 * j], 0x07060302u);
  }
}
DEV u4 load_single(rsrc_t r, unsigned voff, unsigned soff, unsigned rstride) {
  unsigned d[8];
  u4 a;
#pragma unroll
  for (int j = 0; j < 8; ++j) d[j] = ld2(r, voff, soff + j * rstride);
#pragma unroll
  for (int j = 0; j < 4; ++j) a[j] = __builtin_amdgcn_perm(d[2 * j + 1], d[2 * j], 0x05040100u);
  return a;
}
DEV u8 cat(u4 a, u4 b) { return __builtin_shufflevector(a, b, 0, 1, 2, 3, 4, 5, 6, 7); }
DEV u16v cat(u8 a, u8 b) { return __builtin_shufflevector(a, b, 0, 1, 2, 3, 4, 5, 6, 7, 8, 9, 10, 11, 12, 13, 14, 15); }
DEV u32v cat(u8 a, u8 b, u8 c, u8 d) {
  return __builtin_shufflevector(cat(a, b), cat(c, d), 0, 1, 2, 3, 4, 5, 6, 7, 8, 9, 10, 11, 12, 13, 14, 15, 16, 17, 18,
                                 19, 20, 21, 22, 23, 24, 25, 26, 27, 28, 29, 30, 31);
}
DEV u4 srd(const void* p, unsigned bytes) {
  return u4{(unsigned)(uintptr_t)p, (unsigned)((uintptr_t)p >> 32) & 0xffffu, bytes, 0x00020000u};
}
DEV unsigned lds_base(char* sm) { return (unsigned)(uintptr_t)(__attribute__((address_space(3))) char*)sm; }
DEV unsigned swz(unsigned g) { return (g & 1) | ((g & 2) << 1); }
DEV void sched() { __builtin_amdgcn_sched_barrier(0); }

DEV u4 load_mem0(const Params& p, unsigned w, unsigned c, unsigned g) {
  const rsrc_t r = mk(p.mem0, 8192);
  const unsigned cg = 4 * w + g;
  const unsigned voff = ((c >> 1) * 512u + 32u * (cg >> 1) + (cg & 1) + 4u * (c & 1)) * 2u;
  u4 a;
#pragma unroll
  for (int i = 0; i < 4; ++i) a[i] = ld2(r, voff, 16 * i) | (ld2(r, voff, 16 * i + 4) << 16);
  return a;
}
constexpr float SCALE_LOG2E = 0.08838834764831845f * 1.4426950408889634f;

#define A_STAMP(r)
#define A_SAVE(r, slot)
#define A_NEXT
#define A_KXWAIT "s_waitcnt vmcnt(2)\n"
#define A_DIAG_OUT
#define A_DIAG_CLOB
#define A_CLK(r)
#define A_SAVECLK
#define A_DIAG_DECL
#define A_SCLOB "s80", "s81", "s82", "s83", "s84", "s85", "s86", "s87", "s88", "s89", "s90", "s91", "vcc", "scc", "memory"
#define SMF(d, a, b, i) "v_smfmac_f32_16x16x64_bf16 v[" d "], v[" a "], v[" b "], " i "\n"
#define MFM(d, a, b, c) "v_mfma_f32_16x16x32_bf16 v[" d "], v[" a "], v[" b "], " c "\n"
#define DPP_ALL " row_mask:0xf bank_mask:0xf\n"
#define A_POLL_MEM \
  ".Lpoll%=:\n" \
  "buffer_load_dwordx4 v[148:151], v129, %[ws], %[mem] offen sc1\n" \
  "s_sub_u32 %[cap], %[cap], 1\n" \
  "s_cbranch_scc1 .Lexit%=\n" \
  "s_waitcnt vmcnt(0)\n" \
  "ds_write_b128 v130, v[148:151]\n" \
  "v_max3_u32 v152, v148, v149, v150\n" \
  "v_max_u32 v152, v152, v151\n" \
  "v_cmp_eq_u32 vcc, -1, v152\n" \
  "s_cbranch_vccnz .Lpoll%=\n"
#define A_READ_MEM \
  "s_waitcnt lgkmcnt(0)\n" \
  "s_barrier\n" \
  A_CLK("s[72:73]") \
  "ds_read_b128 v[152:155], v131\n" \
  "ds_read_b128 v[156:159], v132\n" \
  "ds_read_b128 v[160:163], v133\n" \
  "ds_read_b128 v[164:167], v134\n" \
  "ds_read_b128 v[168:171], v135\n" \
  "ds_read_b128 v[172:175], v136\n" \
  "ds_read_b128 v[176:179], v137\n"
#define D_S1 0
#define D_S0 0
#define D_VM 0
#define D_V1 0
#define STR_(x) #x
#define SLEEP(x) "s_sleep " STR_(x) "\n"
#define WL(n) "s_waitcnt lgkmcnt(" #n ")\n"
#define WV(n) "s_waitcnt vmcnt(" #n ")\n"

DEV void role_s1(const Params& p, unsigned h, char* sm) {
  constexpr unsigned L_MEM = 0, L_Q = 8192, L_K = 10240;
  const unsigned l = threadIdx.x & 63, w = rfl(threadIdx.x >> 6), c = l & 15, g = l >> 4;
  const rsrc_t ws = mk(p.ws, WS_BYTES);
  bool alive = true;
  STAMP(15, w);

  u8 wa[8], wb[8];
  {
    const rsrc_t wr = mk(w < 4 ? p.wq : p.wk, 4u << 20);
    const unsigned voff = ((16u * (g & 1) + (g >> 1)) * 4096u + h * 128u + (w & 3) * 32u + 2u * c) * 2u;
#pragma unroll
    for (int i = 0; i < 8; ++i) {
      const unsigned t = (w + i) & 7;
      u4 a0, b0, a1, b1;
      load_pair(wr, voff, (64u * t) * 8192u, 16384u, a0, b0);
      load_pair(wr, voff, (64u * t + 32u) * 8192u, 16384u, a1, b1);
      wa[i] = cat(a0, a1);
      wb[i] = cat(b0, b1);
    }
  }
  u4 a0 = load_mem0(p, w, c, g);
  const unsigned cb = wait_init(p, ws, 8, alive);
  STAMP(15, 8 + w);

  const unsigned lb = lds_base(sm);
  u16v k0;
  u8 k1;
  k0[0] = (c & 1) ? 0xeeeeeeeeu : 0x44444444u;
  k0[1] = w * 1024u + l * 16u;
#pragma unroll
  for (int i = 0; i < 8; ++i) k0[2 + i] = lb + L_MEM + ((w + i) & 7) * 1024u + l * 16u;
#pragma unroll
  for (int r = 0; r < 2; ++r)
    k0[10 + r] = lb + (w < 4 ? L_Q : L_K) + (((w & 3) * 4u + (c >> 2)) * 8u + ((2u * g + r) ^ swz(c >> 2))) * 16u + (c & 3) * 4u;
  k0[12] = k0[13] = 0;
  k0[14] = lb + L_Q + (g * 8u + ((c & 7) ^ swz(g))) * 16u;
  k0[15] = lb + L_K + (g * 8u + ((((c >> 2) & 1) * 4u + (c >> 3) * 2u + (c & 1)) ^ swz(g))) * 16u;
  const unsigned piece = ((c & 7) >> 2) * 4u + (h >> 3), hl = h & 7;
  const unsigned p_base = cb + (B_P + (piece >> 1)) * 16384u + (piece & 1) * 4096u;
  const unsigned tok = ((c >> 1) & 1) * 16u + (c >> 3) * 8u + ((c >> 2) & 1) * 2u + (c & 1);
  k1[0] = p_base + (hl >> 1) * 512u + (2u * (hl & 1) + (g >> 1)) * 128u + ((g & 1) * 4u + (c & 3)) * 16u;
  k1[1] = p_base + 2048u + (hl >> 1) * 128u + ((g >> 1) * 4u + (c & 3)) * 16u + (hl & 1) * 8u;
  k1[2] = O_KX + h * 8192u + ((tok >> 4) * 64u + g * 16u + (tok & 15)) * 16u;
  k1[3] = k1[2] + 4096u;
  k1[4] = w == 0 ? F_KX + h * FL : OOB;
  k1[5] = lb + L_K + l * 16u;
  k1[6] = O_KM + h * 2048u + l * 16u;
  k1[7] = F_KM + h * FL;

  u32v kx = {};
  unsigned seen;
  if (w == 0) {
    wait_flags(ws, k1[4], 1u, alive);
    u4 f[8];
#pragma unroll
    for (int i = 0; i < 8; ++i) f[i] = ld16c(ws, k1[2] + (i & 3) * 2048u + (i >> 2) * 64u, 0);
    kx = cat(cat(f[0], f[1]), cat(f[2], f[3]), cat(f[4], f[5]), cat(f[6], f[7]));
  }
  STAMP(15, 16 + w);
  if (w == 0) TSTAMP(1);

  unsigned s = 0, mem = rfl(cb + B_MEM * 16384u - 16384u), cap = rfl(alive ? SPIN_CAP : 0u), kxo = 262144u, kmo = 0;
  u32v t0;
  u8 t1;
  u16v t2;
  A_DIAG_DECL;
  asm volatile(
      "s_mov_b32 s80, 0xff00ff00\n"
      "s_mov_b32 s81, 0xff00ff00\n"
      "s_mov_b32 s82, 0x3e0293ee\n"
      "s_mov_b32 s83, 0\n"
      "s_cmp_eq_u32 %[w], 0\n"
      "s_cselect_b32 s90, 63, 0\n"
      "v_mov_b32 v184, 0\nv_mov_b32 v185, 0\nv_mov_b32 v186, 0\nv_mov_b32 v187, 0\n"
      "v_mov_b32 v188, 0\nv_mov_b32 v189, 0\nv_mov_b32 v190, 0\nv_mov_b32 v191, 0\n"
      "ds_write_b128 v130, v[148:151]\n"
      "s_branch .Lhave%=\n"
      A_POLL_MEM
      ".Lhave%=:\n"
      A_STAMP("s[84:85]") A_CLK("s[70:71]")
      SMF("184:187", "148:151", "0:7", "v128") SMF("188:191", "148:151", "8:15", "v128")
      "buffer_load_dword v248, v244, %[ws], 0 offen sc1\n"
      A_READ_MEM
      WL(6) SMF("184:187", "152:155", "16:23", "v128") SMF("188:191", "152:155", "24:31", "v128")
      WL(5) SMF("184:187", "156:159", "32:39", "v128") SMF("188:191", "156:159", "40:47", "v128")
      WL(4) SMF("184:187", "160:163", "48:55", "v128") SMF("188:191", "160:163", "56:63", "v128")
      WL(3) SMF("184:187", "164:167", "64:71", "v128") SMF("188:191", "164:167", "72:79", "v128")
      WL(2) SMF("184:187", "168:171", "80:87", "v128") SMF("188:191", "168:171", "88:95", "v128")
      WL(1) SMF("184:187", "172:175", "96:103", "v128") SMF("188:191", "172:175", "104:111", "v128")
      WL(0) SMF("184:187", "176:179", "112:119", "v128") SMF("188:191", "176:179", "120:127", "v128")
      A_CLK("s[74:75]")
      "s_cmp_lt_u32 %[s], s90\n"
      "s_nop 5\n"
      "v_add_f32 v152, v184, v185\n"
      "v_add_f32 v154, v186, v187\n"
      "v_add_f32 v153, v188, v189\n"
      "v_add_f32 v155, v190, v191\n"
      "v_cvt_pk_bf16_f32 v152, v152, v153\n"
      "v_cvt_pk_bf16_f32 v154, v154, v155\n"
      "ds_write_b32 v138, v152\n"
      "ds_write_b32 v139, v154\n"
      A_STAMP("s[86:87]") A_CLK("s[76:77]")
      "v_mov_b32 v184, 0\nv_mov_b32 v185, 0\nv_mov_b32 v186, 0\nv_mov_b32 v187, 0\n"
      "v_mov_b32 v188, 0\nv_mov_b32 v189, 0\nv_mov_b32 v190, 0\nv_mov_b32 v191, 0\n"
      "s_waitcnt lgkmcnt(0)\n"
      "s_barrier\n"
      "s_cbranch_scc0 .Lother%=\n"
      A_CLK("s[78:79]")
      "ds_read_b128 v[152:155], v142\n"
      "ds_read_b128 v[168:171], v143\n"
      "ds_read_b128 v[156:159], v142 offset:512\n"
      "ds_read_b128 v[172:175], v143 offset:512\n"
      "ds_read_b128 v[160:163], v142 offset:1024\n"
      "ds_read_b128 v[176:179], v143 offset:1024\n"
      "ds_read_b128 v[164:167], v142 offset:1536\n"
      "ds_read_b128 v[180:183], v143 offset:1536\n"
      WL(7) MFM("224:227", "192:195", "152:155", "0") MFM("228:231", "208:211", "152:155", "0")
      WL(6) MFM("232:235", "168:171", "152:155", "0")
      WL(5) MFM("224:227", "196:199", "156:159", "v[224:227]") MFM("228:231", "212:215", "156:159", "v[228:231]")
      WL(4) MFM("232:235", "172:175", "156:159", "v[232:235]")
      WL(3) MFM("224:227", "200:203", "160:163", "v[224:227]") MFM("228:231", "216:219", "160:163", "v[228:231]")
      WL(2) MFM("232:235", "176:179", "160:163", "v[232:235]")
      WL(1) MFM("224:227", "204:207", "164:167", "v[224:227]") MFM("228:231", "220:223", "164:167", "v[228:231]")
      WL(0) MFM("232:235", "180:183", "164:167", "v[232:235]")
      A_CLK("s[92:93]")
      "s_nop 5\n"
      "v_max3_f32 v152, v224, v225, v226\n"
      "v_max3_f32 v153, v227, v228, v229\n"
      "v_max3_f32 v154, v230, v231, v232\n"
      "v_max3_f32 v152, v152, v153, v233\n"
      "v_max_f32 v153, v152, v154\n"
      "v_max_f32 v152, v152, v154\n"
      "v_cndmask_b32 v156, v224, v228, s[80:81]\n"
      "v_cndmask_b32 v157, v225, v229, s[80:81]\n"
      "v_permlane16_swap_b32 v152, v153\n"
      "v_max_f32 v154, v152, v153\n"
      "v_max_f32 v155, v152, v153\n"
      "v_cndmask_b32 v158, v226, v230, s[80:81]\n"
      "v_cndmask_b32 v159, v227, v231, s[80:81]\n"
      "v_permlane32_swap_b32 v154, v155\n"
      "v_cndmask_b32 v160, v232, v233, s[80:81]\n"
      "v_max_f32 v152, v154, v155\n"
      "v_mul_f32 v152, 0xbe0293ee, v152\n"
      "v_fma_f32 v156, v156, s82, v152\n"
      "v_fma_f32 v157, v157, s82, v152\n"
      "v_fma_f32 v158, v158, s82, v152\n"
      "v_fma_f32 v159, v159, s82, v152\n"
      "v_fma_f32 v160, v160, s82, v152\n"
      "v_exp_f32 v156, v156\n"
      "v_exp_f32 v157, v157\n"
      "v_exp_f32 v158, v158\n"
      "v_exp_f32 v159, v159\n"
      "v_exp_f32 v160, v160\n"
      "v_add_f32 v152, v156, v157\n"
      "v_add_f32 v153, v158, v159\n"
      "v_add_f32 v152, v152, v160\n"
      "v_add_f32 v152, v152, v153\n"
      "v_xor_b32 v156, s83, v156\n"
      "v_xor_b32 v157, s83, v157\n"
      "v_add_f32_dpp v154, v152, v152 row_ror:8" DPP_ALL
      "v_add_f32_dpp v155, v152, v152 row_ror:8" DPP_ALL
      "v_xor_b32 v158, s83, v158\n"
      "v_xor_b32 v159, s83, v159\n"
      "v_permlane16_swap_b32 v154, v155\n"
      "v_add_f32 v152, v154, v155\n"
      "v_add_f32 v153, v154, v155\n"
      "v_xor_b32 v160, s83, v160\n"
      "s_xor_b32 s83, s83, 0x80000000\n"
      "v_permlane32_swap_b32 v152, v153\n"
      "v_add_f32 v152, v152, v153\n"
      "v_rcp_f32 v152, v152\n"
      A_CLK("s[94:95]")
      "s_add_u32 s91, %[s], 2\n"
      "v_mul_f32 v156, v156, v152\n"
      "v_mul_f32 v157, v157, v152\n"
      "v_mul_f32 v158, v158, v152\n"
      "v_mul_f32 v159, v159, v152\n"
      "v_mul_f32 v160, v160, v152\n"
      "v_cvt_pk_bf16_f32 v164, v156, v157\n"
      "v_cvt_pk_bf16_f32 v165, v158, v159\n"
      "v_mov_b32_dpp v161, v160 row_ror:8" DPP_ALL
      "v_mov_b32_dpp v166, v164 row_ror:8" DPP_ALL
      "v_mov_b32_dpp v167, v165 row_ror:8" DPP_ALL
      "v_cvt_pk_bf16_f32 v162, v160, v161\n"
      "v_cvt_pk_bf16_f32 v163, v160, v161\n"
      "s_not_b64 exec, s[80:81]\n"
      "buffer_store_dwordx4 v[164:167], v240, %[ws], 0 offen sc1\n"
      "s_mov_b64 exec, -1\n"
      "v_permlane16_swap_b32 v162, v163\n"
      "s_mov_b32 exec_lo, 0xff\n"
      "s_mov_b32 exec_hi, 0xff\n"
      "buffer_store_dwordx2 v[162:163], v241, %[ws], 0 offen sc1\n"
      "s_mov_b64 exec, -1\n"
      A_STAMP("s[88:89]") A_CLK("s[96:97]") A_SAVE("s84", 0) A_SAVE("s86", 1) A_SAVE("s88", 2) A_SAVECLK
      "s_cmp_ge_u32 %[s], 62\n"
      "s_cbranch_scc1 .Lnext%=\n"
      A_KXWAIT
      ".Lkx%=:\n"
      "v_cmp_gt_u32 vcc, s91, v248\n"
      "s_cbranch_vccz .Lkxok%=\n"
      "buffer_load_dword v248, v244, %[ws], 0 offen sc1\n"
      "s_sub_u32 %[cap], %[cap], 1\n"
      "s_cbranch_scc1 .Lexit%=\n"
      "s_waitcnt vmcnt(0)\n"
      "s_branch .Lkx%=\n"
      ".Lkxok%=:\n"
      "buffer_load_dwordx4 v[192:195], v242, %[ws], %[kxo] offen sc1\n"
      "buffer_load_dwordx4 v[208:211], v242, %[ws], %[kxo] offen offset:64 sc1\n"
      "buffer_load_dwordx4 v[196:199], v242, %[ws], %[kxo] offen offset:2048 sc1\n"
      "buffer_load_dwordx4 v[212:215], v242, %[ws], %[kxo] offen offset:2112 sc1\n"
      "buffer_load_dwordx4 v[200:203], v243, %[ws], %[kxo] offen sc1\n"
      "buffer_load_dwordx4 v[216:219], v243, %[ws], %[kxo] offen offset:64 sc1\n"
      "buffer_load_dwordx4 v[204:207], v243, %[ws], %[kxo] offen offset:2048 sc1\n"
      "buffer_load_dwordx4 v[220:223], v243, %[ws], %[kxo] offen offset:2112 sc1\n"
      "s_add_u32 %[kxo], %[kxo], 0x40000\n"
      SLEEP(D_S0)
      "s_branch .Lnext%=\n"
      ".Lidle%=:\n"
      SLEEP(D_S1)
      "s_branch .Lnext%=\n"
      ".Lother%=:\n"
      "s_cmp_lg_u32 %[w], 6\n"
      "s_cbranch_scc1 .Lidle%=\n"
      "ds_read_b128 v[152:155], v245\n"
      "ds_read_b128 v[156:159], v245 offset:1024\n"
      "s_add_u32 s91, %[s], 1\n"
      "v_mov_b32 v160, s91\n"
      WL(1) "buffer_store_dwordx4 v[152:155], v246, %[ws], %[kmo] offen sc1\n"
      WL(0) "buffer_store_dwordx4 v[156:159], v246, %[ws], %[kmo] offen offset:1024 sc1\n"
      "s_add_u32 %[kmo], %[kmo], 0x10000\n"
      "s_waitcnt vmcnt(0)\n"
      "s_mov_b64 exec, 1\n"
      "buffer_store_dword v160, v247, %[ws], 0 offen sc1\n"
      "s_mov_b64 exec, -1\n"
      SLEEP(D_S0)
      ".Lnext%=:\n"
      A_NEXT
      "s_add_u32 %[mem], %[mem], 0x4000\n"
      "s_add_u32 %[s], %[s], 1\n"
      "s_cmp_lt_u32 %[s], 64\n"
      "s_cbranch_scc1 .Lpoll%=\n"
      ".Lexit%=:\n"
      : [s] "+s"(s), [mem] "+s"(mem), [cap] "+s"(cap), [kxo] "+s"(kxo), [kmo] "+s"(kmo), "+{v[148:151]}"(a0),
        "=&{v[152:183]}"(t0), "=&{v[184:191]}"(t1), "+{v[192:223]}"(kx), "=&{v[224:239]}"(t2), "=&{v248}"(seen) A_DIAG_OUT
      : [ws] "s"(srd(p.ws, WS_BYTES)), [w] "s"(w), "{v[0:31]}"(cat(wa[0], wb[0], wa[1], wb[1])),
        "{v[32:63]}"(cat(wa[2], wb[2], wa[3], wb[3])), "{v[64:95]}"(cat(wa[4], wb[4], wa[5], wb[5])),
        "{v[96:127]}"(cat(wa[6], wb[6], wa[7], wb[7])), "{v[128:143]}"(k0), "{v[240:247]}"(k1)
      : A_SCLOB A_DIAG_CLOB);
}

DEV void load_wo(const Params& p, unsigned h, unsigned w, unsigned c, unsigned g, u4 (&wo)[4][4]) {
  const rsrc_t r = mk(p.wo, 4u << 20);
  const unsigned voff = ((h * 128u + 8u * g) * 512u + 64u * w + 2u * c) * 2u;
#pragma unroll
  for (int b = 0; b < 2; ++b)
#pragma unroll
    for (int f = 0; f < 4; ++f) load_pair(r, voff, f * 32768u + b * 64u, 1024u, wo[2 * b][f], wo[2 * b + 1][f]);
}

DEV void role_vm(const Params& p, unsigned h, char* sm) {
  constexpr unsigned L_MEM = 0, L_V = 8192;
  const unsigned l = threadIdx.x & 63, w = rfl(threadIdx.x >> 6), c = l & 15, g = l >> 4;
  const rsrc_t ws = mk(p.ws, WS_BYTES);
  bool alive = true;

  u8 wv[8], wo[4][2];
  {
    const rsrc_t wr = mk(p.wv, 4u << 20);
    const unsigned voff = ((16u * (g & 1) + (g >> 1)) * 4096u + h * 128u + w * 16u + c) * 2u;
#pragma unroll
    for (int i = 0; i < 8; ++i) {
      const unsigned t = (w + i) & 7;
      wv[i] = cat(load_single(wr, voff, (64u * t) * 8192u, 16384u), load_single(wr, voff, (64u * t + 32u) * 8192u, 16384u));
    }
    u4 t[4][4];
    load_wo(p, h, w, c, g, t);
#pragma unroll
    for (int i = 0; i < 4; ++i) {
      wo[i][0] = cat(t[i][0], t[i][1]);
      wo[i][1] = cat(t[i][2], t[i][3]);
    }
  }
  u4 a0 = load_mem0(p, w, c, g);
  const unsigned cb = wait_init(p, ws, 8, alive);
  STAMP(15, 8 + w);

  const unsigned lb = lds_base(sm);
  u16v k0;
  k0[0] = (c & 1) ? 0xeeeeeeeeu : 0x44444444u;
  k0[1] = w * 1024u + l * 16u;
#pragma unroll
  for (int i = 0; i < 8; ++i) k0[2 + i] = lb + L_MEM + ((w + i) & 7) * 1024u + l * 16u;
  k0[10] = lb + L_V + (w * 16u + 4u * g + ((c >> 1) & 1)) * 16u + (2u * (c >> 2) + (c & 1)) * 2u;
  k0[11] = lb + L_V + l * 16u;
  const unsigned cg = 4u * w + g;
  k0[12] = cb + (B_M + cg) * 16384u + h * 256u + c * 16u;
  k0[13] = O_M2 + cg * 8192u + h * 256u + c * 16u;
  k0[14] = threadIdx.x == 448 ? F_M + h * FL : OOB;
  k0[15] = 0;

  unsigned s = 0, mem = rfl(cb + B_MEM * 16384u - 16384u), cap = rfl(alive ? SPIN_CAP : 0u), m2o = 0;
  u32v t0;
  u16v t1, t2;
  A_DIAG_DECL;
#define VM_ZERO(a) "v_mov_b32 v" #a ", 0\n"
#define VM_ZERO_M \
      VM_ZERO(192) VM_ZERO(193) VM_ZERO(194) VM_ZERO(195) VM_ZERO(196) VM_ZERO(197) VM_ZERO(198) VM_ZERO(199) \
      VM_ZERO(200) VM_ZERO(201) VM_ZERO(202) VM_ZERO(203) VM_ZERO(204) VM_ZERO(205) VM_ZERO(206) VM_ZERO(207)
  asm volatile(
      "s_mov_b32 s83, 0\n"
      VM_ZERO(180) VM_ZERO(184) VM_ZERO(185) VM_ZERO(186) VM_ZERO(187) VM_ZERO_M
      "ds_write_b128 v130, v[148:151]\n"
      "s_branch .Lhave%=\n"
      A_POLL_MEM
      ".Lhave%=:\n"
      A_STAMP("s[84:85]") A_CLK("s[70:71]")
      SMF("184:187", "148:151", "0:7", "v128")
      A_READ_MEM
      WL(6) SMF("184:187", "152:155", "8:15", "v128")
      WL(5) SMF("184:187", "156:159", "16:23", "v128")
      WL(4) SMF("184:187", "160:163", "24:31", "v128")
      WL(3) SMF("184:187", "164:167", "32:39", "v128")
      WL(2) SMF("184:187", "168:171", "40:47", "v128")
      WL(1) SMF("184:187", "172:175", "48:55", "v128")
      WL(0) SMF("184:187", "176:179", "56:63", "v128")
      A_CLK("s[74:75]")
      "s_nop 7\n"
      "v_add_f32 v152, v184, v185\n"
      "v_add_f32 v153, v186, v187\n"
      "v_cvt_pk_bf16_f32 v152, v152, v153\n"
      "ds_write_b16 v138, v152\n"
      "ds_write_b16_d16_hi v138, v152 offset:32\n"
      A_STAMP("s[86:87]") A_CLK("s[76:77]")
      VM_ZERO(184) VM_ZERO(185) VM_ZERO(186) VM_ZERO(187)
      "s_waitcnt lgkmcnt(0)\n"
      "s_barrier\n"
      A_CLK("s[78:79]")
      "ds_read_b128 v[152:155], v139\n"
      "ds_read_b128 v[156:159], v139 offset:1024\n"
      "buffer_store_dword v180, v142, %[ws], 0 offen sc1\n"
      WL(1) SMF("192:195", "152:155", "64:71", "v128") SMF("200:203", "152:155", "96:103", "v128")
      WL(0) SMF("192:195", "156:159", "72:79", "v128") SMF("200:203", "156:159", "104:111", "v128")
      SMF("196:199", "152:155", "80:87", "v128") SMF("204:207", "152:155", "112:119", "v128")
      SMF("196:199", "156:159", "88:95", "v128") SMF("204:207", "156:159", "120:127", "v128")
      A_CLK("s[92:93]")
      "s_nop 2\n"
      "v_add_f32 v192, v192, v193\n"
      "v_add_f32 v200, v200, v201\n"
      "v_add_f32 v194, v194, v195\n"
      "v_add_f32 v202, v202, v203\n"
      "v_cvt_pk_bf16_f32 v160, v192, v194\n"
      "v_cvt_pk_bf16_f32 v162, v200, v202\n"
      "v_bfi_b32 v160, -2, v160, s83\n"
      "v_bfi_b32 v162, -2, v162, s83\n"
      "v_add_f32 v196, v196, v197\n"
      "v_add_f32 v204, v204, v205\n"
      "v_add_f32 v198, v198, v199\n"
      "v_add_f32 v206, v206, v207\n"
      "v_cvt_pk_bf16_f32 v161, v196, v198\n"
      "v_cvt_pk_bf16_f32 v163, v204, v206\n"
      "v_bfi_b32 v161, -2, v161, s83\n"
      "v_bfi_b32 v163, -2, v163, s83\n"
      "v_permlane32_swap_b32 v160, v162\n"
      "s_add_u32 %[s], %[s], 1\n"
      "v_permlane32_swap_b32 v161, v163\n"
      "s_nop 1\n"
      "v_permlane16_swap_b32 v160, v161\n"
      "v_permlane16_swap_b32 v162, v163\n"
      "buffer_store_dwordx4 v[160:163], v140, %[ws], 0 offen sc1\n"
      A_STAMP("s[88:89]") A_CLK("s[94:95]") A_CLK("s[96:97]")
      "buffer_store_dwordx4 v[160:163], v141, %[ws], %[m2o] offen sc1\n"
      "v_mov_b32 v180, %[s]\n"
      VM_ZERO_M
      "s_xor_b32 s83, s83, 1\n"
      "s_add_u32 %[m2o], %[m2o], 0x40000\n"
      "s_add_u32 %[mem], %[mem], 0x4000\n"
      A_NEXT
      SLEEP(D_VM)
      "s_cmp_lt_u32 %[s], 64\n"
      "s_cbranch_scc1 .Lpoll%=\n"
      "s_waitcnt vmcnt(0)\n"
      "s_barrier\n"
      "buffer_store_dword v180, v142, %[ws], 0 offen sc1\n"
      ".Lexit%=:\n"
      : [s] "+s"(s), [mem] "+s"(mem), [cap] "+s"(cap), [m2o] "+s"(m2o), "+{v[148:151]}"(a0), "=&{v[152:183]}"(t0),
        "=&{v[184:199]}"(t1), "=&{v[200:215]}"(t2) A_DIAG_OUT
      : [ws] "s"(srd(p.ws, WS_BYTES)), [w] "s"(w), "{v[0:31]}"(cat(wv[0], wv[1], wv[2], wv[3])),
        "{v[32:63]}"(cat(wv[4], wv[5], wv[6], wv[7])), "{v[64:95]}"(cat(wo[0][0], wo[0][1], wo[1][0], wo[1][1])),
        "{v[96:127]}"(cat(wo[2][0], wo[2][1], wo[3][0], wo[3][1])), "{v[128:143]}"(k0)
      : A_SCLOB A_DIAG_CLOB);
}

DEV void role_v1(const Params& p, unsigned j, char* sm) {
  const unsigned l = threadIdx.x & 63, w = rfl(threadIdx.x >> 6), c = l & 15, g = l >> 4;
  const unsigned cg = j >> 1, rg = j & 1;
  const rsrc_t ws = mk(p.ws, WS_BYTES);
  bool alive = true;
  {
    const unsigned cb0 = crit_base(p);
    const u4 ones = {~0u, ~0u, ~0u, ~0u};
    for (unsigned o = (j * 512u + threadIdx.x) * 16u; o < B_END * 16384u; o += 64u * 512u * 16u) st16c(ones, ws, o, cb0);
    drain();
    unsigned sel = 1;
    if (j == 0 && w == 0) {
      unsigned best[2] = {~0u, ~0u};
      for (int k = 0; k < 8; ++k) {
        const unsigned off = B_PROBE * 16384u + (k & 1) * 8192u + (l & 7) * 16u + (k >> 1) * 1024u;
        const unsigned long long t0 = __builtin_amdgcn_s_memtime();
        asm volatile("s_waitcnt lgkmcnt(0)" ::: "memory");
        st16c(ones, ws, off, cb0);
        drain();
        const unsigned long long t1 = __builtin_amdgcn_s_memtime();
        asm volatile("s_waitcnt lgkmcnt(0)" ::: "memory");
        const unsigned dt = (unsigned)(t1 - t0);
        if (k & 1) best[1] = min(best[1], dt); else best[0] = min(best[0], dt);
      }
      sel = best[1] < best[0] ? 2u : 1u;
    }
    bar();
    if (threadIdx.x == 0) st4c(sel, ws, F_INIT + j * FL, 0);
  }
  if (w >= 4) return;
  const unsigned cb = wait_init(p, ws, 4, alive);
  bar();
  STAMP(15, 8 + w);

  const unsigned lb = lds_base(sm);
  const unsigned piece = rg * 4u + w;
  u16v k0 = {};
  k0[0] = (c & 4) ? 0xeeeeeeeeu : 0x44444444u;
  k0[1] = cb + (B_P + (piece >> 1)) * 16384u + (piece & 1) * 4096u + g * 128u + (c & 7) * 16u + 2048u;
  k0[2] = k0[1] - 2048u + (c >> 3) * 512u;
  k0[3] = cb + (B_M + cg) * 16384u + w * 2048u + l * 16u;
  k0[4] = O_U + (cg * 32u + 8u * w) * 1024u + l * 16u;
  k0[5] = k0[4] + 4096u;
  k0[6] = F_U + (8u * w + (l & 7)) * FL;
  k0[7] = lb + ((g * 2u + ((c >> 1) & 1)) * 8u + 2u * (c >> 2) + (c & 1)) * 16u + w * 4u;
  k0[8] = lb + l * 16u;
  k0[9] = cg * 256u + rg * 128u + (l >> 3) * 16u;

  unsigned s = 0, mem = rfl(cb + B_MEM * 16384u), cap = rfl(alive ? SPIN_CAP : 0u), uo = 0, seen = 0;
  u32v t0, t1;
  A_DIAG_DECL;
#define V1_TAGS(d, a, b, c, OP) "v_bitop3_b32 v" d ", v" a ", v" b ", v" c " bitop3:" OP "\n"
#define V1_ZERO "v_mov_b32 v52, 0\nv_mov_b32 v53, 0\nv_mov_b32 v54, 0\nv_mov_b32 v55, 0\n" \
                "v_mov_b32 v56, 0\nv_mov_b32 v57, 0\nv_mov_b32 v58, 0\nv_mov_b32 v59, 0\n"
#define V1_LOAD_P \
      "buffer_load_dwordx4 v[40:43], v66, %[ws], 0 offen sc1\n" \
      "buffer_load_dwordx4 v[44:47], v66, %[ws], 0 offen offset:1024 sc1\n" \
      "buffer_load_dwordx4 v[48:51], v65, %[ws], 0 offen sc1\n"
#define V1_STEP(P, OP, MWANT, PWANT, NEG) \
      ".Ltop" P "%=:\n" \
      "v_cmp_ge_u32 vcc, %[s], v80\n" \
      "s_cbranch_vccnz .Luw" P "%=\n" \
      "buffer_load_dwordx4 v[0:3], v68, %[ws], %[uo] offen sc1\n" \
      "buffer_load_dwordx4 v[4:7], v68, %[ws], %[uo] offen offset:1024 sc1\n" \
      "buffer_load_dwordx4 v[8:11], v68, %[ws], %[uo] offen offset:2048 sc1\n" \
      "buffer_load_dwordx4 v[12:15], v68, %[ws], %[uo] offen offset:3072 sc1\n" \
      "buffer_load_dwordx4 v[16:19], v69, %[ws], %[uo] offen sc1\n" \
      "buffer_load_dwordx4 v[20:23], v69, %[ws], %[uo] offen offset:1024 sc1\n" \
      "buffer_load_dwordx4 v[24:27], v69, %[ws], %[uo] offen offset:2048 sc1\n" \
      "buffer_load_dwordx4 v[28:31], v69, %[ws], %[uo] offen offset:3072 sc1\n" \
      "buffer_load_dword v80, v70, %[ws], 0 offen sc1\n" \
      "s_add_u32 %[uo], %[uo], 0x100000\n" \
      V1_ZERO \
      SLEEP(D_V1) \
      ".Lboth" P "%=:\n" \
      "buffer_load_dwordx4 v[32:35], v67, %[ws], 0 offen sc1\n" \
      "buffer_load_dwordx4 v[36:39], v67, %[ws], 0 offen offset:1024 sc1\n" \
      V1_LOAD_P \
      WV(3) \
      V1_TAGS("60", "32", "33", "34", OP) V1_TAGS("61", "35", "36", "37", OP) V1_TAGS("60", "60", "38", "39", OP) \
      V1_TAGS("60", "60", "61", "61", OP) \
      "v_and_b32 v60, 1, v60\n" \
      "v_cmp_ne_u32 s[80:81], " MWANT ", v60\n" \
      ".Lp" P "%=:\n" \
      WV(2) SMF("52:55", "40:43", "0:7", "v64") SMF("56:59", "40:43", "8:15", "v64") \
      V1_TAGS("60", "40", "41", "42", OP) \
      WV(1) SMF("52:55", "44:47", "16:23", "v64") SMF("56:59", "44:47", "24:31", "v64") \
      V1_TAGS("60", "60", "43", "44", OP) V1_TAGS("61", "45", "46", "47", OP) \
      WV(0) SMF("52:55", "48:51", "32:39", "v64") \
      V1_TAGS("60", "60", "48", "49", OP) V1_TAGS("61", "61", "50", "51", OP) V1_TAGS("60", "60", "61", "61", OP) \
      "v_and_b32 v60, s82, v60\n" \
      "v_cmp_ne_u32 vcc, " PWANT ", v60\n" \
      "s_or_b64 vcc, vcc, s[80:81]\n" \
      "s_cbranch_scc1 .Lre" P "%=\n" \
      A_STAMP("s[86:87]") A_CLK("s[70:71]") \
      "s_nop 0\n" \
      "v_permlane32_swap_b32 v56, v54\n" \
      "v_permlane32_swap_b32 v57, v55\n" \
      "v_add_f32 v60, v52, v54\n" \
      "v_add_f32 v62, v58, v56\n" \
      "v_add_f32 v61, v53, v55\n" \
      "v_add_f32 v63, v59, v57\n" \
      "v_cndmask_b32 v60, v60, v62, s[84:85]\n" \
      "v_cndmask_b32 v61, v61, v63, s[84:85]\n" \
      "s_cmp_lg_u32 %[w], 0\n" \
      "s_nop 0\n" \
      "v_permlane16_swap_b32 v60, v61\n" \
      "v_add_f32 v60, v60, v61\n" \
      "ds_write_b32 v71, v60 offset:" P "*1024\n" \
      A_CLK("s[72:73]") \
      "s_waitcnt lgkmcnt(0)\n" \
      "s_barrier\n" \
      "s_cbranch_scc1 .Lend" P "%=\n" \
      A_CLK("s[74:75]") \
      "ds_read_b128 v[52:55], v72 offset:" P "*1024\n" \
      "s_waitcnt lgkmcnt(0)\n" \
      A_CLK("s[76:77]") \
      "v_add_f32 v52, v52, v53\n" \
      "v_add_f32 v54, v54, v55\n" \
      "v_add_f32_e64 v52, " NEG "v52, " NEG "v54\n" \
      "s_nop 1\n" \
      "v_mov_b32_dpp v53, v52 quad_perm:[1,0,3,2]" DPP_ALL \
      "v_cvt_pk_bf16_f32 v56, v52, v53\n" \
      "s_nop 1\n" \
      "v_mov_b32_dpp v57, v56 row_shl:2" DPP_ALL \
      "v_mov_b32_dpp v58, v56 row_shl:4" DPP_ALL \
      "v_mov_b32_dpp v59, v56 row_shl:6" DPP_ALL \
      "s_mov_b64 exec, s[90:91]\n" \
      "buffer_store_dwordx4 v[56:59], v73, %[ws], %[mem] offen sc1\n" \
      "s_mov_b64 exec, -1\n" \
      A_STAMP("s[88:89]") A_CLK("s[78:79]") A_SAVE("s86", 1) A_SAVE("s88", 2) A_SAVECLK \
      ".Lend" P "%=:\n" \
      "s_barrier\n" \
      A_NEXT \
      "s_add_u32 %[mem], %[mem], 0x4000\n" \
      "s_add_u32 %[s], %[s], 1\n"
#define V1_SIDE(P) \
      ".Luw" P "%=:\n" \
      "buffer_load_dword v80, v70, %[ws], 0 offen sc1\n" \
      "s_sub_u32 %[cap], %[cap], 1\n" \
      "s_cbranch_scc1 .Lexit%=\n" \
      "s_waitcnt vmcnt(0)\n" \
      "s_branch .Ltop" P "%=\n" \
      ".Lre" P "%=:\n" \
      "s_sub_u32 %[cap], %[cap], 1\n" \
      "s_cbranch_scc1 .Lexit%=\n" \
      "s_cmp_lg_u64 s[80:81], 0\n" \
      V1_ZERO \
      "s_cbranch_scc1 .Lboth" P "%=\n" \
      V1_LOAD_P \
      "s_branch .Lp" P "%=\n"
  asm volatile(
      "s_mov_b32 s82, 0x80008000\n"
      "s_mov_b32 s84, 0\n"
      "s_mov_b32 s85, -1\n"
      "s_mov_b32 s90, 0x01010101\n"
      "s_mov_b32 s91, 0x01010101\n"
      V1_STEP("0", "0xfe", "0", "0", "")
      "s_cmp_ge_u32 %[s], 63\n"
      "s_cbranch_scc1 .Lexit%=\n"
      V1_STEP("1", "0x80", "1", "s82", "-")
      "s_branch .Ltop0%=\n"
      V1_SIDE("0") V1_SIDE("1")
      ".Lexit%=:\n"
      : [s] "+s"(s), [mem] "+s"(mem), [cap] "+s"(cap), [uo] "+s"(uo), "=&{v[0:31]}"(t0), "=&{v[32:63]}"(t1),
        "+{v80}"(seen) A_DIAG_OUT
      : [ws] "s"(srd(p.ws, WS_BYTES)), [w] "s"(w), "{v[64:79]}"(k0)
      : A_SCLOB A_DIAG_CLOB);
}

DEV void load_x(rsrc_t xr, unsigned w, unsigned c, unsigned g, int s, u4 (&xp)[4]) {
  const unsigned voff = ((16u * (w >> 2) + c) * 512u + 128u * (w & 3) + 8u * g) * 2u;
#pragma unroll
  for (int i = 0; i < 4; ++i) xp[i] = ld16(xr, voff + i * 64u, s * 32768u);
}
DEV void put_x(char* sm, unsigned base, unsigned w, unsigned l, const u4 (&xp)[4]) {
#pragma unroll
  for (int i = 0; i < 4; ++i) lds16(sm, base + (4u * w + i) * 1024u + l * 16u) = xp[i];
}

DEV void token_softmax(rsrc_t ws, char* sm, unsigned qb, const u4 (&km)[4], unsigned h, unsigned w, unsigned l, int s) {
  const unsigned c = l & 15, g = l >> 4;
  u4 q[4], k0[4], k1[4];
#pragma unroll
  for (int m = 0; m < 4; ++m) {
    q[m] = lds16(sm, qb + (w * 4u + m) * 1024u);
    k0[m] = lds16(sm, qb + 8192u + m * 1024u);
    k1[m] = lds16(sm, qb + 12288u + m * 1024u);
  }
  sched();
  f4 s0 = {0, 0, 0, 0}, s1 = {0, 0, 0, 0}, sm_ = {0, 0, 0, 0};
#pragma unroll
  for (int m = 0; m < 4; ++m) {
    s0 = mma(k0[m], q[m], s0);
    s1 = mma(k1[m], q[m], s1);
  }
#pragma unroll
  for (int m = 0; m < 4; ++m) sm_ = mma(km[m], q[m], sm_);
  const unsigned tq = 16u * w + c;
  const float ninf = -__builtin_inff();
#pragma unroll
  for (int r = 0; r < 4; ++r) {
    s0[r] = 4u * g + r <= tq ? s0[r] : ninf;
    s1[r] = 16u + 4u * g + r <= tq ? s1[r] : ninf;
  }
  float mx = fmaxf(fmaxf(fmaxf(s0[0], s0[1]), fmaxf(s0[2], s0[3])), fmaxf(fmaxf(s1[0], s1[1]), fmaxf(s1[2], s1[3])));
  mx = group_max(fmaxf(mx, fmaxf(sm_[0], sm_[1])));
  const float mc = -mx * SCALE_LOG2E;
  float e[10];
#pragma unroll
  for (int r = 0; r < 4; ++r) {
    e[r] = __builtin_amdgcn_exp2f(fmaf(s0[r], SCALE_LOG2E, mc));
    e[4 + r] = __builtin_amdgcn_exp2f(fmaf(s1[r], SCALE_LOG2E, mc));
  }
  e[8] = __builtin_amdgcn_exp2f(fmaf(sm_[0], SCALE_LOG2E, mc));
  e[9] = __builtin_amdgcn_exp2f(fmaf(sm_[1], SCALE_LOG2E, mc));
  const float sum = group_sum(((e[0] + e[1]) + (e[2] + e[3])) + ((e[4] + e[5]) + (e[6] + e[7])) + (e[8] + e[9]));
  const float rs = __builtin_amdgcn_rcpf(sum);
  const u4 px = {pk(e[0] * rs, e[1] * rs), pk(e[2] * rs, e[3] * rs), pk(e[4] * rs, e[5] * rs), pk(e[6] * rs, e[7] * rs)};
  st16c(px, ws, O_PX + (w * 32u + h) * 1024u + l * 16u, s * 65536u);
  const unsigned dm = pk(e[8] * rs, e[9] * rs);
  auto r16 = __builtin_amdgcn_permlane16_swap(dm, dm, false, false);
  auto ra = __builtin_amdgcn_permlane32_swap(r16[0], r16[0], false, false);
  auto rb = __builtin_amdgcn_permlane32_swap(r16[1], r16[1], false, false);
  const u4 pm = {ra[0], rb[0], ra[1], rb[1]};
  st16c(pm, ws, g == 0 ? O_PM + (w * 8u + (h >> 2)) * 1024u + ((h & 3) * 16u + c) * 16u : OOB, s * 16384u);
}

DEV void role_s2(const Params& p, unsigned h, char* sm) {
  constexpr int LAG = 4, R = LAG + 1;
  constexpr unsigned L_X = 0, L_QK = 65536;
  const unsigned l = threadIdx.x & 63, w = rfl(threadIdx.x >> 6), c = l & 15, g = l >> 4;
  const rsrc_t ws = mk(p.ws, WS_BYTES), xr = mk(p.bx, 2u << 20);
  bool alive = true;

  u4 xp[4];
  load_x(xr, w, c, g, 0, xp);
  u4 wa[16], wb[16];
  {
    const rsrc_t wr = mk(w < 4 ? p.wq : p.wk, 4u << 20);
    const unsigned voff = (8u * g * 4096u + h * 128u + (w & 3) * 32u + 2u * c) * 2u;
#pragma unroll
    for (int f = 0; f < 16; ++f) load_pair(wr, voff, 32u * f * 8192u, 8192u, wa[f], wb[f]);
  }
  put_x(sm, L_X, w, l, xp);
  load_x(xr, w, c, g, 1, xp);
  bar();
  STAMP(15, 8 + w);
  if (w < 4) __builtin_amdgcn_s_setprio(1);

  const unsigned km_off = w < 2 ? O_KM + h * 2048u + (g * 8u + ((2u * (c >> 2) + (c & 1)) ^ swz(g))) * 16u : OOB;
  const unsigned km_flag = w < 2 ? F_KM + h * FL : OOB;
  const unsigned px_flag = w < 2 && l == 0 ? F_PX + (w * 32u + h) * FL : OOB;
  const unsigned kx_flag = threadIdx.x == 448 ? F_KX + h * FL : OOB;
  const unsigned kx_off = w >= 4 ? O_KX + h * 8192u + (w & 3) * 2048u + l * 16u : OOB;
  unsigned km_seen = 0;
  int slot = 0;
  for (int it = 0; it < NSEG; ++it) {
    const int s = max(it - LAG, 0);
    const bool soft = it >= LAG && w < 2;
    if (soft && !all_lanes(km_seen > (unsigned)s)) wait_flags(ws, km_flag, s + 1, alive);
    if (w == 0 || w == 4) STAMP(3 + (w >> 2) * 4, it);
    u4 km[4];
#pragma unroll
    for (int m = 0; m < 4; ++m) km[m] = ld16c(ws, km_off + m * 512u, s * 65536u);
    km_seen = ld4c(ws, km_flag, 0);
    f4 acc[2][2] = {};
    const unsigned xb = L_X + (it & 1) * 32768u + l * 16u;
    u4 x[3][2];
#pragma unroll
    for (int k = 0; k < 2; ++k)
#pragma unroll
      for (int t = 0; t < 2; ++t) x[k][t] = lds16(sm, xb + (t * 16 + k) * 1024u);
#pragma unroll
    for (int ks = 0; ks < 16; ++ks) {
      sched();
      if (ks + 2 < 16) {
#pragma unroll
        for (int t = 0; t < 2; ++t) x[(ks + 2) % 3][t] = lds16(sm, xb + (t * 16 + ks + 2) * 1024u);
      }
      sched();
#pragma unroll
      for (int t = 0; t < 2; ++t) {
        acc[0][t] = mma(wa[ks], x[ks % 3][t], acc[0][t]);
        acc[1][t] = mma(wb[ks], x[ks % 3][t], acc[1][t]);
      }
    }
    sched();
    asm volatile("s_waitcnt vmcnt(6)" ::: "memory");
    if (w == 0 || w == 4) STAMP(4 + (w >> 2) * 4, it);
    st4c(s, ws, px_flag, 0);
    put_x(sm, L_X + ((it + 1) & 1) * 32768u, w, l, xp);
#pragma unroll
    for (int t = 0; t < 2; ++t) {
      const u4 o = {pk(acc[0][t][0], acc[1][t][0]), pk(acc[0][t][1], acc[1][t][1]), pk(acc[0][t][2], acc[1][t][2]),
                    pk(acc[0][t][3], acc[1][t][3])};
      lds16(sm, L_QK + slot * 16384u + (w >> 2) * 8192u + (t * 4u + (w & 3)) * 1024u + l * 16u) = o;
      st16c(o, ws, kx_off + t * 1024u, it * 262144u);
    }
    load_x(xr, w, c, g, it + 2, xp);
    if (w == 0 || w == 4) STAMP(5 + (w >> 2) * 4, it);
    slot = slot == R - 1 ? 0 : slot + 1;
    if (soft) {
      token_softmax(ws, sm, L_QK + slot * 16384u + l * 16u, km, h, w, l, s);
      if (w == 0) STAMP(2, s);
    }
    if (it < LAG) drain();
    bar();
    st4c(it < LAG ? it + 1 : it, ws, kx_flag, 0);
  }
  if (w >= 2) return;
  for (int s = NSEG - LAG; s < NSEG; ++s) {
    wait_flags(ws, km_flag, s + 1, alive);
    u4 km[4];
#pragma unroll
    for (int m = 0; m < 4; ++m) km[m] = ld16c(ws, km_off + m * 512u, s * 65536u);
    st4c(s, ws, px_flag, 0);
    slot = slot == R - 1 ? 0 : slot + 1;
    token_softmax(ws, sm, L_QK + slot * 16384u + l * 16u, km, h, w, l, s);
    if (w == 0) STAMP(2, s);
  }
  drain();
  st4c(NSEG, ws, px_flag, 0);
}

DEV void role_vu(const Params& p, unsigned h, char* sm) {
  constexpr unsigned L_X = 0, L_V = 65536;
  constexpr int EAGER = 3;
  const unsigned l = threadIdx.x & 63, w = rfl(threadIdx.x >> 6), c = l & 15, g = l >> 4;
  const rsrc_t ws = mk(p.ws, WS_BYTES), xr = mk(p.bx, 2u << 20);

  u4 xp[4];
  load_x(xr, w, c, g, 0, xp);
  u4 wv[16], wo[4][4];
  {
    const rsrc_t wr = mk(p.wv, 4u << 20);
    const unsigned voff = (8u * g * 4096u + h * 128u + w * 16u + c) * 2u;
#pragma unroll
    for (int f = 0; f < 16; ++f) wv[f] = load_single(wr, voff, 32u * f * 8192u, 8192u);
    load_wo(p, h, w, c, g, wo);
  }
  put_x(sm, L_X, w, l, xp);
  load_x(xr, w, c, g, 1, xp);
  bar();
  STAMP(15, 8 + w);

  const unsigned v_off = L_V + ((w >> 1) * 64u + (2u * (w & 1) + (g >> 1)) * 16u + c) * 16u + (g & 1) * 8u;
  const unsigned flag = threadIdx.x == 448 ? F_U + h * FL : OOB;
  for (int s = 0; s < NSEG; ++s) {
    f4 acc[2] = {};
    const unsigned xb = L_X + (s & 1) * 32768u + l * 16u;
    u4 x[4][2];
#pragma unroll
    for (int k = 0; k < 3; ++k)
#pragma unroll
      for (int t = 0; t < 2; ++t) x[k][t] = lds16(sm, xb + (t * 16 + k) * 1024u);
#pragma unroll
    for (int ks = 0; ks < 16; ++ks) {
      sched();
      if (ks + 3 < 16) {
#pragma unroll
        for (int t = 0; t < 2; ++t) x[(ks + 3) & 3][t] = lds16(sm, xb + (t * 16 + ks + 3) * 1024u);
      }
      sched();
#pragma unroll
      for (int t = 0; t < 2; ++t) acc[t] = mma(wv[ks], x[ks & 3][t], acc[t]);
    }
    sched();
#pragma unroll
    for (int t = 0; t < 2; ++t)
      *reinterpret_cast<u2*>(sm + v_off + (s & 1) * 8192u + t * 4096u) = u2{pk(acc[t][0], acc[t][1]), pk(acc[t][2], acc[t][3])};
    if (w == 0) STAMP(3, s);
    asm volatile("s_waitcnt vmcnt(5)" ::: "memory");
    if (w == 0) STAMP(4, s);
    put_x(sm, L_X + ((s + 1) & 1) * 32768u, w, l, xp);
    load_x(xr, w, c, g, s + 2, xp);
    bar();
    st4c(max(s - 1, min(s, EAGER)), ws, flag, 0);
    if (w == 0) STAMP(5, s);
    u4 v[8];
#pragma unroll
    for (int k = 0; k < 8; ++k) v[k] = lds16(sm, L_V + (s & 1) * 8192u + ((k & 1) * 4 + (k >> 1)) * 1024u + l * 16u);
    sched();
    f4 u[4][2] = {};
#pragma unroll
    for (int k = 0; k < 8; ++k)
#pragma unroll
      for (int i = 0; i < 4; ++i) u[i][k & 1] = mma(v[k], wo[i][k >> 1], u[i][k & 1]);
#pragma unroll
    for (int i = 0; i < 4; ++i) {
      const u4 o = {pk(u[i][0][0], u[i][0][1]), pk(u[i][0][2], u[i][0][3]), pk(u[i][1][0], u[i][1][1]),
                    pk(u[i][1][2], u[i][1][3])};
      st16c(o, ws, O_U + ((4u * w + i) * 32u + h) * 1024u + l * 16u, s * 1048576u);
    }
    if (s < EAGER || s == NSEG - 1) {
      drain();
      bar();
      st4c(s + 1, ws, flag, 0);
    }
    if (w == 0) STAMP(2, s);
  }
}

DEV void role_v2(const Params& p, unsigned j, char* sm) {
  constexpr unsigned ROW = 144, PART = 16 * ROW, BUF = 8 * PART;
  const unsigned l = threadIdx.x & 63, w = rfl(threadIdx.x >> 6), c = l & 15, g = l >> 4;
  const unsigned b = j >> 1, qt = j & 1;
  const rsrc_t ws = mk(p.ws, WS_BYTES), outr = mk(p.out, 8u << 20);
  bool alive = true;

  const unsigned hq = 4u * w + (l & 3);
  const unsigned uf_off = F_U + hq * FL;
  const unsigned pf_off = (l & 4) ? F_M + hq * FL : F_PX + (qt * 32u + hq) * FL;
  const unsigned u_off = O_U + (2u * b * 32u + 4u * w) * 1024u + l * 16u;
  const unsigned m_off = O_M2 + 2u * b * 8192u + w * 1024u + l * 16u;
  const unsigned px_off = O_PX + (qt * 32u + 4u * w) * 1024u + l * 16u;
  const unsigned pm_off = O_PM + (qt * 8u + w) * 1024u + l * 16u;
  const unsigned out_off = ((16u * qt + (threadIdx.x >> 5)) * 1024u + 512u + 32u * b + (threadIdx.x & 31)) * 4u;
  unsigned u_seen = wait_flags(ws, uf_off, 1u, alive), p_seen = 0;
  u4 ua[2][4];
#pragma unroll
  for (int k = 0; k < 8; ++k) ua[k >> 2][k & 3] = ld16c(ws, u_off + (k >> 2) * 32768u + (k & 3) * 1024u, 0);

  for (int s = 0; s < NSEG; ++s) {
    if (!all_lanes(p_seen > (unsigned)s)) p_seen = wait_flags(ws, pf_off, s + 1, alive);
    u4 px[4];
#pragma unroll
    for (int i = 0; i < 4; ++i) px[i] = ld16c(ws, px_off + i * 1024u, s * 65536u);
    const u4 pm = ld16c(ws, pm_off, s * 16384u);
    const u4 m0 = ld16c(ws, m_off, s * 262144u), m1 = ld16c(ws, m_off + 8192u, s * 262144u);
    f4 a0 = {0, 0, 0, 0}, a1 = {0, 0, 0, 0};
#pragma unroll
    for (int i = 0; i < 4; ++i) {
      a0 = mma(ua[0][i], px[i], a0);
      a1 = mma(ua[1][i], px[i], a1);
    }
    a0 = mma(m0, pm, a0);
    a1 = mma(m1, pm, a1);
    sched();
    if (s + 1 < NSEG) {
      if (!all_lanes(u_seen > (unsigned)s + 1u)) u_seen = wait_flags(ws, uf_off, s + 2, alive);
#pragma unroll
      for (int k = 0; k < 8; ++k)
        ua[k >> 2][k & 3] = ld16c(ws, u_off + (k >> 2) * 32768u + (k & 3) * 1024u, (s + 1) * 1048576u);
    }
    sched();
    const unsigned rb = (s & 1) * BUF;
    *reinterpret_cast<f4*>(sm + rb + w * PART + c * ROW + g * 32u) = f4{a0[0], a1[0], a0[1], a1[1]};
    *reinterpret_cast<f4*>(sm + rb + w * PART + c * ROW + g * 32u + 16u) = f4{a0[2], a1[2], a0[3], a1[3]};
    bar();
    float v = 0.f;
#pragma unroll
    for (int k = 0; k < 8; ++k)
      v += *reinterpret_cast<const float*>(sm + rb + k * PART + (threadIdx.x >> 5) * ROW + (threadIdx.x & 31) * 4u);
    __builtin_amdgcn_raw_buffer_store_b32(__float_as_uint(rbf(v)), outr, out_off, s * 131072u, 0);
    if (w == 0) STAMP(2, s);
  }
}

DEV f2 cmul(f2 a, f2 b) {
  float re, im;
  asm("v_mul_f32 %0, %3, %5\n\tv_mul_f32 %1, %3, %4\n\tv_fma_f32 %0, %2, %4, -%0\n\tv_fma_f32 %1, %2, %5, %1"
      : "=&v"(re), "=&v"(im) : "v"(a[0]), "v"(a[1]), "v"(b[0]), "v"(b[1]));
  return f2{re, im};
}
DEV f2 cfma(f2 a, f2 b, f2 c) {
  float re, im;
  asm("v_fma_f32 %0, -%3, %5, %6\n\tv_fma_f32 %1, %3, %4, %7\n\tv_fma_f32 %0, %2, %4, %0\n\tv_fma_f32 %1, %2, %5, %1"
      : "=&v"(re), "=&v"(im) : "v"(a[0]), "v"(a[1]), "v"(b[0]), "v"(b[1]), "v"(c[0]), "v"(c[1]));
  return f2{re, im};
}
DEV f2 from_group(float re, float im, unsigned c, int k) {
  return f2{__shfl(re, (int)c + 16 * k, 64), __shfl(im, (int)c + 16 * k, 64)};
}

DEV void role_a(const Params& p, unsigned ci, char* sm) {
  constexpr unsigned GS = 272, KS = 4 * GS, TS = 32 * KS + 32, L_TAB = 4 * TS;
  const unsigned tid = threadIdx.x, l = tid & 63, w = rfl(tid >> 6), c = l & 15, g = l >> 4;
  const rsrc_t ws = mk(p.ws, WS_BYTES), xr = mk(p.ax, 2u << 20), outr = mk(p.out, 8u << 20);
  bool alive = true;

  {
    const unsigned voff = ((64u * ci + 16u * (c >> 2) + 4u * (w >> 1) + (c & 3)) * 512u + 256u * (w & 1) + 8u * g) * 2u;
    u4 xp[8];
#pragma unroll
    for (int i = 0; i < 8; ++i) xp[i] = ld16(xr, voff + i * 64u, 0);
#pragma unroll
    for (int i = 0; i < 8; ++i) lds16(sm, (8u * w + i) * 1024u + l * 16u) = xp[i];
  }
  f2 lam64;
  {
    const float lr = bf2f(((const unsigned short*)p.lre)[tid]), li = bf2f(((const unsigned short*)p.lim)[tid]);
    const float dt = rbf(expf(bf2f(((const unsigned short*)p.ldt)[tid])));
    const float er = expf(dt * lr);
    float sn, cs;
    sincosf(dt * li, &sn, &cs);
    f2 lam = {er * cs, er * sn};
    asm volatile("s_nop 1" : "+v"(lam));
    const float nr = lam[0] - 1.0f, ni = lam[1];
    f2 coef;
    if (fabsf(lr) >= fabsf(li)) {
      const float t = li / lr, d = 1.0f / (lr + li * t);
      coef = f2{smul(nr + smul(ni, t), d), smul(ni - smul(nr, t), d)};
    } else {
      const float t = lr / li, d = 1.0f / (lr * t + li);
      coef = f2{smul(smul(nr, t) + ni, d), smul(smul(ni, t) - nr, d)};
    }
    f2 l16 = cmul(lam, lam);
    l16 = cmul(l16, l16);
    l16 = cmul(l16, l16);
    l16 = cmul(l16, l16);
    lam64 = cmul(l16, l16);
    lam64 = cmul(lam64, lam64);
    *reinterpret_cast<f4*>(sm + L_TAB + tid * 32u) = f4{lam[0], lam[1], coef[0], coef[1]};
    *reinterpret_cast<f2*>(sm + L_TAB + tid * 32u + 16u) = l16;
  }
  bar();

  f4 hr[4][4] = {}, hi[4][4] = {};
  {
    const rsrc_t br = mk(p.bre, 1u << 19), bi = mk(p.bim, 1u << 19);
    const unsigned voff = ((64u * w + c) * 512u + 8u * g) * 2u;
    u4 fr[4], fi[4];
#pragma unroll
    for (int k = 0; k < 4; ++k) {
      fr[k] = ld16(br, voff + k * 64u, 0);
      fi[k] = ld16(bi, voff + k * 64u, 0);
    }
#pragma unroll
    for (int n = 0; n < 64; ++n) {
      const int ct = n >> 4, ks = n & 15;
      const u4 r = fr[n & 3], i = fi[n & 3];
      fence();
      if (n + 4 < 64) {
        fr[n & 3] = ld16(br, voff + ((n + 4) & 15) * 64u, ((n + 4) >> 4) * 16384u);
        fi[n & 3] = ld16(bi, voff + ((n + 4) & 15) * 64u, ((n + 4) >> 4) * 16384u);
      }
#pragma unroll
      for (int t = 0; t < 4; ++t) {
        const u4 x = lds16(sm, (t * 16 + ks) * 1024u + l * 16u);
        hr[ct][t] = mma(x, r, hr[ct][t]);
        hi[ct][t] = mma(x, i, hi[ct][t]);
      }
    }
  }
  __builtin_amdgcn_sched_barrier(0);
  asm volatile("s_nop 7\n\ts_nop 7" ::: "memory");
  __builtin_amdgcn_sched_barrier(0);
#pragma unroll
  for (int ct = 0; ct < 4; ++ct) {
    const unsigned ch = 64u * w + 16u * ct + c;
    const f4 tb = *reinterpret_cast<const f4*>(sm + L_TAB + ch * 32u);
    const f2 l16 = *reinterpret_cast<const f2*>(sm + L_TAB + ch * 32u + 16u);
    const f2 lam = {tb[0], tb[1]}, coef = {tb[2], tb[3]};
    f2 hh = {0.f, 0.f};
#pragma unroll
    for (int i = 0; i < 16; ++i) {
      hh = cfma(lam, hh, cmul(coef, f2{hr[ct][i >> 2][i & 3], hi[ct][i >> 2][i & 3]}));
      hr[ct][i >> 2][i & 3] = hh[0];
      hi[ct][i >> 2][i & 3] = hh[1];
    }
    f2 e = from_group(hh[0], hh[1], c, 0);
#pragma unroll
    for (int k = 1; k < 4; ++k) e = cfma(l16, e, from_group(hh[0], hh[1], c, k));
    if (g == 0) st8c(__builtin_bit_cast(u2, e), ws, O_AE + ci * 4096u + ch * 8u, 0);
  }
  drain();
  bar();
  if (tid == 0) st4c(1u, ws, F_A + ci * FL, 0);
  wait_flags(ws, F_A + (l & 31) * FL, (l & 31) < ci ? 1u : 0u, alive);
  {
    f2 hin = {0.f, 0.f};
#pragma unroll 8
    for (unsigned k = 0; k < ci; ++k) hin = cfma(lam64, hin, __builtin_bit_cast(f2, ld8c(ws, O_AE + tid * 8u, k * 4096u)));
    *reinterpret_cast<f2*>(sm + L_TAB + tid * 32u + 24u) = hin;
  }
  bar();
#pragma unroll
  for (int ct = 0; ct < 4; ++ct) {
    const unsigned ch = 64u * w + 16u * ct + c;
    const f4 t0 = *reinterpret_cast<const f4*>(sm + L_TAB + ch * 32u);
    const f4 t1 = *reinterpret_cast<const f4*>(sm + L_TAB + ch * 32u + 16u);
    const f2 lam = {t0[0], t0[1]}, l16 = {t1[0], t1[1]};
    f2 z = {t1[2], t1[3]};
#pragma unroll
    for (int k = 0; k < 3; ++k) {
      const f2 n = cfma(l16, z, from_group(hr[ct][3][3], hi[ct][3][3], c, k));
      z = (unsigned)k < g ? n : z;
    }
    const unsigned hb = g * TS + (ch >> 5) * KS + ((ch >> 3) & 3) * GS + (ch & 7) * 2u;
#pragma unroll
    for (int i = 0; i < 16; ++i) {
      z = cmul(lam, z);
      const unsigned d = pk(2.0f * (hr[ct][i >> 2][i & 3] + z[0]), -2.0f * (hi[ct][i >> 2][i & 3] + z[1]));
      lds_put2(sm, hb + i * 16u, d);
      lds_put2(sm, hb + 16u * KS + i * 16u, d >> 16);
    }
  }
  bar();
  f4 y[4][4] = {};
  {
    const rsrc_t cr = mk(p.cre, 1u << 19), cm = mk(p.cim, 1u << 19);
    const unsigned voff = ((64u * w + c) * 512u + 8u * g) * 2u;
    u4 fa[2][4];
#pragma unroll
    for (int k = 0; k < 2; ++k)
#pragma unroll
      for (int d = 0; d < 4; ++d) fa[k][d] = ld16(cr, voff + k * 64u, d * 16384u);
#pragma unroll
    for (int ks = 0; ks < 32; ++ks) {
      u4 a[4];
      fence();
#pragma unroll
      for (int d = 0; d < 4; ++d) {
        a[d] = fa[ks & 1][d];
        if (ks + 2 < 32) fa[ks & 1][d] = ld16(ks + 2 < 16 ? cr : cm, voff + ((ks + 2) & 15) * 64u, d * 16384u);
      }
#pragma unroll
      for (int t = 0; t < 4; ++t) {
        const u4 hb = lds16(sm, t * TS + ks * KS + g * GS + c * 16u);
#pragma unroll
        for (int d = 0; d < 4; ++d) y[d][t] = mma(a[d], hb, y[d][t]);
      }
    }
  }
  const rsrc_t dr = mk(p.dsk, 1024);
#pragma unroll
  for (int d = 0; d < 4; ++d) {
    const unsigned col = 64u * w + 16u * d + 4u * g;
    const u2 dk = ld8(dr, col * 2u, 0);
#pragma unroll
    for (int t = 0; t < 4; ++t) {
      const unsigned row = 64u * ci + 16u * t + c;
      const u2 xv = ld8(xr, (row * 512u + col) * 2u, 0);
      const f4 o = {y[d][t][0] + rbf(smul(bf2f(xv[0] & 0xffffu), bf2f(dk[0] & 0xffffu))),
                    y[d][t][1] + rbf(smul(bf2f(xv[0] >> 16), bf2f(dk[0] >> 16))),
                    y[d][t][2] + rbf(smul(bf2f(xv[1] & 0xffffu), bf2f(dk[1] & 0xffffu))),
                    y[d][t][3] + rbf(smul(bf2f(xv[1] >> 16), bf2f(dk[1] >> 16)))};
      __builtin_amdgcn_raw_buffer_store_b128(__builtin_bit_cast(u4, o), outr, (row * 1024u + col) * 4u, 0, 0);
    }
  }
  if (w == 0) STAMP(2, 0);
}

__global__ void __launch_bounds__(512, 2) fused_kernel(Params p) {
  extern __shared__ __attribute__((aligned(16))) char smem[];
  const unsigned x = blockIdx.x & 7, r = blockIdx.x >> 3;
  if (threadIdx.x < 64) TSTAMP(0);
  if (x == 1 || x == 2) {
    role_v1(p, (x - 1) * 32u + r, smem);
  } else if (x < 4) {
    const unsigned h = (x ? 16u : 0u) + (r >> 1);
    if (r & 1) role_vm(p, h, smem); else role_s1(p, h, smem);
  } else {
    const unsigned i = (x - 4) * 8u + (r >> 2);
    switch (r & 3) {
      case 0: role_s2(p, i, smem); break;
      case 1: role_vu(p, i, smem); break;
      case 2: role_v2(p, i, smem); break;
      case 3: role_a(p, i, smem); break;
    }
  }
  if (threadIdx.x < 64) TSTAMP(2);
}

__global__ void prt(const unsigned* dg) {
  unsigned t0 = ~0u, s1 = 0, v1 = 0, e = 0, tl = 0;
  for (int b = 0; b < 256; b++) { const unsigned* q = &dg[b * 1024 + 14 * 64]; t0 = min(t0, q[0]); tl = max(tl, q[0]); e = max(e, q[2]);
    if (((b & 7) == 0 || (b & 7) == 3) && !((b >> 3) & 1)) s1 = max(s1, q[1]);
    if ((b & 7) == 1 || (b & 7) == 2) v1 = max(v1, q[2]); }
  printf("T spread %u start %u step %u tail %u total %u\n", tl - t0, s1 - t0, (v1 - s1) * 10 / 63, e - v1, e - t0);
}
extern "C" void kernel_launch(void* const* d_in, const int* in_sizes, int n_in, void* d_out, int out_size, void* d_ws,
                              size_t ws_size, hipStream_t stream) {
  Params p;
  p.ax = d_in[0]; p.lre = d_in[1]; p.lim = d_in[2]; p.bre = d_in[3]; p.bim = d_in[4]; p.cre = d_in[5];
  p.cim = d_in[6]; p.ldt = d_in[7]; p.dsk = d_in[8]; p.bx = d_in[9]; p.mem0 = d_in[10]; p.wq = d_in[11];
  p.wk = d_in[12]; p.wv = d_in[13]; p.wo = d_in[14];
  p.out = (float*)d_out;
  p.ws = (char*)d_ws;
  (void)hipFuncSetAttribute((const void*)fused_kernel, hipFuncAttributeMaxDynamicSharedMemorySize, DYN_LDS);
  (void)hipMemsetAsync(d_ws, 0, FLAG_BYTES, stream);
  fused_kernel<<<256, 512, DYN_LDS, stream>>>(p);
  prt<<<1, 1, 0, stream>>>((const unsigned*)((char*)d_ws + O_DIAG));
}
```

```cpp
#include <hip/hip_runtime.h>
#include <hip/hip_bf16.h>
#include <stdint.h>

typedef __attribute__((ext_vector_type(2))) unsigned u2;
typedef __attribute__((ext_vector_type(4))) unsigned u4;
typedef __attribute__((ext_vector_type(8))) unsigned u8;
typedef __attribute__((ext_vector_type(16))) unsigned u16v;
typedef __attribute__((ext_vector_type(32))) unsigned u32v;
typedef __attribute__((ext_vector_type(2))) float f2;
typedef __attribute__((ext_vector_type(4))) float f4;
typedef __attribute__((ext_vector_type(2))) __bf16 bf2;
typedef __attribute__((ext_vector_type(8))) __bf16 bf8;
typedef __attribute__((ext_vector_type(16))) __bf16 bf16v;
typedef __amdgpu_buffer_rsrc_t rsrc_t;
#define DEV __device__ __forceinline__

struct Params {
  const void *ax, *lre, *lim, *bre, *bim, *cre, *cim, *ldt, *dsk, *bx, *mem0, *wq, *wk, *wv, *wo;
  float* out;
  char* ws;
};

#define WSLEEP 4
constexpr int NSEG = 64;
constexpr unsigned SPIN_CAP = 400000u;
constexpr unsigned DYN_LDS = 156u * 1024u;

constexpr unsigned FL = 128, F_INIT = 0, F_KM = 64 * FL, F_KX = 96 * FL, F_PX = 128 * FL, F_M = 192 * FL, F_U = 224 * FL,
                   F_A = 256 * FL, FLAG_BYTES = 288 * FL;
constexpr unsigned O_AE = 5u << 20, O_DIAG = 6u << 20;
constexpr unsigned O_KX = 8u << 20, O_KM = 24u << 20, O_PX = 28u << 20, O_PM = 32u << 20, O_M2 = 34u << 20,
                   O_U = 50u << 20, WS_BYTES = 114u << 20;
constexpr unsigned OOB = 1u << 30;
constexpr unsigned B_MEM = 0, B_P = 63, B_M = 67, B_END = 99, B_PROBE = 100;

DEV rsrc_t mk(const void* p, unsigned bytes) {
  return __builtin_amdgcn_make_buffer_rsrc((void*)p, 0, bytes, 0x00020000);
}
DEV unsigned rfl(unsigned v) { return __builtin_amdgcn_readfirstlane(v); }
DEV u4 ld16(rsrc_t r, unsigned v, unsigned s) { return __builtin_amdgcn_raw_buffer_load_b128(r, v, s, 0); }
DEV u2 ld8(rsrc_t r, unsigned v, unsigned s) { return __builtin_amdgcn_raw_buffer_load_b64(r, v, s, 0); }
DEV unsigned ld4(rsrc_t r, unsigned v, unsigned s) { return __builtin_amdgcn_raw_buffer_load_b32(r, v, s, 0); }
DEV unsigned ld2(rsrc_t r, unsigned v, unsigned s) { return __builtin_amdgcn_raw_buffer_load_b16(r, v, s, 0); }
DEV u4 ld16c(rsrc_t r, unsigned v, unsigned s) { return __builtin_amdgcn_raw_buffer_load_b128(r, v, s, 16); }
DEV u2 ld8c(rsrc_t r, unsigned v, unsigned s) { return __builtin_amdgcn_raw_buffer_load_b64(r, v, s, 16); }
DEV unsigned ld4c(rsrc_t r, unsigned v, unsigned s) { return __builtin_amdgcn_raw_buffer_load_b32(r, v, s, 16); }
DEV void st16c(u4 d, rsrc_t r, unsigned v, unsigned s) { __builtin_amdgcn_raw_buffer_store_b128(d, r, v + s, 0, 16); }
DEV void st8c(u2 d, rsrc_t r, unsigned v, unsigned s) { __builtin_amdgcn_raw_buffer_store_b64(d, r, v, s, 16); }
DEV void st4c(unsigned d, rsrc_t r, unsigned v, unsigned s) { __builtin_amdgcn_raw_buffer_store_b32(d, r, v, s, 16); }
DEV void fence() { asm volatile("" ::: "memory"); }
DEV void drain() { asm volatile("s_waitcnt vmcnt(0)" ::: "memory"); }
DEV void bar() { asm volatile("s_waitcnt lgkmcnt(0)\n\ts_barrier" ::: "memory"); }
DEV bool all_lanes(bool ok) { return __builtin_amdgcn_ballot_w64(!ok) == 0ull; }

DEV float bf2f(unsigned h) { return __uint_as_float(h << 16); }
DEV unsigned pk(float a, float b) {
  f2 v = {a, b};
  return __builtin_bit_cast(unsigned, __builtin_convertvector(v, bf2));
}
DEV float rbf(float a) { return bf2f(pk(a, a) & 0xffffu); }
DEV float smul(float a, float b) {
  float r;
  asm("v_mul_f32 %0, %1, %2" : "=v"(r) : "v"(a), "v"(b));
  return r;
}
DEV float sadd(float a, float b) {
  float r;
  asm("v_add_f32 %0, %1, %2" : "=v"(r) : "v"(a), "v"(b));
  return r;
}
DEV f4 mma(u4 a, u4 b, f4 c) {
  return __builtin_amdgcn_mfma_f32_16x16x32_bf16(__builtin_bit_cast(bf8, a), __builtin_bit_cast(bf8, b), c, 0, 0, 0);
}
DEV f4 smma(u4 a, u8 b, f4 c, unsigned idx) {
  return __builtin_amdgcn_smfmac_f32_16x16x64_bf16(__builtin_bit_cast(bf8, a), __builtin_bit_cast(bf16v, b), c,
                                                   (int)idx, 0, 0);
}
DEV float swap32_add(float a, float b) {
  auto r = __builtin_amdgcn_permlane32_swap(__float_as_uint(a), __float_as_uint(b), false, false);
  return __uint_as_float(r[0]) + __uint_as_float(r[1]);
}
DEV float swap16_add(float a, float b) {
  auto r = __builtin_amdgcn_permlane16_swap(__float_as_uint(a), __float_as_uint(b), false, false);
  return __uint_as_float(r[0]) + __uint_as_float(r[1]);
}
DEV float group_max(float m) {
  auto r = __builtin_amdgcn_permlane16_swap(__float_as_uint(m), __float_as_uint(m), false, false);
  m = fmaxf(__uint_as_float(r[0]), __uint_as_float(r[1]));
  auto q = __builtin_amdgcn_permlane32_swap(__float_as_uint(m), __float_as_uint(m), false, false);
  return fmaxf(__uint_as_float(q[0]), __uint_as_float(q[1]));
}
DEV float group_sum(float m) {
  const float t = swap16_add(m, m);
  return swap32_add(t, t);
}
DEV u4& lds16(char* sm, unsigned off) { return *reinterpret_cast<u4*>(sm + off); }
DEV void lds_put2(char* sm, unsigned off, unsigned v) { *reinterpret_cast<unsigned short*>(sm + off) = (unsigned short)v; }

#define STAMP(slot, idx) do { } while (0)

#define TSTAMP(idx) do { } while (0)

DEV unsigned wait_flags(rsrc_t ws, unsigned off, unsigned need, bool& alive) {
  unsigned v = 0;
  for (unsigned it = 0; alive; ++it) {
    fence();
    v = ld4c(ws, off, 0);
    if (all_lanes(v >= need)) break;
    if (it > SPIN_CAP) alive = false;
    __builtin_amdgcn_s_sleep(WSLEEP);
  }
  fence();
  return v;
}
DEV unsigned crit_base(const Params& p) {
  const unsigned lo = (unsigned)(uintptr_t)p.ws;
  return ((lo + 65536u + 0x1fffffu) & ~0x1fffffu) - lo;
}
DEV unsigned wait_init(const Params& p, rsrc_t ws, unsigned n, bool& alive) {
  const unsigned l = threadIdx.x & 63, w = threadIdx.x >> 6;
  const unsigned v = wait_flags(ws, F_INIT + (l ? (l & (64u / n - 1u)) + w * (64u / n) : 0u) * FL, 1u, alive);
  return crit_base(p) + (rfl(v) == 2u ? 8192u : 0u);
}

DEV void load_pair(rsrc_t r, unsigned voff, unsigned soff, unsigned rstride, u4& a, u4& b) {
  unsigned d[8];
#pragma unroll
  for (int j = 0; j < 8; ++j) d[j] = ld4(r, voff, soff + j * rstride);
#pragma unroll
  for (int j = 0; j < 4; ++j) {
    a[j] = __builtin_amdgcn_perm(d[2 * j + 1], d[2 * j], 0x05040100u);
    b[j] = __builtin_amdgcn_perm(d[2 * j + 1], d[2 * j], 0x07060302u);
  }
}
DEV u4 load_single(rsrc_t r, unsigned voff, unsigned soff, unsigned rstride) {
  unsigned d[8];
  u4 a;
#pragma unroll
  for (int j = 0; j < 8; ++j) d[j] = ld2(r, voff, soff + j * rstride);
#pragma unroll
  for (int j = 0; j < 4; ++j) a[j] = __builtin_amdgcn_perm(d[2 * j + 1], d[2 * j], 0x05040100u);
  return a;
}
DEV u8 cat(u4 a, u4 b) { return __builtin_shufflevector(a, b, 0, 1, 2, 3, 4, 5, 6, 7); }
DEV u16v cat(u8 a, u8 b) { return __builtin_shufflevector(a, b, 0, 1, 2, 3, 4, 5, 6, 7, 8, 9, 10, 11, 12, 13, 14, 15); }
DEV u32v cat(u8 a, u8 b, u8 c, u8 d) {
  return __builtin_shufflevector(cat(a, b), cat(c, d), 0, 1, 2, 3, 4, 5, 6, 7, 8, 9, 10, 11, 12, 13, 14, 15, 16, 17, 18,
                                 19, 20, 21, 22, 23, 24, 25, 26, 27, 28, 29, 30, 31);
}
DEV u4 srd(const void* p, unsigned bytes) {
  return u4{(unsigned)(uintptr_t)p, (unsigned)((uintptr_t)p >> 32) & 0xffffu, bytes, 0x00020000u};
}
DEV unsigned lds_base(char* sm) { return (unsigned)(uintptr_t)(__attribute__((address_space(3))) char*)sm; }
DEV unsigned swz(unsigned g) { return (g & 1) | ((g & 2) << 1); }
DEV void sched() { __builtin_amdgcn_sched_barrier(0); }

DEV u4 load_mem0(const Params& p, unsigned w, unsigned c, unsigned g) {
  const rsrc_t r = mk(p.mem0, 8192);
  const unsigned cg = 4 * w + g;
  const unsigned voff = ((c >> 1) * 512u + 32u * (cg >> 1) + (cg & 1) + 4u * (c & 1)) * 2u;
  u4 a;
#pragma unroll
  for (int i = 0; i < 4; ++i) a[i] = ld2(r, voff, 16 * i) | (ld2(r, voff, 16 * i + 4) << 16);
  return a;
}
constexpr float SCALE_LOG2E = 0.08838834764831845f * 1.4426950408889634f;

#define A_STAMP(r)
#define A_SAVE(r, slot)
#define A_NEXT
#define A_KXWAIT "s_waitcnt vmcnt(2)\n"
#define A_DIAG_OUT
#define A_DIAG_CLOB
#define A_CLK(r)
#define A_SAVECLK
#define A_DIAG_DECL
#define A_SCLOB "s80", "s81", "s82", "s83", "s84", "s85", "s86", "s87", "s88", "s89", "s90", "s91", "vcc", "scc", "memory"
#define SMF(d, a, b, i) "v_smfmac_f32_16x16x64_bf16 v[" d "], v[" a "], v[" b "], " i "\n"
#define MFM(d, a, b, c) "v_mfma_f32_16x16x32_bf16 v[" d "], v[" a "], v[" b "], " c "\n"
#define DPP_ALL " row_mask:0xf bank_mask:0xf\n"
#define A_POLL_MEM \
  ".Lpoll%=:\n" \
  "buffer_load_dwordx4 v[148:151], v129, %[ws], %[mem] offen sc1\n" \
  "s_sub_u32 %[cap], %[cap], 1\n" \
  "s_cbranch_scc1 .Lexit%=\n" \
  "s_waitcnt vmcnt(0)\n" \
  "ds_write_b128 v130, v[148:151]\n" \
  "v_max3_u32 v152, v148, v149, v150\n" \
  "v_max_u32 v152, v152, v151\n" \
  "v_cmp_eq_u32 vcc, -1, v152\n" \
  "s_cbranch_vccnz .Lpoll%=\n"
#define A_READ_MEM \
  "s_waitcnt lgkmcnt(0)\n" \
  "s_barrier\n" \
  A_CLK("s[72:73]") \
  "ds_read_b128 v[152:155], v131\n" \
  "ds_read_b128 v[156:159], v132\n" \
  "ds_read_b128 v[160:163], v133\n" \
  "ds_read_b128 v[164:167], v134\n" \
  "ds_read_b128 v[168:171], v135\n" \
  "ds_read_b128 v[172:175], v136\n" \
  "ds_read_b128 v[176:179], v137\n"
#define D_S1 0
#define D_S0 0
#define D_VM 0
#define D_V1 0
#define STR_(x) #x
#define SLEEP(x) "s_sleep " STR_(x) "\n"
#define WL(n) "s_waitcnt lgkmcnt(" #n ")\n"
#define WV(n) "s_waitcnt vmcnt(" #n ")\n"

DEV void role_s1(const Params& p, unsigned h, char* sm) {
  constexpr unsigned L_MEM = 0, L_Q = 8192, L_K = 10240;
  const unsigned l = threadIdx.x & 63, w = rfl(threadIdx.x >> 6), c = l & 15, g = l >> 4;
  const rsrc_t ws = mk(p.ws, WS_BYTES);
  bool alive = true;
  STAMP(15, w);

  u8 wa[8], wb[8];
  {
    const rsrc_t wr = mk(w < 4 ? p.wq : p.wk, 4u << 20);
    const unsigned voff = ((16u * (g & 1) + (g >> 1)) * 4096u + h * 128u + (w & 3) * 32u + 2u * c) * 2u;
#pragma unroll
    for (int i = 0; i < 8; ++i) {
      const unsigned t = (w + i) & 7;
      u4 a0, b0, a1, b1;
      load_pair(wr, voff, (64u * t) * 8192u, 16384u, a0, b0);
      load_pair(wr, voff, (64u * t + 32u) * 8192u, 16384u, a1, b1);
      wa[i] = cat(a0, a1);
      wb[i] = cat(b0, b1);
    }
  }
  u4 a0 = load_mem0(p, w, c, g);
  const unsigned cb = wait_init(p, ws, 8, alive);
  STAMP(15, 8 + w);

  const unsigned lb = lds_base(sm);
  u16v k0;
  u8 k1;
  k0[0] = (c & 1) ? 0xeeeeeeeeu : 0x44444444u;
  k0[1] = w * 1024u + l * 16u;
#pragma unroll
  for (int i = 0; i < 8; ++i) k0[2 + i] = lb + L_MEM + ((w + i) & 7) * 1024u + l * 16u;
#pragma unroll
  for (int r = 0; r < 2; ++r)
    k0[10 + r] = lb + (w < 4 ? L_Q : L_K) + (((w & 3) * 4u + (c >> 2)) * 8u + ((2u * g + r) ^ swz(c >> 2))) * 16u + (c & 3) * 4u;
  k0[12] = k0[13] = 0;
  k0[14] = lb + L_Q + (g * 8u + ((c & 7) ^ swz(g))) * 16u;
  k0[15] = lb + L_K + (g * 8u + ((((c >> 2) & 1) * 4u + (c >> 3) * 2u + (c & 1)) ^ swz(g))) * 16u;
  const unsigned piece = ((c & 7) >> 2) * 4u + (h >> 3), hl = h & 7;
  const unsigned p_base = cb + (B_P + (piece >> 1)) * 16384u + (piece & 1) * 4096u;
  const unsigned tok = ((c >> 1) & 1) * 16u + (c >> 3) * 8u + ((c >> 2) & 1) * 2u + (c & 1);
  k1[0] = p_base + (hl >> 1) * 512u + (2u * (hl & 1) + (g >> 1)) * 128u + ((g & 1) * 4u + (c & 3)) * 16u;
  k1[1] = p_base + 2048u + (hl >> 1) * 128u + ((g >> 1) * 4u + (c & 3)) * 16u + (hl & 1) * 8u;
  k1[2] = O_KX + h * 8192u + ((tok >> 4) * 64u + g * 16u + (tok & 15)) * 16u;
  k1[3] = k1[2] + 4096u;
  k1[4] = w == 0 ? F_KX + h * FL : OOB;
  k1[5] = lb + L_K + l * 16u;
  k1[6] = O_KM + h * 2048u + l * 16u;
  k1[7] = F_KM + h * FL;

  u32v kx = {};
  unsigned seen;
  if (w == 0) {
    wait_flags(ws, k1[4], 1u, alive);
    u4 f[8];
#pragma unroll
    for (int i = 0; i < 8; ++i) f[i] = ld16c(ws, k1[2] + (i & 3) * 2048u + (i >> 2) * 64u, 0);
    kx = cat(cat(f[0], f[1]), cat(f[2], f[3]), cat(f[4], f[5]), cat(f[6], f[7]));
  }
  STAMP(15, 16 + w);
  if (w == 0) TSTAMP(1);

  unsigned s = 0, mem = rfl(cb + B_MEM * 16384u - 16384u), cap = rfl(alive ? SPIN_CAP : 0u), kxo = 262144u, kmo = 0;
  u32v t0;
  u8 t1;
  u16v t2;
  A_DIAG_DECL;
  asm volatile(
      "s_mov_b32 s80, 0xff00ff00\n"
      "s_mov_b32 s81, 0xff00ff00\n"
      "s_mov_b32 s82, 0x3e0293ee\n"
      "s_mov_b32 s83, 0\n"
      "s_cmp_eq_u32 %[w], 0\n"
      "s_cselect_b32 s90, 63, 0\n"
      "v_mov_b32 v184, 0\nv_mov_b32 v185, 0\nv_mov_b32 v186, 0\nv_mov_b32 v187, 0\n"
      "v_mov_b32 v188, 0\nv_mov_b32 v189, 0\nv_mov_b32 v190, 0\nv_mov_b32 v191, 0\n"
      "ds_write_b128 v130, v[148:151]\n"
      "s_branch .Lhave%=\n"
      A_POLL_MEM
      ".Lhave%=:\n"
      A_STAMP("s[84:85]") A_CLK("s[70:71]")
      SMF("184:187", "148:151", "0:7", "v128") SMF("188:191", "148:151", "8:15", "v128")
      "buffer_load_dword v248, v244, %[ws], 0 offen sc1\n"
      A_READ_MEM
      WL(6) SMF("184:187", "152:155", "16:23", "v128") SMF("188:191", "152:155", "24:31", "v128")
      WL(5) SMF("184:187", "156:159", "32:39", "v128") SMF("188:191", "156:159", "40:47", "v128")
      WL(4) SMF("184:187", "160:163", "48:55", "v128") SMF("188:191", "160:163", "56:63", "v128")
      WL(3) SMF("184:187", "164:167", "64:71", "v128") SMF("188:191", "164:167", "72:79", "v128")
      WL(2) SMF("184:187", "168:171", "80:87", "v128") SMF("188:191", "168:171", "88:95", "v128")
      WL(1) SMF("184:187", "172:175", "96:103", "v128") SMF("188:191", "172:175", "104:111", "v128")
      WL(0) SMF("184:187", "176:179", "112:119", "v128") SMF("188:191", "176:179", "120:127", "v128")
      A_CLK("s[74:75]")
      "s_cmp_lt_u32 %[s], s90\n"
      "s_nop 5\n"
      "v_add_f32 v152, v184, v185\n"
      "v_add_f32 v154, v186, v187\n"
      "v_add_f32 v153, v188, v189\n"
      "v_add_f32 v155, v190, v191\n"
      "v_cvt_pk_bf16_f32 v152, v152, v153\n"
      "v_cvt_pk_bf16_f32 v154, v154, v155\n"
      "ds_write_b32 v138, v152\n"
      "ds_write_b32 v139, v154\n"
      A_STAMP("s[86:87]") A_CLK("s[76:77]")
      "v_mov_b32 v184, 0\nv_mov_b32 v185, 0\nv_mov_b32 v186, 0\nv_mov_b32 v187, 0\n"
      "v_mov_b32 v188, 0\nv_mov_b32 v189, 0\nv_mov_b32 v190, 0\nv_mov_b32 v191, 0\n"
      "s_waitcnt lgkmcnt(0)\n"
      "s_barrier\n"
      "s_cbranch_scc0 .Lother%=\n"
      A_CLK("s[78:79]")
      "ds_read_b128 v[152:155], v142\n"
      "ds_read_b128 v[168:171], v143\n"
      "ds_read_b128 v[156:159], v142 offset:512\n"
      "ds_read_b128 v[172:175], v143 offset:512\n"
      "ds_read_b128 v[160:163], v142 offset:1024\n"
      "ds_read_b128 v[176:179], v143 offset:1024\n"
      "ds_read_b128 v[164:167], v142 offset:1536\n"
      "ds_read_b128 v[180:183], v143 offset:1536\n"
      WL(7) MFM("224:227", "192:195", "152:155", "0") MFM("228:231", "208:211", "152:155", "0")
      WL(6) MFM("232:235", "168:171", "152:155", "0")
      WL(5) MFM("224:227", "196:199", "156:159", "v[224:227]") MFM("228:231", "212:215", "156:159", "v[228:231]")
      WL(4) MFM("232:235", "172:175", "156:159", "v[232:235]")
      WL(3) MFM("224:227", "200:203", "160:163", "v[224:227]") MFM("228:231", "216:219", "160:163", "v[228:231]")
      WL(2) MFM("232:235", "176:179", "160:163", "v[232:235]")
      WL(1) MFM("224:227", "204:207", "164:167", "v[224:227]") MFM("228:231", "220:223", "164:167", "v[228:231]")
      WL(0) MFM("232:235", "180:183", "164:167", "v[232:235]")
      A_CLK("s[92:93]")
      "s_nop 5\n"
      "v_max3_f32 v152, v224, v225, v226\n"
      "v_max3_f32 v153, v227, v228, v229\n"
      "v_max3_f32 v154, v230, v231, v232\n"
      "v_max3_f32 v152, v152, v153, v233\n"
      "v_max_f32 v153, v152, v154\n"
      "v_max_f32 v152, v152, v154\n"
      "v_cndmask_b32 v156, v224, v228, s[80:81]\n"
      "v_cndmask_b32 v157, v225, v229, s[80:81]\n"
      "v_permlane16_swap_b32 v152, v153\n"
      "v_max_f32 v154, v152, v153\n"
      "v_max_f32 v155, v152, v153\n"
      "v_cndmask_b32 v158, v226, v230, s[80:81]\n"
      "v_cndmask_b32 v159, v227, v231, s[80:81]\n"
      "v_permlane32_swap_b32 v154, v155\n"
      "v_cndmask_b32 v160, v232, v233, s[80:81]\n"
      "v_max_f32 v152, v154, v155\n"
      "v_mul_f32 v152, 0xbe0293ee, v152\n"
      "v_fma_f32 v156, v156, s82, v152\n"
      "v_fma_f32 v157, v157, s82, v152\n"
      "v_fma_f32 v158, v158, s82, v152\n"
      "v_fma_f32 v159, v159, s82, v152\n"
      "v_fma_f32 v160, v160, s82, v152\n"
      "v_exp_f32 v156, v156\n"
      "v_exp_f32 v157, v157\n"
      "v_exp_f32 v158, v158\n"
      "v_exp_f32 v159, v159\n"
      "v_exp_f32 v160, v160\n"
      "v_add_f32 v152, v156, v157\n"
      "v_add_f32 v153, v158, v159\n"
      "v_add_f32 v152, v152, v160\n"
      "v_add_f32 v152, v152, v153\n"
      "v_xor_b32 v156, s83, v156\n"
      "v_xor_b32 v157, s83, v157\n"
      "v_add_f32_dpp v154, v152, v152 row_ror:8" DPP_ALL
      "v_add_f32_dpp v155, v152, v152 row_ror:8" DPP_ALL
      "v_xor_b32 v158, s83, v158\n"
      "v_xor_b32 v159, s83, v159\n"
      "v_permlane16_swap_b32 v154, v155\n"
      "v_add_f32 v152, v154, v155\n"
      "v_add_f32 v153, v154, v155\n"
      "v_xor_b32 v160, s83, v160\n"
      "s_xor_b32 s83, s83, 0x80000000\n"
      "v_permlane32_swap_b32 v152, v153\n"
      "v_add_f32 v152, v152, v153\n"
      "v_rcp_f32 v152, v152\n"
      A_CLK("s[94:95]")
      "s_add_u32 s91, %[s], 2\n"
      "v_mul_f32 v156, v156, v152\n"
      "v_mul_f32 v157, v157, v152\n"
      "v_mul_f32 v158, v158, v152\n"
      "v_mul_f32 v159, v159, v152\n"
      "v_mul_f32 v160, v160, v152\n"
      "v_cvt_pk_bf16_f32 v164, v156, v157\n"
      "v_cvt_pk_bf16_f32 v165, v158, v159\n"
      "v_mov_b32_dpp v161, v160 row_ror:8" DPP_ALL
      "v_mov_b32_dpp v166, v164 row_ror:8" DPP_ALL
      "v_mov_b32_dpp v167, v165 row_ror:8" DPP_ALL
      "v_cvt_pk_bf16_f32 v162, v160, v161\n"
      "v_cvt_pk_bf16_f32 v163, v160, v161\n"
      "s_not_b64 exec, s[80:81]\n"
      "buffer_store_dwordx4 v[164:167], v240, %[ws], 0 offen sc1\n"
      "s_mov_b64 exec, -1\n"
      "v_permlane16_swap_b32 v162, v163\n"
      "s_mov_b32 exec_lo, 0xff\n"
      "s_mov_b32 exec_hi, 0xff\n"
      "buffer_store_dwordx2 v[162:163], v241, %[ws], 0 offen sc1\n"
      "s_mov_b64 exec, -1\n"
      A_STAMP("s[88:89]") A_CLK("s[96:97]") A_SAVE("s84", 0) A_SAVE("s86", 1) A_SAVE("s88", 2) A_SAVECLK
      "s_cmp_ge_u32 %[s], 62\n"
      "s_cbranch_scc1 .Lnext%=\n"
      A_KXWAIT
      ".Lkx%=:\n"
      "v_cmp_gt_u32 vcc, s91, v248\n"
      "s_cbranch_vccz .Lkxok%=\n"
      "buffer_load_dword v248, v244, %[ws], 0 offen sc1\n"
      "s_sub_u32 %[cap], %[cap], 1\n"
      "s_cbranch_scc1 .Lexit%=\n"
      "s_waitcnt vmcnt(0)\n"
      "s_branch .Lkx%=\n"
      ".Lkxok%=:\n"
      "buffer_load_dwordx4 v[192:195], v242, %[ws], %[kxo] offen sc1\n"
      "buffer_load_dwordx4 v[208:211], v242, %[ws], %[kxo] offen offset:64 sc1\n"
      "buffer_load_dwordx4 v[196:199], v242, %[ws], %[kxo] offen offset:2048 sc1\n"
      "buffer_load_dwordx4 v[212:215], v242, %[ws], %[kxo] offen offset:2112 sc1\n"
      "buffer_load_dwordx4 v[200:203], v243, %[ws], %[kxo] offen sc1\n"
      "buffer_load_dwordx4 v[216:219], v243, %[ws], %[kxo] offen offset:64 sc1\n"
      "buffer_load_dwordx4 v[204:207], v243, %[ws], %[kxo] offen offset:2048 sc1\n"
      "buffer_load_dwordx4 v[220:223], v243, %[ws], %[kxo] offen offset:2112 sc1\n"
      "s_add_u32 %[kxo], %[kxo], 0x40000\n"
      SLEEP(D_S0)
      "s_branch .Lnext%=\n"
      ".Lidle%=:\n"
      SLEEP(D_S1)
      "s_branch .Lnext%=\n"
      ".Lother%=:\n"
      "s_cmp_lg_u32 %[w], 6\n"
      "s_cbranch_scc1 .Lidle%=\n"
      "ds_read_b128 v[152:155], v245\n"
      "ds_read_b128 v[156:159], v245 offset:1024\n"
      "s_add_u32 s91, %[s], 1\n"
      "v_mov_b32 v160, s91\n"
      WL(1) "buffer_store_dwordx4 v[152:155], v246, %[ws], %[kmo] offen sc1\n"
      WL(0) "buffer_store_dwordx4 v[156:159], v246, %[ws], %[kmo] offen offset:1024 sc1\n"
      "s_add_u32 %[kmo], %[kmo], 0x10000\n"
      "s_waitcnt vmcnt(0)\n"
      "s_mov_b64 exec, 1\n"
      "buffer_store_dword v160, v247, %[ws], 0 offen sc1\n"
      "s_mov_b64 exec, -1\n"
      SLEEP(D_S0)
      ".Lnext%=:\n"
      A_NEXT
      "s_add_u32 %[mem], %[mem], 0x4000\n"
      "s_add_u32 %[s], %[s], 1\n"
      "s_cmp_lt_u32 %[s], 64\n"
      "s_cbranch_scc1 .Lpoll%=\n"
      ".Lexit%=:\n"
      : [s] "+s"(s), [mem] "+s"(mem), [cap] "+s"(cap), [kxo] "+s"(kxo), [kmo] "+s"(kmo), "+{v[148:151]}"(a0),
        "=&{v[152:183]}"(t0), "=&{v[184:191]}"(t1), "+{v[192:223]}"(kx), "=&{v[224:239]}"(t2), "=&{v248}"(seen) A_DIAG_OUT
      : [ws] "s"(srd(p.ws, WS_BYTES)), [w] "s"(w), "{v[0:31]}"(cat(wa[0], wb[0], wa[1], wb[1])),
        "{v[32:63]}"(cat(wa[2], wb[2], wa[3], wb[3])), "{v[64:95]}"(cat(wa[4], wb[4], wa[5], wb[5])),
        "{v[96:127]}"(cat(wa[6], wb[6], wa[7], wb[7])), "{v[128:143]}"(k0), "{v[240:247]}"(k1)
      : A_SCLOB A_DIAG_CLOB);
}

DEV void load_wo(const Params& p, unsigned h, unsigned w, unsigned c, unsigned g, u4 (&wo)[4][4]) {
  const rsrc_t r = mk(p.wo, 4u << 20);
  const unsigned voff = ((h * 128u + 8u * g) * 512u + 64u * w + 2u * c) * 2u;
#pragma unroll
  for (int b = 0; b < 2; ++b)
#pragma unroll
    for (int f = 0; f < 4; ++f) load_pair(r, voff, f * 32768u + b * 64u, 1024u, wo[2 * b][f], wo[2 * b + 1][f]);
}

DEV void role_vm(const Params& p, unsigned h, char* sm) {
  constexpr unsigned L_MEM = 0, L_V = 8192;
  const unsigned l = threadIdx.x & 63, w = rfl(threadIdx.x >> 6), c = l & 15, g = l >> 4;
  const rsrc_t ws = mk(p.ws, WS_BYTES);
  bool alive = true;

  u8 wv[8], wo[4][2];
  {
    const rsrc_t wr = mk(p.wv, 4u << 20);
    const unsigned voff = ((16u * (g & 1) + (g >> 1)) * 4096u + h * 128u + w * 16u + c) * 2u;
#pragma unroll
    for (int i = 0; i < 8; ++i) {
      const unsigned t = (w + i) & 7;
      wv[i] = cat(load_single(wr, voff, (64u * t) * 8192u, 16384u), load_single(wr, voff, (64u * t + 32u) * 8192u, 16384u));
    }
    u4 t[4][4];
    load_wo(p, h, w, c, g, t);
#pragma unroll
    for (int i = 0; i < 4; ++i) {
      wo[i][0] = cat(t[i][0], t[i][1]);
      wo[i][1] = cat(t[i][2], t[i][3]);
    }
  }
  u4 a0 = load_mem0(p, w, c, g);
  const unsigned cb = wait_init(p, ws, 8, alive);
  STAMP(15, 8 + w);

  const unsigned lb = lds_base(sm);
  u16v k0;
  k0[0] = (c & 1) ? 0xeeeeeeeeu : 0x44444444u;
  k0[1] = w * 1024u + l * 16u;
#pragma unroll
  for (int i = 0; i < 8; ++i) k0[2 + i] = lb + L_MEM + ((w + i) & 7) * 1024u + l * 16u;
  k0[10] = lb + L_V + (w * 16u + 4u * g + ((c >> 1) & 1)) * 16u + (2u * (c >> 2) + (c & 1)) * 2u;
  k0[11] = lb + L_V + l * 16u;
  const unsigned cg = 4u * w + g;
  k0[12] = cb + (B_M + cg) * 16384u + h * 256u + c * 16u;
  k0[13] = O_M2 + cg * 8192u + h * 256u + c * 16u;
  k0[14] = threadIdx.x == 448 ? F_M + h * FL : OOB;
  k0[15] = 0;

  unsigned s = 0, mem = rfl(cb + B_MEM * 16384u - 16384u), cap = rfl(alive ? SPIN_CAP : 0u), m2o = 0;
  u32v t0;
  u16v t1, t2;
  A_DIAG_DECL;
#define VM_ZERO(a) "v_mov_b32 v" #a ", 0\n"
#define VM_ZERO_M \
      VM_ZERO(192) VM_ZERO(193) VM_ZERO(194) VM_ZERO(195) VM_ZERO(196) VM_ZERO(197) VM_ZERO(198) VM_ZERO(199) \
      VM_ZERO(200) VM_ZERO(201) VM_ZERO(202) VM_ZERO(203) VM_ZERO(204) VM_ZERO(205) VM_ZERO(206) VM_ZERO(207)
  asm volatile(
      "s_mov_b32 s83, 0\n"
      VM_ZERO(180) VM_ZERO(184) VM_ZERO(185) VM_ZERO(186) VM_ZERO(187) VM_ZERO_M
      "ds_write_b128 v130, v[148:151]\n"
      "s_branch .Lhave%=\n"
      A_POLL_MEM
      ".Lhave%=:\n"
      A_STAMP("s[84:85]") A_CLK("s[70:71]")
      SMF("184:187", "148:151", "0:7", "v128")
      A_READ_MEM
      WL(6) SMF("184:187", "152:155", "8:15", "v128")
      WL(5) SMF("184:187", "156:159", "16:23", "v128")
      WL(4) SMF("184:187", "160:163", "24:31", "v128")
      WL(3) SMF("184:187", "164:167", "32:39", "v128")
      WL(2) SMF("184:187", "168:171", "40:47", "v128")
      WL(1) SMF("184:187", "172:175", "48:55", "v128")
      WL(0) SMF("184:187", "176:179", "56:63", "v128")
      A_CLK("s[74:75]")
      "s_nop 7\n"
      "v_add_f32 v152, v184, v185\n"
      "v_add_f32 v153, v186, v187\n"
      "v_cvt_pk_bf16_f32 v152, v152, v153\n"
      "ds_write_b16 v138, v152\n"
      "ds_write_b16_d16_hi v138, v152 offset:32\n"
      A_STAMP("s[86:87]") A_CLK("s[76:77]")
      VM_ZERO(184) VM_ZERO(185) VM_ZERO(186) VM_ZERO(187)
      "s_waitcnt lgkmcnt(0)\n"
      "s_barrier\n"
      A_CLK("s[78:79]")
      "ds_read_b128 v[152:155], v139\n"
      "ds_read_b128 v[156:159], v139 offset:1024\n"
      "buffer_store_dword v180, v142, %[ws], 0 offen sc1\n"
      WL(1) SMF("192:195", "152:155", "64:71", "v128") SMF("200:203", "152:155", "96:103", "v128")
      WL(0) SMF("192:195", "156:159", "72:79", "v128") SMF("200:203", "156:159", "104:111", "v128")
      SMF("196:199", "152:155", "80:87", "v128") SMF("204:207", "152:155", "112:119", "v128")
      SMF("196:199", "156:159", "88:95", "v128") SMF("204:207", "156:159", "120:127", "v128")
      A_CLK("s[92:93]")
      "s_nop 2\n"
      "v_add_f32 v192, v192, v193\n"
      "v_add_f32 v200, v200, v201\n"
      "v_add_f32 v194, v194, v195\n"
      "v_add_f32 v202, v202, v203\n"
      "v_cvt_pk_bf16_f32 v160, v192, v194\n"
      "v_cvt_pk_bf16_f32 v162, v200, v202\n"
      "v_bfi_b32 v160, -2, v160, s83\n"
      "v_bfi_b32 v162, -2, v162, s83\n"
      "v_add_f32 v196, v196, v197\n"
      "v_add_f32 v204, v204, v205\n"
      "v_add_f32 v198, v198, v199\n"
      "v_add_f32 v206, v206, v207\n"
      "v_cvt_pk_bf16_f32 v161, v196, v198\n"
      "v_cvt_pk_bf16_f32 v163, v204, v206\n"
      "v_bfi_b32 v161, -2, v161, s83\n"
      "v_bfi_b32 v163, -2, v163, s83\n"
      "v_permlane32_swap_b32 v160, v162\n"
      "s_add_u32 %[s], %[s], 1\n"
      "v_permlane32_swap_b32 v161, v163\n"
      "s_nop 1\n"
      "v_permlane16_swap_b32 v160, v161\n"
      "v_permlane16_swap_b32 v162, v163\n"
      "buffer_store_dwordx4 v[160:163], v140, %[ws], 0 offen sc1\n"
      A_STAMP("s[88:89]") A_CLK("s[94:95]") A_CLK("s[96:97]")
      "buffer_store_dwordx4 v[160:163], v141, %[ws], %[m2o] offen sc1\n"
      "v_mov_b32 v180, %[s]\n"
      VM_ZERO_M
      "s_xor_b32 s83, s83, 1\n"
      "s_add_u32 %[m2o], %[m2o], 0x40000\n"
      "s_add_u32 %[mem], %[mem], 0x4000\n"
      A_NEXT
      SLEEP(D_VM)
      "s_cmp_lt_u32 %[s], 64\n"
      "s_cbranch_scc1 .Lpoll%=\n"
      "s_waitcnt vmcnt(0)\n"
      "s_barrier\n"
      "buffer_store_dword v180, v142, %[ws], 0 offen sc1\n"
      ".Lexit%=:\n"
      : [s] "+s"(s), [mem] "+s"(mem), [cap] "+s"(cap), [m2o] "+s"(m2o), "+{v[148:151]}"(a0), "=&{v[152:183]}"(t0),
        "=&{v[184:199]}"(t1), "=&{v[200:215]}"(t2) A_DIAG_OUT
      : [ws] "s"(srd(p.ws, WS_BYTES)), [w] "s"(w), "{v[0:31]}"(cat(wv[0], wv[1], wv[2], wv[3])),
        "{v[32:63]}"(cat(wv[4], wv[5], wv[6], wv[7])), "{v[64:95]}"(cat(wo[0][0], wo[0][1], wo[1][0], wo[1][1])),
        "{v[96:127]}"(cat(wo[2][0], wo[2][1], wo[3][0], wo[3][1])), "{v[128:143]}"(k0)
      : A_SCLOB A_DIAG_CLOB);
}

DEV void role_v1(const Params& p, unsigned j, char* sm) {
  const unsigned l = threadIdx.x & 63, w = rfl(threadIdx.x >> 6), c = l & 15, g = l >> 4;
  const unsigned cg = j >> 1, rg = j & 1;
  const rsrc_t ws = mk(p.ws, WS_BYTES);
  bool alive = true;
  {
    const unsigned cb0 = crit_base(p);
    const u4 ones = {~0u, ~0u, ~0u, ~0u};
    for (unsigned o = (j * 512u + threadIdx.x) * 16u; o < B_END * 16384u; o += 64u * 512u * 16u) st16c(ones, ws, o, cb0);
    drain();
    unsigned sel = 1;
    if (j == 0 && w == 0) {
      unsigned best[2] = {~0u, ~0u};
      for (int k = 0; k < 8; ++k) {
        const unsigned off = B_PROBE * 16384u + (k & 1) * 8192u + (l & 7) * 16u + (k >> 1) * 1024u;
        const unsigned long long t0 = __builtin_amdgcn_s_memtime();
        asm volatile("s_waitcnt lgkmcnt(0)" ::: "memory");
        st16c(ones, ws, off, cb0);
        drain();
        const unsigned long long t1 = __builtin_amdgcn_s_memtime();
        asm volatile("s_waitcnt lgkmcnt(0)" ::: "memory");
        const unsigned dt = (unsigned)(t1 - t0);
        if (k & 1) best[1] = min(best[1], dt); else best[0] = min(best[0], dt);
      }
      sel = best[1] < best[0] ? 2u : 1u;
    }
    bar();
    if (threadIdx.x == 0) st4c(sel, ws, F_INIT + j * FL, 0);
  }
  if (w >= 4) return;
  const unsigned cb = wait_init(p, ws, 4, alive);
  bar();
  STAMP(15, 8 + w);

  const unsigned lb = lds_base(sm);
  const unsigned piece = rg * 4u + w;
  u16v k0 = {};
  k0[0] = (c & 4) ? 0xeeeeeeeeu : 0x44444444u;
  k0[1] = cb + (B_P + (piece >> 1)) * 16384u + (piece & 1) * 4096u + g * 128u + (c & 7) * 16u + 2048u;
  k0[2] = k0[1] - 2048u + (c >> 3) * 512u;
  k0[3] = cb + (B_M + cg) * 16384u + w * 2048u + l * 16u;
  k0[4] = O_U + (cg * 32u + 8u * w) * 1024u + l * 16u;
  k0[5] = k0[4] + 4096u;
  k0[6] = F_U + (8u * w + (l & 7)) * FL;
  k0[7] = lb + ((g * 2u + ((c >> 1) & 1)) * 8u + 2u * (c >> 2) + (c & 1)) * 16u + w * 4u;
  k0[8] = lb + l * 16u;
  k0[9] = cg * 256u + rg * 128u + (l >> 3) * 16u;

  unsigned s = 0, mem = rfl(cb + B_MEM * 16384u), cap = rfl(alive ? SPIN_CAP : 0u), uo = 0, seen = 0;
  u32v t0, t1;
  A_DIAG_DECL;
#define V1_TAGS(d, a, b, c, OP) "v_bitop3_b32 v" d ", v" a ", v" b ", v" c " bitop3:" OP "\n"
#define V1_ZERO "v_mov_b32 v52, 0\nv_mov_b32 v53, 0\nv_mov_b32 v54, 0\nv_mov_b32 v55, 0\n" \
                "v_mov_b32 v56, 0\nv_mov_b32 v57, 0\nv_mov_b32 v58, 0\nv_mov_b32 v59, 0\n"
#define V1_LOAD_P \
      "buffer_load_dwordx4 v[40:43], v66, %[ws], 0 offen sc1\n" \
      "buffer_load_dwordx4 v[44:47], v66, %[ws], 0 offen offset:1024 sc1\n" \
      "buffer_load_dwordx4 v[48:51], v65, %[ws], 0 offen sc1\n"
#define V1_STEP(P, OP, MWANT, PWANT, NEG) \
      ".Ltop" P "%=:\n" \
      "v_cmp_ge_u32 vcc, %[s], v80\n" \
      "s_cbranch_vccnz .Luw" P "%=\n" \
      "buffer_load_dwordx4 v[0:3], v68, %[ws], %[uo] offen sc1\n" \
      "buffer_load_dwordx4 v[4:7], v68, %[ws], %[uo] offen offset:1024 sc1\n" \
      "buffer_load_dwordx4 v[8:11], v68, %[ws], %[uo] offen offset:2048 sc1\n" \
      "buffer_load_dwordx4 v[12:15], v68, %[ws], %[uo] offen offset:3072 sc1\n" \
      "buffer_load_dwordx4 v[16:19], v69, %[ws], %[uo] offen sc1\n" \
      "buffer_load_dwordx4 v[20:23], v69, %[ws], %[uo] offen offset:1024 sc1\n" \
      "buffer_load_dwordx4 v[24:27], v69, %[ws], %[uo] offen offset:2048 sc1\n" \
      "buffer_load_dwordx4 v[28:31], v69, %[ws], %[uo] offen offset:3072 sc1\n" \
      "buffer_load_dword v80, v70, %[ws], 0 offen sc1\n" \
      "s_add_u32 %[uo], %[uo], 0x100000\n" \
      V1_ZERO \
      SLEEP(D_V1) \
      ".Lboth" P "%=:\n" \
      "buffer_load_dwordx4 v[32:35], v67, %[ws], 0 offen sc1\n" \
      "buffer_load_dwordx4 v[36:39], v67, %[ws], 0 offen offset:1024 sc1\n" \
      V1_LOAD_P \
      WV(3) \
      V1_TAGS("60", "32", "33", "34", OP) V1_TAGS("61", "35", "36", "37", OP) V1_TAGS("60", "60", "38", "39", OP) \
      V1_TAGS("60", "60", "61", "61", OP) \
      "v_and_b32 v60, 1, v60\n" \
      "v_cmp_ne_u32 s[80:81], " MWANT ", v60\n" \
      ".Lp" P "%=:\n" \
      WV(2) SMF("52:55", "40:43", "0:7", "v64") SMF("56:59", "40:43", "8:15", "v64") \
      V1_TAGS("60", "40", "41", "42", OP) \
      WV(1) SMF("52:55", "44:47", "16:23", "v64") SMF("56:59", "44:47", "24:31", "v64") \
      V1_TAGS("60", "60", "43", "44", OP) V1_TAGS("61", "45", "46", "47", OP) \
      WV(0) SMF("52:55", "48:51", "32:39", "v64") \
      V1_TAGS("60", "60", "48", "49", OP) V1_TAGS("61", "61", "50", "51", OP) V1_TAGS("60", "60", "61", "61", OP) \
      "v_and_b32 v60, s82, v60\n" \
      "v_cmp_ne_u32 vcc, " PWANT ", v60\n" \
      "s_or_b64 vcc, vcc, s[80:81]\n" \
      "s_cbranch_scc1 .Lre" P "%=\n" \
      A_STAMP("s[86:87]") A_CLK("s[70:71]") \
      "s_nop 0\n" \
      "v_permlane32_swap_b32 v56, v54\n" \
      "v_permlane32_swap_b32 v57, v55\n" \
      "v_add_f32 v60, v52, v54\n" \
      "v_add_f32 v62, v58, v56\n" \
      "v_add_f32 v61, v53, v55\n" \
      "v_add_f32 v63, v59, v57\n" \
      "v_cndmask_b32 v60, v60, v62, s[84:85]\n" \
      "v_cndmask_b32 v61, v61, v63, s[84:85]\n" \
      "s_cmp_lg_u32 %[w], 0\n" \
      "s_nop 0\n" \
      "v_permlane16_swap_b32 v60, v61\n" \
      "v_add_f32 v60, v60, v61\n" \
      "ds_write_b32 v71, v60 offset:" P "*1024\n" \
      A_CLK("s[72:73]") \
      "s_waitcnt lgkmcnt(0)\n" \
      "s_barrier\n" \
      "s_cbranch_scc1 .Lend" P "%=\n" \
      A_CLK("s[74:75]") \
      "ds_read_b128 v[52:55], v72 offset:" P "*1024\n" \
      "s_waitcnt lgkmcnt(0)\n" \
      A_CLK("s[76:77]") \
      "v_add_f32 v52, v52, v53\n" \
      "v_add_f32 v54, v54, v55\n" \
      "v_add_f32_e64 v52, " NEG "v52, " NEG "v54\n" \
      "s_nop 1\n" \
      "v_mov_b32_dpp v53, v52 quad_perm:[1,0,3,2]" DPP_ALL \
      "v_cvt_pk_bf16_f32 v56, v52, v53\n" \
      "s_nop 1\n" \
      "v_mov_b32_dpp v57, v56 row_shl:2" DPP_ALL \
      "v_mov_b32_dpp v58, v56 row_shl:4" DPP_ALL \
      "v_mov_b32_dpp v59, v56 row_shl:6" DPP_ALL \
      "s_mov_b64 exec, s[90:91]\n" \
      "buffer_store_dwordx4 v[56:59], v73, %[ws], %[mem] offen sc1\n" \
      "s_mov_b64 exec, -1\n" \
      A_STAMP("s[88:89]") A_CLK("s[78:79]") A_SAVE("s86", 1) A_SAVE("s88", 2) A_SAVECLK \
      ".Lend" P "%=:\n" \
      "s_barrier\n" \
      A_NEXT \
      "s_add_u32 %[mem], %[mem], 0x4000\n" \
      "s_add_u32 %[s], %[s], 1\n"
#define V1_SIDE(P) \
      ".Luw" P "%=:\n" \
      "buffer_load_dword v80, v70, %[ws], 0 offen sc1\n" \
      "s_sub_u32 %[cap], %[cap], 1\n" \
      "s_cbranch_scc1 .Lexit%=\n" \
      "s_waitcnt vmcnt(0)\n" \
      "s_branch .Ltop" P "%=\n" \
      ".Lre" P "%=:\n" \
      "s_sub_u32 %[cap], %[cap], 1\n" \
      "s_cbranch_scc1 .Lexit%=\n" \
      "s_cmp_lg_u64 s[80:81], 0\n" \
      V1_ZERO \
      "s_cbranch_scc1 .Lboth" P "%=\n" \
      V1_LOAD_P \
      "s_branch .Lp" P "%=\n"
  asm volatile(
      "s_mov_b32 s82, 0x80008000\n"
      "s_mov_b32 s84, 0\n"
      "s_mov_b32 s85, -1\n"
      "s_mov_b32 s90, 0x01010101\n"
      "s_mov_b32 s91, 0x01010101\n"
      V1_STEP("0", "0xfe", "0", "0", "")
      "s_cmp_ge_u32 %[s], 63\n"
      "s_cbranch_scc1 .Lexit%=\n"
      V1_STEP("1", "0x80", "1", "s82", "-")
      "s_branch .Ltop0%=\n"
      V1_SIDE("0") V1_SIDE("1")
      ".Lexit%=:\n"
      : [s] "+s"(s), [mem] "+s"(mem), [cap] "+s"(cap), [uo] "+s"(uo), "=&{v[0:31]}"(t0), "=&{v[32:63]}"(t1),
        "+{v80}"(seen) A_DIAG_OUT
      : [ws] "s"(srd(p.ws, WS_BYTES)), [w] "s"(w), "{v[64:79]}"(k0)
      : A_SCLOB A_DIAG_CLOB);
}

DEV void load_x(rsrc_t xr, unsigned w, unsigned c, unsigned g, int s, u4 (&xp)[4]) {
  const unsigned voff = ((16u * (w >> 2) + c) * 512u + 128u * (w & 3) + 8u * g) * 2u;
#pragma unroll
  for (int i = 0; i < 4; ++i) xp[i] = ld16(xr, voff + i * 64u, s * 32768u);
}
DEV void put_x(char* sm, unsigned base, unsigned w, unsigned l, const u4 (&xp)[4]) {
#pragma unroll
  for (int i = 0; i < 4; ++i) lds16(sm, base + (4u * w + i) * 1024u + l * 16u) = xp[i];
}

DEV void token_softmax(rsrc_t ws, char* sm, unsigned qb, const u4 (&km)[4], unsigned h, unsigned w, unsigned l, int s) {
  const unsigned c = l & 15, g = l >> 4;
  u4 q[4], k0[4], k1[4];
#pragma unroll
  for (int m = 0; m < 4; ++m) {
    q[m] = lds16(sm, qb + (w * 4u + m) * 1024u);
    k0[m] = lds16(sm, qb + 8192u + m * 1024u);
    k1[m] = lds16(sm, qb + 12288u + m * 1024u);
  }
  sched();
  f4 s0 = {0, 0, 0, 0}, s1 = {0, 0, 0, 0}, sm_ = {0, 0, 0, 0};
#pragma unroll
  for (int m = 0; m < 4; ++m) {
    s0 = mma(k0[m], q[m], s0);
    s1 = mma(k1[m], q[m], s1);
  }
#pragma unroll
  for (int m = 0; m < 4; ++m) sm_ = mma(km[m], q[m], sm_);
  const unsigned tq = 16u * w + c;
  const float ninf = -__builtin_inff();
#pragma unroll
  for (int r = 0; r < 4; ++r) {
    s0[r] = 4u * g + r <= tq ? s0[r] : ninf;
    s1[r] = 16u + 4u * g + r <= tq ? s1[r] : ninf;
  }
  float mx = fmaxf(fmaxf(fmaxf(s0[0], s0[1]), fmaxf(s0[2], s0[3])), fmaxf(fmaxf(s1[0], s1[1]), fmaxf(s1[2], s1[3])));
  mx = group_max(fmaxf(mx, fmaxf(sm_[0], sm_[1])));
  const float mc = -mx * SCALE_LOG2E;
  float e[10];
#pragma unroll
  for (int r = 0; r < 4; ++r) {
    e[r] = __builtin_amdgcn_exp2f(fmaf(s0[r], SCALE_LOG2E, mc));
    e[4 + r] = __builtin_amdgcn_exp2f(fmaf(s1[r], SCALE_LOG2E, mc));
  }
  e[8] = __builtin_amdgcn_exp2f(fmaf(sm_[0], SCALE_LOG2E, mc));
  e[9] = __builtin_amdgcn_exp2f(fmaf(sm_[1], SCALE_LOG2E, mc));
  const float sum = group_sum(((e[0] + e[1]) + (e[2] + e[3])) + ((e[4] + e[5]) + (e[6] + e[7])) + (e[8] + e[9]));
  const float rs = __builtin_amdgcn_rcpf(sum);
  const u4 px = {pk(e[0] * rs, e[1] * rs), pk(e[2] * rs, e[3] * rs), pk(e[4] * rs, e[5] * rs), pk(e[6] * rs, e[7] * rs)};
  st16c(px, ws, O_PX + (w * 32u + h) * 1024u + l * 16u, s * 65536u);
  const unsigned dm = pk(e[8] * rs, e[9] * rs);
  auto r16 = __builtin_amdgcn_permlane16_swap(dm, dm, false, false);
  auto ra = __builtin_amdgcn_permlane32_swap(r16[0], r16[0], false, false);
  auto rb = __builtin_amdgcn_permlane32_swap(r16[1], r16[1], false, false);
  const u4 pm = {ra[0], rb[0], ra[1], rb[1]};
  st16c(pm, ws, g == 0 ? O_PM + (w * 8u + (h >> 2)) * 1024u + ((h & 3) * 16u + c) * 16u : OOB, s * 16384u);
}

DEV void role_s2(const Params& p, unsigned h, char* sm) {
  constexpr int LAG = 4, R = LAG + 1;
  constexpr unsigned L_X = 0, L_QK = 65536;
  const unsigned l = threadIdx.x & 63, w = rfl(threadIdx.x >> 6), c = l & 15, g = l >> 4;
  const rsrc_t ws = mk(p.ws, WS_BYTES), xr = mk(p.bx, 2u << 20);
  bool alive = true;

  u4 xp[4];
  load_x(xr, w, c, g, 0, xp);
  u4 wa[16], wb[16];
  {
    const rsrc_t wr = mk(w < 4 ? p.wq : p.wk, 4u << 20);
    const unsigned voff = (8u * g * 4096u + h * 128u + (w & 3) * 32u + 2u * c) * 2u;
#pragma unroll
    for (int f = 0; f < 16; ++f) load_pair(wr, voff, 32u * f * 8192u, 8192u, wa[f], wb[f]);
  }
  put_x(sm, L_X, w, l, xp);
  load_x(xr, w, c, g, 1, xp);
  bar();
  STAMP(15, 8 + w);
  if (w < 4) __builtin_amdgcn_s_setprio(1);

  const unsigned km_off = w < 2 ? O_KM + h * 2048u + (g * 8u + ((2u * (c >> 2) + (c & 1)) ^ swz(g))) * 16u : OOB;
  const unsigned km_flag = w < 2 ? F_KM + h * FL : OOB;
  const unsigned px_flag = w < 2 && l == 0 ? F_PX + (w * 32u + h) * FL : OOB;
  const unsigned kx_flag = threadIdx.x == 448 ? F_KX + h * FL : OOB;
  const unsigned kx_off = w >= 4 ? O_KX + h * 8192u + (w & 3) * 2048u + l * 16u : OOB;
  unsigned km_seen = 0;
  int slot = 0;
  for (int it = 0; it < NSEG; ++it) {
    const int s = max(it - LAG, 0);
    const bool soft = it >= LAG && w < 2;
    if (soft && !all_lanes(km_seen > (unsigned)s)) wait_flags(ws, km_flag, s + 1, alive);
    if (w == 0 || w == 4) STAMP(3 + (w >> 2) * 4, it);
    u4 km[4];
#pragma unroll
    for (int m = 0; m < 4; ++m) km[m] = ld16c(ws, km_off + m * 512u, s * 65536u);
    km_seen = ld4c(ws, km_flag, 0);
    f4 acc[2][2] = {};
    const unsigned xb = L_X + (it & 1) * 32768u + l * 16u;
    u4 x[3][2];
#pragma unroll
    for (int k = 0; k < 2; ++k)
#pragma unroll
      for (int t = 0; t < 2; ++t) x[k][t] = lds16(sm, xb + (t * 16 + k) * 1024u);
#pragma unroll
    for (int ks = 0; ks < 16; ++ks) {
      sched();
      if (ks + 2 < 16) {
#pragma unroll
        for (int t = 0; t < 2; ++t) x[(ks + 2) % 3][t] = lds16(sm, xb + (t * 16 + ks + 2) * 1024u);
      }
      sched();
#pragma unroll
      for (int t = 0; t < 2; ++t) {
        acc[0][t] = mma(wa[ks], x[ks % 3][t], acc[0][t]);
        acc[1][t] = mma(wb[ks], x[ks % 3][t], acc[1][t]);
      }
    }
    sched();
    asm volatile("s_waitcnt vmcnt(6)" ::: "memory");
    if (w == 0 || w == 4) STAMP(4 + (w >> 2) * 4, it);
    st4c(s, ws, px_flag, 0);
    put_x(sm, L_X + ((it + 1) & 1) * 32768u, w, l, xp);
#pragma unroll
    for (int t = 0; t < 2; ++t) {
      const u4 o = {pk(acc[0][t][0], acc[1][t][0]), pk(acc[0][t][1], acc[1][t][1]), pk(acc[0][t][2], acc[1][t][2]),
                    pk(acc[0][t][3], acc[1][t][3])};
      lds16(sm, L_QK + slot * 16384u + (w >> 2) * 8192u + (t * 4u + (w & 3)) * 1024u + l * 16u) = o;
      st16c(o, ws, kx_off + t * 1024u, it * 262144u);
    }
    load_x(xr, w, c, g, it + 2, xp);
    if (w == 0 || w == 4) STAMP(5 + (w >> 2) * 4, it);
    slot = slot == R - 1 ? 0 : slot + 1;
    if (soft) {
      token_softmax(ws, sm, L_QK + slot * 16384u + l * 16u, km, h, w, l, s);
      if (w == 0) STAMP(2, s);
    }
    if (it < LAG) drain();
    bar();
    st4c(it < LAG ? it + 1 : it, ws, kx_flag, 0);
  }
  if (w >= 2) return;
  for (int s = NSEG - LAG; s < NSEG; ++s) {
    wait_flags(ws, km_flag, s + 1, alive);
    u4 km[4];
#pragma unroll
    for (int m = 0; m < 4; ++m) km[m] = ld16c(ws, km_off + m * 512u, s * 65536u);
    st4c(s, ws, px_flag, 0);
    slot = slot == R - 1 ? 0 : slot + 1;
    token_softmax(ws, sm, L_QK + slot * 16384u + l * 16u, km, h, w, l, s);
    if (w == 0) STAMP(2, s);
  }
  drain();
  st4c(NSEG, ws, px_flag, 0);
}

DEV void role_vu(const Params& p, unsigned h, char* sm) {
  constexpr unsigned L_X = 0, L_V = 65536;
  constexpr int EAGER = 3;
  const unsigned l = threadIdx.x & 63, w = rfl(threadIdx.x >> 6), c = l & 15, g = l >> 4;
  const rsrc_t ws = mk(p.ws, WS_BYTES), xr = mk(p.bx, 2u << 20);

  u4 xp[4];
  load_x(xr, w, c, g, 0, xp);
  u4 wv[16], wo[4][4];
  {
    const rsrc_t wr = mk(p.wv, 4u << 20);
    const unsigned voff = (8u * g * 4096u + h * 128u + w * 16u + c) * 2u;
#pragma unroll
    for (int f = 0; f < 16; ++f) wv[f] = load_single(wr, voff, 32u * f * 8192u, 8192u);
    load_wo(p, h, w, c, g, wo);
  }
  put_x(sm, L_X, w, l, xp);
  load_x(xr, w, c, g, 1, xp);
  bar();
  STAMP(15, 8 + w);

  const unsigned v_off = L_V + ((w >> 1) * 64u + (2u * (w & 1) + (g >> 1)) * 16u + c) * 16u + (g & 1) * 8u;
  const unsigned flag = threadIdx.x == 448 ? F_U + h * FL : OOB;
  for (int s = 0; s < NSEG; ++s) {
    f4 acc[2] = {};
    const unsigned xb = L_X + (s & 1) * 32768u + l * 16u;
    u4 x[4][2];
#pragma unroll
    for (int k = 0; k < 3; ++k)
#pragma unroll
      for (int t = 0; t < 2; ++t) x[k][t] = lds16(sm, xb + (t * 16 + k) * 1024u);
#pragma unroll
    for (int ks = 0; ks < 16; ++ks) {
      sched();
      if (ks + 3 < 16) {
#pragma unroll
        for (int t = 0; t < 2; ++t) x[(ks + 3) & 3][t] = lds16(sm, xb + (t * 16 + ks + 3) * 1024u);
      }
      sched();
#pragma unroll
      for (int t = 0; t < 2; ++t) acc[t] = mma(wv[ks], x[ks & 3][t], acc[t]);
    }
    sched();
#pragma unroll
    for (int t = 0; t < 2; ++t)
      *reinterpret_cast<u2*>(sm + v_off + (s & 1) * 8192u + t * 4096u) = u2{pk(acc[t][0], acc[t][1]), pk(acc[t][2], acc[t][3])};
    if (w == 0) STAMP(3, s);
    asm volatile("s_waitcnt vmcnt(5)" ::: "memory");
    if (w == 0) STAMP(4, s);
    put_x(sm, L_X + ((s + 1) & 1) * 32768u, w, l, xp);
    load_x(xr, w, c, g, s + 2, xp);
    bar();
    st4c(max(s - 1, min(s, EAGER)), ws, flag, 0);
    if (w == 0) STAMP(5, s);
    u4 v[8];
#pragma unroll
    for (int k = 0; k < 8; ++k) v[k] = lds16(sm, L_V + (s & 1) * 8192u + ((k & 1) * 4 + (k >> 1)) * 1024u + l * 16u);
    sched();
    f4 u[4][2] = {};
#pragma unroll
    for (int k = 0; k < 8; ++k)
#pragma unroll
      for (int i = 0; i < 4; ++i) u[i][k & 1] = mma(v[k], wo[i][k >> 1], u[i][k & 1]);
#pragma unroll
    for (int i = 0; i < 4; ++i) {
      const u4 o = {pk(u[i][0][0], u[i][0][1]), pk(u[i][0][2], u[i][0][3]), pk(u[i][1][0], u[i][1][1]),
                    pk(u[i][1][2], u[i][1][3])};
      st16c(o, ws, O_U + ((4u * w + i) * 32u + h) * 1024u + l * 16u, s * 1048576u);
    }
    if (s < EAGER || s == NSEG - 1) {
      drain();
      bar();
      st4c(s + 1, ws, flag, 0);
    }
    if (w == 0) STAMP(2, s);
  }
}

DEV void role_v2(const Params& p, unsigned j, char* sm) {
  constexpr unsigned ROW = 144, PART = 16 * ROW, BUF = 8 * PART;
  const unsigned l = threadIdx.x & 63, w = rfl(threadIdx.x >> 6), c = l & 15, g = l >> 4;
  const unsigned b = j >> 1, qt = j & 1;
  const rsrc_t ws = mk(p.ws, WS_BYTES), outr = mk(p.out, 8u << 20);
  bool alive = true;

  const unsigned hq = 4u * w + (l & 3);
  const unsigned uf_off = F_U + hq * FL;
  const unsigned pf_off = (l & 4) ? F_M + hq * FL : F_PX + (qt * 32u + hq) * FL;
  const unsigned u_off = O_U + (2u * b * 32u + 4u * w) * 1024u + l * 16u;
  const unsigned m_off = O_M2 + 2u * b * 8192u + w * 1024u + l * 16u;
  const unsigned px_off = O_PX + (qt * 32u + 4u * w) * 1024u + l * 16u;
  const unsigned pm_off = O_PM + (qt * 8u + w) * 1024u + l * 16u;
  const unsigned out_off = ((16u * qt + (threadIdx.x >> 5)) * 1024u + 512u + 32u * b + (threadIdx.x & 31)) * 4u;
  unsigned u_seen = wait_flags(ws, uf_off, 1u, alive), p_seen = 0;
  u4 ua[2][4];
#pragma unroll
  for (int k = 0; k < 8; ++k) ua[k >> 2][k & 3] = ld16c(ws, u_off + (k >> 2) * 32768u + (k & 3) * 1024u, 0);

  for (int s = 0; s < NSEG; ++s) {
    if (!all_lanes(p_seen > (unsigned)s)) p_seen = wait_flags(ws, pf_off, s + 1, alive);
    u4 px[4];
#pragma unroll
    for (int i = 0; i < 4; ++i) px[i] = ld16c(ws, px_off + i * 1024u, s * 65536u);
    const u4 pm = ld16c(ws, pm_off, s * 16384u);
    const u4 m0 = ld16c(ws, m_off, s * 262144u), m1 = ld16c(ws, m_off + 8192u, s * 262144u);
    f4 a0 = {0, 0, 0, 0}, a1 = {0, 0, 0, 0};
#pragma unroll
    for (int i = 0; i < 4; ++i) {
      a0 = mma(ua[0][i], px[i], a0);
      a1 = mma(ua[1][i], px[i], a1);
    }
    a0 = mma(m0, pm, a0);
    a1 = mma(m1, pm, a1);
    sched();
    if (s + 1 < NSEG) {
      if (!all_lanes(u_seen > (unsigned)s + 1u)) u_seen = wait_flags(ws, uf_off, s + 2, alive);
#pragma unroll
      for (int k = 0; k < 8; ++k)
        ua[k >> 2][k & 3] = ld16c(ws, u_off + (k >> 2) * 32768u + (k & 3) * 1024u, (s + 1) * 1048576u);
    }
    sched();
    const unsigned rb = (s & 1) * BUF;
    *reinterpret_cast<f4*>(sm + rb + w * PART + c * ROW + g * 32u) = f4{a0[0], a1[0], a0[1], a1[1]};
    *reinterpret_cast<f4*>(sm + rb + w * PART + c * ROW + g * 32u + 16u) = f4{a0[2], a1[2], a0[3], a1[3]};
    bar();
    float v = 0.f;
#pragma unroll
    for (int k = 0; k < 8; ++k)
      v += *reinterpret_cast<const float*>(sm + rb + k * PART + (threadIdx.x >> 5) * ROW + (threadIdx.x & 31) * 4u);
    __builtin_amdgcn_raw_buffer_store_b32(__float_as_uint(rbf(v)), outr, out_off, s * 131072u, 0);
    if (w == 0) STAMP(2, s);
  }
}

DEV f2 cmul(f2 a, f2 b) {
  float re, im;
  asm("v_mul_f32 %0, %3, %5\n\tv_mul_f32 %1, %3, %4\n\tv_fma_f32 %0, %2, %4, -%0\n\tv_fma_f32 %1, %2, %5, %1"
      : "=&v"(re), "=&v"(im) : "v"(a[0]), "v"(a[1]), "v"(b[0]), "v"(b[1]));
  return f2{re, im};
}
DEV f2 cfma(f2 a, f2 b, f2 c) {
  float re, im;
  asm("v_fma_f32 %0, -%3, %5, %6\n\tv_fma_f32 %1, %3, %4, %7\n\tv_fma_f32 %0, %2, %4, %0\n\tv_fma_f32 %1, %2, %5, %1"
      : "=&v"(re), "=&v"(im) : "v"(a[0]), "v"(a[1]), "v"(b[0]), "v"(b[1]), "v"(c[0]), "v"(c[1]));
  return f2{re, im};
}
DEV f2 from_group(float re, float im, unsigned c, int k) {
  return f2{__shfl(re, (int)c + 16 * k, 64), __shfl(im, (int)c + 16 * k, 64)};
}

DEV void role_a(const Params& p, unsigned ci, char* sm) {
  constexpr unsigned GS = 272, KS = 4 * GS, TS = 32 * KS + 32, L_TAB = 4 * TS;
  const unsigned tid = threadIdx.x, l = tid & 63, w = rfl(tid >> 6), c = l & 15, g = l >> 4;
  const rsrc_t ws = mk(p.ws, WS_BYTES), xr = mk(p.ax, 2u << 20), outr = mk(p.out, 8u << 20);
  bool alive = true;

  {
    const unsigned voff = ((64u * ci + 16u * (c >> 2) + 4u * (w >> 1) + (c & 3)) * 512u + 256u * (w & 1) + 8u * g) * 2u;
    u4 xp[8];
#pragma unroll
    for (int i = 0; i < 8; ++i) xp[i] = ld16(xr, voff + i * 64u, 0);
#pragma unroll
    for (int i = 0; i < 8; ++i) lds16(sm, (8u * w + i) * 1024u + l * 16u) = xp[i];
  }
  f2 lam64;
  {
    const float lr = bf2f(((const unsigned short*)p.lre)[tid]), li = bf2f(((const unsigned short*)p.lim)[tid]);
    const float dt = rbf(expf(bf2f(((const unsigned short*)p.ldt)[tid])));
    const float er = expf(dt * lr);
    float sn, cs;
    sincosf(dt * li, &sn, &cs);
    f2 lam = {er * cs, er * sn};
    asm volatile("s_nop 1" : "+v"(lam));
    const float nr = lam[0] - 1.0f, ni = lam[1];
    f2 coef;
    if (fabsf(lr) >= fabsf(li)) {
      const float t = li / lr, d = 1.0f / (lr + li * t);
      coef = f2{smul(nr + smul(ni, t), d), smul(ni - smul(nr, t), d)};
    } else {
      const float t = lr / li, d = 1.0f / (lr * t + li);
      coef = f2{smul(smul(nr, t) + ni, d), smul(smul(ni, t) - nr, d)};
    }
    f2 l16 = cmul(lam, lam);
    l16 = cmul(l16, l16);
    l16 = cmul(l16, l16);
    l16 = cmul(l16, l16);
    lam64 = cmul(l16, l16);
    lam64 = cmul(lam64, lam64);
    *reinterpret_cast<f4*>(sm + L_TAB + tid * 32u) = f4{lam[0], lam[1], coef[0], coef[1]};
    *reinterpret_cast<f2*>(sm + L_TAB + tid * 32u + 16u) = l16;
  }
  bar();

  f4 hr[4][4] = {}, hi[4][4] = {};
  {
    const rsrc_t br = mk(p.bre, 1u << 19), bi = mk(p.bim, 1u << 19);
    const unsigned voff = ((64u * w + c) * 512u + 8u * g) * 2u;
    u4 fr[4], fi[4];
#pragma unroll
    for (int k = 0; k < 4; ++k) {
      fr[k] = ld16(br, voff + k * 64u, 0);
      fi[k] = ld16(bi, voff + k * 64u, 0);
    }
#pragma unroll
    for (int n = 0; n < 64; ++n) {
      const int ct = n >> 4, ks = n & 15;
      const u4 r = fr[n & 3], i = fi[n & 3];
      fence();
      if (n + 4 < 64) {
        fr[n & 3] = ld16(br, voff + ((n + 4) & 15) * 64u, ((n + 4) >> 4) * 16384u);
        fi[n & 3] = ld16(bi, voff + ((n + 4) & 15) * 64u, ((n + 4) >> 4) * 16384u);
      }
#pragma unroll
      for (int t = 0; t < 4; ++t) {
        const u4 x = lds16(sm, (t * 16 + ks) * 1024u + l * 16u);
        hr[ct][t] = mma(x, r, hr[ct][t]);
        hi[ct][t] = mma(x, i, hi[ct][t]);
      }
    }
  }
  __builtin_amdgcn_sched_barrier(0);
  asm volatile("s_nop 7\n\ts_nop 7" ::: "memory");
  __builtin_amdgcn_sched_barrier(0);
#pragma unroll
  for (int ct = 0; ct < 4; ++ct) {
    const unsigned ch = 64u * w + 16u * ct + c;
    const f4 tb = *reinterpret_cast<const f4*>(sm + L_TAB + ch * 32u);
    const f2 l16 = *reinterpret_cast<const f2*>(sm + L_TAB + ch * 32u + 16u);
    const f2 lam = {tb[0], tb[1]}, coef = {tb[2], tb[3]};
    f2 hh = {0.f, 0.f};
#pragma unroll
    for (int i = 0; i < 16; ++i) {
      hh = cfma(lam, hh, cmul(coef, f2{hr[ct][i >> 2][i & 3], hi[ct][i >> 2][i & 3]}));
      hr[ct][i >> 2][i & 3] = hh[0];
      hi[ct][i >> 2][i & 3] = hh[1];
    }
    f2 e = from_group(hh[0], hh[1], c, 0);
#pragma unroll
    for (int k = 1; k < 4; ++k) e = cfma(l16, e, from_group(hh[0], hh[1], c, k));
    if (g == 0) st8c(__builtin_bit_cast(u2, e), ws, O_AE + ci * 4096u + ch * 8u, 0);
  }
  drain();
  bar();
  if (tid == 0) st4c(1u, ws, F_A + ci * FL, 0);
  wait_flags(ws, F_A + (l & 31) * FL, (l & 31) < ci ? 1u : 0u, alive);
  {
    f2 hin = {0.f, 0.f};
#pragma unroll 8
    for (unsigned k = 0; k < ci; ++k) hin = cfma(lam64, hin, __builtin_bit_cast(f2, ld8c(ws, O_AE + tid * 8u, k * 4096u)));
    *reinterpret_cast<f2*>(sm + L_TAB + tid * 32u + 24u) = hin;
  }
  bar();
#pragma unroll
  for (int ct = 0; ct < 4; ++ct) {
    const unsigned ch = 64u * w + 16u * ct + c;
    const f4 t0 = *reinterpret_cast<const f4*>(sm + L_TAB + ch * 32u);
    const f4 t1 = *reinterpret_cast<const f4*>(sm + L_TAB + ch * 32u + 16u);
    const f2 lam = {t0[0], t0[1]}, l16 = {t1[0], t1[1]};
    f2 z = {t1[2], t1[3]};
#pragma unroll
    for (int k = 0; k < 3; ++k) {
      const f2 n = cfma(l16, z, from_group(hr[ct][3][3], hi[ct][3][3], c, k));
      z = (unsigned)k < g ? n : z;
    }
    const unsigned hb = g * TS + (ch >> 5) * KS + ((ch >> 3) & 3) * GS + (ch & 7) * 2u;
#pragma unroll
    for (int i = 0; i < 16; ++i) {
      z = cmul(lam, z);
      const unsigned d = pk(2.0f * (hr[ct][i >> 2][i & 3] + z[0]), -2.0f * (hi[ct][i >> 2][i & 3] + z[1]));
      lds_put2(sm, hb + i * 16u, d);
      lds_put2(sm, hb + 16u * KS + i * 16u, d >> 16);
    }
  }
  bar();
  f4 y[4][4] = {};
  {
    const rsrc_t cr = mk(p.cre, 1u << 19), cm = mk(p.cim, 1u << 19);
    const unsigned voff = ((64u * w + c) * 512u + 8u * g) * 2u;
    u4 fa[2][4];
#pragma unroll
    for (int k = 0; k < 2; ++k)
#pragma unroll
      for (int d = 0; d < 4; ++d) fa[k][d] = ld16(cr, voff + k * 64u, d * 16384u);
#pragma unroll
    for (int ks = 0; ks < 32; ++ks) {
      u4 a[4];
      fence();
#pragma unroll
      for (int d = 0; d < 4; ++d) {
        a[d] = fa[ks & 1][d];
        if (ks + 2 < 32) fa[ks & 1][d] = ld16(ks + 2 < 16 ? cr : cm, voff + ((ks + 2) & 15) * 64u, d * 16384u);
      }
#pragma unroll
      for (int t = 0; t < 4; ++t) {
        const u4 hb = lds16(sm, t * TS + ks * KS + g * GS + c * 16u);
#pragma unroll
        for (int d = 0; d < 4; ++d) y[d][t] = mma(a[d], hb, y[d][t]);
      }
    }
  }
  const rsrc_t dr = mk(p.dsk, 1024);
#pragma unroll
  for (int d = 0; d < 4; ++d) {
    const unsigned col = 64u * w + 16u * d + 4u * g;
    const u2 dk = ld8(dr, col * 2u, 0);
#pragma unroll
    for (int t = 0; t < 4; ++t) {
      const unsigned row = 64u * ci + 16u * t + c;
      const u2 xv = ld8(xr, (row * 512u + col) * 2u, 0);
      const f4 o = {y[d][t][0] + rbf(smul(bf2f(xv[0] & 0xffffu), bf2f(dk[0] & 0xffffu))),
                    y[d][t][1] + rbf(smul(bf2f(xv[0] >> 16), bf2f(dk[0] >> 16))),
                    y[d][t][2] + rbf(smul(bf2f(xv[1] & 0xffffu), bf2f(dk[1] & 0xffffu))),
                    y[d][t][3] + rbf(smul(bf2f(xv[1] >> 16), bf2f(dk[1] >> 16)))};
      __builtin_amdgcn_raw_buffer_store_b128(__builtin_bit_cast(u4, o), outr, (row * 1024u + col) * 4u, 0, 0);
    }
  }
  if (w == 0) STAMP(2, 0);
}

__global__ void __launch_bounds__(512, 2) fused_kernel(Params p) {
  extern __shared__ __attribute__((aligned(16))) char smem[];
  const unsigned x = blockIdx.x & 7, r = blockIdx.x >> 3;
  if (threadIdx.x < 64) TSTAMP(0);
  if (x == 1 || x == 2) {
    role_v1(p, (x - 1) * 32u + r, smem);
  } else if (x < 4) {
    const unsigned h = (x ? 16u : 0u) + (r >> 1);
    if (r & 1) role_vm(p, h, smem); else role_s1(p, h, smem);
  } else {
    const unsigned i = (x - 4) * 8u + (r >> 2);
    switch (r & 3) {
      case 0: role_s2(p, i, smem); break;
      case 1: role_vu(p, i, smem); break;
      case 2: role_v2(p, i, smem); break;
      case 3: role_a(p, i, smem); break;
    }
  }
  if (threadIdx.x < 64) TSTAMP(2);
}

__global__ void zero_kernel(unsigned* w) { if (threadIdx.x < 288) w[threadIdx.x * 32] = 0; }
extern "C" void kernel_launch(void* const* d_in, const int* in_sizes, int n_in, void* d_out, int out_size, void* d_ws,
                              size_t ws_size, hipStream_t stream) {
  Params p;
  p.ax = d_in[0]; p.lre = d_in[1]; p.lim = d_in[2]; p.bre = d_in[3]; p.bim = d_in[4]; p.cre = d_in[5];
  p.cim = d_in[6]; p.ldt = d_in[7]; p.dsk = d_in[8]; p.bx = d_in[9]; p.mem0 = d_in[10]; p.wq = d_in[11];
  p.wk = d_in[12]; p.wv = d_in[13]; p.wo = d_in[14];
  p.out = (float*)d_out;
  p.ws = (char*)d_ws;
  (void)hipFuncSetAttribute((const void*)fused_kernel, hipFuncAttributeMaxDynamicSharedMemorySize, DYN_LDS);
  zero_kernel<<<1, 320, 0, stream>>>((unsigned*)d_ws);
  fused_kernel<<<256, 512, DYN_LDS, stream>>>(p);
}
```

```cpp
#define TIMER
#include <hip/hip_runtime.h>
#include <hip/hip_bf16.h>
#include <stdint.h>

typedef __attribute__((ext_vector_type(2))) unsigned u2;
typedef __attribute__((ext_vector_type(4))) unsigned u4;
typedef __attribute__((ext_vector_type(8))) unsigned u8;
typedef __attribute__((ext_vector_type(16))) unsigned u16v;
typedef __attribute__((ext_vector_type(32))) unsigned u32v;
typedef __attribute__((ext_vector_type(2))) float f2;
typedef __attribute__((ext_vector_type(4))) float f4;
typedef __attribute__((ext_vector_type(2))) __bf16 bf2;
typedef __attribute__((ext_vector_type(8))) __bf16 bf8;
typedef __attribute__((ext_vector_type(16))) __bf16 bf16v;
typedef __amdgpu_buffer_rsrc_t rsrc_t;
#define DEV __device__ __forceinline__

struct Params {
  const void *ax, *lre, *lim, *bre, *bim, *cre, *cim, *ldt, *dsk, *bx, *mem0, *wq, *wk, *wv, *wo;
  float* out;
  char* ws;
};

#define WSLEEP 4
constexpr int NSEG = 64;
constexpr unsigned SPIN_CAP = 400000u;
constexpr unsigned DYN_LDS = 156u * 1024u;

constexpr unsigned FL = 128, F_INIT = 0, F_KM = 64 * FL, F_KX = 96 * FL, F_PX = 128 * FL, F_M = 192 * FL, F_U = 224 * FL,
                   F_A = 256 * FL, FLAG_BYTES = 288 * FL;
constexpr unsigned O_AE = 5u << 20, O_DIAG = 6u << 20;
constexpr unsigned O_KX = 8u << 20, O_KM = 24u << 20, O_PX = 28u << 20, O_PM = 32u << 20, O_M2 = 34u << 20,
                   O_U = 50u << 20, WS_BYTES = 114u << 20;
constexpr unsigned OOB = 1u << 30;
constexpr unsigned B_MEM = 0, B_P = 63, B_M = 67, B_END = 99, B_PROBE = 100;

DEV rsrc_t mk(const void* p, unsigned bytes) {
  return __builtin_amdgcn_make_buffer_rsrc((void*)p, 0, bytes, 0x00020000);
}
DEV unsigned rfl(unsigned v) { return __builtin_amdgcn_readfirstlane(v); }
DEV u4 ld16(rsrc_t r, unsigned v, unsigned s) { return __builtin_amdgcn_raw_buffer_load_b128(r, v, s, 0); }
DEV u2 ld8(rsrc_t r, unsigned v, unsigned s) { return __builtin_amdgcn_raw_buffer_load_b64(r, v, s, 0); }
DEV unsigned ld4(rsrc_t r, unsigned v, unsigned s) { return __builtin_amdgcn_raw_buffer_load_b32(r, v, s, 0); }
DEV unsigned ld2(rsrc_t r, unsigned v, unsigned s) { return __builtin_amdgcn_raw_buffer_load_b16(r, v, s, 0); }
DEV u4 ld16c(rsrc_t r, unsigned v, unsigned s) { return __builtin_amdgcn_raw_buffer_load_b128(r, v, s, 16); }
DEV u2 ld8c(rsrc_t r, unsigned v, unsigned s) { return __builtin_amdgcn_raw_buffer_load_b64(r, v, s, 16); }
DEV unsigned ld4c(rsrc_t r, unsigned v, unsigned s) { return __builtin_amdgcn_raw_buffer_load_b32(r, v, s, 16); }
DEV void st16c(u4 d, rsrc_t r, unsigned v, unsigned s) { __builtin_amdgcn_raw_buffer_store_b128(d, r, v + s, 0, 16); }
DEV void st8c(u2 d, rsrc_t r, unsigned v, unsigned s) { __builtin_amdgcn_raw_buffer_store_b64(d, r, v, s, 16); }
DEV void st4c(unsigned d, rsrc_t r, unsigned v, unsigned s) { __builtin_amdgcn_raw_buffer_store_b32(d, r, v, s, 16); }
DEV void fence() { asm volatile("" ::: "memory"); }
DEV void drain() { asm volatile("s_waitcnt vmcnt(0)" ::: "memory"); }
DEV void bar() { asm volatile("s_waitcnt lgkmcnt(0)\n\ts_barrier" ::: "memory"); }
DEV bool all_lanes(bool ok) { return __builtin_amdgcn_ballot_w64(!ok) == 0ull; }

DEV float bf2f(unsigned h) { return __uint_as_float(h << 16); }
DEV unsigned pk(float a, float b) {
  f2 v = {a, b};
  return __builtin_bit_cast(unsigned, __builtin_convertvector(v, bf2));
}
DEV float rbf(float a) { return bf2f(pk(a, a) & 0xffffu); }
DEV float smul(float a, float b) {
  float r;
  asm("v_mul_f32 %0, %1, %2" : "=v"(r) : "v"(a), "v"(b));
  return r;
}
DEV float sadd(float a, float b) {
  float r;
  asm("v_add_f32 %0, %1, %2" : "=v"(r) : "v"(a), "v"(b));
  return r;
}
DEV f4 mma(u4 a, u4 b, f4 c) {
  return __builtin_amdgcn_mfma_f32_16x16x32_bf16(__builtin_bit_cast(bf8, a), __builtin_bit_cast(bf8, b), c, 0, 0, 0);
}
DEV f4 smma(u4 a, u8 b, f4 c, unsigned idx) {
  return __builtin_amdgcn_smfmac_f32_16x16x64_bf16(__builtin_bit_cast(bf8, a), __builtin_bit_cast(bf16v, b), c,
                                                   (int)idx, 0, 0);
}
DEV float swap32_add(float a, float b) {
  auto r = __builtin_amdgcn_permlane32_swap(__float_as_uint(a), __float_as_uint(b), false, false);
  return __uint_as_float(r[0]) + __uint_as_float(r[1]);
}
DEV float swap16_add(float a, float b) {
  auto r = __builtin_amdgcn_permlane16_swap(__float_as_uint(a), __float_as_uint(b), false, false);
  return __uint_as_float(r[0]) + __uint_as_float(r[1]);
}
DEV float group_max(float m) {
  auto r = __builtin_amdgcn_permlane16_swap(__float_as_uint(m), __float_as_uint(m), false, false);
  m = fmaxf(__uint_as_float(r[0]), __uint_as_float(r[1]));
  auto q = __builtin_amdgcn_permlane32_swap(__float_as_uint(m), __float_as_uint(m), false, false);
  return fmaxf(__uint_as_float(q[0]), __uint_as_float(q[1]));
}
DEV float group_sum(float m) {
  const float t = swap16_add(m, m);
  return swap32_add(t, t);
}
DEV u4& lds16(char* sm, unsigned off) { return *reinterpret_cast<u4*>(sm + off); }
DEV void lds_put2(char* sm, unsigned off, unsigned v) { *reinterpret_cast<unsigned short*>(sm + off) = (unsigned short)v; }

#define STAMP(slot, idx) do { } while (0)

#define TSTAMP(idx) do { \
  __builtin_amdgcn_raw_buffer_store_b32((unsigned)__builtin_amdgcn_s_memrealtime(), mk(p.ws, WS_BYTES), O_DIAG + (blockIdx.x * 1024u + 14u * 64u + (idx)) * 4u, 0, 0); } while (0)

DEV unsigned wait_flags(rsrc_t ws, unsigned off, unsigned need, bool& alive) {
  unsigned v = 0;
  for (unsigned it = 0; alive; ++it) {
    fence();
    v = ld4c(ws, off, 0);
    if (all_lanes(v >= need)) break;
    if (it > SPIN_CAP) alive = false;
    __builtin_amdgcn_s_sleep(WSLEEP);
  }
  fence();
  return v;
}
DEV unsigned crit_base(const Params& p) {
  const unsigned lo = (unsigned)(uintptr_t)p.ws;
  return ((lo + 65536u + 0x1fffffu) & ~0x1fffffu) - lo;
}
DEV unsigned wait_init(const Params& p, rsrc_t ws, unsigned n, bool& alive) {
  const unsigned l = threadIdx.x & 63, w = threadIdx.x >> 6;
  const unsigned v = wait_flags(ws, F_INIT + (l ? (l & (64u / n - 1u)) + w * (64u / n) : 0u) * FL, 1u, alive);
  return crit_base(p) + (rfl(v) == 2u ? 8192u : 0u);
}

DEV void load_pair(rsrc_t r, unsigned voff, unsigned soff, unsigned rstride, u4& a, u4& b) {
  unsigned d[8];
#pragma unroll
  for (int j = 0; j < 8; ++j) d[j] = ld4(r, voff, soff + j * rstride);
#pragma unroll
  for (int j = 0; j < 4; ++j) {
    a[j] = __builtin_amdgcn_perm(d[2 * j + 1], d[2 * j], 0x05040100u);
    b[j] = __builtin_amdgcn_perm(d[2 * j + 1], d[2 * j], 0x07060302u);
  }
}
DEV u4 load_single(rsrc_t r, unsigned voff, unsigned soff, unsigned rstride) {
  unsigned d[8];
  u4 a;
#pragma unroll
  for (int j = 0; j < 8; ++j) d[j] = ld2(r, voff, soff + j * rstride);
#pragma unroll
  for (int j = 0; j < 4; ++j) a[j] = __builtin_amdgcn_perm(d[2 * j + 1], d[2 * j], 0x05040100u);
  return a;
}
DEV u8 cat(u4 a, u4 b) { return __builtin_shufflevector(a, b, 0, 1, 2, 3, 4, 5, 6, 7); }
DEV u16v cat(u8 a, u8 b) { return __builtin_shufflevector(a, b, 0, 1, 2, 3, 4, 5, 6, 7, 8, 9, 10, 11, 12, 13, 14, 15); }
DEV u32v cat(u8 a, u8 b, u8 c, u8 d) {
  return __builtin_shufflevector(cat(a, b), cat(c, d), 0, 1, 2, 3, 4, 5, 6, 7, 8, 9, 10, 11, 12, 13, 14, 15, 16, 17, 18,
                                 19, 20, 21, 22, 23, 24, 25, 26, 27, 28, 29, 30, 31);
}
DEV u4 srd(const void* p, unsigned bytes) {
  return u4{(unsigned)(uintptr_t)p, (unsigned)((uintptr_t)p >> 32) & 0xffffu, bytes, 0x00020000u};
}
DEV unsigned lds_base(char* sm) { return (unsigned)(uintptr_t)(__attribute__((address_space(3))) char*)sm; }
DEV unsigned swz(unsigned g) { return (g & 1) | ((g & 2) << 1); }
DEV void sched() { __builtin_amdgcn_sched_barrier(0); }

DEV u4 load_mem0(const Params& p, unsigned w, unsigned c, unsigned g) {
  const rsrc_t r = mk(p.mem0, 8192);
  const unsigned cg = 4 * w + g;
  const unsigned voff = ((c >> 1) * 512u + 32u * (cg >> 1) + (cg & 1) + 4u * (c & 1)) * 2u;
  u4 a;
#pragma unroll
  for (int i = 0; i < 4; ++i) a[i] = ld2(r, voff, 16 * i) | (ld2(r, voff, 16 * i + 4) << 16);
  return a;
}
constexpr float SCALE_LOG2E = 0.08838834764831845f * 1.4426950408889634f;

#define A_STAMP(r)
#define A_SAVE(r, slot)
#define A_NEXT
#define A_KXWAIT "s_waitcnt vmcnt(2)\n"
#define A_DIAG_OUT
#define A_DIAG_CLOB
#define A_CLK(r)
#define A_SAVECLK
#define A_DIAG_DECL
#define A_SCLOB "s80", "s81", "s82", "s83", "s84", "s85", "s86", "s87", "s88", "s89", "s90", "s91", "vcc", "scc", "memory"
#define SMF(d, a, b, i) "v_smfmac_f32_16x16x64_bf16 v[" d "], v[" a "], v[" b "], " i "\n"
#define MFM(d, a, b, c) "v_mfma_f32_16x16x32_bf16 v[" d "], v[" a "], v[" b "], " c "\n"
#define DPP_ALL " row_mask:0xf bank_mask:0xf\n"
#define A_POLL_MEM \
  ".Lpoll%=:\n" \
  "buffer_load_dwordx4 v[148:151], v129, %[ws], %[mem] offen sc1\n" \
  "s_sub_u32 %[cap], %[cap], 1\n" \
  "s_cbranch_scc1 .Lexit%=\n" \
  "s_waitcnt vmcnt(0)\n" \
  "ds_write_b128 v130, v[148:151]\n" \
  "v_max3_u32 v152, v148, v149, v150\n" \
  "v_max_u32 v152, v152, v151\n" \
  "v_cmp_eq_u32 vcc, -1, v152\n" \
  "s_cbranch_vccnz .Lpoll%=\n"
#define A_READ_MEM \
  "s_waitcnt lgkmcnt(0)\n" \
  "s_barrier\n" \
  A_CLK("s[72:73]") \
  "ds_read_b128 v[152:155], v131\n" \
  "ds_read_b128 v[156:159], v132\n" \
  "ds_read_b128 v[160:163], v133\n" \
  "ds_read_b128 v[164:167], v134\n" \
  "ds_read_b128 v[168:171], v135\n" \
  "ds_read_b128 v[172:175], v136\n" \
  "ds_read_b128 v[176:179], v137\n"
#define D_S1 0
#define D_S0 0
#define D_VM 0
#define D_V1 0
#define STR_(x) #x
#define SLEEP(x) "s_sleep " STR_(x) "\n"
#define WL(n) "s_waitcnt lgkmcnt(" #n ")\n"
#define WV(n) "s_waitcnt vmcnt(" #n ")\n"

DEV void role_s1(const Params& p, unsigned h, char* sm) {
  constexpr unsigned L_MEM = 0, L_Q = 8192, L_K = 10240;
  const unsigned l = threadIdx.x & 63, w = rfl(threadIdx.x >> 6), c = l & 15, g = l >> 4;
  const rsrc_t ws = mk(p.ws, WS_BYTES);
  bool alive = true;
  STAMP(15, w);

  u8 wa[8], wb[8];
  {
    const rsrc_t wr = mk(w < 4 ? p.wq : p.wk, 4u << 20);
    const unsigned voff = ((16u * (g & 1) + (g >> 1)) * 4096u + h * 128u + (w & 3) * 32u + 2u * c) * 2u;
#pragma unroll
    for (int i = 0; i < 8; ++i) {
      const unsigned t = (w + i) & 7;
      u4 a0, b0, a1, b1;
      load_pair(wr, voff, (64u * t) * 8192u, 16384u, a0, b0);
      load_pair(wr, voff, (64u * t + 32u) * 8192u, 16384u, a1, b1);
      wa[i] = cat(a0, a1);
      wb[i] = cat(b0, b1);
    }
  }
  u4 a0 = load_mem0(p, w, c, g);
  const unsigned cb = wait_init(p, ws, 8, alive);
  STAMP(15, 8 + w);

  const unsigned lb = lds_base(sm);
  u16v k0;
  u8 k1;
  k0[0] = (c & 1) ? 0xeeeeeeeeu : 0x44444444u;
  k0[1] = w * 1024u + l * 16u;
#pragma unroll
  for (int i = 0; i < 8; ++i) k0[2 + i] = lb + L_MEM + ((w + i) & 7) * 1024u + l * 16u;
#pragma unroll
  for (int r = 0; r < 2; ++r)
    k0[10 + r] = lb + (w < 4 ? L_Q : L_K) + (((w & 3) * 4u + (c >> 2)) * 8u + ((2u * g + r) ^ swz(c >> 2))) * 16u + (c & 3) * 4u;
  k0[12] = k0[13] = 0;
  k0[14] = lb + L_Q + (g * 8u + ((c & 7) ^ swz(g))) * 16u;
  k0[15] = lb + L_K + (g * 8u + ((((c >> 2) & 1) * 4u + (c >> 3) * 2u + (c & 1)) ^ swz(g))) * 16u;
  const unsigned piece = ((c & 7) >> 2) * 4u + (h >> 3), hl = h & 7;
  const unsigned p_base = cb + (B_P + (piece >> 1)) * 16384u + (piece & 1) * 4096u;
  const unsigned tok = ((c >> 1) & 1) * 16u + (c >> 3) * 8u + ((c >> 2) & 1) * 2u + (c & 1);
  k1[0] = p_base + (hl >> 1) * 512u + (2u * (hl & 1) + (g >> 1)) * 128u + ((g & 1) * 4u + (c & 3)) * 16u;
  k1[1] = p_base + 2048u + (hl >> 1) * 128u + ((g >> 1) * 4u + (c & 3)) * 16u + (hl & 1) * 8u;
  k1[2] = O_KX + h * 8192u + ((tok >> 4) * 64u + g * 16u + (tok & 15)) * 16u;
  k1[3] = k1[2] + 4096u;
  k1[4] = w == 0 ? F_KX + h * FL : OOB;
  k1[5] = lb + L_K + l * 16u;
  k1[6] = O_KM + h * 2048u + l * 16u;
  k1[7] = F_KM + h * FL;

  u32v kx = {};
  unsigned seen;
  if (w == 0) {
    wait_flags(ws, k1[4], 1u, alive);
    u4 f[8];
#pragma unroll
    for (int i = 0; i < 8; ++i) f[i] = ld16c(ws, k1[2] + (i & 3) * 2048u + (i >> 2) * 64u, 0);
    kx = cat(cat(f[0], f[1]), cat(f[2], f[3]), cat(f[4], f[5]), cat(f[6], f[7]));
  }
  STAMP(15, 16 + w);
  if (w == 0) TSTAMP(1);

  unsigned s = 0, mem = rfl(cb + B_MEM * 16384u - 16384u), cap = rfl(alive ? SPIN_CAP : 0u), kxo = 262144u, kmo = 0;
  u32v t0;
  u8 t1;
  u16v t2;
  A_DIAG_DECL;
  asm volatile(
      "s_mov_b32 s80, 0xff00ff00\n"
      "s_mov_b32 s81, 0xff00ff00\n"
      "s_mov_b32 s82, 0x3e0293ee\n"
      "s_mov_b32 s83, 0\n"
      "s_cmp_eq_u32 %[w], 0\n"
      "s_cselect_b32 s90, 63, 0\n"
      "v_mov_b32 v184, 0\nv_mov_b32 v185, 0\nv_mov_b32 v186, 0\nv_mov_b32 v187, 0\n"
      "v_mov_b32 v188, 0\nv_mov_b32 v189, 0\nv_mov_b32 v190, 0\nv_mov_b32 v191, 0\n"
      "ds_write_b128 v130, v[148:151]\n"
      "s_branch .Lhave%=\n"
      A_POLL_MEM
      ".Lhave%=:\n"
      A_STAMP("s[84:85]") A_CLK("s[70:71]")
      SMF("184:187", "148:151", "0:7", "v128") SMF("188:191", "148:151", "8:15", "v128")
      "buffer_load_dword v248, v244, %[ws], 0 offen sc1\n"
      A_READ_MEM
      WL(6) SMF("184:187", "152:155", "16:23", "v128") SMF("188:191", "152:155", "24:31", "v128")
      WL(5) SMF("184:187", "156:159", "32:39", "v128") SMF("188:191", "156:159", "40:47", "v128")
      WL(4) SMF("184:187", "160:163", "48:55", "v128") SMF("188:191", "160:163", "56:63", "v128")
      WL(3) SMF("184:187", "164:167", "64:71", "v128") SMF("188:191", "164:167", "72:79", "v128")
      WL(2) SMF("184:187", "168:171", "80:87", "v128") SMF("188:191", "168:171", "88:95", "v128")
      WL(1) SMF("184:187", "172:175", "96:103", "v128") SMF("188:191", "172:175", "104:111", "v128")
      WL(0) SMF("184:187", "176:179", "112:119", "v128") SMF("188:191", "176:179", "120:127", "v128")
      A_CLK("s[74:75]")
      "s_cmp_lt_u32 %[s], s90\n"
      "s_nop 5\n"
      "v_add_f32 v152, v184, v185\n"
      "v_add_f32 v154, v186, v187\n"
      "v_add_f32 v153, v188, v189\n"
      "v_add_f32 v155, v190, v191\n"
      "v_cvt_pk_bf16_f32 v152, v152, v153\n"
      "v_cvt_pk_bf16_f32 v154, v154, v155\n"
      "ds_write_b32 v138, v152\n"
      "ds_write_b32 v139, v154\n"
      A_STAMP("s[86:87]") A_CLK("s[76:77]")
      "v_mov_b32 v184, 0\nv_mov_b32 v185, 0\nv_mov_b32 v186, 0\nv_mov_b32 v187, 0\n"
      "v_mov_b32 v188, 0\nv_mov_b32 v189, 0\nv_mov_b32 v190, 0\nv_mov_b32 v191, 0\n"
      "s_waitcnt lgkmcnt(0)\n"
      "s_barrier\n"
      "s_cbranch_scc0 .Lother%=\n"
      A_CLK("s[78:79]")
      "ds_read_b128 v[152:155], v142\n"
      "ds_read_b128 v[168:171], v143\n"
      "ds_read_b128 v[156:159], v142 offset:512\n"
      "ds_read_b128 v[172:175], v143 offset:512\n"
      "ds_read_b128 v[160:163], v142 offset:1024\n"
      "ds_read_b128 v[176:179], v143 offset:1024\n"
      "ds_read_b128 v[164:167], v142 offset:1536\n"
      "ds_read_b128 v[180:183], v143 offset:1536\n"
      WL(7) MFM("224:227", "192:195", "152:155", "0") MFM("228:231", "208:211", "152:155", "0")
      WL(6) MFM("232:235", "168:171", "152:155", "0")
      WL(5) MFM("224:227", "196:199", "156:159", "v[224:227]") MFM("228:231", "212:215", "156:159", "v[228:231]")
      WL(4) MFM("232:235", "172:175", "156:159", "v[232:235]")
      WL(3) MFM("224:227", "200:203", "160:163", "v[224:227]") MFM("228:231", "216:219", "160:163", "v[228:231]")
      WL(2) MFM("232:235", "176:179", "160:163", "v[232:235]")
      WL(1) MFM("224:227", "204:207", "164:167", "v[224:227]") MFM("228:231", "220:223", "164:167", "v[228:231]")
      WL(0) MFM("232:235", "180:183", "164:167", "v[232:235]")
      A_CLK("s[92:93]")
      "s_nop 5\n"
      "v_max3_f32 v152, v224, v225, v226\n"
      "v_max3_f32 v153, v227, v228, v229\n"
      "v_max3_f32 v154, v230, v231, v232\n"
      "v_max3_f32 v152, v152, v153, v233\n"
      "v_max_f32 v153, v152, v154\n"
      "v_max_f32 v152, v152, v154\n"
      "v_cndmask_b32 v156, v224, v228, s[80:81]\n"
      "v_cndmask_b32 v157, v225, v229, s[80:81]\n"
      "v_permlane16_swap_b32 v152, v153\n"
      "v_max_f32 v154, v152, v153\n"
      "v_max_f32 v155, v152, v153\n"
      "v_cndmask_b32 v158, v226, v230, s[80:81]\n"
      "v_cndmask_b32 v159, v227, v231, s[80:81]\n"
      "v_permlane32_swap_b32 v154, v155\n"
      "v_cndmask_b32 v160, v232, v233, s[80:81]\n"
      "v_max_f32 v152, v154, v155\n"
      "v_mul_f32 v152, 0xbe0293ee, v152\n"
      "v_fma_f32 v156, v156, s82, v152\n"
      "v_fma_f32 v157, v157, s82, v152\n"
      "v_fma_f32 v158, v158, s82, v152\n"
      "v_fma_f32 v159, v159, s82, v152\n"
      "v_fma_f32 v160, v160, s82, v152\n"
      "v_exp_f32 v156, v156\n"
      "v_exp_f32 v157, v157\n"
      "v_exp_f32 v158, v158\n"
      "v_exp_f32 v159, v159\n"
      "v_exp_f32 v160, v160\n"
      "v_add_f32 v152, v156, v157\n"
      "v_add_f32 v153, v158, v159\n"
      "v_add_f32 v152, v152, v160\n"
      "v_add_f32 v152, v152, v153\n"
      "v_xor_b32 v156, s83, v156\n"
      "v_xor_b32 v157, s83, v157\n"
      "v_add_f32_dpp v154, v152, v152 row_ror:8" DPP_ALL
      "v_add_f32_dpp v155, v152, v152 row_ror:8" DPP_ALL
      "v_xor_b32 v158, s83, v158\n"
      "v_xor_b32 v159, s83, v159\n"
      "v_permlane16_swap_b32 v154, v155\n"
      "v_add_f32 v152, v154, v155\n"
      "v_add_f32 v153, v154, v155\n"
      "v_xor_b32 v160, s83, v160\n"
      "s_xor_b32 s83, s83, 0x80000000\n"
      "v_permlane32_swap_b32 v152, v153\n"
      "v_add_f32 v152, v152, v153\n"
      "v_rcp_f32 v152, v152\n"
      A_CLK("s[94:95]")
      "s_add_u32 s91, %[s], 2\n"
      "v_mul_f32 v156, v156, v152\n"
      "v_mul_f32 v157, v157, v152\n"
      "v_mul_f32 v158, v158, v152\n"
      "v_mul_f32 v159, v159, v152\n"
      "v_mul_f32 v160, v160, v152\n"
      "v_cvt_pk_bf16_f32 v164, v156, v157\n"
      "v_cvt_pk_bf16_f32 v165, v158, v159\n"
      "v_mov_b32_dpp v161, v160 row_ror:8" DPP_ALL
      "v_mov_b32_dpp v166, v164 row_ror:8" DPP_ALL
      "v_mov_b32_dpp v167, v165 row_ror:8" DPP_ALL
      "v_cvt_pk_bf16_f32 v162, v160, v161\n"
      "v_cvt_pk_bf16_f32 v163, v160, v161\n"
      "s_not_b64 exec, s[80:81]\n"
      "buffer_store_dwordx4 v[164:167], v240, %[ws], 0 offen sc1\n"
      "s_mov_b64 exec, -1\n"
      "v_permlane16_swap_b32 v162, v163\n"
      "s_mov_b32 exec_lo, 0xff\n"
      "s_mov_b32 exec_hi, 0xff\n"
      "buffer_store_dwordx2 v[162:163], v241, %[ws], 0 offen sc1\n"
      "s_mov_b64 exec, -1\n"
      A_STAMP("s[88:89]") A_CLK("s[96:97]") A_SAVE("s84", 0) A_SAVE("s86", 1) A_SAVE("s88", 2) A_SAVECLK
      "s_cmp_ge_u32 %[s], 62\n"
      "s_cbranch_scc1 .Lnext%=\n"
      A_KXWAIT
      ".Lkx%=:\n"
      "v_cmp_gt_u32 vcc, s91, v248\n"
      "s_cbranch_vccz .Lkxok%=\n"
      "buffer_load_dword v248, v244, %[ws], 0 offen sc1\n"
      "s_sub_u32 %[cap], %[cap], 1\n"
      "s_cbranch_scc1 .Lexit%=\n"
      "s_waitcnt vmcnt(0)\n"
      "s_branch .Lkx%=\n"
      ".Lkxok%=:\n"
      "buffer_load_dwordx4 v[192:195], v242, %[ws], %[kxo] offen sc1\n"
      "buffer_load_dwordx4 v[208:211], v242, %[ws], %[kxo] offen offset:64 sc1\n"
      "buffer_load_dwordx4 v[196:199], v242, %[ws], %[kxo] offen offset:2048 sc1\n"
      "buffer_load_dwordx4 v[212:215], v242, %[ws], %[kxo] offen offset:2112 sc1\n"
      "buffer_load_dwordx4 v[200:203], v243, %[ws], %[kxo] offen sc1\n"
      "buffer_load_dwordx4 v[216:219], v243, %[ws], %[kxo] offen offset:64 sc1\n"
      "buffer_load_dwordx4 v[204:207], v243, %[ws], %[kxo] offen offset:2048 sc1\n"
      "buffer_load_dwordx4 v[220:223], v243, %[ws], %[kxo] offen offset:2112 sc1\n"
      "s_add_u32 %[kxo], %[kxo], 0x40000\n"
      SLEEP(D_S0)
      "s_branch .Lnext%=\n"
      ".Lidle%=:\n"
      SLEEP(D_S1)
      "s_branch .Lnext%=\n"
      ".Lother%=:\n"
      "s_cmp_lg_u32 %[w], 6\n"
      "s_cbranch_scc1 .Lidle%=\n"
      "ds_read_b128 v[152:155], v245\n"
      "ds_read_b128 v[156:159], v245 offset:1024\n"
      "s_add_u32 s91, %[s], 1\n"
      "v_mov_b32 v160, s91\n"
      WL(1) "buffer_store_dwordx4 v[152:155], v246, %[ws], %[kmo] offen sc1\n"
      WL(0) "buffer_store_dwordx4 v[156:159], v246, %[ws], %[kmo] offen offset:1024 sc1\n"
      "s_add_u32 %[kmo], %[kmo], 0x10000\n"
      "s_waitcnt vmcnt(0)\n"
      "s_mov_b64 exec, 1\n"
      "buffer_store_dword v160, v247, %[ws], 0 offen sc1\n"
      "s_mov_b64 exec, -1\n"
      SLEEP(D_S0)
      ".Lnext%=:\n"
      A_NEXT
      "s_add_u32 %[mem], %[mem], 0x4000\n"
      "s_add_u32 %[s], %[s], 1\n"
      "s_cmp_lt_u32 %[s], 64\n"
      "s_cbranch_scc1 .Lpoll%=\n"
      ".Lexit%=:\n"
      : [s] "+s"(s), [mem] "+s"(mem), [cap] "+s"(cap), [kxo] "+s"(kxo), [kmo] "+s"(kmo), "+{v[148:151]}"(a0),
        "=&{v[152:183]}"(t0), "=&{v[184:191]}"(t1), "+{v[192:223]}"(kx), "=&{v[224:239]}"(t2), "=&{v248}"(seen) A_DIAG_OUT
      : [ws] "s"(srd(p.ws, WS_BYTES)), [w] "s"(w), "{v[0:31]}"(cat(wa[0], wb[0], wa[1], wb[1])),
        "{v[32:63]}"(cat(wa[2], wb[2], wa[3], wb[3])), "{v[64:95]}"(cat(wa[4], wb[4], wa[5], wb[5])),
        "{v[96:127]}"(cat(wa[6], wb[6], wa[7], wb[7])), "{v[128:143]}"(k0), "{v[240:247]}"(k1)
      : A_SCLOB A_DIAG_CLOB);
}

DEV void load_wo(const Params& p, unsigned h, unsigned w, unsigned c, unsigned g, u4 (&wo)[4][4]) {
  const rsrc_t r = mk(p.wo, 4u << 20);
  const unsigned voff = ((h * 128u + 8u * g) * 512u + 64u * w + 2u * c) * 2u;
#pragma unroll
  for (int b = 0; b < 2; ++b)
#pragma unroll
    for (int f = 0; f < 4; ++f) load_pair(r, voff, f * 32768u + b * 64u, 1024u, wo[2 * b][f], wo[2 * b + 1][f]);
}

DEV void role_vm(const Params& p, unsigned h, char* sm) {
  constexpr unsigned L_MEM = 0, L_V = 8192;
  const unsigned l = threadIdx.x & 63, w = rfl(threadIdx.x >> 6), c = l & 15, g = l >> 4;
  const rsrc_t ws = mk(p.ws, WS_BYTES);
  bool alive = true;

  u8 wv[8], wo[4][2];
  {
    const rsrc_t wr = mk(p.wv, 4u << 20);
    const unsigned voff = ((16u * (g & 1) + (g >> 1)) * 4096u + h * 128u + w * 16u + c) * 2u;
#pragma unroll
    for (int i = 0; i < 8; ++i) {
      const unsigned t = (w + i) & 7;
      wv[i] = cat(load_single(wr, voff, (64u * t) * 8192u, 16384u), load_single(wr, voff, (64u * t + 32u) * 8192u, 16384u));
    }
    u4 t[4][4];
    load_wo(p, h, w, c, g, t);
#pragma unroll
    for (int i = 0; i < 4; ++i) {
      wo[i][0] = cat(t[i][0], t[i][1]);
      wo[i][1] = cat(t[i][2], t[i][3]);
    }
  }
  u4 a0 = load_mem0(p, w, c, g);
  const unsigned cb = wait_init(p, ws, 8, alive);
  STAMP(15, 8 + w);

  const unsigned lb = lds_base(sm);
  u16v k0;
  k0[0] = (c & 1) ? 0xeeeeeeeeu : 0x44444444u;
  k0[1] = w * 1024u + l * 16u;
#pragma unroll
  for (int i = 0; i < 8; ++i) k0[2 + i] = lb + L_MEM + ((w + i) & 7) * 1024u + l * 16u;
  k0[10] = lb + L_V + (w * 16u + 4u * g + ((c >> 1) & 1)) * 16u + (2u * (c >> 2) + (c & 1)) * 2u;
  k0[11] = lb + L_V + l * 16u;
  const unsigned cg = 4u * w + g;
  k0[12] = cb + (B_M + cg) * 16384u + h * 256u + c * 16u;
  k0[13] = O_M2 + cg * 8192u + h * 256u + c * 16u;
  k0[14] = threadIdx.x == 448 ? F_M + h * FL : OOB;
  k0[15] = 0;

  unsigned s = 0, mem = rfl(cb + B_MEM * 16384u - 16384u), cap = rfl(alive ? SPIN_CAP : 0u), m2o = 0;
  u32v t0;
  u16v t1, t2;
  A_DIAG_DECL;
#define VM_ZERO(a) "v_mov_b32 v" #a ", 0\n"
#define VM_ZERO_M \
      VM_ZERO(192) VM_ZERO(193) VM_ZERO(194) VM_ZERO(195) VM_ZERO(196) VM_ZERO(197) VM_ZERO(198) VM_ZERO(199) \
      VM_ZERO(200) VM_ZERO(201) VM_ZERO(202) VM_ZERO(203) VM_ZERO(204) VM_ZERO(205) VM_ZERO(206) VM_ZERO(207)
  asm volatile(
      "s_mov_b32 s83, 0\n"
      VM_ZERO(180) VM_ZERO(184) VM_ZERO(185) VM_ZERO(186) VM_ZERO(187) VM_ZERO_M
      "ds_write_b128 v130, v[148:151]\n"
      "s_branch .Lhave%=\n"
      A_POLL_MEM
      ".Lhave%=:\n"
      A_STAMP("s[84:85]") A_CLK("s[70:71]")
      SMF("184:187", "148:151", "0:7", "v128")
      A_READ_MEM
      WL(6) SMF("184:187", "152:155", "8:15", "v128")
      WL(5) SMF("184:187", "156:159", "16:23", "v128")
      WL(4) SMF("184:187", "160:163", "24:31", "v128")
      WL(3) SMF("184:187", "164:167", "32:39", "v128")
      WL(2) SMF("184:187", "168:171", "40:47", "v128")
      WL(1) SMF("184:187", "172:175", "48:55", "v128")
      WL(0) SMF("184:187", "176:179", "56:63", "v128")
      A_CLK("s[74:75]")
      "s_nop 7\n"
      "v_add_f32 v152, v184, v185\n"
      "v_add_f32 v153, v186, v187\n"
      "v_cvt_pk_bf16_f32 v152, v152, v153\n"
      "ds_write_b16 v138, v152\n"
      "ds_write_b16_d16_hi v138, v152 offset:32\n"
      A_STAMP("s[86:87]") A_CLK("s[76:77]")
      VM_ZERO(184) VM_ZERO(185) VM_ZERO(186) VM_ZERO(187)
      "s_waitcnt lgkmcnt(0)\n"
      "s_barrier\n"
      A_CLK("s[78:79]")
      "ds_read_b128 v[152:155], v139\n"
      "ds_read_b128 v[156:159], v139 offset:1024\n"
      "buffer_store_dword v180, v142, %[ws], 0 offen sc1\n"
      WL(1) SMF("192:195", "152:155", "64:71", "v128") SMF("200:203", "152:155", "96:103", "v128")
      WL(0) SMF("192:195", "156:159", "72:79", "v128") SMF("200:203", "156:159", "104:111", "v128")
      SMF("196:199", "152:155", "80:87", "v128") SMF("204:207", "152:155", "112:119", "v128")
      SMF("196:199", "156:159", "88:95", "v128") SMF("204:207", "156:159", "120:127", "v128")
      A_CLK("s[92:93]")
      "s_nop 2\n"
      "v_add_f32 v192, v192, v193\n"
      "v_add_f32 v200, v200, v201\n"
      "v_add_f32 v194, v194, v195\n"
      "v_add_f32 v202, v202, v203\n"
      "v_cvt_pk_bf16_f32 v160, v192, v194\n"
      "v_cvt_pk_bf16_f32 v162, v200, v202\n"
      "v_bfi_b32 v160, -2, v160, s83\n"
      "v_bfi_b32 v162, -2, v162, s83\n"
      "v_add_f32 v196, v196, v197\n"
      "v_add_f32 v204, v204, v205\n"
      "v_add_f32 v198, v198, v199\n"
      "v_add_f32 v206, v206, v207\n"
      "v_cvt_pk_bf16_f32 v161, v196, v198\n"
      "v_cvt_pk_bf16_f32 v163, v204, v206\n"
      "v_bfi_b32 v161, -2, v161, s83\n"
      "v_bfi_b32 v163, -2, v163, s83\n"
      "v_permlane32_swap_b32 v160, v162\n"
      "s_add_u32 %[s], %[s], 1\n"
      "v_permlane32_swap_b32 v161, v163\n"
      "s_nop 1\n"
      "v_permlane16_swap_b32 v160, v161\n"
      "v_permlane16_swap_b32 v162, v163\n"
      "buffer_store_dwordx4 v[160:163], v140, %[ws], 0 offen sc1\n"
      A_STAMP("s[88:89]") A_CLK("s[94:95]") A_CLK("s[96:97]")
      "buffer_store_dwordx4 v[160:163], v141, %[ws], %[m2o] offen sc1\n"
      "v_mov_b32 v180, %[s]\n"
      VM_ZERO_M
      "s_xor_b32 s83, s83, 1\n"
      "s_add_u32 %[m2o], %[m2o], 0x40000\n"
      "s_add_u32 %[mem], %[mem], 0x4000\n"
      A_NEXT
      SLEEP(D_VM)
      "s_cmp_lt_u32 %[s], 64\n"
      "s_cbranch_scc1 .Lpoll%=\n"
      "s_waitcnt vmcnt(0)\n"
      "s_barrier\n"
      "buffer_store_dword v180, v142, %[ws], 0 offen sc1\n"
      ".Lexit%=:\n"
      : [s] "+s"(s), [mem] "+s"(mem), [cap] "+s"(cap), [m2o] "+s"(m2o), "+{v[148:151]}"(a0), "=&{v[152:183]}"(t0),
        "=&{v[184:199]}"(t1), "=&{v[200:215]}"(t2) A_DIAG_OUT
      : [ws] "s"(srd(p.ws, WS_BYTES)), [w] "s"(w), "{v[0:31]}"(cat(wv[0], wv[1], wv[2], wv[3])),
        "{v[32:63]}"(cat(wv[4], wv[5], wv[6], wv[7])), "{v[64:95]}"(cat(wo[0][0], wo[0][1], wo[1][0], wo[1][1])),
        "{v[96:127]}"(cat(wo[2][0], wo[2][1], wo[3][0], wo[3][1])), "{v[128:143]}"(k0)
      : A_SCLOB A_DIAG_CLOB);
}

DEV void role_v1(const Params& p, unsigned j, char* sm) {
  const unsigned l = threadIdx.x & 63, w = rfl(threadIdx.x >> 6), c = l & 15, g = l >> 4;
  const unsigned cg = j >> 1, rg = j & 1;
  const rsrc_t ws = mk(p.ws, WS_BYTES);
  bool alive = true;
  {
    const unsigned cb0 = crit_base(p);
    const u4 ones = {~0u, ~0u, ~0u, ~0u};
    for (unsigned o = (j * 512u + threadIdx.x) * 16u; o < B_END * 16384u; o += 64u * 512u * 16u) st16c(ones, ws, o, cb0);
    drain();
    unsigned sel = 1;
    if (j == 0 && w == 0) {
      unsigned best[2] = {~0u, ~0u};
      for (int k = 0; k < 8; ++k) {
        const unsigned off = B_PROBE * 16384u + (k & 1) * 8192u + (l & 7) * 16u + (k >> 1) * 1024u;
        const unsigned long long t0 = __builtin_amdgcn_s_memtime();
        asm volatile("s_waitcnt lgkmcnt(0)" ::: "memory");
        st16c(ones, ws, off, cb0);
        drain();
        const unsigned long long t1 = __builtin_amdgcn_s_memtime();
        asm volatile("s_waitcnt lgkmcnt(0)" ::: "memory");
        const unsigned dt = (unsigned)(t1 - t0);
        if (k & 1) best[1] = min(best[1], dt); else best[0] = min(best[0], dt);
      }
      sel = best[1] < best[0] ? 2u : 1u;
    }
    bar();
    if (threadIdx.x == 0) st4c(sel, ws, F_INIT + j * FL, 0);
  }
  if (w >= 4) return;
  const unsigned cb = wait_init(p, ws, 4, alive);
  bar();
  STAMP(15, 8 + w);

  const unsigned lb = lds_base(sm);
  const unsigned piece = rg * 4u + w;
  u16v k0 = {};
  k0[0] = (c & 4) ? 0xeeeeeeeeu : 0x44444444u;
  k0[1] = cb + (B_P + (piece >> 1)) * 16384u + (piece & 1) * 4096u + g * 128u + (c & 7) * 16u + 2048u;
  k0[2] = k0[1] - 2048u + (c >> 3) * 512u;
  k0[3] = cb + (B_M + cg) * 16384u + w * 2048u + l * 16u;
  k0[4] = O_U + (cg * 32u + 8u * w) * 1024u + l * 16u;
  k0[5] = k0[4] + 4096u;
  k0[6] = F_U + (8u * w + (l & 7)) * FL;
  k0[7] = lb + ((g * 2u + ((c >> 1) & 1)) * 8u + 2u * (c >> 2) + (c & 1)) * 16u + w * 4u;
  k0[8] = lb + l * 16u;
  k0[9] = cg * 256u + rg * 128u + (l >> 3) * 16u;

  unsigned s = 0, mem = rfl(cb + B_MEM * 16384u), cap = rfl(alive ? SPIN_CAP : 0u), uo = 0, seen = 0;
  u32v t0, t1;
  A_DIAG_DECL;
#define V1_TAGS(d, a, b, c, OP) "v_bitop3_b32 v" d ", v" a ", v" b ", v" c " bitop3:" OP "\n"
#define V1_ZERO "v_mov_b32 v52, 0\nv_mov_b32 v53, 0\nv_mov_b32 v54, 0\nv_mov_b32 v55, 0\n" \
                "v_mov_b32 v56, 0\nv_mov_b32 v57, 0\nv_mov_b32 v58, 0\nv_mov_b32 v59, 0\n"
#define V1_LOAD_P \
      "buffer_load_dwordx4 v[40:43], v66, %[ws], 0 offen sc1\n" \
      "buffer_load_dwordx4 v[44:47], v66, %[ws], 0 offen offset:1024 sc1\n" \
      "buffer_load_dwordx4 v[48:51], v65, %[ws], 0 offen sc1\n"
#define V1_STEP(P, OP, MWANT, PWANT, NEG) \
      ".Ltop" P "%=:\n" \
      "v_cmp_ge_u32 vcc, %[s], v80\n" \
      "s_cbranch_vccnz .Luw" P "%=\n" \
      "buffer_load_dwordx4 v[0:3], v68, %[ws], %[uo] offen sc1\n" \
      "buffer_load_dwordx4 v[4:7], v68, %[ws], %[uo] offen offset:1024 sc1\n" \
      "buffer_load_dwordx4 v[8:11], v68, %[ws], %[uo] offen offset:2048 sc1\n" \
      "buffer_load_dwordx4 v[12:15], v68, %[ws], %[uo] offen offset:3072 sc1\n" \
      "buffer_load_dwordx4 v[16:19], v69, %[ws], %[uo] offen sc1\n" \
      "buffer_load_dwordx4 v[20:23], v69, %[ws], %[uo] offen offset:1024 sc1\n" \
      "buffer_load_dwordx4 v[24:27], v69, %[ws], %[uo] offen offset:2048 sc1\n" \
      "buffer_load_dwordx4 v[28:31], v69, %[ws], %[uo] offen offset:3072 sc1\n" \
      "buffer_load_dword v80, v70, %[ws], 0 offen sc1\n" \
      "s_add_u32 %[uo], %[uo], 0x100000\n" \
      V1_ZERO \
      SLEEP(D_V1) \
      ".Lboth" P "%=:\n" \
      "buffer_load_dwordx4 v[32:35], v67, %[ws], 0 offen sc1\n" \
      "buffer_load_dwordx4 v[36:39], v67, %[ws], 0 offen offset:1024 sc1\n" \
      V1_LOAD_P \
      WV(3) \
      V1_TAGS("60", "32", "33", "34", OP) V1_TAGS("61", "35", "36", "37", OP) V1_TAGS("60", "60", "38", "39", OP) \
      V1_TAGS("60", "60", "61", "61", OP) \
      "v_and_b32 v60, 1, v60\n" \
      "v_cmp_ne_u32 s[80:81], " MWANT ", v60\n" \
      ".Lp" P "%=:\n" \
      WV(2) SMF("52:55", "40:43", "0:7", "v64") SMF("56:59", "40:43", "8:15", "v64") \
      V1_TAGS("60", "40", "41", "42", OP) \
      WV(1) SMF("52:55", "44:47", "16:23", "v64") SMF("56:59", "44:47", "24:31", "v64") \
      V1_TAGS("60", "60", "43", "44", OP) V1_TAGS("61", "45", "46", "47", OP) \
      WV(0) SMF("52:55", "48:51", "32:39", "v64") \
      V1_TAGS("60", "60", "48", "49", OP) V1_TAGS("61", "61", "50", "51", OP) V1_TAGS("60", "60", "61", "61", OP) \
      "v_and_b32 v60, s82, v60\n" \
      "v_cmp_ne_u32 vcc, " PWANT ", v60\n" \
      "s_or_b64 vcc, vcc, s[80:81]\n" \
      "s_cbranch_scc1 .Lre" P "%=\n" \
      A_STAMP("s[86:87]") A_CLK("s[70:71]") \
      "s_nop 0\n" \
      "v_permlane32_swap_b32 v56, v54\n" \
      "v_permlane32_swap_b32 v57, v55\n" \
      "v_add_f32 v60, v52, v54\n" \
      "v_add_f32 v62, v58, v56\n" \
      "v_add_f32 v61, v53, v55\n" \
      "v_add_f32 v63, v59, v57\n" \
      "v_cndmask_b32 v60, v60, v62, s[84:85]\n" \
      "v_cndmask_b32 v61, v61, v63, s[84:85]\n" \
      "s_cmp_lg_u32 %[w], 0\n" \
      "s_nop 0\n" \
      "v_permlane16_swap_b32 v60, v61\n" \
      "v_add_f32 v60, v60, v61\n" \
      "ds_write_b32 v71, v60 offset:" P "*1024\n" \
      A_CLK("s[72:73]") \
      "s_waitcnt lgkmcnt(0)\n" \
      "s_barrier\n" \
      "s_cbranch_scc1 .Lend" P "%=\n" \
      A_CLK("s[74:75]") \
      "ds_read_b128 v[52:55], v72 offset:" P "*1024\n" \
      "s_waitcnt lgkmcnt(0)\n" \
      A_CLK("s[76:77]") \
      "v_add_f32 v52, v52, v53\n" \
      "v_add_f32 v54, v54, v55\n" \
      "v_add_f32_e64 v52, " NEG "v52, " NEG "v54\n" \
      "s_nop 1\n" \
      "v_mov_b32_dpp v53, v52 quad_perm:[1,0,3,2]" DPP_ALL \
      "v_cvt_pk_bf16_f32 v56, v52, v53\n" \
      "s_nop 1\n" \
      "v_mov_b32_dpp v57, v56 row_shl:2" DPP_ALL \
      "v_mov_b32_dpp v58, v56 row_shl:4" DPP_ALL \
      "v_mov_b32_dpp v59, v56 row_shl:6" DPP_ALL \
      "s_mov_b64 exec, s[90:91]\n" \
      "buffer_store_dwordx4 v[56:59], v73, %[ws], %[mem] offen sc1\n" \
      "s_mov_b64 exec, -1\n" \
      A_STAMP("s[88:89]") A_CLK("s[78:79]") A_SAVE("s86", 1) A_SAVE("s88", 2) A_SAVECLK \
      ".Lend" P "%=:\n" \
      "s_barrier\n" \
      A_NEXT \
      "s_add_u32 %[mem], %[mem], 0x4000\n" \
      "s_add_u32 %[s], %[s], 1\n"
#define V1_SIDE(P) \
      ".Luw" P "%=:\n" \
      "buffer_load_dword v80, v70, %[ws], 0 offen sc1\n" \
      "s_sub_u32 %[cap], %[cap], 1\n" \
      "s_cbranch_scc1 .Lexit%=\n" \
      "s_waitcnt vmcnt(0)\n" \
      "s_branch .Ltop" P "%=\n" \
      ".Lre" P "%=:\n" \
      "s_sub_u32 %[cap], %[cap], 1\n" \
      "s_cbranch_scc1 .Lexit%=\n" \
      "s_cmp_lg_u64 s[80:81], 0\n" \
      V1_ZERO \
      "s_cbranch_scc1 .Lboth" P "%=\n" \
      V1_LOAD_P \
      "s_branch .Lp" P "%=\n"
  asm volatile(
      "s_mov_b32 s82, 0x80008000\n"
      "s_mov_b32 s84, 0\n"
      "s_mov_b32 s85, -1\n"
      "s_mov_b32 s90, 0x01010101\n"
      "s_mov_b32 s91, 0x01010101\n"
      V1_STEP("0", "0xfe", "0", "0", "")
      "s_cmp_ge_u32 %[s], 63\n"
      "s_cbranch_scc1 .Lexit%=\n"
      V1_STEP("1", "0x80", "1", "s82", "-")
      "s_branch .Ltop0%=\n"
      V1_SIDE("0") V1_SIDE("1")
      ".Lexit%=:\n"
      : [s] "+s"(s), [mem] "+s"(mem), [cap] "+s"(cap), [uo] "+s"(uo), "=&{v[0:31]}"(t0), "=&{v[32:63]}"(t1),
        "+{v80}"(seen) A_DIAG_OUT
      : [ws] "s"(srd(p.ws, WS_BYTES)), [w] "s"(w), "{v[64:79]}"(k0)
      : A_SCLOB A_DIAG_CLOB);
}

DEV void load_x(rsrc_t xr, unsigned w, unsigned c, unsigned g, int s, u4 (&xp)[4]) {
  const unsigned voff = ((16u * (w >> 2) + c) * 512u + 128u * (w & 3) + 8u * g) * 2u;
#pragma unroll
  for (int i = 0; i < 4; ++i) xp[i] = ld16(xr, voff + i * 64u, s * 32768u);
}
DEV void put_x(char* sm, unsigned base, unsigned w, unsigned l, const u4 (&xp)[4]) {
#pragma unroll
  for (int i = 0; i < 4; ++i) lds16(sm, base + (4u * w + i) * 1024u + l * 16u) = xp[i];
}

DEV void token_softmax(rsrc_t ws, char* sm, unsigned qb, const u4 (&km)[4], unsigned h, unsigned w, unsigned l, int s) {
  const unsigned c = l & 15, g = l >> 4;
  u4 q[4], k0[4], k1[4];
#pragma unroll
  for (int m = 0; m < 4; ++m) {
    q[m] = lds16(sm, qb + (w * 4u + m) * 1024u);
    k0[m] = lds16(sm, qb + 8192u + m * 1024u);
    k1[m] = lds16(sm, qb + 12288u + m * 1024u);
  }
  sched();
  f4 s0 = {0, 0, 0, 0}, s1 = {0, 0, 0, 0}, sm_ = {0, 0, 0, 0};
#pragma unroll
  for (int m = 0; m < 4; ++m) {
    s0 = mma(k0[m], q[m], s0);
    s1 = mma(k1[m], q[m], s1);
  }
#pragma unroll
  for (int m = 0; m < 4; ++m) sm_ = mma(km[m], q[m], sm_);
  const unsigned tq = 16u * w + c;
  const float ninf = -__builtin_inff();
#pragma unroll
  for (int r = 0; r < 4; ++r) {
    s0[r] = 4u * g + r <= tq ? s0[r] : ninf;
    s1[r] = 16u + 4u * g + r <= tq ? s1[r] : ninf;
  }
  float mx = fmaxf(fmaxf(fmaxf(s0[0], s0[1]), fmaxf(s0[2], s0[3])), fmaxf(fmaxf(s1[0], s1[1]), fmaxf(s1[2], s1[3])));
  mx = group_max(fmaxf(mx, fmaxf(sm_[0], sm_[1])));
  const float mc = -mx * SCALE_LOG2E;
  float e[10];
#pragma unroll
  for (int r = 0; r < 4; ++r) {
    e[r] = __builtin_amdgcn_exp2f(fmaf(s0[r], SCALE_LOG2E, mc));
    e[4 + r] = __builtin_amdgcn_exp2f(fmaf(s1[r], SCALE_LOG2E, mc));
  }
  e[8] = __builtin_amdgcn_exp2f(fmaf(sm_[0], SCALE_LOG2E, mc));
  e[9] = __builtin_amdgcn_exp2f(fmaf(sm_[1], SCALE_LOG2E, mc));
  const float sum = group_sum(((e[0] + e[1]) + (e[2] + e[3])) + ((e[4] + e[5]) + (e[6] + e[7])) + (e[8] + e[9]));
  const float rs = __builtin_amdgcn_rcpf(sum);
  const u4 px = {pk(e[0] * rs, e[1] * rs), pk(e[2] * rs, e[3] * rs), pk(e[4] * rs, e[5] * rs), pk(e[6] * rs, e[7] * rs)};
  st16c(px, ws, O_PX + (w * 32u + h) * 1024u + l * 16u, s * 65536u);
  const unsigned dm = pk(e[8] * rs, e[9] * rs);
  auto r16 = __builtin_amdgcn_permlane16_swap(dm, dm, false, false);
  auto ra = __builtin_amdgcn_permlane32_swap(r16[0], r16[0], false, false);
  auto rb = __builtin_amdgcn_permlane32_swap(r16[1], r16[1], false, false);
  const u4 pm = {ra[0], rb[0], ra[1], rb[1]};
  st16c(pm, ws, g == 0 ? O_PM + (w * 8u + (h >> 2)) * 1024u + ((h & 3) * 16u + c) * 16u : OOB, s * 16384u);
}

DEV void role_s2(const Params& p, unsigned h, char* sm) {
  constexpr int LAG = 4, R = LAG + 1;
  constexpr unsigned L_X = 0, L_QK = 65536;
  const unsigned l = threadIdx.x & 63, w = rfl(threadIdx.x >> 6), c = l & 15, g = l >> 4;
  const rsrc_t ws = mk(p.ws, WS_BYTES), xr = mk(p.bx, 2u << 20);
  bool alive = true;

  u4 xp[4];
  load_x(xr, w, c, g, 0, xp);
  u4 wa[16], wb[16];
  {
    const rsrc_t wr = mk(w < 4 ? p.wq : p.wk, 4u << 20);
    const unsigned voff = (8u * g * 4096u + h * 128u + (w & 3) * 32u + 2u * c) * 2u;
#pragma unroll
    for (int f = 0; f < 16; ++f) load_pair(wr, voff, 32u * f * 8192u, 8192u, wa[f], wb[f]);
  }
  put_x(sm, L_X, w, l, xp);
  load_x(xr, w, c, g, 1, xp);
  bar();
  STAMP(15, 8 + w);
  if (w < 4) __builtin_amdgcn_s_setprio(1);

  const unsigned km_off = w < 2 ? O_KM + h * 2048u + (g * 8u + ((2u * (c >> 2) + (c & 1)) ^ swz(g))) * 16u : OOB;
  const unsigned km_flag = w < 2 ? F_KM + h * FL : OOB;
  const unsigned px_flag = w < 2 && l == 0 ? F_PX + (w * 32u + h) * FL : OOB;
  const unsigned kx_flag = threadIdx.x == 448 ? F_KX + h * FL : OOB;
  const unsigned kx_off = w >= 4 ? O_KX + h * 8192u + (w & 3) * 2048u + l * 16u : OOB;
  unsigned km_seen = 0;
  int slot = 0;
  for (int it = 0; it < NSEG; ++it) {
    const int s = max(it - LAG, 0);
    const bool soft = it >= LAG && w < 2;
    if (soft && !all_lanes(km_seen > (unsigned)s)) wait_flags(ws, km_flag, s + 1, alive);
    if (w == 0 || w == 4) STAMP(3 + (w >> 2) * 4, it);
    u4 km[4];
#pragma unroll
    for (int m = 0; m < 4; ++m) km[m] = ld16c(ws, km_off + m * 512u, s * 65536u);
    km_seen = ld4c(ws, km_flag, 0);
    f4 acc[2][2] = {};
    const unsigned xb = L_X + (it & 1) * 32768u + l * 16u;
    u4 x[3][2];
#pragma unroll
    for (int k = 0; k < 2; ++k)
#pragma unroll
      for (int t = 0; t < 2; ++t) x[k][t] = lds16(sm, xb + (t * 16 + k) * 1024u);
#pragma unroll
    for (int ks = 0; ks < 16; ++ks) {
      sched();
      if (ks + 2 < 16) {
#pragma unroll
        for (int t = 0; t < 2; ++t) x[(ks + 2) % 3][t] = lds16(sm, xb + (t * 16 + ks + 2) * 1024u);
      }
      sched();
#pragma unroll
      for (int t = 0; t < 2; ++t) {
        acc[0][t] = mma(wa[ks], x[ks % 3][t], acc[0][t]);
        acc[1][t] = mma(wb[ks], x[ks % 3][t], acc[1][t]);
      }
    }
    sched();
    asm volatile("s_waitcnt vmcnt(6)" ::: "memory");
    if (w == 0 || w == 4) STAMP(4 + (w >> 2) * 4, it);
    st4c(s, ws, px_flag, 0);
    put_x(sm, L_X + ((it + 1) & 1) * 32768u, w, l, xp);
#pragma unroll
    for (int t = 0; t < 2; ++t) {
      const u4 o = {pk(acc[0][t][0], acc[1][t][0]), pk(acc[0][t][1], acc[1][t][1]), pk(acc[0][t][2], acc[1][t][2]),
                    pk(acc[0][t][3], acc[1][t][3])};
      lds16(sm, L_QK + slot * 16384u + (w >> 2) * 8192u + (t * 4u + (w & 3)) * 1024u + l * 16u) = o;
      st16c(o, ws, kx_off + t * 1024u, it * 262144u);
    }
    load_x(xr, w, c, g, it + 2, xp);
    if (w == 0 || w == 4) STAMP(5 + (w >> 2) * 4, it);
    slot = slot == R - 1 ? 0 : slot + 1;
    if (soft) {
      token_softmax(ws, sm, L_QK + slot * 16384u + l * 16u, km, h, w, l, s);
      if (w == 0) STAMP(2, s);
    }
    if (it < LAG) drain();
    bar();
    st4c(it < LAG ? it + 1 : it, ws, kx_flag, 0);
  }
  if (w >= 2) return;
  for (int s = NSEG - LAG; s < NSEG; ++s) {
    wait_flags(ws, km_flag, s + 1, alive);
    u4 km[4];
#pragma unroll
    for (int m = 0; m < 4; ++m) km[m] = ld16c(ws, km_off + m * 512u, s * 65536u);
    st4c(s, ws, px_flag, 0);
    slot = slot == R - 1 ? 0 : slot + 1;
    token_softmax(ws, sm, L_QK + slot * 16384u + l * 16u, km, h, w, l, s);
    if (w == 0) STAMP(2, s);
  }
  drain();
  st4c(NSEG, ws, px_flag, 0);
}

DEV void role_vu(const Params& p, unsigned h, char* sm) {
  constexpr unsigned L_X = 0, L_V = 65536;
  constexpr int EAGER = 3;
  const unsigned l = threadIdx.x & 63, w = rfl(threadIdx.x >> 6), c = l & 15, g = l >> 4;
  const rsrc_t ws = mk(p.ws, WS_BYTES), xr = mk(p.bx, 2u << 20);

  u4 xp[4];
  load_x(xr, w, c, g, 0, xp);
  u4 wv[16], wo[4][4];
  {
    const rsrc_t wr = mk(p.wv, 4u << 20);
    const unsigned voff = (8u * g * 4096u + h * 128u + w * 16u + c) * 2u;
#pragma unroll
    for (int f = 0; f < 16; ++f) wv[f] = load_single(wr, voff, 32u * f * 8192u, 8192u);
    load_wo(p, h, w, c, g, wo);
  }
  put_x(sm, L_X, w, l, xp);
  load_x(xr, w, c, g, 1, xp);
  bar();
  STAMP(15, 8 + w);

  const unsigned v_off = L_V + ((w >> 1) * 64u + (2u * (w & 1) + (g >> 1)) * 16u + c) * 16u + (g & 1) * 8u;
  const unsigned flag = threadIdx.x == 448 ? F_U + h * FL : OOB;
  for (int s = 0; s < NSEG; ++s) {
    f4 acc[2] = {};
    const unsigned xb = L_X + (s & 1) * 32768u + l * 16u;
    u4 x[4][2];
#pragma unroll
    for (int k = 0; k < 3; ++k)
#pragma unroll
      for (int t = 0; t < 2; ++t) x[k][t] = lds16(sm, xb + (t * 16 + k) * 1024u);
#pragma unroll
    for (int ks = 0; ks < 16; ++ks) {
      sched();
      if (ks + 3 < 16) {
#pragma unroll
        for (int t = 0; t < 2; ++t) x[(ks + 3) & 3][t] = lds16(sm, xb + (t * 16 + ks + 3) * 1024u);
      }
      sched();
#pragma unroll
      for (int t = 0; t < 2; ++t) acc[t] = mma(wv[ks], x[ks & 3][t], acc[t]);
    }
    sched();
#pragma unroll
    for (int t = 0; t < 2; ++t)
      *reinterpret_cast<u2*>(sm + v_off + (s & 1) * 8192u + t * 4096u) = u2{pk(acc[t][0], acc[t][1]), pk(acc[t][2], acc[t][3])};
    if (w == 0) STAMP(3, s);
    asm volatile("s_waitcnt vmcnt(5)" ::: "memory");
    if (w == 0) STAMP(4, s);
    put_x(sm, L_X + ((s + 1) & 1) * 32768u, w, l, xp);
    load_x(xr, w, c, g, s + 2, xp);
    bar();
    st4c(max(s - 1, min(s, EAGER)), ws, flag, 0);
    if (w == 0) STAMP(5, s);
    u4 v[8];
#pragma unroll
    for (int k = 0; k < 8; ++k) v[k] = lds16(sm, L_V + (s & 1) * 8192u + ((k & 1) * 4 + (k >> 1)) * 1024u + l * 16u);
    sched();
    f4 u[4][2] = {};
#pragma unroll
    for (int k = 0; k < 8; ++k)
#pragma unroll
      for (int i = 0; i < 4; ++i) u[i][k & 1] = mma(v[k], wo[i][k >> 1], u[i][k & 1]);
#pragma unroll
    for (int i = 0; i < 4; ++i) {
      const u4 o = {pk(u[i][0][0], u[i][0][1]), pk(u[i][0][2], u[i][0][3]), pk(u[i][1][0], u[i][1][1]),
                    pk(u[i][1][2], u[i][1][3])};
      st16c(o, ws, O_U + ((4u * w + i) * 32u + h) * 1024u + l * 16u, s * 1048576u);
    }
    if (s < EAGER || s == NSEG - 1) {
      drain();
      bar();
      st4c(s + 1, ws, flag, 0);
    }
    if (w == 0) STAMP(2, s);
  }
}

DEV void role_v2(const Params& p, unsigned j, char* sm) {
  constexpr unsigned ROW = 144, PART = 16 * ROW, BUF = 8 * PART;
  const unsigned l = threadIdx.x & 63, w = rfl(threadIdx.x >> 6), c = l & 15, g = l >> 4;
  const unsigned b = j >> 1, qt = j & 1;
  const rsrc_t ws = mk(p.ws, WS_BYTES), outr = mk(p.out, 8u << 20);
  bool alive = true;

  const unsigned hq = 4u * w + (l & 3);
  const unsigned uf_off = F_U + hq * FL;
  const unsigned pf_off = (l & 4) ? F_M + hq * FL : F_PX + (qt * 32u + hq) * FL;
  const unsigned u_off = O_U + (2u * b * 32u + 4u * w) * 1024u + l * 16u;
  const unsigned m_off = O_M2 + 2u * b * 8192u + w * 1024u + l * 16u;
  const unsigned px_off = O_PX + (qt * 32u + 4u * w) * 1024u + l * 16u;
  const unsigned pm_off = O_PM + (qt * 8u + w) * 1024u + l * 16u;
  const unsigned out_off = ((16u * qt + (threadIdx.x >> 5)) * 1024u + 512u + 32u * b + (threadIdx.x & 31)) * 4u;
  unsigned u_seen = wait_flags(ws, uf_off, 1u, alive), p_seen = 0;
  u4 ua[2][4];
#pragma unroll
  for (int k = 0; k < 8; ++k) ua[k >> 2][k & 3] = ld16c(ws, u_off + (k >> 2) * 32768u + (k & 3) * 1024u, 0);

  for (int s = 0; s < NSEG; ++s) {
    if (!all_lanes(p_seen > (unsigned)s)) p_seen = wait_flags(ws, pf_off, s + 1, alive);
    u4 px[4];
#pragma unroll
    for (int i = 0; i < 4; ++i) px[i] = ld16c(ws, px_off + i * 1024u, s * 65536u);
    const u4 pm = ld16c(ws, pm_off, s * 16384u);
    const u4 m0 = ld16c(ws, m_off, s * 262144u), m1 = ld16c(ws, m_off + 8192u, s * 262144u);
    f4 a0 = {0, 0, 0, 0}, a1 = {0, 0, 0, 0};
#pragma unroll
    for (int i = 0; i < 4; ++i) {
      a0 = mma(ua[0][i], px[i], a0);
      a1 = mma(ua[1][i], px[i], a1);
    }
    a0 = mma(m0, pm, a0);
    a1 = mma(m1, pm, a1);
    sched();
    if (s + 1 < NSEG) {
      if (!all_lanes(u_seen > (unsigned)s + 1u)) u_seen = wait_flags(ws, uf_off, s + 2, alive);
#pragma unroll
      for (int k = 0; k < 8; ++k)
        ua[k >> 2][k & 3] = ld16c(ws, u_off + (k >> 2) * 32768u + (k & 3) * 1024u, (s + 1) * 1048576u);
    }
    sched();
    const unsigned rb = (s & 1) * BUF;
    *reinterpret_cast<f4*>(sm + rb + w * PART + c * ROW + g * 32u) = f4{a0[0], a1[0], a0[1], a1[1]};
    *reinterpret_cast<f4*>(sm + rb + w * PART + c * ROW + g * 32u + 16u) = f4{a0[2], a1[2], a0[3], a1[3]};
    bar();
    float v = 0.f;
#pragma unroll
    for (int k = 0; k < 8; ++k)
      v += *reinterpret_cast<const float*>(sm + rb + k * PART + (threadIdx.x >> 5) * ROW + (threadIdx.x & 31) * 4u);
    __builtin_amdgcn_raw_buffer_store_b32(__float_as_uint(rbf(v)), outr, out_off, s * 131072u, 0);
    if (w == 0) STAMP(2, s);
  }
}

DEV f2 cmul(f2 a, f2 b) {
  float re, im;
  asm("v_mul_f32 %0, %3, %5\n\tv_mul_f32 %1, %3, %4\n\tv_fma_f32 %0, %2, %4, -%0\n\tv_fma_f32 %1, %2, %5, %1"
      : "=&v"(re), "=&v"(im) : "v"(a[0]), "v"(a[1]), "v"(b[0]), "v"(b[1]));
  return f2{re, im};
}
DEV f2 cfma(f2 a, f2 b, f2 c) {
  float re, im;
  asm("v_fma_f32 %0, -%3, %5, %6\n\tv_fma_f32 %1, %3, %4, %7\n\tv_fma_f32 %0, %2, %4, %0\n\tv_fma_f32 %1, %2, %5, %1"
      : "=&v"(re), "=&v"(im) : "v"(a[0]), "v"(a[1]), "v"(b[0]), "v"(b[1]), "v"(c[0]), "v"(c[1]));
  return f2{re, im};
}
DEV f2 from_group(float re, float im, unsigned c, int k) {
  return f2{__shfl(re, (int)c + 16 * k, 64), __shfl(im, (int)c + 16 * k, 64)};
}

DEV void role_a(const Params& p, unsigned ci, char* sm) {
  constexpr unsigned GS = 272, KS = 4 * GS, TS = 32 * KS + 32, L_TAB = 4 * TS;
  const unsigned tid = threadIdx.x, l = tid & 63, w = rfl(tid >> 6), c = l & 15, g = l >> 4;
  const rsrc_t ws = mk(p.ws, WS_BYTES), xr = mk(p.ax, 2u << 20), outr = mk(p.out, 8u << 20);
  bool alive = true;

  {
    const unsigned voff = ((64u * ci + 16u * (c >> 2) + 4u * (w >> 1) + (c & 3)) * 512u + 256u * (w & 1) + 8u * g) * 2u;
    u4 xp[8];
#pragma unroll
    for (int i = 0; i < 8; ++i) xp[i] = ld16(xr, voff + i * 64u, 0);
#pragma unroll
    for (int i = 0; i < 8; ++i) lds16(sm, (8u * w + i) * 1024u + l * 16u) = xp[i];
  }
  f2 lam64;
  {
    const float lr = bf2f(((const unsigned short*)p.lre)[tid]), li = bf2f(((const unsigned short*)p.lim)[tid]);
    const float dt = rbf(expf(bf2f(((const unsigned short*)p.ldt)[tid])));
    const float er = expf(dt * lr);
    float sn, cs;
    sincosf(dt * li, &sn, &cs);
    f2 lam = {er * cs, er * sn};
    asm volatile("s_nop 1" : "+v"(lam));
    const float nr = lam[0] - 1.0f, ni = lam[1];
    f2 coef;
    if (fabsf(lr) >= fabsf(li)) {
      const float t = li / lr, d = 1.0f / (lr + li * t);
      coef = f2{smul(nr + smul(ni, t), d), smul(ni - smul(nr, t), d)};
    } else {
      const float t = lr / li, d = 1.0f / (lr * t + li);
      coef = f2{smul(smul(nr, t) + ni, d), smul(smul(ni, t) - nr, d)};
    }
    f2 l16 = cmul(lam, lam);
    l16 = cmul(l16, l16);
    l16 = cmul(l16, l16);
    l16 = cmul(l16, l16);
    lam64 = cmul(l16, l16);
    lam64 = cmul(lam64, lam64);
    *reinterpret_cast<f4*>(sm + L_TAB + tid * 32u) = f4{lam[0], lam[1], coef[0], coef[1]};
    *reinterpret_cast<f2*>(sm + L_TAB + tid * 32u + 16u) = l16;
  }
  bar();

  f4 hr[4][4] = {}, hi[4][4] = {};
  {
    const rsrc_t br = mk(p.bre, 1u << 19), bi = mk(p.bim, 1u << 19);
    const unsigned voff = ((64u * w + c) * 512u + 8u * g) * 2u;
    u4 fr[4], fi[4];
#pragma unroll
    for (int k = 0; k < 4; ++k) {
      fr[k] = ld16(br, voff + k * 64u, 0);
      fi[k] = ld16(bi, voff + k * 64u, 0);
    }
#pragma unroll
    for (int n = 0; n < 64; ++n) {
      const int ct = n >> 4, ks = n & 15;
      const u4 r = fr[n & 3], i = fi[n & 3];
      fence();
      if (n + 4 < 64) {
        fr[n & 3] = ld16(br, voff + ((n + 4) & 15) * 64u, ((n + 4) >> 4) * 16384u);
        fi[n & 3] = ld16(bi, voff + ((n + 4) & 15) * 64u, ((n + 4) >> 4) * 16384u);
      }
#pragma unroll
      for (int t = 0; t < 4; ++t) {
        const u4 x = lds16(sm, (t * 16 + ks) * 1024u + l * 16u);
        hr[ct][t] = mma(x, r, hr[ct][t]);
        hi[ct][t] = mma(x, i, hi[ct][t]);
      }
    }
  }
  __builtin_amdgcn_sched_barrier(0);
  asm volatile("s_nop 7\n\ts_nop 7" ::: "memory");
  __builtin_amdgcn_sched_barrier(0);
#pragma unroll
  for (int ct = 0; ct < 4; ++ct) {
    const unsigned ch = 64u * w + 16u * ct + c;
    const f4 tb = *reinterpret_cast<const f4*>(sm + L_TAB + ch * 32u);
    const f2 l16 = *reinterpret_cast<const f2*>(sm + L_TAB + ch * 32u + 16u);
    const f2 lam = {tb[0], tb[1]}, coef = {tb[2], tb[3]};
    f2 hh = {0.f, 0.f};
#pragma unroll
    for (int i = 0; i < 16; ++i) {
      hh = cfma(lam, hh, cmul(coef, f2{hr[ct][i >> 2][i & 3], hi[ct][i >> 2][i & 3]}));
      hr[ct][i >> 2][i & 3] = hh[0];
      hi[ct][i >> 2][i & 3] = hh[1];
    }
    f2 e = from_group(hh[0], hh[1], c, 0);
#pragma unroll
    for (int k = 1; k < 4; ++k) e = cfma(l16, e, from_group(hh[0], hh[1], c, k));
    if (g == 0) st8c(__builtin_bit_cast(u2, e), ws, O_AE + ci * 4096u + ch * 8u, 0);
  }
  drain();
  bar();
  if (tid == 0) st4c(1u, ws, F_A + ci * FL, 0);
  wait_flags(ws, F_A + (l & 31) * FL, (l & 31) < ci ? 1u : 0u, alive);
  {
    f2 hin = {0.f, 0.f};
#pragma unroll 8
    for (unsigned k = 0; k < ci; ++k) hin = cfma(lam64, hin, __builtin_bit_cast(f2, ld8c(ws, O_AE + tid * 8u, k * 4096u)));
    *reinterpret_cast<f2*>(sm + L_TAB + tid * 32u + 24u) = hin;
  }
  bar();
#pragma unroll
  for (int ct = 0; ct < 4; ++ct) {
    const unsigned ch = 64u * w + 16u * ct + c;
    const f4 t0 = *reinterpret_cast<const f4*>(sm + L_TAB + ch * 32u);
    const f4 t1 = *reinterpret_cast<const f4*>(sm + L_TAB + ch * 32u + 16u);
    const f2 lam = {t0[0], t0[1]}, l16 = {t1[0], t1[1]};
    f2 z = {t1[2], t1[3]};
#pragma unroll
    for (int k = 0; k < 3; ++k) {
      const f2 n = cfma(l16, z, from_group(hr[ct][3][3], hi[ct][3][3], c, k));
      z = (unsigned)k < g ? n : z;
    }
    const unsigned hb = g * TS + (ch >> 5) * KS + ((ch >> 3) & 3) * GS + (ch & 7) * 2u;
#pragma unroll
    for (int i = 0; i < 16; ++i) {
      z = cmul(lam, z);
      const unsigned d = pk(2.0f * (hr[ct][i >> 2][i & 3] + z[0]), -2.0f * (hi[ct][i >> 2][i & 3] + z[1]));
      lds_put2(sm, hb + i * 16u, d);
      lds_put2(sm, hb + 16u * KS + i * 16u, d >> 16);
    }
  }
  bar();
  f4 y[4][4] = {};
  {
    const rsrc_t cr = mk(p.cre, 1u << 19), cm = mk(p.cim, 1u << 19);
    const unsigned voff = ((64u * w + c) * 512u + 8u * g) * 2u;
    u4 fa[2][4];
#pragma unroll
    for (int k = 0; k < 2; ++k)
#pragma unroll
      for (int d = 0; d < 4; ++d) fa[k][d] = ld16(cr, voff + k * 64u, d * 16384u);
#pragma unroll
    for (int ks = 0; ks < 32; ++ks) {
      u4 a[4];
      fence();
#pragma unroll
      for (int d = 0; d < 4; ++d) {
        a[d] = fa[ks & 1][d];
        if (ks + 2 < 32) fa[ks & 1][d] = ld16(ks + 2 < 16 ? cr : cm, voff + ((ks + 2) & 15) * 64u, d * 16384u);
      }
#pragma unroll
      for (int t = 0; t < 4; ++t) {
        const u4 hb = lds16(sm, t * TS + ks * KS + g * GS + c * 16u);
#pragma unroll
        for (int d = 0; d < 4; ++d) y[d][t] = mma(a[d], hb, y[d][t]);
      }
    }
  }
  const rsrc_t dr = mk(p.dsk, 1024);
#pragma unroll
  for (int d = 0; d < 4; ++d) {
    const unsigned col = 64u * w + 16u * d + 4u * g;
    const u2 dk = ld8(dr, col * 2u, 0);
#pragma unroll
    for (int t = 0; t < 4; ++t) {
      const unsigned row = 64u * ci + 16u * t + c;
      const u2 xv = ld8(xr, (row * 512u + col) * 2u, 0);
      const f4 o = {y[d][t][0] + rbf(smul(bf2f(xv[0] & 0xffffu), bf2f(dk[0] & 0xffffu))),
                    y[d][t][1] + rbf(smul(bf2f(xv[0] >> 16), bf2f(dk[0] >> 16))),
                    y[d][t][2] + rbf(smul(bf2f(xv[1] & 0xffffu), bf2f(dk[1] & 0xffffu))),
                    y[d][t][3] + rbf(smul(bf2f(xv[1] >> 16), bf2f(dk[1] >> 16)))};
      __builtin_amdgcn_raw_buffer_store_b128(__builtin_bit_cast(u4, o), outr, (row * 1024u + col) * 4u, 0, 0);
    }
  }
  if (w == 0) STAMP(2, 0);
}

__global__ void __launch_bounds__(512, 2) fused_kernel(Params p) {
  extern __shared__ __attribute__((aligned(16))) char smem[];
  const unsigned x = blockIdx.x & 7, r = blockIdx.x >> 3;
  if (threadIdx.x < 64) TSTAMP(0);
  if (x == 1 || x == 2) {
    role_v1(p, (x - 1) * 32u + r, smem);
  } else if (x < 4) {
    const unsigned h = (x ? 16u : 0u) + (r >> 1);
    if (r & 1) role_vm(p, h, smem); else role_s1(p, h, smem);
  } else {
    const unsigned i = (x - 4) * 8u + (r >> 2);
    switch (r & 3) {
      case 0: role_s2(p, i, smem); break;
      case 1: role_vu(p, i, smem); break;
      case 2: role_v2(p, i, smem); break;
      case 3: role_a(p, i, smem); break;
    }
  }
  if (threadIdx.x < 64) TSTAMP(2);
}

__global__ void zero_kernel(unsigned* w) { if (threadIdx.x < 288) w[threadIdx.x * 32] = 0; if (threadIdx.x == 300) w[(O_DIAG >> 2) + 15 * 64] = (unsigned)__builtin_amdgcn_s_memrealtime(); }

__global__ void prt(const unsigned* dg, int mode) {
  __shared__ unsigned m[4];
  const int b = threadIdx.x;
  if (b == 0) { m[0] = ~0u; m[1] = 0; m[2] = 0; m[3] = 0; }
  __syncthreads();
  const unsigned* q = &dg[b * 1024 + 14 * 64];
  atomicMin(&m[0], q[0]);
  atomicMax(&m[3], q[2]);
  if (((b & 7) == 0 || (b & 7) == 3) && !((b >> 3) & 1)) atomicMax(&m[1], q[1]);
  if ((b & 7) == 1 || (b & 7) == 2) atomicMax(&m[2], q[2]);
  __syncthreads();
  unsigned v = 0;
  if (mode == 1) v = (m[1] - m[0]) * 10;
  if (mode == 2) v = m[2] - m[1];
  if (mode == 3) v = (m[3] - m[2]) * 10;
  if (mode == 4) v = m[3] - m[0];
  v = min(v, 60000u);
  __syncthreads();
  if (b == 0) m[0] = dg[15 * 64];
  __syncthreads();
  for (int i = 0; i < 2000000; ++i) {
    if ((unsigned)__builtin_amdgcn_s_memrealtime() - m[0] >= 30000u + v) break;
    __builtin_amdgcn_s_sleep(2);
  }
}
extern "C" void kernel_launch(void* const* d_in, const int* in_sizes, int n_in, void* d_out, int out_size, void* d_ws,
                              size_t ws_size, hipStream_t stream) {
  Params p;
  p.ax = d_in[0]; p.lre = d_in[1]; p.lim = d_in[2]; p.bre = d_in[3]; p.bim = d_in[4]; p.cre = d_in[5];
  p.cim = d_in[6]; p.ldt = d_in[7]; p.dsk = d_in[8]; p.bx = d_in[9]; p.mem0 = d_in[10]; p.wq = d_in[11];
  p.wk = d_in[12]; p.wv = d_in[13]; p.wo = d_in[14];
  p.out = (float*)d_out;
  p.ws = (char*)d_ws;
  (void)hipFuncSetAttribute((const void*)fused_kernel, hipFuncAttributeMaxDynamicSharedMemorySize, DYN_LDS);
  zero_kernel<<<1, 320, 0, stream>>>((unsigned*)d_ws);
  fused_kernel<<<256, 512, DYN_LDS, stream>>>(p);
  prt<<<1, 256, 0, stream>>>((const unsigned*)((char*)d_ws + O_DIAG), 0);
}
```

```cpp
#include <hip/hip_runtime.h>
#include <hip/hip_bf16.h>
#include <stdint.h>

typedef __attribute__((ext_vector_type(2))) unsigned u2;
typedef __attribute__((ext_vector_type(4))) unsigned u4;
typedef __attribute__((ext_vector_type(8))) unsigned u8;
typedef __attribute__((ext_vector_type(16))) unsigned u16v;
typedef __attribute__((ext_vector_type(32))) unsigned u32v;
typedef __attribute__((ext_vector_type(2))) float f2;
typedef __attribute__((ext_vector_type(4))) float f4;
typedef __attribute__((ext_vector_type(2))) __bf16 bf2;
typedef __attribute__((ext_vector_type(8))) __bf16 bf8;
typedef __attribute__((ext_vector_type(16))) __bf16 bf16v;
typedef __amdgpu_buffer_rsrc_t rsrc_t;
#define DEV __device__ __forceinline__

struct Params {
  const void *ax, *lre, *lim, *bre, *bim, *cre, *cim, *ldt, *dsk, *bx, *mem0, *wq, *wk, *wv, *wo;
  float* out;
  char* ws;
};

#define WSLEEP 4
constexpr int NSEG = 64;
constexpr unsigned SPIN_CAP = 400000u;
constexpr unsigned DYN_LDS = 156u * 1024u;

constexpr unsigned FL = 128, F_KM = 0, F_KX = 32 * FL, F_PX = 64 * FL, F_M = 128 * FL, F_U = 160 * FL, F_A = 192 * FL,
                   N_FLAGS = 224;
constexpr unsigned H_X = 256 * FL, H_Y = 512 * FL, H_END = 768 * FL;
constexpr unsigned O_AE = 5u << 20, O_DIAG = 6u << 20;
constexpr unsigned O_KX = 8u << 20, O_KM = 24u << 20, O_PX = 28u << 20, O_PM = 32u << 20, O_M2 = 34u << 20,
                   O_U = 50u << 20, WS_BYTES = 114u << 20;
constexpr unsigned OOB = 1u << 30;
constexpr unsigned B_MEM = 0, B_P = 63, B_M = 67, B_END = 99, B_PROBE = 100;

enum { R_S1, R_VM, R_V1, R_S2, R_VU, R_V2, R_A };
__host__ __device__ inline unsigned role_of(unsigned b, unsigned& i) {
  const unsigned x = b & 7, r = b >> 3;
  if (x >= 4) {
    i = (x - 4) * 8u + (r >> 2);
    return R_S2 + (r & 3);
  }
  if (x == 1 || x == 2) {
    i = (x - 1) * 32u + r;
    return R_V1;
  }
  i = (x ? 16u : 0u) + (r >> 1);
  return r & 1;
}

DEV rsrc_t mk(const void* p, unsigned bytes) {
  return __builtin_amdgcn_make_buffer_rsrc((void*)p, 0, bytes, 0x00020000);
}
DEV unsigned rfl(unsigned v) { return __builtin_amdgcn_readfirstlane(v); }
DEV u4 ld16(rsrc_t r, unsigned v, unsigned s) { return __builtin_amdgcn_raw_buffer_load_b128(r, v, s, 0); }
#define WAUX 0
DEV u4 ld16w(rsrc_t r, unsigned v, unsigned s) { return __builtin_amdgcn_raw_buffer_load_b128(r, v, s, WAUX); }
DEV u2 ld8(rsrc_t r, unsigned v, unsigned s) { return __builtin_amdgcn_raw_buffer_load_b64(r, v, s, 0); }
DEV unsigned ld4(rsrc_t r, unsigned v, unsigned s) { return __builtin_amdgcn_raw_buffer_load_b32(r, v, s, 0); }
DEV unsigned ld2(rsrc_t r, unsigned v, unsigned s) { return __builtin_amdgcn_raw_buffer_load_b16(r, v, s, 0); }
DEV u4 ld16c(rsrc_t r, unsigned v, unsigned s) { return __builtin_amdgcn_raw_buffer_load_b128(r, v, s, 16); }
DEV u2 ld8c(rsrc_t r, unsigned v, unsigned s) { return __builtin_amdgcn_raw_buffer_load_b64(r, v, s, 16); }
DEV unsigned ld4c(rsrc_t r, unsigned v, unsigned s) { return __builtin_amdgcn_raw_buffer_load_b32(r, v, s, 16); }
DEV void st16c(u4 d, rsrc_t r, unsigned v, unsigned s) { __builtin_amdgcn_raw_buffer_store_b128(d, r, v + s, 0, 16); }
DEV void st8c(u2 d, rsrc_t r, unsigned v, unsigned s) { __builtin_amdgcn_raw_buffer_store_b64(d, r, v, s, 16); }
DEV void st4c(unsigned d, rsrc_t r, unsigned v, unsigned s) { __builtin_amdgcn_raw_buffer_store_b32(d, r, v, s, 16); }
DEV void fence() { asm volatile("" ::: "memory"); }
DEV void drain() { asm volatile("s_waitcnt vmcnt(0)" ::: "memory"); }
DEV void bar() { asm volatile("s_waitcnt lgkmcnt(0)\n\ts_barrier" ::: "memory"); }
DEV bool all_lanes(bool ok) { return __builtin_amdgcn_ballot_w64(!ok) == 0ull; }

DEV float bf2f(unsigned h) { return __uint_as_float(h << 16); }
DEV unsigned pk(float a, float b) {
  f2 v = {a, b};
  return __builtin_bit_cast(unsigned, __builtin_convertvector(v, bf2));
}
DEV float rbf(float a) { return bf2f(pk(a, a) & 0xffffu); }
DEV float smul(float a, float b) {
  float r;
  asm("v_mul_f32 %0, %1, %2" : "=v"(r) : "v"(a), "v"(b));
  return r;
}
DEV float sadd(float a, float b) {
  float r;
  asm("v_add_f32 %0, %1, %2" : "=v"(r) : "v"(a), "v"(b));
  return r;
}
DEV f4 mma(u4 a, u4 b, f4 c) {
  return __builtin_amdgcn_mfma_f32_16x16x32_bf16(__builtin_bit_cast(bf8, a), __builtin_bit_cast(bf8, b), c, 0, 0, 0);
}
DEV f4 smma(u4 a, u8 b, f4 c, unsigned idx) {
  return __builtin_amdgcn_smfmac_f32_16x16x64_bf16(__builtin_bit_cast(bf8, a), __builtin_bit_cast(bf16v, b), c,
                                                   (int)idx, 0, 0);
}
DEV float swap32_add(float a, float b) {
  auto r = __builtin_amdgcn_permlane32_swap(__float_as_uint(a), __float_as_uint(b), false, false);
  return __uint_as_float(r[0]) + __uint_as_float(r[1]);
}
DEV float swap16_add(float a, float b) {
  auto r = __builtin_amdgcn_permlane16_swap(__float_as_uint(a), __float_as_uint(b), false, false);
  return __uint_as_float(r[0]) + __uint_as_float(r[1]);
}
DEV float group_max(float m) {
  auto r = __builtin_amdgcn_permlane16_swap(__float_as_uint(m), __float_as_uint(m), false, false);
  m = fmaxf(__uint_as_float(r[0]), __uint_as_float(r[1]));
  auto q = __builtin_amdgcn_permlane32_swap(__float_as_uint(m), __float_as_uint(m), false, false);
  return fmaxf(__uint_as_float(q[0]), __uint_as_float(q[1]));
}
DEV float group_sum(float m) {
  const float t = swap16_add(m, m);
  return swap32_add(t, t);
}
DEV u4& lds16(char* sm, unsigned off) { return *reinterpret_cast<u4*>(sm + off); }
DEV void lds_put2(char* sm, unsigned off, unsigned v) { *reinterpret_cast<unsigned short*>(sm + off) = (unsigned short)v; }

#define STAMP(slot, idx) do { } while (0)

#define TSTAMP(idx) do { } while (0)

DEV unsigned wait_flags(rsrc_t ws, unsigned off, unsigned need, bool& alive) {
  unsigned v = 0;
  for (unsigned it = 0; alive; ++it) {
    fence();
    v = ld4c(ws, off, 0);
    if (all_lanes(v >= need)) break;
    if (it > SPIN_CAP) alive = false;
    __builtin_amdgcn_s_sleep(WSLEEP);
  }
  fence();
  return v;
}
DEV unsigned crit_base(const Params& p) {
  const unsigned lo = (unsigned)(uintptr_t)p.ws;
  return ((lo + H_END + 0x1fffffu) & ~0x1fffffu) - lo;
}
struct Hs { unsigned x, a; };
constexpr unsigned L_HS = DYN_LDS - 16u;
DEV unsigned& lds4(char* sm, unsigned off) { return *reinterpret_cast<unsigned*>(sm + off); }
DEV Hs hs_begin(rsrc_t ws) {
  const unsigned o = threadIdx.x == 0 ? blockIdx.x * FL : OOB;
  return Hs{ld4c(ws, o + H_Y, 0), ld4c(ws, o + H_X + 4u, 0)};
}
DEV void hs_post(rsrc_t ws, Hs& h, char* sm) {
  h.x = (h.x & ~3u) + 4u;
  st4c(h.x, ws, threadIdx.x == 0 ? blockIdx.x * FL + H_X : OOB, 0);
  lds4(sm, threadIdx.x == 0 ? L_HS : L_HS + 4u) = h.x;
}
DEV unsigned hs_echo(rsrc_t ws) { return ld4c(ws, blockIdx.x * FL + H_Y, 0); }
DEV unsigned hs_wait(rsrc_t ws, char* sm, unsigned e, unsigned stage, bool& alive) {
  const unsigned x = rfl(lds4(sm, L_HS));
  unsigned v = rfl(e) - x;
  for (unsigned it = 0; alive && (v < stage || v > 3u); ++it) {
    if (it > SPIN_CAP) alive = false;
    __builtin_amdgcn_s_sleep(WSLEEP);
    fence();
    v = rfl(hs_echo(ws)) - x;
  }
  fence();
  return v;
}
DEV void hs_ack(rsrc_t ws, const Hs& h, unsigned d) {
  st4c(h.a + d, ws, threadIdx.x == 0 ? blockIdx.x * FL + H_X + 4u : OOB, 0);
}
DEV unsigned window(const Params& p, unsigned stage) { return crit_base(p) + (stage == 3u ? 8192u : 0u); }

DEV void stage_slice(rsrc_t r, unsigned h, unsigned row0, unsigned l, u4 (&d)[16]) {
  const unsigned v = (l >> 4) * 8192u + h * 256u + ((l & 15) ^ ((l >> 4) << 1)) * 16u;
#pragma unroll
  for (int i = 0; i < 16; ++i) d[i] = ld16w(r, v ^ ((i & 2) * 64u), (row0 + 4u * i) * 8192u);
}
DEV void stage_wo(rsrc_t r, unsigned h, unsigned w, unsigned l, u4 (&d)[16]) {
#pragma unroll
  for (int i = 0; i < 16; ++i)
    d[i] = ld16w(r, (l ^ (((i & 3) | ((i >> 1) & 4)) << 1)) * 16u, (h * 128u + w * 16u + i) * 1024u);
}
DEV void put_image(char* sm, unsigned w, unsigned l, const u4 (&d)[16]) {
#pragma unroll
  for (int i = 0; i < 16; ++i) lds16(sm, (16u * w + i) * 1024u + l * 16u) = d[i];
}
typedef __attribute__((ext_vector_type(4))) short s4;
DEV u2 lds_tr(char* sm, unsigned off) {
  return __builtin_bit_cast(u2, __builtin_amdgcn_ds_read_tr16_b64_v4i16((__attribute__((address_space(3))) s4*)(sm + off)));
}
DEV unsigned frag_base(unsigned pitch, unsigned cb, unsigned l) {
  const unsigned g = l >> 4, q = (l >> 2) & 3, p = l & 3;
  return (8u * g + q) * pitch + ((2u * cb + (p >> 1)) ^ ((q | ((g & 1) << 2)) << 1)) * 16u + (p & 1) * 8u;
}
DEV u4 frag(char* sm, unsigned off, unsigned pitch) {
  const u2 a = lds_tr(sm, off), b = lds_tr(sm, off + 4u * pitch);
  return u4{a[0], a[1], b[0], b[1]};
}
DEV u8 cat(u4 a, u4 b) { return __builtin_shufflevector(a, b, 0, 1, 2, 3, 4, 5, 6, 7); }
DEV u16v cat(u8 a, u8 b) { return __builtin_shufflevector(a, b, 0, 1, 2, 3, 4, 5, 6, 7, 8, 9, 10, 11, 12, 13, 14, 15); }
DEV u32v cat(u8 a, u8 b, u8 c, u8 d) {
  return __builtin_shufflevector(cat(a, b), cat(c, d), 0, 1, 2, 3, 4, 5, 6, 7, 8, 9, 10, 11, 12, 13, 14, 15, 16, 17, 18,
                                 19, 20, 21, 22, 23, 24, 25, 26, 27, 28, 29, 30, 31);
}
DEV u4 srd(const void* p, unsigned bytes) {
  return u4{(unsigned)(uintptr_t)p, (unsigned)((uintptr_t)p >> 32) & 0xffffu, bytes, 0x00020000u};
}
DEV unsigned lds_base(char* sm) { return (unsigned)(uintptr_t)(__attribute__((address_space(3))) char*)sm; }
DEV unsigned swz(unsigned g) { return (g & 1) | ((g & 2) << 1); }
DEV void sched() { __builtin_amdgcn_sched_barrier(0); }

DEV u4 load_mem0(const Params& p, unsigned w, unsigned c, unsigned g) {
  const rsrc_t r = mk(p.mem0, 8192);
  const unsigned voff = ((c >> 1) * 512u + 64u * w + 16u * g + 2u * (c & 1)) * 2u;
  u4 a;
#pragma unroll
  for (int i = 0; i < 4; ++i) a[i] = ld4(r, voff, 8 * i);
  return a;
}
constexpr float SCALE_LOG2E = 0.08838834764831845f * 1.4426950408889634f;

#define A_STAMP(r)
#define A_SAVE(r, slot)
#define A_NEXT
#define A_KXWAIT "s_waitcnt vmcnt(2)\n"
#define A_DIAG_OUT
#define A_DIAG_CLOB
#define A_CLK(r)
#define A_SAVECLK
#define A_DIAG_DECL
#define A_SCLOB "s80", "s81", "s82", "s83", "s84", "s85", "s86", "s87", "s88", "s89", "s90", "s91", "vcc", "scc", "memory"
#define SMF(d, a, b, i) "v_smfmac_f32_16x16x64_bf16 v[" d "], v[" a "], v[" b "], " i "\n"
#define MFM(d, a, b, c) "v_mfma_f32_16x16x32_bf16 v[" d "], v[" a "], v[" b "], " c "\n"
#define DPP_ALL " row_mask:0xf bank_mask:0xf\n"
#define A_POLL_MEM \
  ".Lpoll%=:\n" \
  "buffer_load_dwordx4 v[148:151], v129, %[ws], %[mem] offen sc1\n" \
  "s_sub_u32 %[cap], %[cap], 1\n" \
  "s_cbranch_scc1 .Lexit%=\n" \
  "s_waitcnt vmcnt(0)\n" \
  "ds_write_b128 v130, v[148:151]\n" \
  "v_max3_u32 v152, v148, v149, v150\n" \
  "v_max_u32 v152, v152, v151\n" \
  "v_cmp_eq_u32 vcc, -1, v152\n" \
  "s_cbranch_vccnz .Lpoll%=\n"
#define A_READ_MEM \
  "s_waitcnt lgkmcnt(0)\n" \
  "s_barrier\n" \
  A_CLK("s[72:73]") \
  "ds_read_b128 v[152:155], v131\n" \
  "ds_read_b128 v[156:159], v132\n" \
  "ds_read_b128 v[160:163], v133\n" \
  "ds_read_b128 v[164:167], v134\n" \
  "ds_read_b128 v[168:171], v135\n" \
  "ds_read_b128 v[172:175], v136\n" \
  "ds_read_b128 v[176:179], v137\n"
#define D_S1 0
#define D_S0 0
#define D_VM 0
#define D_V1 0
#define D_U 0
#define STR_(x) #x
#define SLEEP(x) "s_sleep " STR_(x) "\n"
#define WL(n) "s_waitcnt lgkmcnt(" #n ")\n"
#define WV(n) "s_waitcnt vmcnt(" #n ")\n"

DEV void role_s1(const Params& p, unsigned h, char* sm) {
  constexpr unsigned L_MEM = 131072, L_Q = L_MEM + 8192, L_K = L_MEM + 10240;
  const unsigned l = threadIdx.x & 63, w = rfl(threadIdx.x >> 6), c = l & 15, g = l >> 4;
  const rsrc_t ws = mk(p.ws, WS_BYTES);
  bool alive = true;
  STAMP(15, w);

  u8 wa[8], wb[8];
  u4 a0;
  unsigned cb, echo;
  {
    const rsrc_t wr = mk(w < 4 ? p.wq : p.wk, 4u << 20);
    Hs hs = hs_begin(ws);
    u4 d0[16], d1[16];
    stage_slice(wr, h, 64u * w, l, d0);
    fence();
    hs_post(ws, hs, sm);
    fence();
    stage_slice(wr, h, 64u * (w ^ 4u), l, d1);
    a0 = load_mem0(p, w, c, g);
    const unsigned fa = (w >> 2) * 65536u + frag_base(256u, 2u * (w & 3), l), fb = fa ^ 32u;
#pragma unroll
    for (int k = 0; k < 2; ++k) {
      put_image(sm, w, l, k ? d1 : d0);
      bar();
#pragma unroll
      for (int i = 0; i < 4; ++i) {
        const unsigned o = ((w ^ i) & 3) * 16384u;
        wa[4 * k + i] = cat(frag(sm, fa + o, 256u), frag(sm, fa + o + 8192u, 256u));
        wb[4 * k + i] = cat(frag(sm, fb + o, 256u), frag(sm, fb + o + 8192u, 256u));
      }
      if (k == 0) {
        bar();
        echo = hs_echo(ws);
      }
    }
    cb = window(p, hs_wait(ws, sm, echo, 2u, alive));
    hs_ack(ws, hs, 1u);
  }
  STAMP(15, 8 + w);

  const unsigned lb = lds_base(sm);
  u16v k0;
  u8 k1;
  k0[0] = (c & 1) ? 0xeeeeeeeeu : 0x44444444u;
  k0[1] = w * 1024u + l * 16u;
#pragma unroll
  for (int i = 0; i < 8; ++i) k0[2 + i] = lb + L_MEM + (w ^ i) * 1024u + l * 16u;
#pragma unroll
  for (int r = 0; r < 2; ++r)
    k0[10 + r] = lb + (w < 4 ? L_Q : L_K) + (((w & 3) * 4u + (c >> 2)) * 8u + ((2u * g + r) ^ swz(c >> 2))) * 16u + (c & 3) * 4u;
  k0[12] = k0[13] = 0;
  k0[14] = lb + L_Q + (g * 8u + ((c & 7) ^ swz(g))) * 16u;
  k0[15] = lb + L_K + (g * 8u + ((((c >> 2) & 1) * 4u + (c >> 3) * 2u + (c & 1)) ^ swz(g))) * 16u;
  const unsigned piece = ((c & 7) >> 2) * 4u + (h >> 3), hl = h & 7;
  const unsigned p_base = cb + (B_P + (piece >> 1)) * 16384u + (piece & 1) * 4096u;
  const unsigned tok = ((c >> 1) & 1) * 16u + (c >> 3) * 8u + ((c >> 2) & 1) * 2u + (c & 1);
  k1[0] = p_base + (hl >> 1) * 512u + (2u * (hl & 1) + (g >> 1)) * 128u + ((g & 1) * 4u + (c & 3)) * 16u;
  k1[1] = p_base + 2048u + (hl >> 1) * 128u + ((g >> 1) * 4u + (c & 3)) * 16u + (hl & 1) * 8u;
  k1[2] = O_KX + h * 8192u + ((tok >> 4) * 64u + g * 16u + (tok & 15)) * 16u;
  k1[3] = k1[2] + 4096u;
  k1[4] = w == 0 ? F_KX + h * FL : OOB;
  k1[5] = lb + L_K + l * 16u;
  k1[6] = O_KM + h * 2048u + l * 16u;
  k1[7] = F_KM + h * FL;

  u32v kx = {};
  unsigned seen;
  if (w == 0) {
    wait_flags(ws, k1[4], 1u, alive);
    u4 f[8];
#pragma unroll
    for (int i = 0; i < 8; ++i) f[i] = ld16c(ws, k1[2] + (i & 3) * 2048u + (i >> 2) * 64u, 0);
    kx = cat(cat(f[0], f[1]), cat(f[2], f[3]), cat(f[4], f[5]), cat(f[6], f[7]));
  }
  STAMP(15, 16 + w);
  if (w == 0) TSTAMP(1);

  unsigned s = 0, mem = rfl(cb + B_MEM * 16384u - 16384u), cap = rfl(alive ? SPIN_CAP : 0u), kxo = 262144u, kmo = 0;
  u32v t0;
  u8 t1;
  u16v t2;
  A_DIAG_DECL;
  asm volatile(
      "s_mov_b32 s80, 0xff00ff00\n"
      "s_mov_b32 s81, 0xff00ff00\n"
      "s_mov_b32 s82, 0x3e0293ee\n"
      "s_mov_b32 s83, 0\n"
      "s_cmp_eq_u32 %[w], 0\n"
      "s_cselect_b32 s90, 63, 0\n"
      "v_mov_b32 v184, 0\nv_mov_b32 v185, 0\nv_mov_b32 v186, 0\nv_mov_b32 v187, 0\n"
      "v_mov_b32 v188, 0\nv_mov_b32 v189, 0\nv_mov_b32 v190, 0\nv_mov_b32 v191, 0\n"
      "ds_write_b128 v130, v[148:151]\n"
      "s_branch .Lhave%=\n"
      A_POLL_MEM
      ".Lhave%=:\n"
      A_STAMP("s[84:85]") A_CLK("s[70:71]")
      SMF("184:187", "148:151", "0:7", "v128") SMF("188:191", "148:151", "8:15", "v128")
      "buffer_load_dword v248, v244, %[ws], 0 offen sc1\n"
      A_READ_MEM
      WL(6) SMF("184:187", "152:155", "16:23", "v128") SMF("188:191", "152:155", "24:31", "v128")
      WL(5) SMF("184:187", "156:159", "32:39", "v128") SMF("188:191", "156:159", "40:47", "v128")
      WL(4) SMF("184:187", "160:163", "48:55", "v128") SMF("188:191", "160:163", "56:63", "v128")
      WL(3) SMF("184:187", "164:167", "64:71", "v128") SMF("188:191", "164:167", "72:79", "v128")
      WL(2) SMF("184:187", "168:171", "80:87", "v128") SMF("188:191", "168:171", "88:95", "v128")
      WL(1) SMF("184:187", "172:175", "96:103", "v128") SMF("188:191", "172:175", "104:111", "v128")
      WL(0) SMF("184:187", "176:179", "112:119", "v128") SMF("188:191", "176:179", "120:127", "v128")
      A_CLK("s[74:75]")
      "s_cmp_lt_u32 %[s], s90\n"
      "s_nop 5\n"
      "v_add_f32 v152, v184, v185\n"
      "v_add_f32 v154, v186, v187\n"
      "v_add_f32 v153, v188, v189\n"
      "v_add_f32 v155, v190, v191\n"
      "v_cvt_pk_bf16_f32 v152, v152, v153\n"
      "v_cvt_pk_bf16_f32 v154, v154, v155\n"
      "ds_write_b32 v138, v152\n"
      "ds_write_b32 v139, v154\n"
      A_STAMP("s[86:87]") A_CLK("s[76:77]")
      "v_mov_b32 v184, 0\nv_mov_b32 v185, 0\nv_mov_b32 v186, 0\nv_mov_b32 v187, 0\n"
      "v_mov_b32 v188, 0\nv_mov_b32 v189, 0\nv_mov_b32 v190, 0\nv_mov_b32 v191, 0\n"
      "s_waitcnt lgkmcnt(0)\n"
      "s_barrier\n"
      "s_cbranch_scc0 .Lother%=\n"
      A_CLK("s[78:79]")
      "ds_read_b128 v[152:155], v142\n"
      "ds_read_b128 v[168:171], v143\n"
      "ds_read_b128 v[156:159], v142 offset:512\n"
      "ds_read_b128 v[172:175], v143 offset:512\n"
      "ds_read_b128 v[160:163], v142 offset:1024\n"
      "ds_read_b128 v[176:179], v143 offset:1024\n"
      "ds_read_b128 v[164:167], v142 offset:1536\n"
      "ds_read_b128 v[180:183], v143 offset:1536\n"
      WL(7) MFM("224:227", "192:195", "152:155", "0") MFM("228:231", "208:211", "152:155", "0")
      WL(6) MFM("232:235", "168:171", "152:155", "0")
      WL(5) MFM("224:227", "196:199", "156:159", "v[224:227]") MFM("228:231", "212:215", "156:159", "v[228:231]")
      WL(4) MFM("232:235", "172:175", "156:159", "v[232:235]")
      WL(3) MFM("224:227", "200:203", "160:163", "v[224:227]") MFM("228:231", "216:219", "160:163", "v[228:231]")
      WL(2) MFM("232:235", "176:179", "160:163", "v[232:235]")
      WL(1) MFM("224:227", "204:207", "164:167", "v[224:227]") MFM("228:231", "220:223", "164:167", "v[228:231]")
      WL(0) MFM("232:235", "180:183", "164:167", "v[232:235]")
      A_CLK("s[92:93]")
      "s_nop 5\n"
      "v_max3_f32 v152, v224, v225, v226\n"
      "v_max3_f32 v153, v227, v228, v229\n"
      "v_max3_f32 v154, v230, v231, v232\n"
      "v_max3_f32 v152, v152, v153, v233\n"
      "v_max_f32 v153, v152, v154\n"
      "v_max_f32 v152, v152, v154\n"
      "v_cndmask_b32 v156, v224, v228, s[80:81]\n"
      "v_cndmask_b32 v157, v225, v229, s[80:81]\n"
      "v_permlane16_swap_b32 v152, v153\n"
      "v_max_f32 v154, v152, v153\n"
      "v_max_f32 v155, v152, v153\n"
      "v_cndmask_b32 v158, v226, v230, s[80:81]\n"
      "v_cndmask_b32 v159, v227, v231, s[80:81]\n"
      "v_permlane32_swap_b32 v154, v155\n"
      "v_cndmask_b32 v160, v232, v233, s[80:81]\n"
      "v_max_f32 v152, v154, v155\n"
      "v_mul_f32 v152, 0xbe0293ee, v152\n"
      "v_fma_f32 v156, v156, s82, v152\n"
      "v_fma_f32 v157, v157, s82, v152\n"
      "v_fma_f32 v158, v158, s82, v152\n"
      "v_fma_f32 v159, v159, s82, v152\n"
      "v_fma_f32 v160, v160, s82, v152\n"
      "v_exp_f32 v156, v156\n"
      "v_exp_f32 v157, v157\n"
      "v_exp_f32 v158, v158\n"
      "v_exp_f32 v159, v159\n"
      "v_exp_f32 v160, v160\n"
      "v_add_f32 v152, v156, v157\n"
      "v_add_f32 v153, v158, v159\n"
      "v_add_f32 v152, v152, v160\n"
      "v_add_f32 v152, v152, v153\n"
      "v_xor_b32 v156, s83, v156\n"
      "v_xor_b32 v157, s83, v157\n"
      "v_add_f32_dpp v154, v152, v152 row_ror:8" DPP_ALL
      "v_add_f32_dpp v155, v152, v152 row_ror:8" DPP_ALL
      "v_xor_b32 v158, s83, v158\n"
      "v_xor_b32 v159, s83, v159\n"
      "v_permlane16_swap_b32 v154, v155\n"
      "v_add_f32 v152, v154, v155\n"
      "v_add_f32 v153, v154, v155\n"
      "v_xor_b32 v160, s83, v160\n"
      "s_xor_b32 s83, s83, 0x80000000\n"
      "v_permlane32_swap_b32 v152, v153\n"
      "v_add_f32 v152, v152, v153\n"
      "v_rcp_f32 v152, v152\n"
      A_CLK("s[94:95]")
      "s_add_u32 s91, %[s], 2\n"
      "v_mul_f32 v156, v156, v152\n"
      "v_mul_f32 v157, v157, v152\n"
      "v_mul_f32 v158, v158, v152\n"
      "v_mul_f32 v159, v159, v152\n"
      "v_mul_f32 v160, v160, v152\n"
      "v_cvt_pk_bf16_f32 v164, v156, v157\n"
      "v_cvt_pk_bf16_f32 v165, v158, v159\n"
      "v_mov_b32_dpp v161, v160 row_ror:8" DPP_ALL
      "v_mov_b32_dpp v166, v164 row_ror:8" DPP_ALL
      "v_mov_b32_dpp v167, v165 row_ror:8" DPP_ALL
      "v_cvt_pk_bf16_f32 v162, v160, v161\n"
      "v_cvt_pk_bf16_f32 v163, v160, v161\n"
      "s_not_b64 exec, s[80:81]\n"
      "buffer_store_dwordx4 v[164:167], v240, %[ws], 0 offen sc1\n"
      "s_mov_b64 exec, -1\n"
      "v_permlane16_swap_b32 v162, v163\n"
      "s_mov_b32 exec_lo, 0xff\n"
      "s_mov_b32 exec_hi, 0xff\n"
      "buffer_store_dwordx2 v[162:163], v241, %[ws], 0 offen sc1\n"
      "s_mov_b64 exec, -1\n"
      A_STAMP("s[88:89]") A_CLK("s[96:97]") A_SAVE("s84", 0) A_SAVE("s86", 1) A_SAVE("s88", 2) A_SAVECLK
      "s_cmp_ge_u32 %[s], 62\n"
      "s_cbranch_scc1 .Lnext%=\n"
      A_KXWAIT
      ".Lkx%=:\n"
      "v_cmp_gt_u32 vcc, s91, v248\n"
      "s_cbranch_vccz .Lkxok%=\n"
      "buffer_load_dword v248, v244, %[ws], 0 offen sc1\n"
      "s_sub_u32 %[cap], %[cap], 1\n"
      "s_cbranch_scc1 .Lexit%=\n"
      "s_waitcnt vmcnt(0)\n"
      "s_branch .Lkx%=\n"
      ".Lkxok%=:\n"
      "buffer_load_dwordx4 v[192:195], v242, %[ws], %[kxo] offen sc1\n"
      "buffer_load_dwordx4 v[208:211], v242, %[ws], %[kxo] offen offset:64 sc1\n"
      "buffer_load_dwordx4 v[196:199], v242, %[ws], %[kxo] offen offset:2048 sc1\n"
      "buffer_load_dwordx4 v[212:215], v242, %[ws], %[kxo] offen offset:2112 sc1\n"
      "buffer_load_dwordx4 v[200:203], v243, %[ws], %[kxo] offen sc1\n"
      "buffer_load_dwordx4 v[216:219], v243, %[ws], %[kxo] offen offset:64 sc1\n"
      "buffer_load_dwordx4 v[204:207], v243, %[ws], %[kxo] offen offset:2048 sc1\n"
      "buffer_load_dwordx4 v[220:223], v243, %[ws], %[kxo] offen offset:2112 sc1\n"
      "s_add_u32 %[kxo], %[kxo], 0x40000\n"
      SLEEP(D_S0)
      "s_branch .Lnext%=\n"
      ".Lidle%=:\n"
      SLEEP(D_S1)
      "s_branch .Lnext%=\n"
      ".Lother%=:\n"
      "s_cmp_lg_u32 %[w], 6\n"
      "s_cbranch_scc1 .Lidle%=\n"
      "ds_read_b128 v[152:155], v245\n"
      "ds_read_b128 v[156:159], v245 offset:1024\n"
      "s_add_u32 s91, %[s], 1\n"
      "v_mov_b32 v160, s91\n"
      WL(1) "buffer_store_dwordx4 v[152:155], v246, %[ws], %[kmo] offen sc1\n"
      WL(0) "buffer_store_dwordx4 v[156:159], v246, %[ws], %[kmo] offen offset:1024 sc1\n"
      "s_add_u32 %[kmo], %[kmo], 0x10000\n"
      "s_waitcnt vmcnt(0)\n"
      "s_mov_b64 exec, 1\n"
      "buffer_store_dword v160, v247, %[ws], 0 offen sc1\n"
      "s_mov_b64 exec, -1\n"
      SLEEP(D_S0)
      ".Lnext%=:\n"
      A_NEXT
      "s_add_u32 %[mem], %[mem], 0x4000\n"
      "s_add_u32 %[s], %[s], 1\n"
      "s_cmp_lt_u32 %[s], 64\n"
      "s_cbranch_scc1 .Lpoll%=\n"
      ".Lexit%=:\n"
      : [s] "+s"(s), [mem] "+s"(mem), [cap] "+s"(cap), [kxo] "+s"(kxo), [kmo] "+s"(kmo), "+{v[148:151]}"(a0),
        "=&{v[152:183]}"(t0), "=&{v[184:191]}"(t1), "+{v[192:223]}"(kx), "=&{v[224:239]}"(t2), "=&{v248}"(seen) A_DIAG_OUT
      : [ws] "s"(srd(p.ws, WS_BYTES)), [w] "s"(w), "{v[0:31]}"(cat(wa[0], wb[0], wa[1], wb[1])),
        "{v[32:63]}"(cat(wa[2], wb[2], wa[3], wb[3])), "{v[64:95]}"(cat(wa[4], wb[4], wa[5], wb[5])),
        "{v[96:127]}"(cat(wa[6], wb[6], wa[7], wb[7])), "{v[128:143]}"(k0), "{v[240:247]}"(k1)
      : A_SCLOB A_DIAG_CLOB);
}

DEV void read_wo(char* sm, unsigned w, unsigned l, u4 (&wo)[4][4]) {
#pragma unroll
  for (int i = 0; i < 4; ++i) {
    const unsigned fa = frag_base(1024u, 4u * w + i, l);
#pragma unroll
    for (int f = 0; f < 4; ++f) wo[i][f] = frag(sm, fa + f * 32768u, 1024u);
  }
}

DEV void role_vm(const Params& p, unsigned h, char* sm) {
  constexpr unsigned L_MEM = 131072, L_V = L_MEM + 8192;
  const unsigned l = threadIdx.x & 63, w = rfl(threadIdx.x >> 6), c = l & 15, g = l >> 4;
  const rsrc_t ws = mk(p.ws, WS_BYTES);
  bool alive = true;

  u8 wv[8], wo[4][2];
  u4 a0;
  unsigned cb;
  {
    Hs hs = hs_begin(ws);
    u4 d0[16], d1[16], t[4][4];
    stage_slice(mk(p.wv, 4u << 20), h, 64u * w, l, d0);
    fence();
    hs_post(ws, hs, sm);
    fence();
    stage_wo(mk(p.wo, 4u << 20), h, w, l, d1);
    a0 = load_mem0(p, w, c, g);
    put_image(sm, w, l, d0);
    bar();
    const unsigned fa = frag_base(256u, w, l);
#pragma unroll
    for (int i = 0; i < 8; ++i) {
      const unsigned o = fa + (w ^ i) * 16384u;
      wv[i] = cat(frag(sm, o, 256u), frag(sm, o + 8192u, 256u));
    }
    bar();
    const unsigned echo = hs_echo(ws);
    put_image(sm, w, l, d1);
    bar();
    read_wo(sm, w, l, t);
    cb = window(p, hs_wait(ws, sm, echo, 2u, alive));
    hs_ack(ws, hs, 1u);
#pragma unroll
    for (int i = 0; i < 4; ++i) {
      wo[i][0] = cat(t[i][0], t[i][1]);
      wo[i][1] = cat(t[i][2], t[i][3]);
    }
  }
  STAMP(15, 8 + w);

  const unsigned lb = lds_base(sm);
  u16v k0;
  k0[0] = (c & 1) ? 0xeeeeeeeeu : 0x44444444u;
  k0[1] = w * 1024u + l * 16u;
#pragma unroll
  for (int i = 0; i < 8; ++i) k0[2 + i] = lb + L_MEM + (w ^ i) * 1024u + l * 16u;
  k0[10] = lb + L_V + (w * 16u + 4u * g + ((c >> 1) & 1)) * 16u + (2u * (c >> 2) + (c & 1)) * 2u;
  k0[11] = lb + L_V + l * 16u;
  const unsigned cg = 4u * w + g;
  k0[12] = cb + (B_M + cg) * 16384u + h * 256u + c * 16u;
  k0[13] = O_M2 + cg * 8192u + h * 256u + c * 16u;
  k0[14] = threadIdx.x == 448 ? F_M + h * FL : OOB;
  k0[15] = 0;

  unsigned s = 0, mem = rfl(cb + B_MEM * 16384u - 16384u), cap = rfl(alive ? SPIN_CAP : 0u), m2o = 0;
  u32v t0;
  u16v t1, t2;
  A_DIAG_DECL;
#define VM_ZERO(a) "v_mov_b32 v" #a ", 0\n"
#define VM_ZERO_M \
      VM_ZERO(192) VM_ZERO(193) VM_ZERO(194) VM_ZERO(195) VM_ZERO(196) VM_ZERO(197) VM_ZERO(198) VM_ZERO(199) \
      VM_ZERO(200) VM_ZERO(201) VM_ZERO(202) VM_ZERO(203) VM_ZERO(204) VM_ZERO(205) VM_ZERO(206) VM_ZERO(207)
  asm volatile(
      "s_mov_b32 s83, 0\n"
      VM_ZERO(180) VM_ZERO(184) VM_ZERO(185) VM_ZERO(186) VM_ZERO(187) VM_ZERO_M
      "ds_write_b128 v130, v[148:151]\n"
      "s_branch .Lhave%=\n"
      A_POLL_MEM
      ".Lhave%=:\n"
      A_STAMP("s[84:85]") A_CLK("s[70:71]")
      SMF("184:187", "148:151", "0:7", "v128")
      A_READ_MEM
      WL(6) SMF("184:187", "152:155", "8:15", "v128")
      WL(5) SMF("184:187", "156:159", "16:23", "v128")
      WL(4) SMF("184:187", "160:163", "24:31", "v128")
      WL(3) SMF("184:187", "164:167", "32:39", "v128")
      WL(2) SMF("184:187", "168:171", "40:47", "v128")
      WL(1) SMF("184:187", "172:175", "48:55", "v128")
      WL(0) SMF("184:187", "176:179", "56:63", "v128")
      A_CLK("s[74:75]")
      "s_nop 7\n"
      "v_add_f32 v152, v184, v185\n"
      "v_add_f32 v153, v186, v187\n"
      "v_cvt_pk_bf16_f32 v152, v152, v153\n"
      "ds_write_b16 v138, v152\n"
      "ds_write_b16_d16_hi v138, v152 offset:32\n"
      A_STAMP("s[86:87]") A_CLK("s[76:77]")
      VM_ZERO(184) VM_ZERO(185) VM_ZERO(186) VM_ZERO(187)
      "s_waitcnt lgkmcnt(0)\n"
      "s_barrier\n"
      A_CLK("s[78:79]")
      "ds_read_b128 v[152:155], v139\n"
      "ds_read_b128 v[156:159], v139 offset:1024\n"
      "buffer_store_dword v180, v142, %[ws], 0 offen sc1\n"
      WL(1) SMF("192:195", "152:155", "64:71", "v128") SMF("200:203", "152:155", "96:103", "v128")
      WL(0) SMF("192:195", "156:159", "72:79", "v128") SMF("200:203", "156:159", "104:111", "v128")
      SMF("196:199", "152:155", "80:87", "v128") SMF("204:207", "152:155", "112:119", "v128")
      SMF("196:199", "156:159", "88:95", "v128") SMF("204:207", "156:159", "120:127", "v128")
      A_CLK("s[92:93]")
      "s_nop 2\n"
      "v_add_f32 v192, v192, v193\n"
      "v_add_f32 v200, v200, v201\n"
      "v_add_f32 v194, v194, v195\n"
      "v_add_f32 v202, v202, v203\n"
      "v_cvt_pk_bf16_f32 v160, v192, v194\n"
      "v_cvt_pk_bf16_f32 v162, v200, v202\n"
      "v_bfi_b32 v160, -2, v160, s83\n"
      "v_bfi_b32 v162, -2, v162, s83\n"
      "v_add_f32 v196, v196, v197\n"
      "v_add_f32 v204, v204, v205\n"
      "v_add_f32 v198, v198, v199\n"
      "v_add_f32 v206, v206, v207\n"
      "v_cvt_pk_bf16_f32 v161, v196, v198\n"
      "v_cvt_pk_bf16_f32 v163, v204, v206\n"
      "v_bfi_b32 v161, -2, v161, s83\n"
      "v_bfi_b32 v163, -2, v163, s83\n"
      "v_permlane32_swap_b32 v160, v162\n"
      "s_add_u32 %[s], %[s], 1\n"
      "v_permlane32_swap_b32 v161, v163\n"
      "s_nop 1\n"
      "v_permlane16_swap_b32 v160, v161\n"
      "v_permlane16_swap_b32 v162, v163\n"
      "buffer_store_dwordx4 v[160:163], v140, %[ws], 0 offen sc1\n"
      A_STAMP("s[88:89]") A_CLK("s[94:95]") A_CLK("s[96:97]")
      "buffer_store_dwordx4 v[160:163], v141, %[ws], %[m2o] offen sc1\n"
      "v_mov_b32 v180, %[s]\n"
      VM_ZERO_M
      "s_xor_b32 s83, s83, 1\n"
      "s_add_u32 %[m2o], %[m2o], 0x40000\n"
      "s_add_u32 %[mem], %[mem], 0x4000\n"
      A_NEXT
      SLEEP(D_VM)
      "s_cmp_lt_u32 %[s], 64\n"
      "s_cbranch_scc1 .Lpoll%=\n"
      "s_waitcnt vmcnt(0)\n"
      "s_barrier\n"
      "buffer_store_dword v180, v142, %[ws], 0 offen sc1\n"
      ".Lexit%=:\n"
      : [s] "+s"(s), [mem] "+s"(mem), [cap] "+s"(cap), [m2o] "+s"(m2o), "+{v[148:151]}"(a0), "=&{v[152:183]}"(t0),
        "=&{v[184:199]}"(t1), "=&{v[200:215]}"(t2) A_DIAG_OUT
      : [ws] "s"(srd(p.ws, WS_BYTES)), [w] "s"(w), "{v[0:31]}"(cat(wv[0], wv[1], wv[2], wv[3])),
        "{v[32:63]}"(cat(wv[4], wv[5], wv[6], wv[7])), "{v[64:95]}"(cat(wo[0][0], wo[0][1], wo[1][0], wo[1][1])),
        "{v[96:127]}"(cat(wo[2][0], wo[2][1], wo[3][0], wo[3][1])), "{v[128:143]}"(k0)
      : A_SCLOB A_DIAG_CLOB);
}

#define PROBES 6
constexpr unsigned PROBER = 32, PROBER_BLOCK = 2;
DEV void lead(rsrc_t ws) {
  const unsigned l = threadIdx.x & 63;
  unsigned i, a0[4], last[4];
  const bool init = role_of(l, i) == R_V1;
#pragma unroll
  for (int k = 0; k < 4; ++k) {
    a0[k] = ld4c(ws, H_X + (k * 64u + l) * FL + 4u, 0);
    st4c(0u, ws, k * 64u + l < N_FLAGS ? (k * 64u + l) * FL : OOB, 0);
  }
  drain();
  for (unsigned it = 0; it < SPIN_CAP; ++it) {
    fence();
    u2 v[4];
#pragma unroll
    for (int k = 0; k < 4; ++k) v[k] = ld8c(ws, H_X + (k * 64u + l) * FL, 0);
    const bool done = v[0][1] != a0[0] && v[1][1] != a0[1] && v[2][1] != a0[2] && v[3][1] != a0[3];
    const unsigned half = __builtin_amdgcn_readlane(v[0][1] - a0[0], PROBER_BLOCK);
    const unsigned stage = all_lanes(done || !init) ? (half == 2u ? 3u : 2u) : 1u;
#pragma unroll
    for (int k = 0; k < 4; ++k) {
      const unsigned e = (v[k][0] & ~3u) | stage;
      st4c(e, ws, it == 0 || e != last[k] ? H_Y + (k * 64u + l) * FL : OOB, 0);
      last[k] = e;
    }
    if (all_lanes(done)) break;
  }
}
DEV void role_v1(const Params& p, unsigned j, char* sm) {
  const unsigned l = threadIdx.x & 63, w = rfl(threadIdx.x >> 6), c = l & 15, g = l >> 4;
  const unsigned cg = j >> 1, rg = j & 1;
  const rsrc_t ws = mk(p.ws, WS_BYTES);
  bool alive = true;
  if (j == 0 && w == 7) {
    lead(ws);
    return;
  }
  const unsigned cb0 = crit_base(p);
  const u4 ones = {~0u, ~0u, ~0u, ~0u};
  Hs hs = hs_begin(ws);
  if (j) {
    for (unsigned o = ((j - 1u) * 512u + threadIdx.x) * 16u; o < B_END * 16384u; o += 63u * 512u * 16u)
      st16c(ones, ws, o, cb0);
  }
  hs_post(ws, hs, sm);
  drain();
  unsigned half = 0;
  if (j == PROBER && w == 0) {
    unsigned best[2] = {~0u, ~0u};
    for (int k = 0; k < PROBES; ++k) {
      const unsigned off = B_PROBE * 16384u + (k & 1) * 8192u + (l & 7) * 16u + (k >> 1) * 1024u;
      const unsigned long long t0 = __builtin_amdgcn_s_memtime();
      asm volatile("s_waitcnt lgkmcnt(0)" ::: "memory");
      st16c(ones, ws, off, cb0);
      drain();
      const unsigned long long t1 = __builtin_amdgcn_s_memtime();
      asm volatile("s_waitcnt lgkmcnt(0)" ::: "memory");
      const unsigned dt = (unsigned)(t1 - t0);
      if (k & 1) best[1] = min(best[1], dt); else best[0] = min(best[0], dt);
    }
    half = best[1] < best[0];
  }
  if (j) bar();
  if (w >= 4) return;
  if (w == 0) {
    hs_wait(ws, sm, hs_echo(ws), 1u, alive);
    hs_ack(ws, hs, 1u + half);
  }
  if (j == 0) bar();
  const unsigned cb = window(p, hs_wait(ws, sm, hs_echo(ws), 2u, alive));
  STAMP(15, 8 + w);

  const unsigned lb = lds_base(sm);
  const unsigned piece = rg * 4u + w;
  u16v k0 = {};
  k0[0] = (c & 4) ? 0xeeeeeeeeu : 0x44444444u;
  k0[1] = cb + (B_P + (piece >> 1)) * 16384u + (piece & 1) * 4096u + g * 128u + (c & 7) * 16u + 2048u;
  k0[2] = k0[1] - 2048u + (c >> 3) * 512u;
  k0[3] = cb + (B_M + cg) * 16384u + w * 2048u + l * 16u;
  k0[4] = O_U + (cg * 32u + 8u * w) * 1024u + l * 16u;
  k0[5] = k0[4] + 4096u;
  k0[6] = F_U + (8u * w + (l & 7)) * FL;
  k0[7] = lb + ((g * 2u + ((c >> 1) & 1)) * 8u + 2u * (c >> 2) + (c & 1)) * 16u + w * 4u;
  k0[8] = lb + l * 16u;
  k0[9] = cg * 256u + rg * 128u + (l >> 3) * 16u;

  unsigned s = 0, mem = rfl(cb + B_MEM * 16384u), cap = rfl(alive ? SPIN_CAP : 0u), uo = 0, seen = 0;
  u32v t0, t1;
  A_DIAG_DECL;
#define V1_TAGS(d, a, b, c, OP) "v_bitop3_b32 v" d ", v" a ", v" b ", v" c " bitop3:" OP "\n"
#define V1_ZERO "v_mov_b32 v52, 0\nv_mov_b32 v53, 0\nv_mov_b32 v54, 0\nv_mov_b32 v55, 0\n" \
                "v_mov_b32 v56, 0\nv_mov_b32 v57, 0\nv_mov_b32 v58, 0\nv_mov_b32 v59, 0\n"
#define V1_LOAD_P \
      "buffer_load_dwordx4 v[40:43], v66, %[ws], 0 offen sc1\n" \
      "buffer_load_dwordx4 v[44:47], v66, %[ws], 0 offen offset:1024 sc1\n" \
      "buffer_load_dwordx4 v[48:51], v65, %[ws], 0 offen sc1\n"
#define V1_STEP(P, OP, MWANT, PWANT, NEG) \
      ".Ltop" P "%=:\n" \
      "v_cmp_ge_u32 vcc, %[s], v80\n" \
      "s_cbranch_vccnz .Luw" P "%=\n" \
      SLEEP(D_U) \
      "buffer_load_dwordx4 v[0:3], v68, %[ws], %[uo] offen sc1\n" \
      "buffer_load_dwordx4 v[4:7], v68, %[ws], %[uo] offen offset:1024 sc1\n" \
      "buffer_load_dwordx4 v[8:11], v68, %[ws], %[uo] offen offset:2048 sc1\n" \
      "buffer_load_dwordx4 v[12:15], v68, %[ws], %[uo] offen offset:3072 sc1\n" \
      "buffer_load_dwordx4 v[16:19], v69, %[ws], %[uo] offen sc1\n" \
      "buffer_load_dwordx4 v[20:23], v69, %[ws], %[uo] offen offset:1024 sc1\n" \
      "buffer_load_dwordx4 v[24:27], v69, %[ws], %[uo] offen offset:2048 sc1\n" \
      "buffer_load_dwordx4 v[28:31], v69, %[ws], %[uo] offen offset:3072 sc1\n" \
      "buffer_load_dword v80, v70, %[ws], 0 offen sc1\n" \
      "s_add_u32 %[uo], %[uo], 0x100000\n" \
      V1_ZERO \
      SLEEP(D_V1) \
      ".Lboth" P "%=:\n" \
      "buffer_load_dwordx4 v[32:35], v67, %[ws], 0 offen sc1\n" \
      "buffer_load_dwordx4 v[36:39], v67, %[ws], 0 offen offset:1024 sc1\n" \
      V1_LOAD_P \
      WV(3) \
      V1_TAGS("60", "32", "33", "34", OP) V1_TAGS("61", "35", "36", "37", OP) V1_TAGS("60", "60", "38", "39", OP) \
      V1_TAGS("60", "60", "61", "61", OP) \
      "v_and_b32 v60, 1, v60\n" \
      "v_cmp_ne_u32 s[80:81], " MWANT ", v60\n" \
      ".Lp" P "%=:\n" \
      WV(2) SMF("52:55", "40:43", "0:7", "v64") SMF("56:59", "40:43", "8:15", "v64") \
      V1_TAGS("60", "40", "41", "42", OP) \
      WV(1) SMF("52:55", "44:47", "16:23", "v64") SMF("56:59", "44:47", "24:31", "v64") \
      V1_TAGS("60", "60", "43", "44", OP) V1_TAGS("61", "45", "46", "47", OP) \
      WV(0) SMF("52:55", "48:51", "32:39", "v64") \
      V1_TAGS("60", "60", "48", "49", OP) V1_TAGS("61", "61", "50", "51", OP) V1_TAGS("60", "60", "61", "61", OP) \
      "v_and_b32 v60, s82, v60\n" \
      "v_cmp_ne_u32 vcc, " PWANT ", v60\n" \
      "s_or_b64 vcc, vcc, s[80:81]\n" \
      "s_cbranch_scc1 .Lre" P "%=\n" \
      A_STAMP("s[86:87]") A_CLK("s[70:71]") \
      "s_nop 0\n" \
      "v_permlane32_swap_b32 v56, v54\n" \
      "v_permlane32_swap_b32 v57, v55\n" \
      "v_add_f32 v60, v52, v54\n" \
      "v_add_f32 v62, v58, v56\n" \
      "v_add_f32 v61, v53, v55\n" \
      "v_add_f32 v63, v59, v57\n" \
      "v_cndmask_b32 v60, v60, v62, s[84:85]\n" \
      "v_cndmask_b32 v61, v61, v63, s[84:85]\n" \
      "s_cmp_lg_u32 %[w], 0\n" \
      "s_nop 0\n" \
      "v_permlane16_swap_b32 v60, v61\n" \
      "v_add_f32 v60, v60, v61\n" \
      "ds_write_b32 v71, v60 offset:" P "*1024\n" \
      A_CLK("s[72:73]") \
      "s_waitcnt lgkmcnt(0)\n" \
      "s_barrier\n" \
      "s_cbranch_scc1 .Lend" P "%=\n" \
      A_CLK("s[74:75]") \
      "ds_read_b128 v[52:55], v72 offset:" P "*1024\n" \
      "s_waitcnt lgkmcnt(0)\n" \
      A_CLK("s[76:77]") \
      "v_add_f32 v52, v52, v53\n" \
      "v_add_f32 v54, v54, v55\n" \
      "v_add_f32_e64 v52, " NEG "v52, " NEG "v54\n" \
      "s_nop 1\n" \
      "v_mov_b32_dpp v53, v52 quad_perm:[1,0,3,2]" DPP_ALL \
      "v_cvt_pk_bf16_f32 v56, v52, v53\n" \
      "s_nop 1\n" \
      "v_mov_b32_dpp v57, v56 row_shl:2" DPP_ALL \
      "v_mov_b32_dpp v58, v56 row_shl:4" DPP_ALL \
      "v_mov_b32_dpp v59, v56 row_shl:6" DPP_ALL \
      "s_mov_b64 exec, s[90:91]\n" \
      "buffer_store_dwordx4 v[56:59], v73, %[ws], %[mem] offen sc1\n" \
      "s_mov_b64 exec, -1\n" \
      A_STAMP("s[88:89]") A_CLK("s[78:79]") A_SAVE("s86", 1) A_SAVE("s88", 2) A_SAVECLK \
      ".Lend" P "%=:\n" \
      "s_barrier\n" \
      A_NEXT \
      "s_add_u32 %[mem], %[mem], 0x4000\n" \
      "s_add_u32 %[s], %[s], 1\n"
#define V1_SIDE(P) \
      ".Luw" P "%=:\n" \
      "buffer_load_dword v80, v70, %[ws], 0 offen sc1\n" \
      "s_sub_u32 %[cap], %[cap], 1\n" \
      "s_cbranch_scc1 .Lexit%=\n" \
      "s_waitcnt vmcnt(0)\n" \
      "s_branch .Ltop" P "%=\n" \
      ".Lre" P "%=:\n" \
      "s_sub_u32 %[cap], %[cap], 1\n" \
      "s_cbranch_scc1 .Lexit%=\n" \
      "s_cmp_lg_u64 s[80:81], 0\n" \
      V1_ZERO \
      "s_cbranch_scc1 .Lboth" P "%=\n" \
      V1_LOAD_P \
      "s_branch .Lp" P "%=\n"
  asm volatile(
      "s_mov_b32 s82, 0x80008000\n"
      "s_mov_b32 s84, 0\n"
      "s_mov_b32 s85, -1\n"
      "s_mov_b32 s90, 0x01010101\n"
      "s_mov_b32 s91, 0x01010101\n"
      V1_STEP("0", "0xfe", "0", "0", "")
      "s_cmp_ge_u32 %[s], 63\n"
      "s_cbranch_scc1 .Lexit%=\n"
      V1_STEP("1", "0x80", "1", "s82", "-")
      "s_branch .Ltop0%=\n"
      V1_SIDE("0") V1_SIDE("1")
      ".Lexit%=:\n"
      : [s] "+s"(s), [mem] "+s"(mem), [cap] "+s"(cap), [uo] "+s"(uo), "=&{v[0:31]}"(t0), "=&{v[32:63]}"(t1),
        "+{v80}"(seen) A_DIAG_OUT
      : [ws] "s"(srd(p.ws, WS_BYTES)), [w] "s"(w), "{v[64:79]}"(k0)
      : A_SCLOB A_DIAG_CLOB);
}

DEV void load_x(rsrc_t xr, unsigned w, unsigned c, unsigned g, int s, u4 (&xp)[4]) {
  const unsigned voff = ((16u * (w >> 2) + c) * 512u + 128u * (w & 3) + 8u * g) * 2u;
#pragma unroll
  for (int i = 0; i < 4; ++i) xp[i] = ld16(xr, voff + i * 64u, s * 32768u);
}
DEV void put_x(char* sm, unsigned base, unsigned w, unsigned l, const u4 (&xp)[4]) {
#pragma unroll
  for (int i = 0; i < 4; ++i) lds16(sm, base + (4u * w + i) * 1024u + l * 16u) = xp[i];
}

DEV void token_softmax(rsrc_t ws, char* sm, unsigned qb, const u4 (&km)[4], unsigned h, unsigned w, unsigned l, int s) {
  const unsigned c = l & 15, g = l >> 4;
  u4 q[4], k0[4], k1[4];
#pragma unroll
  for (int m = 0; m < 4; ++m) {
    q[m] = lds16(sm, qb + (w * 4u + m) * 1024u);
    k0[m] = lds16(sm, qb + 8192u + m * 1024u);
    k1[m] = lds16(sm, qb + 12288u + m * 1024u);
  }
  sched();
  f4 s0 = {0, 0, 0, 0}, s1 = {0, 0, 0, 0}, sm_ = {0, 0, 0, 0};
#pragma unroll
  for (int m = 0; m < 4; ++m) {
    s0 = mma(k0[m], q[m], s0);
    s1 = mma(k1[m], q[m], s1);
  }
#pragma unroll
  for (int m = 0; m < 4; ++m) sm_ = mma(km[m], q[m], sm_);
  const unsigned tq = 16u * w + c;
  const float ninf = -__builtin_inff();
#pragma unroll
  for (int r = 0; r < 4; ++r) {
    s0[r] = 4u * g + r <= tq ? s0[r] : ninf;
    s1[r] = 16u + 4u * g + r <= tq ? s1[r] : ninf;
  }
  float mx = fmaxf(fmaxf(fmaxf(s0[0], s0[1]), fmaxf(s0[2], s0[3])), fmaxf(fmaxf(s1[0], s1[1]), fmaxf(s1[2], s1[3])));
  mx = group_max(fmaxf(mx, fmaxf(sm_[0], sm_[1])));
  const float mc = -mx * SCALE_LOG2E;
  float e[10];
#pragma unroll
  for (int r = 0; r < 4; ++r) {
    e[r] = __builtin_amdgcn_exp2f(fmaf(s0[r], SCALE_LOG2E, mc));
    e[4 + r] = __builtin_amdgcn_exp2f(fmaf(s1[r], SCALE_LOG2E, mc));
  }
  e[8] = __builtin_amdgcn_exp2f(fmaf(sm_[0], SCALE_LOG2E, mc));
  e[9] = __builtin_amdgcn_exp2f(fmaf(sm_[1], SCALE_LOG2E, mc));
  const float sum = group_sum(((e[0] + e[1]) + (e[2] + e[3])) + ((e[4] + e[5]) + (e[6] + e[7])) + (e[8] + e[9]));
  const float rs = __builtin_amdgcn_rcpf(sum);
  const u4 px = {pk(e[0] * rs, e[1] * rs), pk(e[2] * rs, e[3] * rs), pk(e[4] * rs, e[5] * rs), pk(e[6] * rs, e[7] * rs)};
  st16c(px, ws, O_PX + (w * 32u + h) * 1024u + l * 16u, s * 65536u);
  const unsigned dm = pk(e[8] * rs, e[9] * rs);
  auto r16 = __builtin_amdgcn_permlane16_swap(dm, dm, false, false);
  auto ra = __builtin_amdgcn_permlane32_swap(r16[0], r16[0], false, false);
  auto rb = __builtin_amdgcn_permlane32_swap(r16[1], r16[1], false, false);
  const u4 pm = {ra[0], rb[0], ra[1], rb[1]};
  st16c(pm, ws, g == 0 ? O_PM + (w * 8u + (h >> 2)) * 1024u + ((h & 3) * 16u + c) * 16u : OOB, s * 16384u);
}

DEV void role_s2(const Params& p, unsigned h, char* sm) {
  constexpr int LAG = 4, R = LAG + 1;
  constexpr unsigned L_X = 0, L_QK = 65536;
  const unsigned l = threadIdx.x & 63, w = rfl(threadIdx.x >> 6), c = l & 15, g = l >> 4;
  const rsrc_t ws = mk(p.ws, WS_BYTES), xr = mk(p.bx, 2u << 20);
  bool alive = true;

  u4 xp[4], wa[16], wb[16];
  {
    const rsrc_t wr = mk(w < 4 ? p.wq : p.wk, 4u << 20);
    Hs hs = hs_begin(ws);
    u4 d0[16], d1[16];
    stage_slice(wr, h, 64u * (w & 3), l, d0);
    fence();
    hs_post(ws, hs, sm);
    fence();
    stage_slice(wr, h, 64u * (w & 3) + 256u, l, d1);
    load_x(xr, w, c, g, 0, xp);
    unsigned echo;
    const unsigned fa = (w >> 2) * 65536u + frag_base(256u, 2u * (w & 3), l), fb = fa ^ 32u;
#pragma unroll
    for (int k = 0; k < 2; ++k) {
      put_image(sm, w, l, k ? d1 : d0);
      bar();
#pragma unroll
      for (int i = 0; i < 8; ++i) {
        wa[8 * k + i] = frag(sm, fa + i * 8192u, 256u);
        wb[8 * k + i] = frag(sm, fb + i * 8192u, 256u);
      }
      if (k == 0) echo = hs_echo(ws);
      else hs_wait(ws, sm, echo, 1u, alive);
      bar();
    }
    hs_ack(ws, hs, 1u);
  }
  put_x(sm, L_X, w, l, xp);
  load_x(xr, w, c, g, 1, xp);
  bar();
  STAMP(15, 8 + w);
  if (w < 4) __builtin_amdgcn_s_setprio(1);

  const unsigned km_off = w < 2 ? O_KM + h * 2048u + (g * 8u + ((2u * (c >> 2) + (c & 1)) ^ swz(g))) * 16u : OOB;
  const unsigned km_flag = w < 2 ? F_KM + h * FL : OOB;
  const unsigned px_flag = w < 2 && l == 0 ? F_PX + (w * 32u + h) * FL : OOB;
  const unsigned kx_flag = threadIdx.x == 448 ? F_KX + h * FL : OOB;
  const unsigned kx_off = w >= 4 ? O_KX + h * 8192u + (w & 3) * 2048u + l * 16u : OOB;
  unsigned km_seen = 0;
  int slot = 0;
  for (int it = 0; it < NSEG; ++it) {
    const int s = max(it - LAG, 0);
    const bool soft = it >= LAG && w < 2;
    if (soft && !all_lanes(km_seen > (unsigned)s)) wait_flags(ws, km_flag, s + 1, alive);
    if (w == 0 || w == 4) STAMP(3 + (w >> 2) * 4, it);
    u4 km[4];
#pragma unroll
    for (int m = 0; m < 4; ++m) km[m] = ld16c(ws, km_off + m * 512u, s * 65536u);
    km_seen = ld4c(ws, km_flag, 0);
    f4 acc[2][2] = {};
    const unsigned xb = L_X + (it & 1) * 32768u + l * 16u;
    u4 x[3][2];
#pragma unroll
    for (int k = 0; k < 2; ++k)
#pragma unroll
      for (int t = 0; t < 2; ++t) x[k][t] = lds16(sm, xb + (t * 16 + k) * 1024u);
#pragma unroll
    for (int ks = 0; ks < 16; ++ks) {
      sched();
      if (ks + 2 < 16) {
#pragma unroll
        for (int t = 0; t < 2; ++t) x[(ks + 2) % 3][t] = lds16(sm, xb + (t * 16 + ks + 2) * 1024u);
      }
      sched();
#pragma unroll
      for (int t = 0; t < 2; ++t) {
        acc[0][t] = mma(wa[ks], x[ks % 3][t], acc[0][t]);
        acc[1][t] = mma(wb[ks], x[ks % 3][t], acc[1][t]);
      }
    }
    sched();
    asm volatile("s_waitcnt vmcnt(6)" ::: "memory");
    if (w == 0 || w == 4) STAMP(4 + (w >> 2) * 4, it);
    st4c(s, ws, px_flag, 0);
    put_x(sm, L_X + ((it + 1) & 1) * 32768u, w, l, xp);
#pragma unroll
    for (int t = 0; t < 2; ++t) {
      const u4 o = {pk(acc[0][t][0], acc[1][t][0]), pk(acc[0][t][1], acc[1][t][1]), pk(acc[0][t][2], acc[1][t][2]),
                    pk(acc[0][t][3], acc[1][t][3])};
      lds16(sm, L_QK + slot * 16384u + (w >> 2) * 8192u + (t * 4u + (w & 3)) * 1024u + l * 16u) = o;
      st16c(o, ws, kx_off + t * 1024u, it * 262144u);
    }
    load_x(xr, w, c, g, it + 2, xp);
    if (w == 0 || w == 4) STAMP(5 + (w >> 2) * 4, it);
    slot = slot == R - 1 ? 0 : slot + 1;
    if (soft) {
      token_softmax(ws, sm, L_QK + slot * 16384u + l * 16u, km, h, w, l, s);
      if (w == 0) STAMP(2, s);
    }
    if (it < LAG) asm volatile("s_waitcnt vmcnt(4)" ::: "memory");
    bar();
    st4c(it < LAG ? it + 1 : it, ws, kx_flag, 0);
  }
  if (w >= 2) return;
  for (int s = NSEG - LAG; s < NSEG; ++s) {
    wait_flags(ws, km_flag, s + 1, alive);
    u4 km[4];
#pragma unroll
    for (int m = 0; m < 4; ++m) km[m] = ld16c(ws, km_off + m * 512u, s * 65536u);
    st4c(s, ws, px_flag, 0);
    slot = slot == R - 1 ? 0 : slot + 1;
    token_softmax(ws, sm, L_QK + slot * 16384u + l * 16u, km, h, w, l, s);
    if (w == 0) STAMP(2, s);
  }
  drain();
  st4c(NSEG, ws, px_flag, 0);
}

DEV void role_vu(const Params& p, unsigned h, char* sm) {
  constexpr unsigned L_X = 0, L_V = 65536;
  constexpr int EAGER = 3;
  const unsigned l = threadIdx.x & 63, w = rfl(threadIdx.x >> 6), c = l & 15, g = l >> 4;
  const rsrc_t ws = mk(p.ws, WS_BYTES), xr = mk(p.bx, 2u << 20);

  u4 xp[4], wv[16], wo[4][4];
  {
    bool alive = true;
    Hs hs = hs_begin(ws);
    u4 d0[16], d1[16];
    stage_slice(mk(p.wv, 4u << 20), h, 64u * w, l, d0);
    fence();
    hs_post(ws, hs, sm);
    fence();
    stage_wo(mk(p.wo, 4u << 20), h, w, l, d1);
    load_x(xr, w, c, g, 0, xp);
    put_image(sm, w, l, d0);
    bar();
    const unsigned fa = frag_base(256u, w, l);
#pragma unroll
    for (int f = 0; f < 16; ++f) wv[f] = frag(sm, fa + f * 8192u, 256u);
    const unsigned echo = hs_echo(ws);
    bar();
    put_image(sm, w, l, d1);
    bar();
    read_wo(sm, w, l, wo);
    hs_wait(ws, sm, echo, 1u, alive);
    hs_ack(ws, hs, 1u);
    bar();
  }
  put_x(sm, L_X, w, l, xp);
  load_x(xr, w, c, g, 1, xp);
  bar();
  STAMP(15, 8 + w);

  const unsigned v_off = L_V + ((w >> 1) * 64u + (2u * (w & 1) + (g >> 1)) * 16u + c) * 16u + (g & 1) * 8u;
  const unsigned flag = threadIdx.x == 448 ? F_U + h * FL : OOB;
  for (int s = 0; s < NSEG; ++s) {
    f4 acc[2] = {};
    const unsigned xb = L_X + (s & 1) * 32768u + l * 16u;
    u4 x[4][2];
#pragma unroll
    for (int k = 0; k < 3; ++k)
#pragma unroll
      for (int t = 0; t < 2; ++t) x[k][t] = lds16(sm, xb + (t * 16 + k) * 1024u);
#pragma unroll
    for (int ks = 0; ks < 16; ++ks) {
      sched();
      if (ks + 3 < 16) {
#pragma unroll
        for (int t = 0; t < 2; ++t) x[(ks + 3) & 3][t] = lds16(sm, xb + (t * 16 + ks + 3) * 1024u);
      }
      sched();
#pragma unroll
      for (int t = 0; t < 2; ++t) acc[t] = mma(wv[ks], x[ks & 3][t], acc[t]);
    }
    sched();
#pragma unroll
    for (int t = 0; t < 2; ++t)
      *reinterpret_cast<u2*>(sm + v_off + (s & 1) * 8192u + t * 4096u) = u2{pk(acc[t][0], acc[t][1]), pk(acc[t][2], acc[t][3])};
    if (w == 0) STAMP(3, s);
    asm volatile("s_waitcnt vmcnt(5)" ::: "memory");
    if (w == 0) STAMP(4, s);
    put_x(sm, L_X + ((s + 1) & 1) * 32768u, w, l, xp);
    load_x(xr, w, c, g, s + 2, xp);
    bar();
    st4c(max(s - 1, min(s, EAGER)), ws, flag, 0);
    if (w == 0) STAMP(5, s);
    u4 v[8];
#pragma unroll
    for (int k = 0; k < 8; ++k) v[k] = lds16(sm, L_V + (s & 1) * 8192u + ((k & 1) * 4 + (k >> 1)) * 1024u + l * 16u);
    sched();
    f4 u[4][2] = {};
#pragma unroll
    for (int k = 0; k < 8; ++k)
#pragma unroll
      for (int i = 0; i < 4; ++i) u[i][k & 1] = mma(v[k], wo[i][k >> 1], u[i][k & 1]);
#pragma unroll
    for (int i = 0; i < 4; ++i) {
      const u4 o = {pk(u[i][0][0], u[i][0][1]), pk(u[i][0][2], u[i][0][3]), pk(u[i][1][0], u[i][1][1]),
                    pk(u[i][1][2], u[i][1][3])};
      st16c(o, ws, O_U + ((4u * w + i) * 32u + h) * 1024u + l * 16u, s * 1048576u);
    }
    if (s < EAGER || s == NSEG - 1) {
      drain();
      bar();
      st4c(s + 1, ws, flag, 0);
    }
    if (w == 0) STAMP(2, s);
  }
}

DEV void role_v2(const Params& p, unsigned j, char* sm) {
  constexpr unsigned ROW = 144, PART = 16 * ROW, BUF = 8 * PART;
  const unsigned l = threadIdx.x & 63, w = rfl(threadIdx.x >> 6), c = l & 15, g = l >> 4;
  const unsigned b = j >> 1, qt = j & 1;
  const rsrc_t ws = mk(p.ws, WS_BYTES), outr = mk(p.out, 8u << 20);
  bool alive = true;

  const unsigned hq = 4u * w + (l & 3);
  const unsigned uf_off = F_U + hq * FL;
  const unsigned pf_off = (l & 4) ? F_M + hq * FL : F_PX + (qt * 32u + hq) * FL;
  const unsigned u_off = O_U + (2u * b * 32u + 4u * w) * 1024u + l * 16u;
  const unsigned m_off = O_M2 + 2u * b * 8192u + w * 1024u + l * 16u;
  const unsigned px_off = O_PX + (qt * 32u + 4u * w) * 1024u + l * 16u;
  const unsigned pm_off = O_PM + (qt * 8u + w) * 1024u + l * 16u;
  const unsigned out_off = ((16u * qt + (threadIdx.x >> 5)) * 1024u + 512u + 32u * b + (threadIdx.x & 31)) * 4u;
  {
    Hs hs = hs_begin(ws);
    hs_post(ws, hs, sm);
    bar();
    hs_wait(ws, sm, hs_echo(ws), 2u, alive);
    hs_ack(ws, hs, 1u);
  }
  unsigned u_seen = wait_flags(ws, uf_off, 1u, alive), p_seen = 0;
  u4 ua[2][4];
#pragma unroll
  for (int k = 0; k < 8; ++k) ua[k >> 2][k & 3] = ld16c(ws, u_off + (k >> 2) * 32768u + (k & 3) * 1024u, 0);

  for (int s = 0; s < NSEG; ++s) {
    if (!all_lanes(p_seen > (unsigned)s)) p_seen = wait_flags(ws, pf_off, s + 1, alive);
    u4 px[4];
#pragma unroll
    for (int i = 0; i < 4; ++i) px[i] = ld16c(ws, px_off + i * 1024u, s * 65536u);
    const u4 pm = ld16c(ws, pm_off, s * 16384u);
    const u4 m0 = ld16c(ws, m_off, s * 262144u), m1 = ld16c(ws, m_off + 8192u, s * 262144u);
    f4 a0 = {0, 0, 0, 0}, a1 = {0, 0, 0, 0};
#pragma unroll
    for (int i = 0; i < 4; ++i) {
      a0 = mma(ua[0][i], px[i], a0);
      a1 = mma(ua[1][i], px[i], a1);
    }
    a0 = mma(m0, pm, a0);
    a1 = mma(m1, pm, a1);
    sched();
    if (s + 1 < NSEG) {
      if (!all_lanes(u_seen > (unsigned)s + 1u)) u_seen = wait_flags(ws, uf_off, s + 2, alive);
#pragma unroll
      for (int k = 0; k < 8; ++k)
        ua[k >> 2][k & 3] = ld16c(ws, u_off + (k >> 2) * 32768u + (k & 3) * 1024u, (s + 1) * 1048576u);
    }
    sched();
    const unsigned rb = (s & 1) * BUF;
    *reinterpret_cast<f4*>(sm + rb + w * PART + c * ROW + g * 16u) = a0;
    *reinterpret_cast<f4*>(sm + rb + w * PART + c * ROW + g * 16u + 64u) = a1;
    bar();
    float v = 0.f;
#pragma unroll
    for (int k = 0; k < 8; ++k)
      v += *reinterpret_cast<const float*>(sm + rb + k * PART + (threadIdx.x >> 5) * ROW + (threadIdx.x & 31) * 4u);
    __builtin_amdgcn_raw_buffer_store_b32(__float_as_uint(rbf(v)), outr, out_off, s * 131072u, 0);
    if (w == 0) STAMP(2, s);
  }
}

DEV f2 cmul(f2 a, f2 b) {
  float re, im;
  asm("v_mul_f32 %0, %3, %5\n\tv_mul_f32 %1, %3, %4\n\tv_fma_f32 %0, %2, %4, -%0\n\tv_fma_f32 %1, %2, %5, %1"
      : "=&v"(re), "=&v"(im) : "v"(a[0]), "v"(a[1]), "v"(b[0]), "v"(b[1]));
  return f2{re, im};
}
DEV f2 cfma(f2 a, f2 b, f2 c) {
  float re, im;
  asm("v_fma_f32 %0, -%3, %5, %6\n\tv_fma_f32 %1, %3, %4, %7\n\tv_fma_f32 %0, %2, %4, %0\n\tv_fma_f32 %1, %2, %5, %1"
      : "=&v"(re), "=&v"(im) : "v"(a[0]), "v"(a[1]), "v"(b[0]), "v"(b[1]), "v"(c[0]), "v"(c[1]));
  return f2{re, im};
}
DEV f2 from_group(float re, float im, unsigned c, int k) {
  return f2{__shfl(re, (int)c + 16 * k, 64), __shfl(im, (int)c + 16 * k, 64)};
}

DEV void role_a(const Params& p, unsigned ci, char* sm) {
  constexpr unsigned GS = 272, KS = 4 * GS, TS = 32 * KS + 32, L_TAB = 4 * TS;
  const unsigned tid = threadIdx.x, l = tid & 63, w = rfl(tid >> 6), c = l & 15, g = l >> 4;
  const rsrc_t ws = mk(p.ws, WS_BYTES), xr = mk(p.ax, 2u << 20), outr = mk(p.out, 8u << 20);
  bool alive = true;
  {
    Hs hs = hs_begin(ws);
    hs_post(ws, hs, sm);
    if (w == 0) {
      hs_wait(ws, sm, hs_echo(ws), 1u, alive);
      hs_ack(ws, hs, 1u);
      wait_flags(ws, F_U + (l & 31) * FL, 1u, alive);
    }
    bar();
  }
  {
    const unsigned voff = ((64u * ci + 16u * (c >> 2) + 4u * (w >> 1) + (c & 3)) * 512u + 256u * (w & 1) + 8u * g) * 2u;
    u4 xp[8];
#pragma unroll
    for (int i = 0; i < 8; ++i) xp[i] = ld16(xr, voff + i * 64u, 0);
#pragma unroll
    for (int i = 0; i < 8; ++i) lds16(sm, (8u * w + i) * 1024u + l * 16u) = xp[i];
  }
  f2 lam64;
  {
    const float lr = bf2f(((const unsigned short*)p.lre)[tid]), li = bf2f(((const unsigned short*)p.lim)[tid]);
    const float dt = rbf(expf(bf2f(((const unsigned short*)p.ldt)[tid])));
    const float er = expf(dt * lr);
    float sn, cs;
    sincosf(dt * li, &sn, &cs);
    f2 lam = {er * cs, er * sn};
    asm volatile("s_nop 1" : "+v"(lam));
    const float nr = lam[0] - 1.0f, ni = lam[1];
    f2 coef;
    if (fabsf(lr) >= fabsf(li)) {
      const float t = li / lr, d = 1.0f / (lr + li * t);
      coef = f2{smul(nr + smul(ni, t), d), smul(ni - smul(nr, t), d)};
    } else {
      const float t = lr / li, d = 1.0f / (lr * t + li);
      coef = f2{smul(smul(nr, t) + ni, d), smul(smul(ni, t) - nr, d)};
    }
    f2 l16 = cmul(lam, lam);
    l16 = cmul(l16, l16);
    l16 = cmul(l16, l16);
    l16 = cmul(l16, l16);
    lam64 = cmul(l16, l16);
    lam64 = cmul(lam64, lam64);
    *reinterpret_cast<f4*>(sm + L_TAB + tid * 32u) = f4{lam[0], lam[1], coef[0], coef[1]};
    *reinterpret_cast<f2*>(sm + L_TAB + tid * 32u + 16u) = l16;
  }
  bar();

  f4 hr[4][4] = {}, hi[4][4] = {};
  {
    const rsrc_t br = mk(p.bre, 1u << 19), bi = mk(p.bim, 1u << 19);
    const unsigned voff = ((64u * w + c) * 512u + 8u * g) * 2u;
    u4 fr[4], fi[4];
#pragma unroll
    for (int k = 0; k < 4; ++k) {
      fr[k] = ld16(br, voff + k * 64u, 0);
      fi[k] = ld16(bi, voff + k * 64u, 0);
    }
#pragma unroll
    for (int n = 0; n < 64; ++n) {
      const int ct = n >> 4, ks = n & 15;
      const u4 r = fr[n & 3], i = fi[n & 3];
      fence();
      if (n + 4 < 64) {
        fr[n & 3] = ld16(br, voff + ((n + 4) & 15) * 64u, ((n + 4) >> 4) * 16384u);
        fi[n & 3] = ld16(bi, voff + ((n + 4) & 15) * 64u, ((n + 4) >> 4) * 16384u);
      }
#pragma unroll
      for (int t = 0; t < 4; ++t) {
        const u4 x = lds16(sm, (t * 16 + ks) * 1024u + l * 16u);
        hr[ct][t] = mma(x, r, hr[ct][t]);
        hi[ct][t] = mma(x, i, hi[ct][t]);
      }
    }
  }
  __builtin_amdgcn_sched_barrier(0);
  asm volatile("s_nop 7\n\ts_nop 7" ::: "memory");
  __builtin_amdgcn_sched_barrier(0);
#pragma unroll
  for (int ct = 0; ct < 4; ++ct) {
    const unsigned ch = 64u * w + 16u * ct + c;
    const f4 tb = *reinterpret_cast<const f4*>(sm + L_TAB + ch * 32u);
    const f2 l16 = *reinterpret_cast<const f2*>(sm + L_TAB + ch * 32u + 16u);
    const f2 lam = {tb[0], tb[1]}, coef = {tb[2], tb[3]};
    f2 hh = {0.f, 0.f};
#pragma unroll
    for (int i = 0; i < 16; ++i) {
      hh = cfma(lam, hh, cmul(coef, f2{hr[ct][i >> 2][i & 3], hi[ct][i >> 2][i & 3]}));
      hr[ct][i >> 2][i & 3] = hh[0];
      hi[ct][i >> 2][i & 3] = hh[1];
    }
    f2 e = from_group(hh[0], hh[1], c, 0);
#pragma unroll
    for (int k = 1; k < 4; ++k) e = cfma(l16, e, from_group(hh[0], hh[1], c, k));
    if (g == 0) st8c(__builtin_bit_cast(u2, e), ws, O_AE + ci * 4096u + ch * 8u, 0);
  }
  drain();
  bar();
  if (tid == 0) st4c(1u, ws, F_A + ci * FL, 0);
  wait_flags(ws, F_A + (l & 31) * FL, (l & 31) < ci ? 1u : 0u, alive);
  {
    f2 hin = {0.f, 0.f};
#pragma unroll 8
    for (unsigned k = 0; k < ci; ++k) hin = cfma(lam64, hin, __builtin_bit_cast(f2, ld8c(ws, O_AE + tid * 8u, k * 4096u)));
    *reinterpret_cast<f2*>(sm + L_TAB + tid * 32u + 24u) = hin;
  }
  bar();
#pragma unroll
  for (int ct = 0; ct < 4; ++ct) {
    const unsigned ch = 64u * w + 16u * ct + c;
    const f4 t0 = *reinterpret_cast<const f4*>(sm + L_TAB + ch * 32u);
    const f4 t1 = *reinterpret_cast<const f4*>(sm + L_TAB + ch * 32u + 16u);
    const f2 lam = {t0[0], t0[1]}, l16 = {t1[0], t1[1]};
    f2 z = {t1[2], t1[3]};
#pragma unroll
    for (int k = 0; k < 3; ++k) {
      const f2 n = cfma(l16, z, from_group(hr[ct][3][3], hi[ct][3][3], c, k));
      z = (unsigned)k < g ? n : z;
    }
    const unsigned hb = g * TS + (ch >> 5) * KS + ((ch >> 3) & 3) * GS + (ch & 7) * 2u;
#pragma unroll
    for (int i = 0; i < 16; ++i) {
      z = cmul(lam, z);
      const unsigned d = pk(2.0f * (hr[ct][i >> 2][i & 3] + z[0]), -2.0f * (hi[ct][i >> 2][i & 3] + z[1]));
      lds_put2(sm, hb + i * 16u, d);
      lds_put2(sm, hb + 16u * KS + i * 16u, d >> 16);
    }
  }
  bar();
  f4 y[4][4] = {};
  {
    const rsrc_t cr = mk(p.cre, 1u << 19), cm = mk(p.cim, 1u << 19);
    const unsigned voff = ((64u * w + c) * 512u + 8u * g) * 2u;
    u4 fa[2][4];
#pragma unroll
    for (int k = 0; k < 2; ++k)
#pragma unroll
      for (int d = 0; d < 4; ++d) fa[k][d] = ld16(cr, voff + k * 64u, d * 16384u);
#pragma unroll
    for (int ks = 0; ks < 32; ++ks) {
      u4 a[4];
      fence();
#pragma unroll
      for (int d = 0; d < 4; ++d) {
        a[d] = fa[ks & 1][d];
        if (ks + 2 < 32) fa[ks & 1][d] = ld16(ks + 2 < 16 ? cr : cm, voff + ((ks + 2) & 15) * 64u, d * 16384u);
      }
#pragma unroll
      for (int t = 0; t < 4; ++t) {
        const u4 hb = lds16(sm, t * TS + ks * KS + g * GS + c * 16u);
#pragma unroll
        for (int d = 0; d < 4; ++d) y[d][t] = mma(a[d], hb, y[d][t]);
      }
    }
  }
  const rsrc_t dr = mk(p.dsk, 1024);
#pragma unroll
  for (int d = 0; d < 4; ++d) {
    const unsigned col = 64u * w + 16u * d + 4u * g;
    const u2 dk = ld8(dr, col * 2u, 0);
#pragma unroll
    for (int t = 0; t < 4; ++t) {
      const unsigned row = 64u * ci + 16u * t + c;
      const u2 xv = ld8(xr, (row * 512u + col) * 2u, 0);
      const f4 o = {y[d][t][0] + rbf(smul(bf2f(xv[0] & 0xffffu), bf2f(dk[0] & 0xffffu))),
                    y[d][t][1] + rbf(smul(bf2f(xv[0] >> 16), bf2f(dk[0] >> 16))),
                    y[d][t][2] + rbf(smul(bf2f(xv[1] & 0xffffu), bf2f(dk[1] & 0xffffu))),
                    y[d][t][3] + rbf(smul(bf2f(xv[1] >> 16), bf2f(dk[1] >> 16)))};
      __builtin_amdgcn_raw_buffer_store_b128(__builtin_bit_cast(u4, o), outr, (row * 1024u + col) * 4u, 0, 0);
    }
  }
  if (w == 0) STAMP(2, 0);
}

__global__ void __launch_bounds__(512, 2) fused_kernel(Params p) {
  extern __shared__ __attribute__((aligned(16))) char smem[];
  unsigned i;
  const unsigned role = role_of(blockIdx.x, i);
  if (threadIdx.x < 64) TSTAMP(0);
  switch (role) {
    case R_S1: role_s1(p, i, smem); break;
    case R_VM: role_vm(p, i, smem); break;
    case R_V1: role_v1(p, i, smem); break;
    case R_S2: role_s2(p, i, smem); break;
    case R_VU: role_vu(p, i, smem); break;
    case R_V2: role_v2(p, i, smem); break;
    case R_A: role_a(p, i, smem); break;
  }
  if (threadIdx.x < 64) TSTAMP(2);
}

extern "C" void kernel_launch(void* const* d_in, const int* in_sizes, int n_in, void* d_out, int out_size, void* d_ws,
                              size_t ws_size, hipStream_t stream) {
  Params p;
  p.ax = d_in[0]; p.lre = d_in[1]; p.lim = d_in[2]; p.bre = d_in[3]; p.bim = d_in[4]; p.cre = d_in[5];
  p.cim = d_in[6]; p.ldt = d_in[7]; p.dsk = d_in[8]; p.bx = d_in[9]; p.mem0 = d_in[10]; p.wq = d_in[11];
  p.wk = d_in[12]; p.wv = d_in[13]; p.wo = d_in[14];
  p.out = (float*)d_out;
  p.ws = (char*)d_ws;
  (void)hipFuncSetAttribute((const void*)fused_kernel, hipFuncAttributeMaxDynamicSharedMemorySize, DYN_LDS);
  fused_kernel<<<256, 512, DYN_LDS, stream>>>(p);
}
```

```cpp
#include <hip/hip_runtime.h>
#include <hip/hip_bf16.h>
#include <stdint.h>

typedef __attribute__((ext_vector_type(2))) unsigned u2;
typedef __attribute__((ext_vector_type(4))) unsigned u4;
typedef __attribute__((ext_vector_type(8))) unsigned u8;
typedef __attribute__((ext_vector_type(16))) unsigned u16v;
typedef __attribute__((ext_vector_type(32))) unsigned u32v;
typedef __attribute__((ext_vector_type(2))) float f2;
typedef __attribute__((ext_vector_type(4))) float f4;
typedef __attribute__((ext_vector_type(2))) __bf16 bf2;
typedef __attribute__((ext_vector_type(8))) __bf16 bf8;
typedef __attribute__((ext_vector_type(16))) __bf16 bf16v;
typedef __amdgpu_buffer_rsrc_t rsrc_t;
#define DEV __device__ __forceinline__

struct Params {
  const void *ax, *lre, *lim, *bre, *bim, *cre, *cim, *ldt, *dsk, *bx, *mem0, *wq, *wk, *wv, *wo;
  float* out;
  char* ws;
};

#define WSLEEP 4
constexpr int NSEG = 64, LAST = NSEG - 1;
#define NFAST 3
constexpr int FAST = NSEG - NFAST;
constexpr unsigned SPIN_CAP = 400000u;
constexpr unsigned DYN_LDS = 156u * 1024u;

constexpr unsigned FL = 128, F_KM = 0, F_PX = 32 * FL, F_M = 96 * FL, F_A = 128 * FL, F_KX = 192 * FL, F_U = 320 * FL,
                   N_FLAGS = 576;
constexpr unsigned H_X = N_FLAGS * FL, H_Y = H_X + 256 * FL, H_END = H_Y + 256 * FL;
constexpr unsigned O_AE = 5u << 20, O_DIAG = 6u << 20;
constexpr unsigned O_KX = 8u << 20, O_KM = 24u << 20, O_PX = 28u << 20, O_PM = 32u << 20, O_M2 = 34u << 20,
                   O_U = 50u << 20, WS_BYTES = 114u << 20;
constexpr unsigned OOB = 1u << 30;
constexpr unsigned B_MEM = 0, B_P = 63, B_M = 67, B_END = 99, B_PROBE = 100;

enum { R_S1, R_VM, R_V1, R_S2, R_VU, R_V2, R_A };
__host__ __device__ inline unsigned role_of(unsigned b, unsigned& i) {
  const unsigned x = b & 7, r = b >> 3;
  if (x >= 4) {
    i = (x - 4) * 8u + (r >> 2);
    return R_S2 + (r & 3);
  }
  if (x == 1 || x == 2) {
    i = (x - 1) * 32u + r;
    return R_V1;
  }
  i = (x ? 16u : 0u) + (r >> 1);
  return r & 1;
}

DEV rsrc_t mk(const void* p, unsigned bytes) {
  return __builtin_amdgcn_make_buffer_rsrc((void*)p, 0, bytes, 0x00020000);
}
DEV unsigned rfl(unsigned v) { return __builtin_amdgcn_readfirstlane(v); }
DEV u4 ld16(rsrc_t r, unsigned v, unsigned s) { return __builtin_amdgcn_raw_buffer_load_b128(r, v, s, 0); }
#define WAUX 0
DEV u4 ld16w(rsrc_t r, unsigned v, unsigned s) { return __builtin_amdgcn_raw_buffer_load_b128(r, v, s, WAUX); }
DEV u2 ld8(rsrc_t r, unsigned v, unsigned s) { return __builtin_amdgcn_raw_buffer_load_b64(r, v, s, 0); }
DEV unsigned ld4(rsrc_t r, unsigned v, unsigned s) { return __builtin_amdgcn_raw_buffer_load_b32(r, v, s, 0); }
DEV unsigned ld2(rsrc_t r, unsigned v, unsigned s) { return __builtin_amdgcn_raw_buffer_load_b16(r, v, s, 0); }
DEV u4 ld16c(rsrc_t r, unsigned v, unsigned s) { return __builtin_amdgcn_raw_buffer_load_b128(r, v, s, 16); }
DEV u2 ld8c(rsrc_t r, unsigned v, unsigned s) { return __builtin_amdgcn_raw_buffer_load_b64(r, v, s, 16); }
DEV unsigned ld4c(rsrc_t r, unsigned v, unsigned s) { return __builtin_amdgcn_raw_buffer_load_b32(r, v, s, 16); }
DEV void st16c(u4 d, rsrc_t r, unsigned v, unsigned s) { __builtin_amdgcn_raw_buffer_store_b128(d, r, v + s, 0, 16); }
DEV void st8c(u2 d, rsrc_t r, unsigned v, unsigned s) { __builtin_amdgcn_raw_buffer_store_b64(d, r, v, s, 16); }
DEV void st4c(unsigned d, rsrc_t r, unsigned v, unsigned s) { __builtin_amdgcn_raw_buffer_store_b32(d, r, v, s, 16); }
DEV void fence() { asm volatile("" ::: "memory"); }
DEV void drain() { asm volatile("s_waitcnt vmcnt(0)" ::: "memory"); }
DEV void bar() { asm volatile("s_waitcnt lgkmcnt(0)\n\ts_barrier" ::: "memory"); }
DEV bool all_lanes(bool ok) { return __builtin_amdgcn_ballot_w64(!ok) == 0ull; }

DEV float bf2f(unsigned h) { return __uint_as_float(h << 16); }
DEV unsigned pk(float a, float b) {
  f2 v = {a, b};
  return __builtin_bit_cast(unsigned, __builtin_convertvector(v, bf2));
}
DEV float rbf(float a) { return bf2f(pk(a, a) & 0xffffu); }
DEV float smul(float a, float b) {
  float r;
  asm("v_mul_f32 %0, %1, %2" : "=v"(r) : "v"(a), "v"(b));
  return r;
}
DEV float sadd(float a, float b) {
  float r;
  asm("v_add_f32 %0, %1, %2" : "=v"(r) : "v"(a), "v"(b));
  return r;
}
DEV f4 mma(u4 a, u4 b, f4 c) {
  return __builtin_amdgcn_mfma_f32_16x16x32_bf16(__builtin_bit_cast(bf8, a), __builtin_bit_cast(bf8, b), c, 0, 0, 0);
}
DEV f4 smma(u4 a, u8 b, f4 c, unsigned idx) {
  return __builtin_amdgcn_smfmac_f32_16x16x64_bf16(__builtin_bit_cast(bf8, a), __builtin_bit_cast(bf16v, b), c,
                                                   (int)idx, 0, 0);
}
DEV float swap32_add(float a, float b) {
  auto r = __builtin_amdgcn_permlane32_swap(__float_as_uint(a), __float_as_uint(b), false, false);
  return __uint_as_float(r[0]) + __uint_as_float(r[1]);
}
DEV float swap16_add(float a, float b) {
  auto r = __builtin_amdgcn_permlane16_swap(__float_as_uint(a), __float_as_uint(b), false, false);
  return __uint_as_float(r[0]) + __uint_as_float(r[1]);
}
DEV float group_max(float m) {
  auto r = __builtin_amdgcn_permlane16_swap(__float_as_uint(m), __float_as_uint(m), false, false);
  m = fmaxf(__uint_as_float(r[0]), __uint_as_float(r[1]));
  auto q = __builtin_amdgcn_permlane32_swap(__float_as_uint(m), __float_as_uint(m), false, false);
  return fmaxf(__uint_as_float(q[0]), __uint_as_float(q[1]));
}
DEV float group_sum(float m) {
  const float t = swap16_add(m, m);
  return swap32_add(t, t);
}
DEV u4& lds16(char* sm, unsigned off) { return *reinterpret_cast<u4*>(sm + off); }
DEV void lds_put2(char* sm, unsigned off, unsigned v) { *reinterpret_cast<unsigned short*>(sm + off) = (unsigned short)v; }

#define STAMP(slot, idx) do { } while (0)

#define TSTAMP(idx) do { } while (0)

DEV unsigned wait_flags(rsrc_t ws, unsigned off, unsigned need, bool& alive) {
  unsigned v = 0;
  for (unsigned it = 0; alive; ++it) {
    fence();
    v = ld4c(ws, off, 0);
    if (all_lanes(v >= need)) break;
    if (it > SPIN_CAP) alive = false;
    __builtin_amdgcn_s_sleep(WSLEEP);
  }
  fence();
  return v;
}
DEV bool filled(u4 v) { return max(max(v[0], v[1]), max(v[2], v[3])) != ~0u; }
DEV unsigned crit_base(const Params& p) {
  const unsigned lo = (unsigned)(uintptr_t)p.ws;
  return ((lo + H_END + 0x1fffffu) & ~0x1fffffu) - lo;
}
struct Hs { unsigned x, a; };
constexpr unsigned L_HS = DYN_LDS - 16u;
DEV unsigned& lds4(char* sm, unsigned off) { return *reinterpret_cast<unsigned*>(sm + off); }
DEV Hs hs_begin(rsrc_t ws) {
  const unsigned o = threadIdx.x == 0 ? blockIdx.x * FL : OOB;
  return Hs{ld4c(ws, o + H_Y, 0), ld4c(ws, o + H_X + 4u, 0)};
}
DEV void hs_post(rsrc_t ws, Hs& h, char* sm) {
  h.x = (h.x & ~3u) + 4u;
  st4c(h.x, ws, threadIdx.x == 0 ? blockIdx.x * FL + H_X : OOB, 0);
  lds4(sm, threadIdx.x == 0 ? L_HS : L_HS + 4u) = h.x;
}
DEV unsigned hs_echo(rsrc_t ws) { return ld4c(ws, blockIdx.x * FL + H_Y, 0); }
DEV unsigned hs_wait(rsrc_t ws, char* sm, unsigned e, unsigned stage, bool& alive) {
  const unsigned x = rfl(lds4(sm, L_HS));
  unsigned v = rfl(e) - x;
  for (unsigned it = 0; alive && (v < stage || v > 3u); ++it) {
    if (it > SPIN_CAP) alive = false;
    __builtin_amdgcn_s_sleep(WSLEEP);
    fence();
    v = rfl(hs_echo(ws)) - x;
  }
  fence();
  return v;
}
DEV void hs_ack(rsrc_t ws, const Hs& h, unsigned d) {
  st4c(h.a + d, ws, threadIdx.x == 0 ? blockIdx.x * FL + H_X + 4u : OOB, 0);
}
DEV unsigned window(const Params& p, unsigned stage) { return crit_base(p) + (stage == 3u ? 8192u : 0u); }

DEV void stage_slice(rsrc_t r, unsigned h, unsigned row0, unsigned l, u4 (&d)[16]) {
  const unsigned v = (l >> 4) * 8192u + h * 256u + ((l & 15) ^ ((l >> 4) << 1)) * 16u;
#pragma unroll
  for (int i = 0; i < 16; ++i) d[i] = ld16w(r, v ^ ((i & 2) * 64u), (row0 + 4u * i) * 8192u);
}
DEV void stage_wo(rsrc_t r, unsigned h, unsigned w, unsigned l, u4 (&d)[16]) {
#pragma unroll
  for (int i = 0; i < 16; ++i)
    d[i] = ld16w(r, (l ^ (((i & 3) | ((i >> 1) & 4)) << 1)) * 16u, (h * 128u + w * 16u + i) * 1024u);
}
DEV void put_image(char* sm, unsigned w, unsigned l, const u4 (&d)[16]) {
#pragma unroll
  for (int i = 0; i < 16; ++i) lds16(sm, (16u * w + i) * 1024u + l * 16u) = d[i];
}
typedef __attribute__((ext_vector_type(4))) short s4;
DEV u2 lds_tr(char* sm, unsigned off) {
  return __builtin_bit_cast(u2, __builtin_amdgcn_ds_read_tr16_b64_v4i16((__attribute__((address_space(3))) s4*)(sm + off)));
}
DEV unsigned frag_base(unsigned pitch, unsigned cb, unsigned l) {
  const unsigned g = l >> 4, q = (l >> 2) & 3, p = l & 3;
  return (8u * g + q) * pitch + ((2u * cb + (p >> 1)) ^ ((q | ((g & 1) << 2)) << 1)) * 16u + (p & 1) * 8u;
}
DEV u4 frag(char* sm, unsigned off, unsigned pitch) {
  const u2 a = lds_tr(sm, off), b = lds_tr(sm, off + 4u * pitch);
  return u4{a[0], a[1], b[0], b[1]};
}
DEV u8 cat(u4 a, u4 b) { return __builtin_shufflevector(a, b, 0, 1, 2, 3, 4, 5, 6, 7); }
DEV u16v cat(u8 a, u8 b) { return __builtin_shufflevector(a, b, 0, 1, 2, 3, 4, 5, 6, 7, 8, 9, 10, 11, 12, 13, 14, 15); }
DEV u32v cat(u8 a, u8 b, u8 c, u8 d) {
  return __builtin_shufflevector(cat(a, b), cat(c, d), 0, 1, 2, 3, 4, 5, 6, 7, 8, 9, 10, 11, 12, 13, 14, 15, 16, 17, 18,
                                 19, 20, 21, 22, 23, 24, 25, 26, 27, 28, 29, 30, 31);
}
DEV u4 srd(const void* p, unsigned bytes) {
  return u4{(unsigned)(uintptr_t)p, (unsigned)((uintptr_t)p >> 32) & 0xffffu, bytes, 0x00020000u};
}
DEV unsigned lds_base(char* sm) { return (unsigned)(uintptr_t)(__attribute__((address_space(3))) char*)sm; }
DEV unsigned swz(unsigned g) { return (g & 1) | ((g & 2) << 1); }
DEV void sched() { __builtin_amdgcn_sched_barrier(0); }

DEV u4 load_mem0(const Params& p, unsigned w, unsigned c, unsigned g) {
  const rsrc_t r = mk(p.mem0, 8192);
  const unsigned voff = ((c >> 1) * 512u + 64u * w + 16u * g + 2u * (c & 1)) * 2u;
  u4 a;
#pragma unroll
  for (int i = 0; i < 4; ++i) a[i] = ld4(r, voff, 8 * i);
  return a;
}
constexpr float SCALE_LOG2E = 0.08838834764831845f * 1.4426950408889634f;

#define A_STEP
#define A_STAMP(r)
#define A_SAVE(r, slot)
#define A_NEXT
#define A_KXWAIT "s_waitcnt vmcnt(2)\n"
#define A_DIAG_OUT
#define A_DIAG_CLOB
#define A_CLK(r)
#define A_SAVECLK
#define A_DIAG_DECL
#define A_SCLOB "s80", "s81", "s82", "s83", "s84", "s85", "s86", "s87", "s88", "s89", "s90", "s91", "vcc", "scc", "memory"
#define SMF(d, a, b, i) "v_smfmac_f32_16x16x64_bf16 v[" d "], v[" a "], v[" b "], " i "\n"
#define MFM(d, a, b, c) "v_mfma_f32_16x16x32_bf16 v[" d "], v[" a "], v[" b "], " c "\n"
#define DPP_ALL " row_mask:0xf bank_mask:0xf\n"
#define A_POLL_MEM \
  ".Lpoll%=:\n" \
  "buffer_load_dwordx4 v[148:151], v129, %[ws], %[mem] offen sc1\n" \
  "s_sub_u32 %[cap], %[cap], 1\n" \
  "s_cbranch_scc1 .Lexit%=\n" \
  "s_waitcnt vmcnt(0)\n" \
  "ds_write_b128 v130, v[148:151]\n" \
  "v_max3_u32 v152, v148, v149, v150\n" \
  "v_max_u32 v152, v152, v151\n" \
  "v_cmp_eq_u32 vcc, -1, v152\n" \
  "s_cbranch_vccnz .Lpoll%=\n"
#define A_READ_MEM \
  "s_waitcnt lgkmcnt(0)\n" \
  "s_barrier\n" \
  A_CLK("s[72:73]") \
  "ds_read_b128 v[152:155], v131\n" \
  "ds_read_b128 v[156:159], v132\n" \
  "ds_read_b128 v[160:163], v133\n" \
  "ds_read_b128 v[164:167], v134\n" \
  "ds_read_b128 v[168:171], v135\n" \
  "ds_read_b128 v[172:175], v136\n" \
  "ds_read_b128 v[176:179], v137\n"
#define D_S1 0
#define D_S0 0
#define D_VM 0
#define D_V1 0
#define D_U 8
#define STR_(x) #x
#define SLEEP(x) "s_sleep " STR_(x) "\n"
#define WL(n) "s_waitcnt lgkmcnt(" #n ")\n"
#define WV(n) "s_waitcnt vmcnt(" #n ")\n"

DEV void role_s1(const Params& p, unsigned h, char* sm) {
  constexpr unsigned L_MEM = 131072, L_Q = L_MEM + 8192, L_K = L_MEM + 10240;
  const unsigned l = threadIdx.x & 63, w = rfl(threadIdx.x >> 6), c = l & 15, g = l >> 4;
  const rsrc_t ws = mk(p.ws, WS_BYTES);
  bool alive = true;
  STAMP(15, w);

  u8 wa[8], wb[8];
  u4 a0;
  unsigned cb, echo;
  {
    const rsrc_t wr = mk(w < 4 ? p.wq : p.wk, 4u << 20);
    Hs hs = hs_begin(ws);
    u4 d0[16], d1[16];
    stage_slice(wr, h, 64u * w, l, d0);
    fence();
    hs_post(ws, hs, sm);
    fence();
    stage_slice(wr, h, 64u * (w ^ 4u), l, d1);
    a0 = load_mem0(p, w, c, g);
    const unsigned fa = (w >> 2) * 65536u + frag_base(256u, 2u * (w & 3), l), fb = fa ^ 32u;
#pragma unroll
    for (int k = 0; k < 2; ++k) {
      put_image(sm, w, l, k ? d1 : d0);
      bar();
#pragma unroll
      for (int i = 0; i < 4; ++i) {
        const unsigned o = ((w ^ i) & 3) * 16384u;
        wa[4 * k + i] = cat(frag(sm, fa + o, 256u), frag(sm, fa + o + 8192u, 256u));
        wb[4 * k + i] = cat(frag(sm, fb + o, 256u), frag(sm, fb + o + 8192u, 256u));
      }
      if (k == 0) {
        bar();
        echo = hs_echo(ws);
      }
    }
    cb = window(p, hs_wait(ws, sm, echo, 2u, alive));
    hs_ack(ws, hs, 1u);
  }
  STAMP(15, 8 + w);

  const unsigned lb = lds_base(sm);
  u16v k0;
  u8 k1;
  k0[0] = (c & 1) ? 0xeeeeeeeeu : 0x44444444u;
  k0[1] = w * 1024u + l * 16u;
#pragma unroll
  for (int i = 0; i < 8; ++i) k0[2 + i] = lb + L_MEM + (w ^ i) * 1024u + l * 16u;
#pragma unroll
  for (int r = 0; r < 2; ++r)
    k0[10 + r] = lb + (w < 4 ? L_Q : L_K) + (((w & 3) * 4u + (c >> 2)) * 8u + ((2u * g + r) ^ swz(c >> 2))) * 16u + (c & 3) * 4u;
  k0[12] = k0[13] = 0;
  k0[14] = lb + L_Q + (g * 8u + ((c & 7) ^ swz(g))) * 16u;
  k0[15] = lb + L_K + (g * 8u + ((((c >> 2) & 1) * 4u + (c >> 3) * 2u + (c & 1)) ^ swz(g))) * 16u;
  const unsigned piece = ((c & 7) >> 2) * 4u + (h >> 3), hl = h & 7;
  const unsigned p_base = cb + (B_P + (piece >> 1)) * 16384u + (piece & 1) * 4096u;
  const unsigned tok = ((c >> 1) & 1) * 16u + (c >> 3) * 8u + ((c >> 2) & 1) * 2u + (c & 1);
  k1[0] = p_base + (hl >> 1) * 512u + (2u * (hl & 1) + (g >> 1)) * 128u + ((g & 1) * 4u + (c & 3)) * 16u;
  k1[1] = p_base + 2048u + (hl >> 1) * 128u + ((g >> 1) * 4u + (c & 3)) * 16u + (hl & 1) * 8u;
  k1[2] = O_KX + h * 8192u + ((tok >> 4) * 64u + g * 16u + (tok & 15)) * 16u;
  k1[3] = k1[2] + 4096u;
  k1[4] = w == 0 ? F_KX + (h * 4u + (l & 3)) * FL : OOB;
  k1[5] = lb + L_K + l * 16u;
  k1[6] = O_KM + h * 2048u + l * 16u;
  k1[7] = F_KM + h * FL;

  u32v kx = {};
  unsigned seen;
  if (w == 0) {
    wait_flags(ws, k1[4], 1u, alive);
    u4 f[8];
#pragma unroll
    for (int i = 0; i < 8; ++i) f[i] = ld16c(ws, k1[2] + (i & 3) * 2048u + (i >> 2) * 64u, 0);
    kx = cat(cat(f[0], f[1]), cat(f[2], f[3]), cat(f[4], f[5]), cat(f[6], f[7]));
  }
  STAMP(15, 16 + w);
  if (w == 0) TSTAMP(1);

  unsigned s = 0, mem = rfl(cb + B_MEM * 16384u - 16384u), cap = rfl(alive ? SPIN_CAP : 0u), kxo = 262144u, kmo = 0;
  u32v t0;
  u8 t1;
  u16v t2;
  A_DIAG_DECL;
  asm volatile(
      "s_mov_b32 s80, 0xff00ff00\n"
      "s_mov_b32 s81, 0xff00ff00\n"
      "s_mov_b32 s82, 0x3e0293ee\n"
      "s_mov_b32 s83, 0\n"
      "s_cmp_eq_u32 %[w], 0\n"
      "s_cselect_b32 s90, 63, 0\n"
      "v_mov_b32 v184, 0\nv_mov_b32 v185, 0\nv_mov_b32 v186, 0\nv_mov_b32 v187, 0\n"
      "v_mov_b32 v188, 0\nv_mov_b32 v189, 0\nv_mov_b32 v190, 0\nv_mov_b32 v191, 0\n"
      "ds_write_b128 v130, v[148:151]\n"
      "s_branch .Lhave%=\n"
      A_POLL_MEM
      ".Lhave%=:\n"
      A_STAMP("s[84:85]") A_CLK("s[70:71]")
      SMF("184:187", "148:151", "0:7", "v128") SMF("188:191", "148:151", "8:15", "v128")
      "buffer_load_dword v248, v244, %[ws], 0 offen sc1\n"
      A_READ_MEM
      WL(6) SMF("184:187", "152:155", "16:23", "v128") SMF("188:191", "152:155", "24:31", "v128")
      WL(5) SMF("184:187", "156:159", "32:39", "v128") SMF("188:191", "156:159", "40:47", "v128")
      WL(4) SMF("184:187", "160:163", "48:55", "v128") SMF("188:191", "160:163", "56:63", "v128")
      WL(3) SMF("184:187", "164:167", "64:71", "v128") SMF("188:191", "164:167", "72:79", "v128")
      WL(2) SMF("184:187", "168:171", "80:87", "v128") SMF("188:191", "168:171", "88:95", "v128")
      WL(1) SMF("184:187", "172:175", "96:103", "v128") SMF("188:191", "172:175", "104:111", "v128")
      WL(0) SMF("184:187", "176:179", "112:119", "v128") SMF("188:191", "176:179", "120:127", "v128")
      A_CLK("s[74:75]")
      "s_cmp_lt_u32 %[s], s90\n"
      "s_nop 5\n"
      "v_add_f32 v152, v184, v185\n"
      "v_add_f32 v154, v186, v187\n"
      "v_add_f32 v153, v188, v189\n"
      "v_add_f32 v155, v190, v191\n"
      "v_cvt_pk_bf16_f32 v152, v152, v153\n"
      "v_cvt_pk_bf16_f32 v154, v154, v155\n"
      "ds_write_b32 v138, v152\n"
      "ds_write_b32 v139, v154\n"
      A_STAMP("s[86:87]") A_CLK("s[76:77]")
      "v_mov_b32 v184, 0\nv_mov_b32 v185, 0\nv_mov_b32 v186, 0\nv_mov_b32 v187, 0\n"
      "v_mov_b32 v188, 0\nv_mov_b32 v189, 0\nv_mov_b32 v190, 0\nv_mov_b32 v191, 0\n"
      "s_waitcnt lgkmcnt(0)\n"
      "s_barrier\n"
      "s_cbranch_scc0 .Lother%=\n"
      A_CLK("s[78:79]")
      "ds_read_b128 v[152:155], v142\n"
      "ds_read_b128 v[168:171], v143\n"
      "ds_read_b128 v[156:159], v142 offset:512\n"
      "ds_read_b128 v[172:175], v143 offset:512\n"
      "ds_read_b128 v[160:163], v142 offset:1024\n"
      "ds_read_b128 v[176:179], v143 offset:1024\n"
      "ds_read_b128 v[164:167], v142 offset:1536\n"
      "ds_read_b128 v[180:183], v143 offset:1536\n"
      WL(7) MFM("224:227", "192:195", "152:155", "0") MFM("228:231", "208:211", "152:155", "0")
      WL(6) MFM("232:235", "168:171", "152:155", "0")
      WL(5) MFM("224:227", "196:199", "156:159", "v[224:227]") MFM("228:231", "212:215", "156:159", "v[228:231]")
      WL(4) MFM("232:235", "172:175", "156:159", "v[232:235]")
      WL(3) MFM("224:227", "200:203", "160:163", "v[224:227]") MFM("228:231", "216:219", "160:163", "v[228:231]")
      WL(2) MFM("232:235", "176:179", "160:163", "v[232:235]")
      WL(1) MFM("224:227", "204:207", "164:167", "v[224:227]") MFM("228:231", "220:223", "164:167", "v[228:231]")
      WL(0) MFM("232:235", "180:183", "164:167", "v[232:235]")
      A_CLK("s[92:93]")
      "s_nop 5\n"
      "v_max3_f32 v152, v224, v225, v226\n"
      "v_max3_f32 v153, v227, v228, v229\n"
      "v_max3_f32 v154, v230, v231, v232\n"
      "v_max3_f32 v152, v152, v153, v233\n"
      "v_max_f32 v153, v152, v154\n"
      "v_max_f32 v152, v152, v154\n"
      "v_cndmask_b32 v156, v224, v228, s[80:81]\n"
      "v_cndmask_b32 v157, v225, v229, s[80:81]\n"
      "v_permlane16_swap_b32 v152, v153\n"
      "v_max_f32 v154, v152, v153\n"
      "v_max_f32 v155, v152, v153\n"
      "v_cndmask_b32 v158, v226, v230, s[80:81]\n"
      "v_cndmask_b32 v159, v227, v231, s[80:81]\n"
      "v_permlane32_swap_b32 v154, v155\n"
      "v_cndmask_b32 v160, v232, v233, s[80:81]\n"
      "v_max_f32 v152, v154, v155\n"
      "v_mul_f32 v152, 0xbe0293ee, v152\n"
      "v_fma_f32 v156, v156, s82, v152\n"
      "v_fma_f32 v157, v157, s82, v152\n"
      "v_fma_f32 v158, v158, s82, v152\n"
      "v_fma_f32 v159, v159, s82, v152\n"
      "v_fma_f32 v160, v160, s82, v152\n"
      "v_exp_f32 v156, v156\n"
      "v_exp_f32 v157, v157\n"
      "v_exp_f32 v158, v158\n"
      "v_exp_f32 v159, v159\n"
      "v_exp_f32 v160, v160\n"
      "v_add_f32 v152, v156, v157\n"
      "v_add_f32 v153, v158, v159\n"
      "v_add_f32 v152, v152, v160\n"
      "v_add_f32 v152, v152, v153\n"
      "v_xor_b32 v156, s83, v156\n"
      "v_xor_b32 v157, s83, v157\n"
      "v_add_f32_dpp v154, v152, v152 row_ror:8" DPP_ALL
      "v_add_f32_dpp v155, v152, v152 row_ror:8" DPP_ALL
      "v_xor_b32 v158, s83, v158\n"
      "v_xor_b32 v159, s83, v159\n"
      "v_permlane16_swap_b32 v154, v155\n"
      "v_add_f32 v152, v154, v155\n"
      "v_add_f32 v153, v154, v155\n"
      "v_xor_b32 v160, s83, v160\n"
      "s_xor_b32 s83, s83, 0x80000000\n"
      "v_permlane32_swap_b32 v152, v153\n"
      "v_add_f32 v152, v152, v153\n"
      "v_rcp_f32 v152, v152\n"
      A_CLK("s[94:95]")
      "s_add_u32 s91, %[s], 2\n"
      "v_mul_f32 v156, v156, v152\n"
      "v_mul_f32 v157, v157, v152\n"
      "v_mul_f32 v158, v158, v152\n"
      "v_mul_f32 v159, v159, v152\n"
      "v_mul_f32 v160, v160, v152\n"
      "v_cvt_pk_bf16_f32 v164, v156, v157\n"
      "v_cvt_pk_bf16_f32 v165, v158, v159\n"
      "v_mov_b32_dpp v161, v160 row_ror:8" DPP_ALL
      "v_mov_b32_dpp v166, v164 row_ror:8" DPP_ALL
      "v_mov_b32_dpp v167, v165 row_ror:8" DPP_ALL
      "v_cvt_pk_bf16_f32 v162, v160, v161\n"
      "v_cvt_pk_bf16_f32 v163, v160, v161\n"
      "s_not_b64 exec, s[80:81]\n"
      "buffer_store_dwordx4 v[164:167], v240, %[ws], 0 offen sc1\n"
      "s_mov_b64 exec, -1\n"
      "v_permlane16_swap_b32 v162, v163\n"
      "s_mov_b32 exec_lo, 0xff\n"
      "s_mov_b32 exec_hi, 0xff\n"
      "buffer_store_dwordx2 v[162:163], v241, %[ws], 0 offen sc1\n"
      "s_mov_b64 exec, -1\n"
      A_STAMP("s[88:89]") A_CLK("s[96:97]") A_SAVE("s84", 0) A_SAVE("s86", 1) A_SAVE("s88", 2) A_SAVECLK
      "s_cmp_ge_u32 %[s], 62\n"
      "s_cbranch_scc1 .Lnext%=\n"
      A_KXWAIT
      ".Lkx%=:\n"
      "v_cmp_gt_u32 vcc, s91, v248\n"
      "s_cbranch_vccz .Lkxok%=\n"
      "buffer_load_dword v248, v244, %[ws], 0 offen sc1\n"
      "s_sub_u32 %[cap], %[cap], 1\n"
      "s_cbranch_scc1 .Lexit%=\n"
      "s_waitcnt vmcnt(0)\n"
      "s_branch .Lkx%=\n"
      ".Lkxok%=:\n"
      "buffer_load_dwordx4 v[192:195], v242, %[ws], %[kxo] offen sc1\n"
      "buffer_load_dwordx4 v[208:211], v242, %[ws], %[kxo] offen offset:64 sc1\n"
      "buffer_load_dwordx4 v[196:199], v242, %[ws], %[kxo] offen offset:2048 sc1\n"
      "buffer_load_dwordx4 v[212:215], v242, %[ws], %[kxo] offen offset:2112 sc1\n"
      "buffer_load_dwordx4 v[200:203], v243, %[ws], %[kxo] offen sc1\n"
      "buffer_load_dwordx4 v[216:219], v243, %[ws], %[kxo] offen offset:64 sc1\n"
      "buffer_load_dwordx4 v[204:207], v243, %[ws], %[kxo] offen offset:2048 sc1\n"
      "buffer_load_dwordx4 v[220:223], v243, %[ws], %[kxo] offen offset:2112 sc1\n"
      "s_add_u32 %[kxo], %[kxo], 0x40000\n"
      SLEEP(D_S0)
      "s_branch .Lnext%=\n"
      ".Lidle%=:\n"
      SLEEP(D_S1)
      "s_branch .Lnext%=\n"
      ".Lother%=:\n"
      "s_cmp_lg_u32 %[w], 6\n"
      "s_cbranch_scc1 .Lidle%=\n"
      "ds_read_b128 v[152:155], v245\n"
      "ds_read_b128 v[156:159], v245 offset:1024\n"
      "s_add_u32 s91, %[s], 1\n"
      "v_mov_b32 v160, s91\n"
      WL(1) "buffer_store_dwordx4 v[152:155], v246, %[ws], %[kmo] offen sc1\n"
      WL(0) "buffer_store_dwordx4 v[156:159], v246, %[ws], %[kmo] offen offset:1024 sc1\n"
      "s_add_u32 %[kmo], %[kmo], 0x10000\n"
      "s_waitcnt vmcnt(0)\n"
      "s_mov_b64 exec, 1\n"
      "buffer_store_dword v160, v247, %[ws], 0 offen sc1\n"
      "s_mov_b64 exec, -1\n"
      SLEEP(D_S0)
      ".Lnext%=:\n"
      A_NEXT
      "s_add_u32 %[mem], %[mem], 0x4000\n"
      "s_add_u32 %[s], %[s], 1\n"
      "s_cmp_lt_u32 %[s], 64\n"
      "s_cbranch_scc1 .Lpoll%=\n"
      ".Lexit%=:\n"
      : [s] "+s"(s), [mem] "+s"(mem), [cap] "+s"(cap), [kxo] "+s"(kxo), [kmo] "+s"(kmo), "+{v[148:151]}"(a0),
        "=&{v[152:183]}"(t0), "=&{v[184:191]}"(t1), "+{v[192:223]}"(kx), "=&{v[224:239]}"(t2), "=&{v248}"(seen) A_DIAG_OUT
      : [ws] "s"(srd(p.ws, WS_BYTES)), [w] "s"(w), "{v[0:31]}"(cat(wa[0], wb[0], wa[1], wb[1])),
        "{v[32:63]}"(cat(wa[2], wb[2], wa[3], wb[3])), "{v[64:95]}"(cat(wa[4], wb[4], wa[5], wb[5])),
        "{v[96:127]}"(cat(wa[6], wb[6], wa[7], wb[7])), "{v[128:143]}"(k0), "{v[240:247]}"(k1)
      : A_SCLOB A_DIAG_CLOB);
}

DEV void read_wo(char* sm, unsigned w, unsigned l, u4 (&wo)[4][4]) {
#pragma unroll
  for (int i = 0; i < 4; ++i) {
    const unsigned fa = frag_base(1024u, 4u * w + i, l);
#pragma unroll
    for (int f = 0; f < 4; ++f) wo[i][f] = frag(sm, fa + f * 32768u, 1024u);
  }
}

DEV void role_vm(const Params& p, unsigned h, char* sm) {
  constexpr unsigned L_MEM = 131072, L_V = L_MEM + 8192;
  const unsigned l = threadIdx.x & 63, w = rfl(threadIdx.x >> 6), c = l & 15, g = l >> 4;
  const rsrc_t ws = mk(p.ws, WS_BYTES);
  bool alive = true;

  u8 wv[8], wo[4][2];
  u4 a0;
  unsigned cb;
  {
    Hs hs = hs_begin(ws);
    u4 d0[16], d1[16], t[4][4];
    stage_slice(mk(p.wv, 4u << 20), h, 64u * w, l, d0);
    fence();
    hs_post(ws, hs, sm);
    fence();
    stage_wo(mk(p.wo, 4u << 20), h, w, l, d1);
    a0 = load_mem0(p, w, c, g);
    put_image(sm, w, l, d0);
    bar();
    const unsigned fa = frag_base(256u, w, l);
#pragma unroll
    for (int i = 0; i < 8; ++i) {
      const unsigned o = fa + (w ^ i) * 16384u;
      wv[i] = cat(frag(sm, o, 256u), frag(sm, o + 8192u, 256u));
    }
    bar();
    const unsigned echo = hs_echo(ws);
    put_image(sm, w, l, d1);
    bar();
    read_wo(sm, w, l, t);
    cb = window(p, hs_wait(ws, sm, echo, 2u, alive));
    hs_ack(ws, hs, 1u);
#pragma unroll
    for (int i = 0; i < 4; ++i) {
      wo[i][0] = cat(t[i][0], t[i][1]);
      wo[i][1] = cat(t[i][2], t[i][3]);
    }
  }
  STAMP(15, 8 + w);

  const unsigned lb = lds_base(sm);
  u16v k0;
  k0[0] = (c & 1) ? 0xeeeeeeeeu : 0x44444444u;
  k0[1] = w * 1024u + l * 16u;
#pragma unroll
  for (int i = 0; i < 8; ++i) k0[2 + i] = lb + L_MEM + (w ^ i) * 1024u + l * 16u;
  k0[10] = lb + L_V + (w * 16u + 4u * g + ((c >> 1) & 1)) * 16u + (2u * (c >> 2) + (c & 1)) * 2u;
  k0[11] = lb + L_V + l * 16u;
  const unsigned cg = 4u * w + g;
  k0[12] = cb + (B_M + cg) * 16384u + h * 256u + c * 16u;
  k0[13] = O_M2 + cg * 8192u + h * 256u + c * 16u;
  k0[14] = threadIdx.x == 448 ? F_M + h * FL : OOB;
  k0[15] = 0;

  unsigned s = 0, mem = rfl(cb + B_MEM * 16384u - 16384u), cap = rfl(alive ? SPIN_CAP : 0u), m2o = 0;
  u32v t0;
  u16v t1, t2;
  A_DIAG_DECL;
#define VM_ZERO(a) "v_mov_b32 v" #a ", 0\n"
#define VM_ZERO_M \
      VM_ZERO(192) VM_ZERO(193) VM_ZERO(194) VM_ZERO(195) VM_ZERO(196) VM_ZERO(197) VM_ZERO(198) VM_ZERO(199) \
      VM_ZERO(200) VM_ZERO(201) VM_ZERO(202) VM_ZERO(203) VM_ZERO(204) VM_ZERO(205) VM_ZERO(206) VM_ZERO(207)
  asm volatile(
      "s_mov_b32 s83, 0\n"
      VM_ZERO(180) VM_ZERO(184) VM_ZERO(185) VM_ZERO(186) VM_ZERO(187) VM_ZERO_M
      "ds_write_b128 v130, v[148:151]\n"
      "s_branch .Lhave%=\n"
      A_POLL_MEM
      ".Lhave%=:\n"
      A_STAMP("s[84:85]") A_CLK("s[70:71]")
      SMF("184:187", "148:151", "0:7", "v128")
      A_READ_MEM
      WL(6) SMF("184:187", "152:155", "8:15", "v128")
      WL(5) SMF("184:187", "156:159", "16:23", "v128")
      WL(4) SMF("184:187", "160:163", "24:31", "v128")
      WL(3) SMF("184:187", "164:167", "32:39", "v128")
      WL(2) SMF("184:187", "168:171", "40:47", "v128")
      WL(1) SMF("184:187", "172:175", "48:55", "v128")
      WL(0) SMF("184:187", "176:179", "56:63", "v128")
      A_CLK("s[74:75]")
      "s_nop 7\n"
      "v_add_f32 v152, v184, v185\n"
      "v_add_f32 v153, v186, v187\n"
      "v_cvt_pk_bf16_f32 v152, v152, v153\n"
      "ds_write_b16 v138, v152\n"
      "ds_write_b16_d16_hi v138, v152 offset:32\n"
      A_STAMP("s[86:87]") A_CLK("s[76:77]")
      VM_ZERO(184) VM_ZERO(185) VM_ZERO(186) VM_ZERO(187)
      "s_waitcnt lgkmcnt(0)\n"
      "s_barrier\n"
      A_CLK("s[78:79]")
      "ds_read_b128 v[152:155], v139\n"
      "ds_read_b128 v[156:159], v139 offset:1024\n"
      "buffer_store_dword v180, v142, %[ws], 0 offen sc1\n"
      WL(1) SMF("192:195", "152:155", "64:71", "v128") SMF("200:203", "152:155", "96:103", "v128")
      WL(0) SMF("192:195", "156:159", "72:79", "v128") SMF("200:203", "156:159", "104:111", "v128")
      SMF("196:199", "152:155", "80:87", "v128") SMF("204:207", "152:155", "112:119", "v128")
      SMF("196:199", "156:159", "88:95", "v128") SMF("204:207", "156:159", "120:127", "v128")
      A_CLK("s[92:93]")
      "s_nop 2\n"
      "v_add_f32 v192, v192, v193\n"
      "v_add_f32 v200, v200, v201\n"
      "v_add_f32 v194, v194, v195\n"
      "v_add_f32 v202, v202, v203\n"
      "v_cvt_pk_bf16_f32 v160, v192, v194\n"
      "v_cvt_pk_bf16_f32 v162, v200, v202\n"
      "v_bfi_b32 v160, -2, v160, s83\n"
      "v_bfi_b32 v162, -2, v162, s83\n"
      "v_add_f32 v196, v196, v197\n"
      "v_add_f32 v204, v204, v205\n"
      "v_add_f32 v198, v198, v199\n"
      "v_add_f32 v206, v206, v207\n"
      "v_cvt_pk_bf16_f32 v161, v196, v198\n"
      "v_cvt_pk_bf16_f32 v163, v204, v206\n"
      "v_bfi_b32 v161, -2, v161, s83\n"
      "v_bfi_b32 v163, -2, v163, s83\n"
      "v_permlane32_swap_b32 v160, v162\n"
      "s_add_u32 %[s], %[s], 1\n"
      "v_permlane32_swap_b32 v161, v163\n"
      "s_nop 1\n"
      "v_permlane16_swap_b32 v160, v161\n"
      "v_permlane16_swap_b32 v162, v163\n"
      "buffer_store_dwordx4 v[160:163], v140, %[ws], 0 offen sc1\n"
      A_STAMP("s[88:89]") A_CLK("s[94:95]") A_CLK("s[96:97]")
      "buffer_store_dwordx4 v[160:163], v141, %[ws], %[m2o] offen sc1\n"
      "v_mov_b32 v180, %[s]\n"
      VM_ZERO_M
      "s_xor_b32 s83, s83, 1\n"
      "s_add_u32 %[m2o], %[m2o], 0x40000\n"
      "s_add_u32 %[mem], %[mem], 0x4000\n"
      A_NEXT
      SLEEP(D_VM)
      "s_cmp_lt_u32 %[s], 64\n"
      "s_cbranch_scc1 .Lpoll%=\n"
      "s_waitcnt vmcnt(0)\n"
      "s_barrier\n"
      "buffer_store_dword v180, v142, %[ws], 0 offen sc1\n"
      ".Lexit%=:\n"
      : [s] "+s"(s), [mem] "+s"(mem), [cap] "+s"(cap), [m2o] "+s"(m2o), "+{v[148:151]}"(a0), "=&{v[152:183]}"(t0),
        "=&{v[184:199]}"(t1), "=&{v[200:215]}"(t2) A_DIAG_OUT
      : [ws] "s"(srd(p.ws, WS_BYTES)), [w] "s"(w), "{v[0:31]}"(cat(wv[0], wv[1], wv[2], wv[3])),
        "{v[32:63]}"(cat(wv[4], wv[5], wv[6], wv[7])), "{v[64:95]}"(cat(wo[0][0], wo[0][1], wo[1][0], wo[1][1])),
        "{v[96:127]}"(cat(wo[2][0], wo[2][1], wo[3][0], wo[3][1])), "{v[128:143]}"(k0)
      : A_SCLOB A_DIAG_CLOB);
}

#define PROBES 6
constexpr unsigned PROBER = 32, PROBER_BLOCK = 2;
DEV void lead(rsrc_t ws) {
  const unsigned l = threadIdx.x & 63;
  unsigned i, a0[4], last[4];
  const bool init = role_of(l, i) == R_V1;
#pragma unroll
  for (int k = 0; k < 4; ++k) {
    a0[k] = ld4c(ws, H_X + (k * 64u + l) * FL + 4u, 0);
  }
#pragma unroll
  for (unsigned k = 0; k < N_FLAGS / 64; ++k) st4c(0u, ws, (k * 64u + l) * FL, 0);
  drain();
  for (unsigned it = 0; it < SPIN_CAP; ++it) {
    fence();
    u2 v[4];
#pragma unroll
    for (int k = 0; k < 4; ++k) v[k] = ld8c(ws, H_X + (k * 64u + l) * FL, 0);
    const bool done = v[0][1] != a0[0] && v[1][1] != a0[1] && v[2][1] != a0[2] && v[3][1] != a0[3];
    const unsigned half = __builtin_amdgcn_readlane(v[0][1] - a0[0], PROBER_BLOCK);
    const unsigned stage = all_lanes(done || !init) ? (half == 2u ? 3u : 2u) : 1u;
#pragma unroll
    for (int k = 0; k < 4; ++k) {
      const unsigned e = (v[k][0] & ~3u) | stage;
      st4c(e, ws, it == 0 || e != last[k] ? H_Y + (k * 64u + l) * FL : OOB, 0);
      last[k] = e;
    }
    if (all_lanes(done)) break;
  }
}
DEV void role_v1(const Params& p, unsigned j, char* sm) {
  const unsigned l = threadIdx.x & 63, w = rfl(threadIdx.x >> 6), c = l & 15, g = l >> 4;
  const unsigned cg = j >> 1, rg = j & 1;
  const rsrc_t ws = mk(p.ws, WS_BYTES);
  bool alive = true;
  if (j == 0 && w == 7) {
    lead(ws);
    return;
  }
  const unsigned cb0 = crit_base(p);
  const u4 ones = {~0u, ~0u, ~0u, ~0u};
  Hs hs = hs_begin(ws);
  if (j) {
    for (unsigned o = ((j - 1u) * 512u + threadIdx.x) * 16u; o < B_END * 16384u; o += 63u * 512u * 16u)
      st16c(ones, ws, o, cb0);
    constexpr unsigned N = (NSEG - FAST) * 65536u;
    for (unsigned o = ((j - 1u) * 512u + threadIdx.x) * 16u; o < 2u * N + N / 4u; o += 63u * 512u * 16u)
      st16c(ones, ws, o + (o < N ? O_KM + FAST * 65536u : o < 2u * N ? O_PX + FAST * 65536u - N : O_PM + FAST * 16384u - 2u * N), 0);
  }
  hs_post(ws, hs, sm);
  drain();
  unsigned half = 0;
  if (j == PROBER && w == 0) {
    unsigned best[2] = {~0u, ~0u};
    for (int k = 0; k < PROBES; ++k) {
      const unsigned off = B_PROBE * 16384u + (k & 1) * 8192u + (l & 7) * 16u + (k >> 1) * 1024u;
      const unsigned long long t0 = __builtin_amdgcn_s_memtime();
      asm volatile("s_waitcnt lgkmcnt(0)" ::: "memory");
      st16c(ones, ws, off, cb0);
      drain();
      const unsigned long long t1 = __builtin_amdgcn_s_memtime();
      asm volatile("s_waitcnt lgkmcnt(0)" ::: "memory");
      const unsigned dt = (unsigned)(t1 - t0);
      if (k & 1) best[1] = min(best[1], dt); else best[0] = min(best[0], dt);
    }
    half = best[1] < best[0];
  }
  if (j) bar();
  if (w >= 4) return;
  if (w == 0) {
    hs_wait(ws, sm, hs_echo(ws), 1u, alive);
    hs_ack(ws, hs, 1u + half);
  }
  if (j == 0) bar();
  const unsigned cb = window(p, hs_wait(ws, sm, hs_echo(ws), 2u, alive));
  STAMP(15, 8 + w);

  const unsigned lb = lds_base(sm);
  const unsigned piece = rg * 4u + w;
  u16v k0 = {};
  k0[0] = (c & 4) ? 0xeeeeeeeeu : 0x44444444u;
  k0[1] = cb + (B_P + (piece >> 1)) * 16384u + (piece & 1) * 4096u + g * 128u + (c & 7) * 16u + 2048u;
  k0[2] = k0[1] - 2048u + (c >> 3) * 512u;
  k0[3] = cb + (B_M + cg) * 16384u + w * 2048u + l * 16u;
  k0[4] = O_U + (cg * 32u + 8u * w) * 1024u + l * 16u;
  k0[5] = k0[4] + 4096u;
  k0[6] = F_U + ((8u * w + (l & 7)) * 8u + (cg >> 2)) * FL;
  k0[7] = lb + ((g * 2u + ((c >> 1) & 1)) * 8u + 2u * (c >> 2) + (c & 1)) * 16u + w * 4u;
  k0[8] = lb + l * 16u;
  k0[9] = cg * 256u + rg * 128u + (l >> 3) * 16u;

  unsigned s = 0, mem = rfl(cb + B_MEM * 16384u), cap = rfl(alive ? SPIN_CAP : 0u), uo = 0, seen = 0;
  u32v t0, t1;
  A_DIAG_DECL;
#define V1_TAGS(d, a, b, c, OP) "v_bitop3_b32 v" d ", v" a ", v" b ", v" c " bitop3:" OP "\n"
#define V1_ZERO "v_mov_b32 v52, 0\nv_mov_b32 v53, 0\nv_mov_b32 v54, 0\nv_mov_b32 v55, 0\n" \
                "v_mov_b32 v56, 0\nv_mov_b32 v57, 0\nv_mov_b32 v58, 0\nv_mov_b32 v59, 0\n"
#define V1_LOAD_P \
      "buffer_load_dwordx4 v[40:43], v66, %[ws], 0 offen sc1\n" \
      "buffer_load_dwordx4 v[44:47], v66, %[ws], 0 offen offset:1024 sc1\n" \
      "buffer_load_dwordx4 v[48:51], v65, %[ws], 0 offen sc1\n"
#define V1_STEP(P, OP, MWANT, PWANT, NEG) \
      ".Ltop" P "%=:\n" \
      "v_cmp_ge_u32 vcc, %[s], v80\n" \
      "s_cbranch_vccnz .Luw" P "%=\n" \
      SLEEP(D_U) \
      "buffer_load_dwordx4 v[0:3], v68, %[ws], %[uo] offen sc1\n" \
      "buffer_load_dwordx4 v[4:7], v68, %[ws], %[uo] offen offset:1024 sc1\n" \
      "buffer_load_dwordx4 v[8:11], v68, %[ws], %[uo] offen offset:2048 sc1\n" \
      "buffer_load_dwordx4 v[12:15], v68, %[ws], %[uo] offen offset:3072 sc1\n" \
      "buffer_load_dwordx4 v[16:19], v69, %[ws], %[uo] offen sc1\n" \
      "buffer_load_dwordx4 v[20:23], v69, %[ws], %[uo] offen offset:1024 sc1\n" \
      "buffer_load_dwordx4 v[24:27], v69, %[ws], %[uo] offen offset:2048 sc1\n" \
      "buffer_load_dwordx4 v[28:31], v69, %[ws], %[uo] offen offset:3072 sc1\n" \
      "buffer_load_dword v80, v70, %[ws], 0 offen sc1\n" \
      "s_add_u32 %[uo], %[uo], 0x100000\n" \
      V1_ZERO \
      SLEEP(D_V1) \
      ".Lboth" P "%=:\n" \
      "buffer_load_dwordx4 v[32:35], v67, %[ws], 0 offen sc1\n" \
      "buffer_load_dwordx4 v[36:39], v67, %[ws], 0 offen offset:1024 sc1\n" \
      V1_LOAD_P \
      WV(3) \
      V1_TAGS("60", "32", "33", "34", OP) V1_TAGS("61", "35", "36", "37", OP) V1_TAGS("60", "60", "38", "39", OP) \
      V1_TAGS("60", "60", "61", "61", OP) \
      "v_and_b32 v60, 1, v60\n" \
      "v_cmp_ne_u32 s[80:81], " MWANT ", v60\n" \
      ".Lp" P "%=:\n" \
      WV(2) SMF("52:55", "40:43", "0:7", "v64") SMF("56:59", "40:43", "8:15", "v64") \
      V1_TAGS("60", "40", "41", "42", OP) \
      WV(1) SMF("52:55", "44:47", "16:23", "v64") SMF("56:59", "44:47", "24:31", "v64") \
      V1_TAGS("60", "60", "43", "44", OP) V1_TAGS("61", "45", "46", "47", OP) \
      WV(0) SMF("52:55", "48:51", "32:39", "v64") \
      V1_TAGS("60", "60", "48", "49", OP) V1_TAGS("61", "61", "50", "51", OP) V1_TAGS("60", "60", "61", "61", OP) \
      "v_and_b32 v60, s82, v60\n" \
      "v_cmp_ne_u32 vcc, " PWANT ", v60\n" \
      "s_or_b64 vcc, vcc, s[80:81]\n" \
      "s_cbranch_scc1 .Lre" P "%=\n" \
      A_STAMP("s[86:87]") A_CLK("s[70:71]") \
      "s_nop 0\n" \
      "v_permlane32_swap_b32 v56, v54\n" \
      "v_permlane32_swap_b32 v57, v55\n" \
      "v_add_f32 v60, v52, v54\n" \
      "v_add_f32 v62, v58, v56\n" \
      "v_add_f32 v61, v53, v55\n" \
      "v_add_f32 v63, v59, v57\n" \
      "v_cndmask_b32 v60, v60, v62, s[84:85]\n" \
      "v_cndmask_b32 v61, v61, v63, s[84:85]\n" \
      "s_cmp_lg_u32 %[w], 0\n" \
      "s_nop 0\n" \
      "v_permlane16_swap_b32 v60, v61\n" \
      "v_add_f32 v60, v60, v61\n" \
      "ds_write_b32 v71, v60 offset:" P "*1024\n" \
      A_CLK("s[72:73]") \
      "s_waitcnt lgkmcnt(0)\n" \
      "s_barrier\n" \
      "s_cbranch_scc1 .Lend" P "%=\n" \
      A_CLK("s[74:75]") \
      "ds_read_b128 v[52:55], v72 offset:" P "*1024\n" \
      "s_waitcnt lgkmcnt(0)\n" \
      A_CLK("s[76:77]") \
      "v_add_f32 v52, v52, v53\n" \
      "v_add_f32 v54, v54, v55\n" \
      "v_add_f32_e64 v52, " NEG "v52, " NEG "v54\n" \
      "s_nop 1\n" \
      "v_mov_b32_dpp v53, v52 quad_perm:[1,0,3,2]" DPP_ALL \
      "v_cvt_pk_bf16_f32 v56, v52, v53\n" \
      "s_nop 1\n" \
      "v_mov_b32_dpp v57, v56 row_shl:2" DPP_ALL \
      "v_mov_b32_dpp v58, v56 row_shl:4" DPP_ALL \
      "v_mov_b32_dpp v59, v56 row_shl:6" DPP_ALL \
      "s_mov_b64 exec, s[90:91]\n" \
      "buffer_store_dwordx4 v[56:59], v73, %[ws], %[mem] offen sc1\n" \
      "s_mov_b64 exec, -1\n" \
      A_STEP A_STAMP("s[88:89]") A_CLK("s[78:79]") A_SAVE("s86", 1) A_SAVE("s88", 2) A_SAVECLK \
      ".Lend" P "%=:\n" \
      "s_barrier\n" \
      A_NEXT \
      "s_add_u32 %[mem], %[mem], 0x4000\n" \
      "s_add_u32 %[s], %[s], 1\n"
#define V1_SIDE(P) \
      ".Luw" P "%=:\n" \
      "buffer_load_dword v80, v70, %[ws], 0 offen sc1\n" \
      "s_sub_u32 %[cap], %[cap], 1\n" \
      "s_cbranch_scc1 .Lexit%=\n" \
      "s_waitcnt vmcnt(0)\n" \
      "s_branch .Ltop" P "%=\n" \
      ".Lre" P "%=:\n" \
      "s_sub_u32 %[cap], %[cap], 1\n" \
      "s_cbranch_scc1 .Lexit%=\n" \
      "s_cmp_lg_u64 s[80:81], 0\n" \
      V1_ZERO \
      "s_cbranch_scc1 .Lboth" P "%=\n" \
      V1_LOAD_P \
      "s_branch .Lp" P "%=\n"
  asm volatile(
      "s_mov_b32 s82, 0x80008000\n"
      "s_mov_b32 s84, 0\n"
      "s_mov_b32 s85, -1\n"
      "s_mov_b32 s90, 0x01010101\n"
      "s_mov_b32 s91, 0x01010101\n"
      V1_STEP("0", "0xfe", "0", "0", "")
      "s_cmp_ge_u32 %[s], 63\n"
      "s_cbranch_scc1 .Lexit%=\n"
      V1_STEP("1", "0x80", "1", "s82", "-")
      "s_branch .Ltop0%=\n"
      V1_SIDE("0") V1_SIDE("1")
      ".Lexit%=:\n"
      : [s] "+s"(s), [mem] "+s"(mem), [cap] "+s"(cap), [uo] "+s"(uo), "=&{v[0:31]}"(t0), "=&{v[32:63]}"(t1),
        "+{v80}"(seen) A_DIAG_OUT
      : [ws] "s"(srd(p.ws, WS_BYTES)), [w] "s"(w), "{v[64:79]}"(k0)
      : A_SCLOB A_DIAG_CLOB);
}

DEV void load_x(rsrc_t xr, unsigned w, unsigned c, unsigned g, int s, u4 (&xp)[4]) {
  const unsigned voff = ((16u * (w >> 2) + c) * 512u + 128u * (w & 3) + 8u * g) * 2u;
#pragma unroll
  for (int i = 0; i < 4; ++i) xp[i] = ld16(xr, voff + i * 64u, s * 32768u);
}
DEV void put_x(char* sm, unsigned base, unsigned w, unsigned l, const u4 (&xp)[4]) {
#pragma unroll
  for (int i = 0; i < 4; ++i) lds16(sm, base + (4u * w + i) * 1024u + l * 16u) = xp[i];
}

DEV void token_softmax(rsrc_t ws, char* sm, unsigned qb, const u4 (&km)[4], unsigned h, unsigned w, unsigned l, int s) {
  const unsigned c = l & 15, g = l >> 4;
  u4 q[4], k0[4], k1[4];
#pragma unroll
  for (int m = 0; m < 4; ++m) {
    q[m] = lds16(sm, qb + (w * 4u + m) * 1024u);
    k0[m] = lds16(sm, qb + 8192u + m * 1024u);
    k1[m] = lds16(sm, qb + 12288u + m * 1024u);
  }
  sched();
  f4 s0 = {0, 0, 0, 0}, s1 = {0, 0, 0, 0}, sm_ = {0, 0, 0, 0};
#pragma unroll
  for (int m = 0; m < 4; ++m) {
    s0 = mma(k0[m], q[m], s0);
    s1 = mma(k1[m], q[m], s1);
  }
#pragma unroll
  for (int m = 0; m < 4; ++m) sm_ = mma(km[m], q[m], sm_);
  const unsigned tq = 16u * w + c;
  const float ninf = -__builtin_inff();
#pragma unroll
  for (int r = 0; r < 4; ++r) {
    s0[r] = 4u * g + r <= tq ? s0[r] : ninf;
    s1[r] = 16u + 4u * g + r <= tq ? s1[r] : ninf;
  }
  float mx = fmaxf(fmaxf(fmaxf(s0[0], s0[1]), fmaxf(s0[2], s0[3])), fmaxf(fmaxf(s1[0], s1[1]), fmaxf(s1[2], s1[3])));
  mx = group_max(fmaxf(mx, fmaxf(sm_[0], sm_[1])));
  const float mc = -mx * SCALE_LOG2E;
  float e[10];
#pragma unroll
  for (int r = 0; r < 4; ++r) {
    e[r] = __builtin_amdgcn_exp2f(fmaf(s0[r], SCALE_LOG2E, mc));
    e[4 + r] = __builtin_amdgcn_exp2f(fmaf(s1[r], SCALE_LOG2E, mc));
  }
  e[8] = __builtin_amdgcn_exp2f(fmaf(sm_[0], SCALE_LOG2E, mc));
  e[9] = __builtin_amdgcn_exp2f(fmaf(sm_[1], SCALE_LOG2E, mc));
  const float sum = group_sum(((e[0] + e[1]) + (e[2] + e[3])) + ((e[4] + e[5]) + (e[6] + e[7])) + (e[8] + e[9]));
  const float rs = __builtin_amdgcn_rcpf(sum);
  const u4 px = {pk(e[0] * rs, e[1] * rs), pk(e[2] * rs, e[3] * rs), pk(e[4] * rs, e[5] * rs), pk(e[6] * rs, e[7] * rs)};
  st16c(px, ws, O_PX + (w * 32u + h) * 1024u + l * 16u, s * 65536u);
  const unsigned dm = pk(e[8] * rs, e[9] * rs);
  auto r16 = __builtin_amdgcn_permlane16_swap(dm, dm, false, false);
  auto ra = __builtin_amdgcn_permlane32_swap(r16[0], r16[0], false, false);
  auto rb = __builtin_amdgcn_permlane32_swap(r16[1], r16[1], false, false);
  const u4 pm = {ra[0], rb[0], ra[1], rb[1]};
  st16c(pm, ws, g == 0 ? O_PM + (w * 8u + (h >> 2)) * 1024u + ((h & 3) * 16u + c) * 16u : OOB, s * 16384u);
}

DEV void role_s2(const Params& p, unsigned h, char* sm) {
#define S2_MID 5
#define S2_SW 5
  constexpr int LAG = 4, R = LAG + 1;
  constexpr unsigned L_X = 0, L_QK = 65536;
  const unsigned l = threadIdx.x & 63, w = rfl(threadIdx.x >> 6), c = l & 15, g = l >> 4;
  const rsrc_t ws = mk(p.ws, WS_BYTES), xr = mk(p.bx, 2u << 20);
  bool alive = true;

  u4 xp[4], wa[16], wb[16];
  {
    const rsrc_t wr = mk(w < 4 ? p.wq : p.wk, 4u << 20);
    Hs hs = hs_begin(ws);
    u4 d0[16], d1[16];
    stage_slice(wr, h, 64u * (w & 3), l, d0);
    fence();
    hs_post(ws, hs, sm);
    fence();
    stage_slice(wr, h, 64u * (w & 3) + 256u, l, d1);
    load_x(xr, w, c, g, 0, xp);
    unsigned echo;
    const unsigned fa = (w >> 2) * 65536u + frag_base(256u, 2u * (w & 3), l), fb = fa ^ 32u;
#pragma unroll
    for (int k = 0; k < 2; ++k) {
      put_image(sm, w, l, k ? d1 : d0);
      bar();
#pragma unroll
      for (int i = 0; i < 8; ++i) {
        wa[8 * k + i] = frag(sm, fa + i * 8192u, 256u);
        wb[8 * k + i] = frag(sm, fb + i * 8192u, 256u);
      }
      if (k == 0) echo = hs_echo(ws);
      else hs_wait(ws, sm, echo, 1u, alive);
      bar();
    }
    hs_ack(ws, hs, 1u);
  }
  put_x(sm, L_X, w, l, xp);
  load_x(xr, w, c, g, 1, xp);
  bar();
  STAMP(15, 8 + w);
  if (w >= 4) __builtin_amdgcn_s_setprio(1);

  const unsigned km_off = w < 2 ? O_KM + h * 2048u + (g * 8u + ((2u * (c >> 2) + (c & 1)) ^ swz(g))) * 16u : OOB;
  const unsigned km_flag = w < 2 ? F_KM + h * FL : OOB;
  const unsigned px_flag = w < 2 && l == 0 ? F_PX + (w * 32u + h) * FL : OOB;
  const unsigned kx_flag = w >= 4 && l == 0 ? F_KX + (h * 4u + (w & 3)) * FL : OOB;
  const unsigned kx_off = w >= 4 ? O_KX + h * 8192u + (w & 3) * 2048u + l * 16u : OOB;
  unsigned km_seen = 0;
  int slot = 0;
  for (int it = 0; it < NSEG; ++it) {
    const int s = max(it - LAG, 0);
    const bool soft = it >= LAG && w < 2;
    if (it == S2_SW && w < 4) __builtin_amdgcn_s_setprio(2);
    if (soft && !all_lanes(km_seen > (unsigned)s)) wait_flags(ws, km_flag, s + 1, alive);
    if (w == 0 || w == 4) STAMP(3 + (w >> 2) * 4, it);
    u4 km[4];
#pragma unroll
    for (int m = 0; m < 4; ++m) km[m] = ld16c(ws, km_off + m * 512u, s * 65536u);
    km_seen = ld4c(ws, km_flag, 0);
    f4 acc[2][2] = {};
    const unsigned xb = L_X + (it & 1) * 32768u + l * 16u;
    u4 x[3][2];
#pragma unroll
    for (int k = 0; k < 2; ++k)
#pragma unroll
      for (int t = 0; t < 2; ++t) x[k][t] = lds16(sm, xb + (t * 16 + k) * 1024u);
#pragma unroll
    for (int ks = 0; ks < 16; ++ks) {
      sched();
      if (ks + 2 < 16) {
#pragma unroll
        for (int t = 0; t < 2; ++t) x[(ks + 2) % 3][t] = lds16(sm, xb + (t * 16 + ks + 2) * 1024u);
      }
      if (ks == S2_MID) {
        asm volatile("s_waitcnt vmcnt(9)" ::: "memory");
        st4c(it, ws, kx_flag, 0);
      }
      sched();
#pragma unroll
      for (int t = 0; t < 2; ++t) {
        acc[0][t] = mma(wa[ks], x[ks % 3][t], acc[0][t]);
        acc[1][t] = mma(wb[ks], x[ks % 3][t], acc[1][t]);
      }
    }
    sched();
    asm volatile("s_waitcnt vmcnt(6)" ::: "memory");
    if (w == 0 || w == 4) STAMP(4 + (w >> 2) * 4, it);
    st4c(s, ws, px_flag, 0);
    put_x(sm, L_X + ((it + 1) & 1) * 32768u, w, l, xp);
#pragma unroll
    for (int t = 0; t < 2; ++t) {
      const u4 o = {pk(acc[0][t][0], acc[1][t][0]), pk(acc[0][t][1], acc[1][t][1]), pk(acc[0][t][2], acc[1][t][2]),
                    pk(acc[0][t][3], acc[1][t][3])};
      lds16(sm, L_QK + slot * 16384u + (w >> 2) * 8192u + (t * 4u + (w & 3)) * 1024u + l * 16u) = o;
      st16c(o, ws, kx_off + t * 1024u, it * 262144u);
    }
    load_x(xr, w, c, g, it + 2, xp);
    if (w == 0 || w == 4) STAMP(5 + (w >> 2) * 4, it);
    slot = slot == R - 1 ? 0 : slot + 1;
    if (soft) {
      token_softmax(ws, sm, L_QK + slot * 16384u + l * 16u, km, h, w, l, s);
      if (w == 0) STAMP(2, s);
    }
    bar();
  }
  if (w >= 2) return;
  for (int s = NSEG - LAG; s < NSEG; ++s) {
    if (s <= FAST) {
      drain();
      st4c(s, ws, px_flag, 0);
    }
    if (s < FAST) wait_flags(ws, km_flag, s + 1, alive);
    u4 km[4];
    for (unsigned it = 0; it < SPIN_CAP; ++it) {
      fence();
#pragma unroll
      for (int m = 0; m < 4; ++m) km[m] = ld16c(ws, km_off + m * 512u, s * 65536u);
      if (s < FAST || all_lanes(filled(km[0]) && filled(km[1]) && filled(km[2]) && filled(km[3]))) break;
    }
    slot = slot == R - 1 ? 0 : slot + 1;
    token_softmax(ws, sm, L_QK + slot * 16384u + l * 16u, km, h, w, l, s);
    if (w == 0) STAMP(2, s);
  }
}

DEV void role_vu(const Params& p, unsigned h, char* sm) {
  constexpr unsigned L_X = 0, L_V = 65536;
  const unsigned l = threadIdx.x & 63, w = rfl(threadIdx.x >> 6), c = l & 15, g = l >> 4;
  const rsrc_t ws = mk(p.ws, WS_BYTES), xr = mk(p.bx, 2u << 20);

  u4 xp[4], wv[16], wo[4][4];
  {
    bool alive = true;
    Hs hs = hs_begin(ws);
    u4 d0[16], d1[16];
    stage_slice(mk(p.wv, 4u << 20), h, 64u * w, l, d0);
    fence();
    hs_post(ws, hs, sm);
    fence();
    stage_wo(mk(p.wo, 4u << 20), h, w, l, d1);
    load_x(xr, w, c, g, 0, xp);
    put_image(sm, w, l, d0);
    bar();
    const unsigned fa = frag_base(256u, w, l);
#pragma unroll
    for (int f = 0; f < 16; ++f) wv[f] = frag(sm, fa + f * 8192u, 256u);
    const unsigned echo = hs_echo(ws);
    bar();
    put_image(sm, w, l, d1);
    bar();
    read_wo(sm, w, l, wo);
    hs_wait(ws, sm, echo, 1u, alive);
    hs_ack(ws, hs, 1u);
    bar();
  }
  put_x(sm, L_X, w, l, xp);
  load_x(xr, w, c, g, 1, xp);
  bar();
  STAMP(15, 8 + w);

  const unsigned v_off = L_V + ((w >> 1) * 64u + (2u * (w & 1) + (g >> 1)) * 16u + c) * 16u + (g & 1) * 8u;
  const unsigned flag = l == 0 ? F_U + (h * 8u + w) * FL : OOB;
  for (int s = 0; s < NSEG; ++s) {
    f4 acc[2] = {};
    const unsigned xb = L_X + (s & 1) * 32768u + l * 16u;
    u4 x[4][2];
#pragma unroll
    for (int k = 0; k < 3; ++k)
#pragma unroll
      for (int t = 0; t < 2; ++t) x[k][t] = lds16(sm, xb + (t * 16 + k) * 1024u);
#pragma unroll
    for (int ks = 0; ks < 16; ++ks) {
      sched();
      if (ks + 3 < 16) {
#pragma unroll
        for (int t = 0; t < 2; ++t) x[(ks + 3) & 3][t] = lds16(sm, xb + (t * 16 + ks + 3) * 1024u);
      }
      sched();
#pragma unroll
      for (int t = 0; t < 2; ++t) acc[t] = mma(wv[ks], x[ks & 3][t], acc[t]);
    }
    sched();
#pragma unroll
    for (int t = 0; t < 2; ++t)
      *reinterpret_cast<u2*>(sm + v_off + (s & 1) * 8192u + t * 4096u) = u2{pk(acc[t][0], acc[t][1]), pk(acc[t][2], acc[t][3])};
    if (w == 0) STAMP(3, s);
    drain();
    st4c(s, ws, flag, 0);
    if (w == 0) STAMP(4, s);
    put_x(sm, L_X + ((s + 1) & 1) * 32768u, w, l, xp);
    load_x(xr, w, c, g, s + 2, xp);
    bar();
    if (w == 0) STAMP(5, s);
    u4 v[8];
#pragma unroll
    for (int k = 0; k < 8; ++k) v[k] = lds16(sm, L_V + (s & 1) * 8192u + ((k & 1) * 4 + (k >> 1)) * 1024u + l * 16u);
    sched();
    f4 u[4][2] = {};
#pragma unroll
    for (int k = 0; k < 8; ++k)
#pragma unroll
      for (int i = 0; i < 4; ++i) u[i][k & 1] = mma(v[k], wo[i][k >> 1], u[i][k & 1]);
#pragma unroll
    for (int i = 0; i < 4; ++i) {
      const u4 o = {pk(u[i][0][0], u[i][0][1]), pk(u[i][0][2], u[i][0][3]), pk(u[i][1][0], u[i][1][1]),
                    pk(u[i][1][2], u[i][1][3])};
      st16c(o, ws, O_U + ((4u * w + i) * 32u + h) * 1024u + l * 16u, s * 1048576u);
    }
    if (w == 0) STAMP(2, s);
  }
  drain();
  st4c(NSEG, ws, flag, 0);
}

DEV void role_v2(const Params& p, unsigned j, char* sm) {
  constexpr unsigned ROW = 144, PART = 16 * ROW, BUF = 8 * PART;
  const unsigned l = threadIdx.x & 63, w = rfl(threadIdx.x >> 6), c = l & 15, g = l >> 4;
  const unsigned b = j >> 1, qt = j & 1;
  const rsrc_t ws = mk(p.ws, WS_BYTES), outr = mk(p.out, 8u << 20);
  bool alive = true;

  const unsigned hq = 4u * w + (l & 3);
  const unsigned uf_off = F_U + (hq * 8u + (b >> 1)) * FL;
  const unsigned pf_off = (l & 4) ? F_M + hq * FL : F_PX + (qt * 32u + hq) * FL;
  const unsigned u_off = O_U + (2u * b * 32u + 4u * w) * 1024u + l * 16u;
  const unsigned m_off = O_M2 + 2u * b * 8192u + w * 1024u + l * 16u;
  const unsigned px_off = O_PX + (qt * 32u + 4u * w) * 1024u + l * 16u;
  const unsigned pm_off = O_PM + (qt * 8u + w) * 1024u + l * 16u;
  const unsigned out_off = ((16u * qt + (threadIdx.x >> 5)) * 1024u + 512u + 32u * b + (threadIdx.x & 31)) * 4u;
  unsigned ring;
  {
    Hs hs = hs_begin(ws);
    hs_post(ws, hs, sm);
    bar();
    ring = window(p, hs_wait(ws, sm, hs_echo(ws), 2u, alive)) + (B_M + 2u * b) * 16384u + w * 1024u + l * 16u;
    hs_ack(ws, hs, 1u);
  }
  unsigned u_seen = wait_flags(ws, uf_off, 1u, alive), p_seen = 0;
  u4 ua[2][4];
#pragma unroll
  for (int k = 0; k < 8; ++k) ua[k >> 2][k & 3] = ld16c(ws, u_off + (k >> 2) * 32768u + (k & 3) * 1024u, 0);

  for (int s = 0; s < NSEG; ++s) {
    const unsigned need = s < FAST || ((l & 4) && s < LAST) ? s + 1 : 0;
    if (!all_lanes(p_seen >= need)) p_seen = wait_flags(ws, pf_off, need, alive);
    const unsigned mo = s < LAST ? m_off + s * 262144u : ring, ms = s < LAST ? 8192u : 16384u;
    u4 px[4], pm, m0, m1;
    for (unsigned it = 0; it < SPIN_CAP; ++it) {
      fence();
#pragma unroll
      for (int i = 0; i < 4; ++i) px[i] = ld16c(ws, px_off + i * 1024u, s * 65536u);
      pm = ld16c(ws, pm_off, s * 16384u);
      m0 = ld16c(ws, mo, 0);
      m1 = ld16c(ws, mo, ms);
      if (s < FAST) break;
      const bool tag = s < LAST || (m0[0] & m0[1] & m0[2] & m0[3] & m1[0] & m1[1] & m1[2] & m1[3] & 1u);
      if (all_lanes(tag && filled(px[0]) && filled(px[1]) && filled(px[2]) && filled(px[3]) && filled(pm))) break;
    }
    f4 a0 = {0, 0, 0, 0}, a1 = {0, 0, 0, 0};
#pragma unroll
    for (int i = 0; i < 4; ++i) {
      a0 = mma(ua[0][i], px[i], a0);
      a1 = mma(ua[1][i], px[i], a1);
    }
    a0 = mma(m0, pm, a0);
    a1 = mma(m1, pm, a1);
    sched();
    if (s + 1 < NSEG) {
      if (!all_lanes(u_seen > (unsigned)s + 1u)) u_seen = wait_flags(ws, uf_off, s + 2, alive);
#pragma unroll
      for (int k = 0; k < 8; ++k)
        ua[k >> 2][k & 3] = ld16c(ws, u_off + (k >> 2) * 32768u + (k & 3) * 1024u, (s + 1) * 1048576u);
    }
    sched();
    const unsigned rb = (s & 1) * BUF;
    *reinterpret_cast<f4*>(sm + rb + w * PART + c * ROW + g * 16u) = a0;
    *reinterpret_cast<f4*>(sm + rb + w * PART + c * ROW + g * 16u + 64u) = a1;
    bar();
    float v = 0.f;
#pragma unroll
    for (int k = 0; k < 8; ++k)
      v += *reinterpret_cast<const float*>(sm + rb + k * PART + (threadIdx.x >> 5) * ROW + (threadIdx.x & 31) * 4u);
    __builtin_amdgcn_raw_buffer_store_b32(__float_as_uint(rbf(v)), outr, out_off, s * 131072u, 0);
    if (w == 0) STAMP(2, s);
  }
}

DEV f2 cmul(f2 a, f2 b) {
  float re, im;
  asm("v_mul_f32 %0, %3, %5\n\tv_mul_f32 %1, %3, %4\n\tv_fma_f32 %0, %2, %4, -%0\n\tv_fma_f32 %1, %2, %5, %1"
      : "=&v"(re), "=&v"(im) : "v"(a[0]), "v"(a[1]), "v"(b[0]), "v"(b[1]));
  return f2{re, im};
}
DEV f2 cfma(f2 a, f2 b, f2 c) {
  float re, im;
  asm("v_fma_f32 %0, -%3, %5, %6\n\tv_fma_f32 %1, %3, %4, %7\n\tv_fma_f32 %0, %2, %4, %0\n\tv_fma_f32 %1, %2, %5, %1"
      : "=&v"(re), "=&v"(im) : "v"(a[0]), "v"(a[1]), "v"(b[0]), "v"(b[1]), "v"(c[0]), "v"(c[1]));
  return f2{re, im};
}
DEV f2 from_group(float re, float im, unsigned c, int k) {
  return f2{__shfl(re, (int)c + 16 * k, 64), __shfl(im, (int)c + 16 * k, 64)};
}

DEV void role_a(const Params& p, unsigned ci, char* sm) {
  constexpr unsigned GS = 272, KS = 4 * GS, TS = 32 * KS + 32, L_TAB = 4 * TS;
  const unsigned tid = threadIdx.x, l = tid & 63, w = rfl(tid >> 6), c = l & 15, g = l >> 4;
  const rsrc_t ws = mk(p.ws, WS_BYTES), xr = mk(p.ax, 2u << 20), outr = mk(p.out, 8u << 20);
  bool alive = true;
  {
    Hs hs = hs_begin(ws);
    hs_post(ws, hs, sm);
    if (w == 0) {
      hs_wait(ws, sm, hs_echo(ws), 1u, alive);
      hs_ack(ws, hs, 1u);
      wait_flags(ws, F_U + (l & 31) * 8u * FL, 1u, alive);
    }
    bar();
  }
  {
    const unsigned voff = ((64u * ci + 16u * (c >> 2) + 4u * (w >> 1) + (c & 3)) * 512u + 256u * (w & 1) + 8u * g) * 2u;
    u4 xp[8];
#pragma unroll
    for (int i = 0; i < 8; ++i) xp[i] = ld16(xr, voff + i * 64u, 0);
#pragma unroll
    for (int i = 0; i < 8; ++i) lds16(sm, (8u * w + i) * 1024u + l * 16u) = xp[i];
  }
  f2 lam64;
  {
    const float lr = bf2f(((const unsigned short*)p.lre)[tid]), li = bf2f(((const unsigned short*)p.lim)[tid]);
    const float dt = rbf(expf(bf2f(((const unsigned short*)p.ldt)[tid])));
    const float er = expf(dt * lr);
    float sn, cs;
    sincosf(dt * li, &sn, &cs);
    f2 lam = {er * cs, er * sn};
    asm volatile("s_nop 1" : "+v"(lam));
    const float nr = lam[0] - 1.0f, ni = lam[1];
    f2 coef;
    if (fabsf(lr) >= fabsf(li)) {
      const float t = li / lr, d = 1.0f / (lr + li * t);
      coef = f2{smul(nr + smul(ni, t), d), smul(ni - smul(nr, t), d)};
    } else {
      const float t = lr / li, d = 1.0f / (lr * t + li);
      coef = f2{smul(smul(nr, t) + ni, d), smul(smul(ni, t) - nr, d)};
    }
    f2 l16 = cmul(lam, lam);
    l16 = cmul(l16, l16);
    l16 = cmul(l16, l16);
    l16 = cmul(l16, l16);
    lam64 = cmul(l16, l16);
    lam64 = cmul(lam64, lam64);
    *reinterpret_cast<f4*>(sm + L_TAB + tid * 32u) = f4{lam[0], lam[1], coef[0], coef[1]};
    *reinterpret_cast<f2*>(sm + L_TAB + tid * 32u + 16u) = l16;
  }
  bar();

  f4 hr[4][4] = {}, hi[4][4] = {};
  {
    const rsrc_t br = mk(p.bre, 1u << 19), bi = mk(p.bim, 1u << 19);
    const unsigned voff = ((64u * w + c) * 512u + 8u * g) * 2u;
    u4 fr[4], fi[4];
#pragma unroll
    for (int k = 0; k < 4; ++k) {
      fr[k] = ld16(br, voff + k * 64u, 0);
      fi[k] = ld16(bi, voff + k * 64u, 0);
    }
#pragma unroll
    for (int n = 0; n < 64; ++n) {
      const int ct = n >> 4, ks = n & 15;
      const u4 r = fr[n & 3], i = fi[n & 3];
      fence();
      if (n + 4 < 64) {
        fr[n & 3] = ld16(br, voff + ((n + 4) & 15) * 64u, ((n + 4) >> 4) * 16384u);
        fi[n & 3] = ld16(bi, voff + ((n + 4) & 15) * 64u, ((n + 4) >> 4) * 16384u);
      }
#pragma unroll
      for (int t = 0; t < 4; ++t) {
        const u4 x = lds16(sm, (t * 16 + ks) * 1024u + l * 16u);
        hr[ct][t] = mma(x, r, hr[ct][t]);
        hi[ct][t] = mma(x, i, hi[ct][t]);
      }
    }
  }
  __builtin_amdgcn_sched_barrier(0);
  asm volatile("s_nop 7\n\ts_nop 7" ::: "memory");
  __builtin_amdgcn_sched_barrier(0);
#pragma unroll
  for (int ct = 0; ct < 4; ++ct) {
    const unsigned ch = 64u * w + 16u * ct + c;
    const f4 tb = *reinterpret_cast<const f4*>(sm + L_TAB + ch * 32u);
    const f2 l16 = *reinterpret_cast<const f2*>(sm + L_TAB + ch * 32u + 16u);
    const f2 lam = {tb[0], tb[1]}, coef = {tb[2], tb[3]};
    f2 hh = {0.f, 0.f};
#pragma unroll
    for (int i = 0; i < 16; ++i) {
      hh = cfma(lam, hh, cmul(coef, f2{hr[ct][i >> 2][i & 3], hi[ct][i >> 2][i & 3]}));
      hr[ct][i >> 2][i & 3] = hh[0];
      hi[ct][i >> 2][i & 3] = hh[1];
    }
    f2 e = from_group(hh[0], hh[1], c, 0);
#pragma unroll
    for (int k = 1; k < 4; ++k) e = cfma(l16, e, from_group(hh[0], hh[1], c, k));
    if (g == 0) st8c(__builtin_bit_cast(u2, e), ws, O_AE + ci * 4096u + ch * 8u, 0);
  }
  drain();
  bar();
  if (tid == 0) st4c(1u, ws, F_A + ci * FL, 0);
  wait_flags(ws, F_A + (l & 31) * FL, (l & 31) < ci ? 1u : 0u, alive);
  {
    f2 hin = {0.f, 0.f};
#pragma unroll 8
    for (unsigned k = 0; k < ci; ++k) hin = cfma(lam64, hin, __builtin_bit_cast(f2, ld8c(ws, O_AE + tid * 8u, k * 4096u)));
    *reinterpret_cast<f2*>(sm + L_TAB + tid * 32u + 24u) = hin;
  }
  bar();
#pragma unroll
  for (int ct = 0; ct < 4; ++ct) {
    const unsigned ch = 64u * w + 16u * ct + c;
    const f4 t0 = *reinterpret_cast<const f4*>(sm + L_TAB + ch * 32u);
    const f4 t1 = *reinterpret_cast<const f4*>(sm + L_TAB + ch * 32u + 16u);
    const f2 lam = {t0[0], t0[1]}, l16 = {t1[0], t1[1]};
    f2 z = {t1[2], t1[3]};
#pragma unroll
    for (int k = 0; k < 3; ++k) {
      const f2 n = cfma(l16, z, from_group(hr[ct][3][3], hi[ct][3][3], c, k));
      z = (unsigned)k < g ? n : z;
    }
    const unsigned hb = g * TS + (ch >> 5) * KS + ((ch >> 3) & 3) * GS + (ch & 7) * 2u;
#pragma unroll
    for (int i = 0; i < 16; ++i) {
      z = cmul(lam, z);
      const unsigned d = pk(2.0f * (hr[ct][i >> 2][i & 3] + z[0]), -2.0f * (hi[ct][i >> 2][i & 3] + z[1]));
      lds_put2(sm, hb + i * 16u, d);
      lds_put2(sm, hb + 16u * KS + i * 16u, d >> 16);
    }
  }
  bar();
  f4 y[4][4] = {};
  {
    const rsrc_t cr = mk(p.cre, 1u << 19), cm = mk(p.cim, 1u << 19);
    const unsigned voff = ((64u * w + c) * 512u + 8u * g) * 2u;
    u4 fa[2][4];
#pragma unroll
    for (int k = 0; k < 2; ++k)
#pragma unroll
      for (int d = 0; d < 4; ++d) fa[k][d] = ld16(cr, voff + k * 64u, d * 16384u);
#pragma unroll
    for (int ks = 0; ks < 32; ++ks) {
      u4 a[4];
      fence();
#pragma unroll
      for (int d = 0; d < 4; ++d) {
        a[d] = fa[ks & 1][d];
        if (ks + 2 < 32) fa[ks & 1][d] = ld16(ks + 2 < 16 ? cr : cm, voff + ((ks + 2) & 15) * 64u, d * 16384u);
      }
#pragma unroll
      for (int t = 0; t < 4; ++t) {
        const u4 hb = lds16(sm, t * TS + ks * KS + g * GS + c * 16u);
#pragma unroll
        for (int d = 0; d < 4; ++d) y[d][t] = mma(a[d], hb, y[d][t]);
      }
    }
  }
  const rsrc_t dr = mk(p.dsk, 1024);
#pragma unroll
  for (int d = 0; d < 4; ++d) {
    const unsigned col = 64u * w + 16u * d + 4u * g;
    const u2 dk = ld8(dr, col * 2u, 0);
#pragma unroll
    for (int t = 0; t < 4; ++t) {
      const unsigned row = 64u * ci + 16u * t + c;
      const u2 xv = ld8(xr, (row * 512u + col) * 2u, 0);
      const f4 o = {y[d][t][0] + rbf(smul(bf2f(xv[0] & 0xffffu), bf2f(dk[0] & 0xffffu))),
                    y[d][t][1] + rbf(smul(bf2f(xv[0] >> 16), bf2f(dk[0] >> 16))),
                    y[d][t][2] + rbf(smul(bf2f(xv[1] & 0xffffu), bf2f(dk[1] & 0xffffu))),
                    y[d][t][3] + rbf(smul(bf2f(xv[1] >> 16), bf2f(dk[1] >> 16)))};
      __builtin_amdgcn_raw_buffer_store_b128(__builtin_bit_cast(u4, o), outr, (row * 1024u + col) * 4u, 0, 0);
    }
  }
  if (w == 0) STAMP(2, 0);
}

__global__ void __launch_bounds__(512, 2) fused_kernel(Params p) {
  extern __shared__ __attribute__((aligned(16))) char smem[];
  unsigned i;
  const unsigned role = role_of(blockIdx.x, i);
  if (threadIdx.x < 64) TSTAMP(0);
  switch (role) {
    case R_S1: role_s1(p, i, smem); break;
    case R_VM: role_vm(p, i, smem); break;
    case R_V1: role_v1(p, i, smem); break;
    case R_S2: role_s2(p, i, smem); break;
    case R_VU: role_vu(p, i, smem); break;
    case R_V2: role_v2(p, i, smem); break;
    case R_A: role_a(p, i, smem); break;
  }
  if (threadIdx.x < 64) TSTAMP(2);
}

extern "C" void kernel_launch(void* const* d_in, const int* in_sizes, int n_in, void* d_out, int out_size, void* d_ws,
                              size_t ws_size, hipStream_t stream) {
  Params p;
  p.ax = d_in[0]; p.lre = d_in[1]; p.lim = d_in[2]; p.bre = d_in[3]; p.bim = d_in[4]; p.cre = d_in[5];
  p.cim = d_in[6]; p.ldt = d_in[7]; p.dsk = d_in[8]; p.bx = d_in[9]; p.mem0 = d_in[10]; p.wq = d_in[11];
  p.wk = d_in[12]; p.wv = d_in[13]; p.wo = d_in[14];
  p.out = (float*)d_out;
  p.ws = (char*)d_ws;
  (void)hipFuncSetAttribute((const void*)fused_kernel, hipFuncAttributeMaxDynamicSharedMemorySize, DYN_LDS);
  fused_kernel<<<256, 512, DYN_LDS, stream>>>(p);
}
```

```cpp
#define TIMER
#include <hip/hip_runtime.h>
#include <hip/hip_bf16.h>
#include <stdint.h>

typedef __attribute__((ext_vector_type(2))) unsigned u2;
typedef __attribute__((ext_vector_type(4))) unsigned u4;
typedef __attribute__((ext_vector_type(8))) unsigned u8;
typedef __attribute__((ext_vector_type(16))) unsigned u16v;
typedef __attribute__((ext_vector_type(32))) unsigned u32v;
typedef __attribute__((ext_vector_type(2))) float f2;
typedef __attribute__((ext_vector_type(4))) float f4;
typedef __attribute__((ext_vector_type(2))) __bf16 bf2;
typedef __attribute__((ext_vector_type(8))) __bf16 bf8;
typedef __attribute__((ext_vector_type(16))) __bf16 bf16v;
typedef __amdgpu_buffer_rsrc_t rsrc_t;
#define DEV __device__ __forceinline__

struct Params {
  const void *ax, *lre, *lim, *bre, *bim, *cre, *cim, *ldt, *dsk, *bx, *mem0, *wq, *wk, *wv, *wo;
  float* out;
  char* ws;
};

#define WSLEEP 4
constexpr int NSEG = 64, LAST = NSEG - 1;
#define NFAST 3
constexpr int FAST = NSEG - NFAST;
constexpr unsigned SPIN_CAP = 400000u;
constexpr unsigned DYN_LDS = 156u * 1024u;

constexpr unsigned FL = 128, F_KM = 0, F_PX = 32 * FL, F_M = 96 * FL, F_A = 128 * FL, F_KX = 192 * FL, F_U = 320 * FL,
                   N_FLAGS = 576;
constexpr unsigned H_X = N_FLAGS * FL, H_Y = H_X + 256 * FL, H_END = H_Y + 256 * FL;
constexpr unsigned O_AE = 5u << 20, O_DIAG = 6u << 20;
constexpr unsigned O_KX = 8u << 20, O_KM = 24u << 20, O_PX = 28u << 20, O_PM = 32u << 20, O_M2 = 34u << 20,
                   O_U = 50u << 20, WS_BYTES = 114u << 20;
constexpr unsigned OOB = 1u << 30;
constexpr unsigned B_MEM = 0, B_P = 63, B_M = 67, B_END = 99, B_PROBE = 100;

enum { R_S1, R_VM, R_V1, R_S2, R_VU, R_V2, R_A };
__host__ __device__ inline unsigned role_of(unsigned b, unsigned& i) {
  const unsigned x = b & 7, r = b >> 3;
  if (x >= 4) {
    i = (x - 4) * 8u + (r >> 2);
    return R_S2 + (r & 3);
  }
  if (x == 1 || x == 2) {
    i = (x - 1) * 32u + r;
    return R_V1;
  }
  i = (x ? 16u : 0u) + (r >> 1);
  return r & 1;
}

DEV rsrc_t mk(const void* p, unsigned bytes) {
  return __builtin_amdgcn_make_buffer_rsrc((void*)p, 0, bytes, 0x00020000);
}
DEV unsigned rfl(unsigned v) { return __builtin_amdgcn_readfirstlane(v); }
DEV u4 ld16(rsrc_t r, unsigned v, unsigned s) { return __builtin_amdgcn_raw_buffer_load_b128(r, v, s, 0); }
#define WAUX 0
DEV u4 ld16w(rsrc_t r, unsigned v, unsigned s) { return __builtin_amdgcn_raw_buffer_load_b128(r, v, s, WAUX); }
DEV u2 ld8(rsrc_t r, unsigned v, unsigned s) { return __builtin_amdgcn_raw_buffer_load_b64(r, v, s, 0); }
DEV unsigned ld4(rsrc_t r, unsigned v, unsigned s) { return __builtin_amdgcn_raw_buffer_load_b32(r, v, s, 0); }
DEV unsigned ld2(rsrc_t r, unsigned v, unsigned s) { return __builtin_amdgcn_raw_buffer_load_b16(r, v, s, 0); }
DEV u4 ld16c(rsrc_t r, unsigned v, unsigned s) { return __builtin_amdgcn_raw_buffer_load_b128(r, v, s, 16); }
DEV u2 ld8c(rsrc_t r, unsigned v, unsigned s) { return __builtin_amdgcn_raw_buffer_load_b64(r, v, s, 16); }
DEV unsigned ld4c(rsrc_t r, unsigned v, unsigned s) { return __builtin_amdgcn_raw_buffer_load_b32(r, v, s, 16); }
DEV void st16c(u4 d, rsrc_t r, unsigned v, unsigned s) { __builtin_amdgcn_raw_buffer_store_b128(d, r, v + s, 0, 16); }
DEV void st8c(u2 d, rsrc_t r, unsigned v, unsigned s) { __builtin_amdgcn_raw_buffer_store_b64(d, r, v, s, 16); }
DEV void st4c(unsigned d, rsrc_t r, unsigned v, unsigned s) { __builtin_amdgcn_raw_buffer_store_b32(d, r, v, s, 16); }
DEV void fence() { asm volatile("" ::: "memory"); }
DEV void drain() { asm volatile("s_waitcnt vmcnt(0)" ::: "memory"); }
DEV void bar() { asm volatile("s_waitcnt lgkmcnt(0)\n\ts_barrier" ::: "memory"); }
DEV bool all_lanes(bool ok) { return __builtin_amdgcn_ballot_w64(!ok) == 0ull; }

DEV float bf2f(unsigned h) { return __uint_as_float(h << 16); }
DEV unsigned pk(float a, float b) {
  f2 v = {a, b};
  return __builtin_bit_cast(unsigned, __builtin_convertvector(v, bf2));
}
DEV float rbf(float a) { return bf2f(pk(a, a) & 0xffffu); }
DEV float smul(float a, float b) {
  float r;
  asm("v_mul_f32 %0, %1, %2" : "=v"(r) : "v"(a), "v"(b));
  return r;
}
DEV float sadd(float a, float b) {
  float r;
  asm("v_add_f32 %0, %1, %2" : "=v"(r) : "v"(a), "v"(b));
  return r;
}
DEV f4 mma(u4 a, u4 b, f4 c) {
  return __builtin_amdgcn_mfma_f32_16x16x32_bf16(__builtin_bit_cast(bf8, a), __builtin_bit_cast(bf8, b), c, 0, 0, 0);
}
DEV f4 smma(u4 a, u8 b, f4 c, unsigned idx) {
  return __builtin_amdgcn_smfmac_f32_16x16x64_bf16(__builtin_bit_cast(bf8, a), __builtin_bit_cast(bf16v, b), c,
                                                   (int)idx, 0, 0);
}
DEV float swap32_add(float a, float b) {
  auto r = __builtin_amdgcn_permlane32_swap(__float_as_uint(a), __float_as_uint(b), false, false);
  return __uint_as_float(r[0]) + __uint_as_float(r[1]);
}
DEV float swap16_add(float a, float b) {
  auto r = __builtin_amdgcn_permlane16_swap(__float_as_uint(a), __float_as_uint(b), false, false);
  return __uint_as_float(r[0]) + __uint_as_float(r[1]);
}
DEV float group_max(float m) {
  auto r = __builtin_amdgcn_permlane16_swap(__float_as_uint(m), __float_as_uint(m), false, false);
  m = fmaxf(__uint_as_float(r[0]), __uint_as_float(r[1]));
  auto q = __builtin_amdgcn_permlane32_swap(__float_as_uint(m), __float_as_uint(m), false, false);
  return fmaxf(__uint_as_float(q[0]), __uint_as_float(q[1]));
}
DEV float group_sum(float m) {
  const float t = swap16_add(m, m);
  return swap32_add(t, t);
}
DEV u4& lds16(char* sm, unsigned off) { return *reinterpret_cast<u4*>(sm + off); }
DEV void lds_put2(char* sm, unsigned off, unsigned v) { *reinterpret_cast<unsigned short*>(sm + off) = (unsigned short)v; }

#define STAMP(slot, idx) do { } while (0)

#define TSTAMP(idx) do { \
  __builtin_amdgcn_raw_buffer_store_b32((unsigned)__builtin_amdgcn_s_memrealtime(), mk(p.ws, WS_BYTES), O_DIAG + (blockIdx.x * 1024u + 14u * 64u + (idx)) * 4u, 0, 0); } while (0)

DEV unsigned wait_flags(rsrc_t ws, unsigned off, unsigned need, bool& alive) {
  unsigned v = 0;
  for (unsigned it = 0; alive; ++it) {
    fence();
    v = ld4c(ws, off, 0);
    if (all_lanes(v >= need)) break;
    if (it > SPIN_CAP) alive = false;
    __builtin_amdgcn_s_sleep(WSLEEP);
  }
  fence();
  return v;
}
DEV bool filled(u4 v) { return max(max(v[0], v[1]), max(v[2], v[3])) != ~0u; }
DEV unsigned crit_base(const Params& p) {
  const unsigned lo = (unsigned)(uintptr_t)p.ws;
  return ((lo + H_END + 0x1fffffu) & ~0x1fffffu) - lo;
}
struct Hs { unsigned x, a; };
constexpr unsigned L_HS = DYN_LDS - 16u;
DEV unsigned& lds4(char* sm, unsigned off) { return *reinterpret_cast<unsigned*>(sm + off); }
DEV Hs hs_begin(rsrc_t ws) {
  const unsigned o = threadIdx.x == 0 ? blockIdx.x * FL : OOB;
  return Hs{ld4c(ws, o + H_Y, 0), ld4c(ws, o + H_X + 4u, 0)};
}
DEV void hs_post(rsrc_t ws, Hs& h, char* sm) {
  h.x = (h.x & ~3u) + 4u;
  st4c(h.x, ws, threadIdx.x == 0 ? blockIdx.x * FL + H_X : OOB, 0);
  lds4(sm, threadIdx.x == 0 ? L_HS : L_HS + 4u) = h.x;
}
DEV unsigned hs_echo(rsrc_t ws) { return ld4c(ws, blockIdx.x * FL + H_Y, 0); }
DEV unsigned hs_wait(rsrc_t ws, char* sm, unsigned e, unsigned stage, bool& alive) {
  const unsigned x = rfl(lds4(sm, L_HS));
  unsigned v = rfl(e) - x;
  for (unsigned it = 0; alive && (v < stage || v > 3u); ++it) {
    if (it > SPIN_CAP) alive = false;
    __builtin_amdgcn_s_sleep(WSLEEP);
    fence();
    v = rfl(hs_echo(ws)) - x;
  }
  fence();
  return v;
}
DEV void hs_ack(rsrc_t ws, const Hs& h, unsigned d) {
  st4c(h.a + d, ws, threadIdx.x == 0 ? blockIdx.x * FL + H_X + 4u : OOB, 0);
}
DEV unsigned window(const Params& p, unsigned stage) { return crit_base(p) + (stage == 3u ? 8192u : 0u); }

DEV void stage_slice(rsrc_t r, unsigned h, unsigned row0, unsigned l, u4 (&d)[16]) {
  const unsigned v = (l >> 4) * 8192u + h * 256u + ((l & 15) ^ ((l >> 4) << 1)) * 16u;
#pragma unroll
  for (int i = 0; i < 16; ++i) d[i] = ld16w(r, v ^ ((i & 2) * 64u), (row0 + 4u * i) * 8192u);
}
DEV void stage_wo(rsrc_t r, unsigned h, unsigned w, unsigned l, u4 (&d)[16]) {
#pragma unroll
  for (int i = 0; i < 16; ++i)
    d[i] = ld16w(r, (l ^ (((i & 3) | ((i >> 1) & 4)) << 1)) * 16u, (h * 128u + w * 16u + i) * 1024u);
}
DEV void put_image(char* sm, unsigned w, unsigned l, const u4 (&d)[16]) {
#pragma unroll
  for (int i = 0; i < 16; ++i) lds16(sm, (16u * w + i) * 1024u + l * 16u) = d[i];
}
typedef __attribute__((ext_vector_type(4))) short s4;
DEV u2 lds_tr(char* sm, unsigned off) {
  return __builtin_bit_cast(u2, __builtin_amdgcn_ds_read_tr16_b64_v4i16((__attribute__((address_space(3))) s4*)(sm + off)));
}
DEV unsigned frag_base(unsigned pitch, unsigned cb, unsigned l) {
  const unsigned g = l >> 4, q = (l >> 2) & 3, p = l & 3;
  return (8u * g + q) * pitch + ((2u * cb + (p >> 1)) ^ ((q | ((g & 1) << 2)) << 1)) * 16u + (p & 1) * 8u;
}
DEV u4 frag(char* sm, unsigned off, unsigned pitch) {
  const u2 a = lds_tr(sm, off), b = lds_tr(sm, off + 4u * pitch);
  return u4{a[0], a[1], b[0], b[1]};
}
DEV u8 cat(u4 a, u4 b) { return __builtin_shufflevector(a, b, 0, 1, 2, 3, 4, 5, 6, 7); }
DEV u16v cat(u8 a, u8 b) { return __builtin_shufflevector(a, b, 0, 1, 2, 3, 4, 5, 6, 7, 8, 9, 10, 11, 12, 13, 14, 15); }
DEV u32v cat(u8 a, u8 b, u8 c, u8 d) {
  return __builtin_shufflevector(cat(a, b), cat(c, d), 0, 1, 2, 3, 4, 5, 6, 7, 8, 9, 10, 11, 12, 13, 14, 15, 16, 17, 18,
                                 19, 20, 21, 22, 23, 24, 25, 26, 27, 28, 29, 30, 31);
}
DEV u4 srd(const void* p, unsigned bytes) {
  return u4{(unsigned)(uintptr_t)p, (unsigned)((uintptr_t)p >> 32) & 0xffffu, bytes, 0x00020000u};
}
DEV unsigned lds_base(char* sm) { return (unsigned)(uintptr_t)(__attribute__((address_space(3))) char*)sm; }
DEV unsigned swz(unsigned g) { return (g & 1) | ((g & 2) << 1); }
DEV void sched() { __builtin_amdgcn_sched_barrier(0); }

DEV u4 load_mem0(const Params& p, unsigned w, unsigned c, unsigned g) {
  const rsrc_t r = mk(p.mem0, 8192);
  const unsigned voff = ((c >> 1) * 512u + 64u * w + 16u * g + 2u * (c & 1)) * 2u;
  u4 a;
#pragma unroll
  for (int i = 0; i < 4; ++i) a[i] = ld4(r, voff, 8 * i);
  return a;
}
constexpr float SCALE_LOG2E = 0.08838834764831845f * 1.4426950408889634f;

#define A_STAMP(r)
#define A_SAVE(r, slot)
#define A_NEXT "s_add_u32 %[dg], %[dg], 4\n"
#define A_KXWAIT "s_waitcnt vmcnt(2)\n"
#define A_DIAG_OUT , [dg] "+s"(dg)
#define A_DIAG_CLOB , "v254", "v255"
#define A_CLK(r)
#define A_SAVECLK
#define A_DIAG_DECL unsigned dg = rfl(O_DIAG + blockIdx.x * 4096u)
#define A_STEP "s_memrealtime s[88:89]\ns_waitcnt lgkmcnt(0)\nv_mov_b32 v254, s88\nv_mov_b32 v255, 0\ns_mov_b64 exec, 1\n" \
  "buffer_store_dword v254, v255, %[ws], %[dg] offen\ns_mov_b64 exec, -1\n"
#define A_SCLOB "s80", "s81", "s82", "s83", "s84", "s85", "s86", "s87", "s88", "s89", "s90", "s91", "vcc", "scc", "memory"
#define SMF(d, a, b, i) "v_smfmac_f32_16x16x64_bf16 v[" d "], v[" a "], v[" b "], " i "\n"
#define MFM(d, a, b, c) "v_mfma_f32_16x16x32_bf16 v[" d "], v[" a "], v[" b "], " c "\n"
#define DPP_ALL " row_mask:0xf bank_mask:0xf\n"
#define A_POLL_MEM \
  ".Lpoll%=:\n" \
  "buffer_load_dwordx4 v[148:151], v129, %[ws], %[mem] offen sc1\n" \
  "s_sub_u32 %[cap], %[cap], 1\n" \
  "s_cbranch_scc1 .Lexit%=\n" \
  "s_waitcnt vmcnt(0)\n" \
  "ds_write_b128 v130, v[148:151]\n" \
  "v_max3_u32 v152, v148, v149, v150\n" \
  "v_max_u32 v152, v152, v151\n" \
  "v_cmp_eq_u32 vcc, -1, v152\n" \
  "s_cbranch_vccnz .Lpoll%=\n"
#define A_READ_MEM \
  "s_waitcnt lgkmcnt(0)\n" \
  "s_barrier\n" \
  A_CLK("s[72:73]") \
  "ds_read_b128 v[152:155], v131\n" \
  "ds_read_b128 v[156:159], v132\n" \
  "ds_read_b128 v[160:163], v133\n" \
  "ds_read_b128 v[164:167], v134\n" \
  "ds_read_b128 v[168:171], v135\n" \
  "ds_read_b128 v[172:175], v136\n" \
  "ds_read_b128 v[176:179], v137\n"
#define D_S1 0
#define D_S0 0
#define D_VM 0
#define D_V1 0
#define D_U 8
#define STR_(x) #x
#define SLEEP(x) "s_sleep " STR_(x) "\n"
#define WL(n) "s_waitcnt lgkmcnt(" #n ")\n"
#define WV(n) "s_waitcnt vmcnt(" #n ")\n"

DEV void role_s1(const Params& p, unsigned h, char* sm) {
  constexpr unsigned L_MEM = 131072, L_Q = L_MEM + 8192, L_K = L_MEM + 10240;
  const unsigned l = threadIdx.x & 63, w = rfl(threadIdx.x >> 6), c = l & 15, g = l >> 4;
  const rsrc_t ws = mk(p.ws, WS_BYTES);
  bool alive = true;
  STAMP(15, w);

  u8 wa[8], wb[8];
  u4 a0;
  unsigned cb, echo;
  {
    const rsrc_t wr = mk(w < 4 ? p.wq : p.wk, 4u << 20);
    Hs hs = hs_begin(ws);
    u4 d0[16], d1[16];
    stage_slice(wr, h, 64u * w, l, d0);
    fence();
    hs_post(ws, hs, sm);
    fence();
    stage_slice(wr, h, 64u * (w ^ 4u), l, d1);
    a0 = load_mem0(p, w, c, g);
    const unsigned fa = (w >> 2) * 65536u + frag_base(256u, 2u * (w & 3), l), fb = fa ^ 32u;
#pragma unroll
    for (int k = 0; k < 2; ++k) {
      put_image(sm, w, l, k ? d1 : d0);
      bar();
#pragma unroll
      for (int i = 0; i < 4; ++i) {
        const unsigned o = ((w ^ i) & 3) * 16384u;
        wa[4 * k + i] = cat(frag(sm, fa + o, 256u), frag(sm, fa + o + 8192u, 256u));
        wb[4 * k + i] = cat(frag(sm, fb + o, 256u), frag(sm, fb + o + 8192u, 256u));
      }
      if (k == 0) {
        bar();
        echo = hs_echo(ws);
      }
    }
    cb = window(p, hs_wait(ws, sm, echo, 2u, alive));
    hs_ack(ws, hs, 1u);
  }
  STAMP(15, 8 + w);

  const unsigned lb = lds_base(sm);
  u16v k0;
  u8 k1;
  k0[0] = (c & 1) ? 0xeeeeeeeeu : 0x44444444u;
  k0[1] = w * 1024u + l * 16u;
#pragma unroll
  for (int i = 0; i < 8; ++i) k0[2 + i] = lb + L_MEM + (w ^ i) * 1024u + l * 16u;
#pragma unroll
  for (int r = 0; r < 2; ++r)
    k0[10 + r] = lb + (w < 4 ? L_Q : L_K) + (((w & 3) * 4u + (c >> 2)) * 8u + ((2u * g + r) ^ swz(c >> 2))) * 16u + (c & 3) * 4u;
  k0[12] = k0[13] = 0;
  k0[14] = lb + L_Q + (g * 8u + ((c & 7) ^ swz(g))) * 16u;
  k0[15] = lb + L_K + (g * 8u + ((((c >> 2) & 1) * 4u + (c >> 3) * 2u + (c & 1)) ^ swz(g))) * 16u;
  const unsigned piece = ((c & 7) >> 2) * 4u + (h >> 3), hl = h & 7;
  const unsigned p_base = cb + (B_P + (piece >> 1)) * 16384u + (piece & 1) * 4096u;
  const unsigned tok = ((c >> 1) & 1) * 16u + (c >> 3) * 8u + ((c >> 2) & 1) * 2u + (c & 1);
  k1[0] = p_base + (hl >> 1) * 512u + (2u * (hl & 1) + (g >> 1)) * 128u + ((g & 1) * 4u + (c & 3)) * 16u;
  k1[1] = p_base + 2048u + (hl >> 1) * 128u + ((g >> 1) * 4u + (c & 3)) * 16u + (hl & 1) * 8u;
  k1[2] = O_KX + h * 8192u + ((tok >> 4) * 64u + g * 16u + (tok & 15)) * 16u;
  k1[3] = k1[2] + 4096u;
  k1[4] = w == 0 ? F_KX + (h * 4u + (l & 3)) * FL : OOB;
  k1[5] = lb + L_K + l * 16u;
  k1[6] = O_KM + h * 2048u + l * 16u;
  k1[7] = F_KM + h * FL;

  u32v kx = {};
  unsigned seen;
  if (w == 0) {
    wait_flags(ws, k1[4], 1u, alive);
    u4 f[8];
#pragma unroll
    for (int i = 0; i < 8; ++i) f[i] = ld16c(ws, k1[2] + (i & 3) * 2048u + (i >> 2) * 64u, 0);
    kx = cat(cat(f[0], f[1]), cat(f[2], f[3]), cat(f[4], f[5]), cat(f[6], f[7]));
  }
  STAMP(15, 16 + w);
  if (w == 0) TSTAMP(1);

  unsigned s = 0, mem = rfl(cb + B_MEM * 16384u - 16384u), cap = rfl(alive ? SPIN_CAP : 0u), kxo = 262144u, kmo = 0;
  u32v t0;
  u8 t1;
  u16v t2;
  A_DIAG_DECL;
  asm volatile(
      "s_mov_b32 s80, 0xff00ff00\n"
      "s_mov_b32 s81, 0xff00ff00\n"
      "s_mov_b32 s82, 0x3e0293ee\n"
      "s_mov_b32 s83, 0\n"
      "s_cmp_eq_u32 %[w], 0\n"
      "s_cselect_b32 s90, 63, 0\n"
      "v_mov_b32 v184, 0\nv_mov_b32 v185, 0\nv_mov_b32 v186, 0\nv_mov_b32 v187, 0\n"
      "v_mov_b32 v188, 0\nv_mov_b32 v189, 0\nv_mov_b32 v190, 0\nv_mov_b32 v191, 0\n"
      "ds_write_b128 v130, v[148:151]\n"
      "s_branch .Lhave%=\n"
      A_POLL_MEM
      ".Lhave%=:\n"
      A_STAMP("s[84:85]") A_CLK("s[70:71]")
      SMF("184:187", "148:151", "0:7", "v128") SMF("188:191", "148:151", "8:15", "v128")
      "buffer_load_dword v248, v244, %[ws], 0 offen sc1\n"
      A_READ_MEM
      WL(6) SMF("184:187", "152:155", "16:23", "v128") SMF("188:191", "152:155", "24:31", "v128")
      WL(5) SMF("184:187", "156:159", "32:39", "v128") SMF("188:191", "156:159", "40:47", "v128")
      WL(4) SMF("184:187", "160:163", "48:55", "v128") SMF("188:191", "160:163", "56:63", "v128")
      WL(3) SMF("184:187", "164:167", "64:71", "v128") SMF("188:191", "164:167", "72:79", "v128")
      WL(2) SMF("184:187", "168:171", "80:87", "v128") SMF("188:191", "168:171", "88:95", "v128")
      WL(1) SMF("184:187", "172:175", "96:103", "v128") SMF("188:191", "172:175", "104:111", "v128")
      WL(0) SMF("184:187", "176:179", "112:119", "v128") SMF("188:191", "176:179", "120:127", "v128")
      A_CLK("s[74:75]")
      "s_cmp_lt_u32 %[s], s90\n"
      "s_nop 5\n"
      "v_add_f32 v152, v184, v185\n"
      "v_add_f32 v154, v186, v187\n"
      "v_add_f32 v153, v188, v189\n"
      "v_add_f32 v155, v190, v191\n"
      "v_cvt_pk_bf16_f32 v152, v152, v153\n"
      "v_cvt_pk_bf16_f32 v154, v154, v155\n"
      "ds_write_b32 v138, v152\n"
      "ds_write_b32 v139, v154\n"
      A_STAMP("s[86:87]") A_CLK("s[76:77]")
      "v_mov_b32 v184, 0\nv_mov_b32 v185, 0\nv_mov_b32 v186, 0\nv_mov_b32 v187, 0\n"
      "v_mov_b32 v188, 0\nv_mov_b32 v189, 0\nv_mov_b32 v190, 0\nv_mov_b32 v191, 0\n"
      "s_waitcnt lgkmcnt(0)\n"
      "s_barrier\n"
      "s_cbranch_scc0 .Lother%=\n"
      A_CLK("s[78:79]")
      "ds_read_b128 v[152:155], v142\n"
      "ds_read_b128 v[168:171], v143\n"
      "ds_read_b128 v[156:159], v142 offset:512\n"
      "ds_read_b128 v[172:175], v143 offset:512\n"
      "ds_read_b128 v[160:163], v142 offset:1024\n"
      "ds_read_b128 v[176:179], v143 offset:1024\n"
      "ds_read_b128 v[164:167], v142 offset:1536\n"
      "ds_read_b128 v[180:183], v143 offset:1536\n"
      WL(7) MFM("224:227", "192:195", "152:155", "0") MFM("228:231", "208:211", "152:155", "0")
      WL(6) MFM("232:235", "168:171", "152:155", "0")
      WL(5) MFM("224:227", "196:199", "156:159", "v[224:227]") MFM("228:231", "212:215", "156:159", "v[228:231]")
      WL(4) MFM("232:235", "172:175", "156:159", "v[232:235]")
      WL(3) MFM("224:227", "200:203", "160:163", "v[224:227]") MFM("228:231", "216:219", "160:163", "v[228:231]")
      WL(2) MFM("232:235", "176:179", "160:163", "v[232:235]")
      WL(1) MFM("224:227", "204:207", "164:167", "v[224:227]") MFM("228:231", "220:223", "164:167", "v[228:231]")
      WL(0) MFM("232:235", "180:183", "164:167", "v[232:235]")
      A_CLK("s[92:93]")
      "s_nop 5\n"
      "v_max3_f32 v152, v224, v225, v226\n"
      "v_max3_f32 v153, v227, v228, v229\n"
      "v_max3_f32 v154, v230, v231, v232\n"
      "v_max3_f32 v152, v152, v153, v233\n"
      "v_max_f32 v153, v152, v154\n"
      "v_max_f32 v152, v152, v154\n"
      "v_cndmask_b32 v156, v224, v228, s[80:81]\n"
      "v_cndmask_b32 v157, v225, v229, s[80:81]\n"
      "v_permlane16_swap_b32 v152, v153\n"
      "v_max_f32 v154, v152, v153\n"
      "v_max_f32 v155, v152, v153\n"
      "v_cndmask_b32 v158, v226, v230, s[80:81]\n"
      "v_cndmask_b32 v159, v227, v231, s[80:81]\n"
      "v_permlane32_swap_b32 v154, v155\n"
      "v_cndmask_b32 v160, v232, v233, s[80:81]\n"
      "v_max_f32 v152, v154, v155\n"
      "v_mul_f32 v152, 0xbe0293ee, v152\n"
      "v_fma_f32 v156, v156, s82, v152\n"
      "v_fma_f32 v157, v157, s82, v152\n"
      "v_fma_f32 v158, v158, s82, v152\n"
      "v_fma_f32 v159, v159, s82, v152\n"
      "v_fma_f32 v160, v160, s82, v152\n"
      "v_exp_f32 v156, v156\n"
      "v_exp_f32 v157, v157\n"
      "v_exp_f32 v158, v158\n"
      "v_exp_f32 v159, v159\n"
      "v_exp_f32 v160, v160\n"
      "v_add_f32 v152, v156, v157\n"
      "v_add_f32 v153, v158, v159\n"
      "v_add_f32 v152, v152, v160\n"
      "v_add_f32 v152, v152, v153\n"
      "v_xor_b32 v156, s83, v156\n"
      "v_xor_b32 v157, s83, v157\n"
      "v_add_f32_dpp v154, v152, v152 row_ror:8" DPP_ALL
      "v_add_f32_dpp v155, v152, v152 row_ror:8" DPP_ALL
      "v_xor_b32 v158, s83, v158\n"
      "v_xor_b32 v159, s83, v159\n"
      "v_permlane16_swap_b32 v154, v155\n"
      "v_add_f32 v152, v154, v155\n"
      "v_add_f32 v153, v154, v155\n"
      "v_xor_b32 v160, s83, v160\n"
      "s_xor_b32 s83, s83, 0x80000000\n"
      "v_permlane32_swap_b32 v152, v153\n"
      "v_add_f32 v152, v152, v153\n"
      "v_rcp_f32 v152, v152\n"
      A_CLK("s[94:95]")
      "s_add_u32 s91, %[s], 2\n"
      "v_mul_f32 v156, v156, v152\n"
      "v_mul_f32 v157, v157, v152\n"
      "v_mul_f32 v158, v158, v152\n"
      "v_mul_f32 v159, v159, v152\n"
      "v_mul_f32 v160, v160, v152\n"
      "v_cvt_pk_bf16_f32 v164, v156, v157\n"
      "v_cvt_pk_bf16_f32 v165, v158, v159\n"
      "v_mov_b32_dpp v161, v160 row_ror:8" DPP_ALL
      "v_mov_b32_dpp v166, v164 row_ror:8" DPP_ALL
      "v_mov_b32_dpp v167, v165 row_ror:8" DPP_ALL
      "v_cvt_pk_bf16_f32 v162, v160, v161\n"
      "v_cvt_pk_bf16_f32 v163, v160, v161\n"
      "s_not_b64 exec, s[80:81]\n"
      "buffer_store_dwordx4 v[164:167], v240, %[ws], 0 offen sc1\n"
      "s_mov_b64 exec, -1\n"
      "v_permlane16_swap_b32 v162, v163\n"
      "s_mov_b32 exec_lo, 0xff\n"
      "s_mov_b32 exec_hi, 0xff\n"
      "buffer_store_dwordx2 v[162:163], v241, %[ws], 0 offen sc1\n"
      "s_mov_b64 exec, -1\n"
      A_STAMP("s[88:89]") A_CLK("s[96:97]") A_SAVE("s84", 0) A_SAVE("s86", 1) A_SAVE("s88", 2) A_SAVECLK
      "s_cmp_ge_u32 %[s], 62\n"
      "s_cbranch_scc1 .Lnext%=\n"
      A_KXWAIT
      ".Lkx%=:\n"
      "v_cmp_gt_u32 vcc, s91, v248\n"
      "s_cbranch_vccz .Lkxok%=\n"
      "buffer_load_dword v248, v244, %[ws], 0 offen sc1\n"
      "s_sub_u32 %[cap], %[cap], 1\n"
      "s_cbranch_scc1 .Lexit%=\n"
      "s_waitcnt vmcnt(0)\n"
      "s_branch .Lkx%=\n"
      ".Lkxok%=:\n"
      "buffer_load_dwordx4 v[192:195], v242, %[ws], %[kxo] offen sc1\n"
      "buffer_load_dwordx4 v[208:211], v242, %[ws], %[kxo] offen offset:64 sc1\n"
      "buffer_load_dwordx4 v[196:199], v242, %[ws], %[kxo] offen offset:2048 sc1\n"
      "buffer_load_dwordx4 v[212:215], v242, %[ws], %[kxo] offen offset:2112 sc1\n"
      "buffer_load_dwordx4 v[200:203], v243, %[ws], %[kxo] offen sc1\n"
      "buffer_load_dwordx4 v[216:219], v243, %[ws], %[kxo] offen offset:64 sc1\n"
      "buffer_load_dwordx4 v[204:207], v243, %[ws], %[kxo] offen offset:2048 sc1\n"
      "buffer_load_dwordx4 v[220:223], v243, %[ws], %[kxo] offen offset:2112 sc1\n"
      "s_add_u32 %[kxo], %[kxo], 0x40000\n"
      SLEEP(D_S0)
      "s_branch .Lnext%=\n"
      ".Lidle%=:\n"
      SLEEP(D_S1)
      "s_branch .Lnext%=\n"
      ".Lother%=:\n"
      "s_cmp_lg_u32 %[w], 6\n"
      "s_cbranch_scc1 .Lidle%=\n"
      "ds_read_b128 v[152:155], v245\n"
      "ds_read_b128 v[156:159], v245 offset:1024\n"
      "s_add_u32 s91, %[s], 1\n"
      "v_mov_b32 v160, s91\n"
      WL(1) "buffer_store_dwordx4 v[152:155], v246, %[ws], %[kmo] offen sc1\n"
      WL(0) "buffer_store_dwordx4 v[156:159], v246, %[ws], %[kmo] offen offset:1024 sc1\n"
      "s_add_u32 %[kmo], %[kmo], 0x10000\n"
      "s_waitcnt vmcnt(0)\n"
      "s_mov_b64 exec, 1\n"
      "buffer_store_dword v160, v247, %[ws], 0 offen sc1\n"
      "s_mov_b64 exec, -1\n"
      SLEEP(D_S0)
      ".Lnext%=:\n"
      A_NEXT
      "s_add_u32 %[mem], %[mem], 0x4000\n"
      "s_add_u32 %[s], %[s], 1\n"
      "s_cmp_lt_u32 %[s], 64\n"
      "s_cbranch_scc1 .Lpoll%=\n"
      ".Lexit%=:\n"
      : [s] "+s"(s), [mem] "+s"(mem), [cap] "+s"(cap), [kxo] "+s"(kxo), [kmo] "+s"(kmo), "+{v[148:151]}"(a0),
        "=&{v[152:183]}"(t0), "=&{v[184:191]}"(t1), "+{v[192:223]}"(kx), "=&{v[224:239]}"(t2), "=&{v248}"(seen) A_DIAG_OUT
      : [ws] "s"(srd(p.ws, WS_BYTES)), [w] "s"(w), "{v[0:31]}"(cat(wa[0], wb[0], wa[1], wb[1])),
        "{v[32:63]}"(cat(wa[2], wb[2], wa[3], wb[3])), "{v[64:95]}"(cat(wa[4], wb[4], wa[5], wb[5])),
        "{v[96:127]}"(cat(wa[6], wb[6], wa[7], wb[7])), "{v[128:143]}"(k0), "{v[240:247]}"(k1)
      : A_SCLOB A_DIAG_CLOB);
}

DEV void read_wo(char* sm, unsigned w, unsigned l, u4 (&wo)[4][4]) {
#pragma unroll
  for (int i = 0; i < 4; ++i) {
    const unsigned fa = frag_base(1024u, 4u * w + i, l);
#pragma unroll
    for (int f = 0; f < 4; ++f) wo[i][f] = frag(sm, fa + f * 32768u, 1024u);
  }
}

DEV void role_vm(const Params& p, unsigned h, char* sm) {
  constexpr unsigned L_MEM = 131072, L_V = L_MEM + 8192;
  const unsigned l = threadIdx.x & 63, w = rfl(threadIdx.x >> 6), c = l & 15, g = l >> 4;
  const rsrc_t ws = mk(p.ws, WS_BYTES);
  bool alive = true;

  u8 wv[8], wo[4][2];
  u4 a0;
  unsigned cb;
  {
    Hs hs = hs_begin(ws);
    u4 d0[16], d1[16], t[4][4];
    stage_slice(mk(p.wv, 4u << 20), h, 64u * w, l, d0);
    fence();
    hs_post(ws, hs, sm);
    fence();
    stage_wo(mk(p.wo, 4u << 20), h, w, l, d1);
    a0 = load_mem0(p, w, c, g);
    put_image(sm, w, l, d0);
    bar();
    const unsigned fa = frag_base(256u, w, l);
#pragma unroll
    for (int i = 0; i < 8; ++i) {
      const unsigned o = fa + (w ^ i) * 16384u;
      wv[i] = cat(frag(sm, o, 256u), frag(sm, o + 8192u, 256u));
    }
    bar();
    const unsigned echo = hs_echo(ws);
    put_image(sm, w, l, d1);
    bar();
    read_wo(sm, w, l, t);
    cb = window(p, hs_wait(ws, sm, echo, 2u, alive));
    hs_ack(ws, hs, 1u);
#pragma unroll
    for (int i = 0; i < 4; ++i) {
      wo[i][0] = cat(t[i][0], t[i][1]);
      wo[i][1] = cat(t[i][2], t[i][3]);
    }
  }
  STAMP(15, 8 + w);

  const unsigned lb = lds_base(sm);
  u16v k0;
  k0[0] = (c & 1) ? 0xeeeeeeeeu : 0x44444444u;
  k0[1] = w * 1024u + l * 16u;
#pragma unroll
  for (int i = 0; i < 8; ++i) k0[2 + i] = lb + L_MEM + (w ^ i) * 1024u + l * 16u;
  k0[10] = lb + L_V + (w * 16u + 4u * g + ((c >> 1) & 1)) * 16u + (2u * (c >> 2) + (c & 1)) * 2u;
  k0[11] = lb + L_V + l * 16u;
  const unsigned cg = 4u * w + g;
  k0[12] = cb + (B_M + cg) * 16384u + h * 256u + c * 16u;
  k0[13] = O_M2 + cg * 8192u + h * 256u + c * 16u;
  k0[14] = threadIdx.x == 448 ? F_M + h * FL : OOB;
  k0[15] = 0;

  unsigned s = 0, mem = rfl(cb + B_MEM * 16384u - 16384u), cap = rfl(alive ? SPIN_CAP : 0u), m2o = 0;
  u32v t0;
  u16v t1, t2;
  A_DIAG_DECL;
#define VM_ZERO(a) "v_mov_b32 v" #a ", 0\n"
#define VM_ZERO_M \
      VM_ZERO(192) VM_ZERO(193) VM_ZERO(194) VM_ZERO(195) VM_ZERO(196) VM_ZERO(197) VM_ZERO(198) VM_ZERO(199) \
      VM_ZERO(200) VM_ZERO(201) VM_ZERO(202) VM_ZERO(203) VM_ZERO(204) VM_ZERO(205) VM_ZERO(206) VM_ZERO(207)
  asm volatile(
      "s_mov_b32 s83, 0\n"
      VM_ZERO(180) VM_ZERO(184) VM_ZERO(185) VM_ZERO(186) VM_ZERO(187) VM_ZERO_M
      "ds_write_b128 v130, v[148:151]\n"
      "s_branch .Lhave%=\n"
      A_POLL_MEM
      ".Lhave%=:\n"
      A_STAMP("s[84:85]") A_CLK("s[70:71]")
      SMF("184:187", "148:151", "0:7", "v128")
      A_READ_MEM
      WL(6) SMF("184:187", "152:155", "8:15", "v128")
      WL(5) SMF("184:187", "156:159", "16:23", "v128")
      WL(4) SMF("184:187", "160:163", "24:31", "v128")
      WL(3) SMF("184:187", "164:167", "32:39", "v128")
      WL(2) SMF("184:187", "168:171", "40:47", "v128")
      WL(1) SMF("184:187", "172:175", "48:55", "v128")
      WL(0) SMF("184:187", "176:179", "56:63", "v128")
      A_CLK("s[74:75]")
      "s_nop 7\n"
      "v_add_f32 v152, v184, v185\n"
      "v_add_f32 v153, v186, v187\n"
      "v_cvt_pk_bf16_f32 v152, v152, v153\n"
      "ds_write_b16 v138, v152\n"
      "ds_write_b16_d16_hi v138, v152 offset:32\n"
      A_STAMP("s[86:87]") A_CLK("s[76:77]")
      VM_ZERO(184) VM_ZERO(185) VM_ZERO(186) VM_ZERO(187)
      "s_waitcnt lgkmcnt(0)\n"
      "s_barrier\n"
      A_CLK("s[78:79]")
      "ds_read_b128 v[152:155], v139\n"
      "ds_read_b128 v[156:159], v139 offset:1024\n"
      "buffer_store_dword v180, v142, %[ws], 0 offen sc1\n"
      WL(1) SMF("192:195", "152:155", "64:71", "v128") SMF("200:203", "152:155", "96:103", "v128")
      WL(0) SMF("192:195", "156:159", "72:79", "v128") SMF("200:203", "156:159", "104:111", "v128")
      SMF("196:199", "152:155", "80:87", "v128") SMF("204:207", "152:155", "112:119", "v128")
      SMF("196:199", "156:159", "88:95", "v128") SMF("204:207", "156:159", "120:127", "v128")
      A_CLK("s[92:93]")
      "s_nop 2\n"
      "v_add_f32 v192, v192, v193\n"
      "v_add_f32 v200, v200, v201\n"
      "v_add_f32 v194, v194, v195\n"
      "v_add_f32 v202, v202, v203\n"
      "v_cvt_pk_bf16_f32 v160, v192, v194\n"
      "v_cvt_pk_bf16_f32 v162, v200, v202\n"
      "v_bfi_b32 v160, -2, v160, s83\n"
      "v_bfi_b32 v162, -2, v162, s83\n"
      "v_add_f32 v196, v196, v197\n"
      "v_add_f32 v204, v204, v205\n"
      "v_add_f32 v198, v198, v199\n"
      "v_add_f32 v206, v206, v207\n"
      "v_cvt_pk_bf16_f32 v161, v196, v198\n"
      "v_cvt_pk_bf16_f32 v163, v204, v206\n"
      "v_bfi_b32 v161, -2, v161, s83\n"
      "v_bfi_b32 v163, -2, v163, s83\n"
      "v_permlane32_swap_b32 v160, v162\n"
      "s_add_u32 %[s], %[s], 1\n"
      "v_permlane32_swap_b32 v161, v163\n"
      "s_nop 1\n"
      "v_permlane16_swap_b32 v160, v161\n"
      "v_permlane16_swap_b32 v162, v163\n"
      "buffer_store_dwordx4 v[160:163], v140, %[ws], 0 offen sc1\n"
      A_STAMP("s[88:89]") A_CLK("s[94:95]") A_CLK("s[96:97]")
      "buffer_store_dwordx4 v[160:163], v141, %[ws], %[m2o] offen sc1\n"
      "v_mov_b32 v180, %[s]\n"
      VM_ZERO_M
      "s_xor_b32 s83, s83, 1\n"
      "s_add_u32 %[m2o], %[m2o], 0x40000\n"
      "s_add_u32 %[mem], %[mem], 0x4000\n"
      A_NEXT
      SLEEP(D_VM)
      "s_cmp_lt_u32 %[s], 64\n"
      "s_cbranch_scc1 .Lpoll%=\n"
      "s_waitcnt vmcnt(0)\n"
      "s_barrier\n"
      "buffer_store_dword v180, v142, %[ws], 0 offen sc1\n"
      ".Lexit%=:\n"
      : [s] "+s"(s), [mem] "+s"(mem), [cap] "+s"(cap), [m2o] "+s"(m2o), "+{v[148:151]}"(a0), "=&{v[152:183]}"(t0),
        "=&{v[184:199]}"(t1), "=&{v[200:215]}"(t2) A_DIAG_OUT
      : [ws] "s"(srd(p.ws, WS_BYTES)), [w] "s"(w), "{v[0:31]}"(cat(wv[0], wv[1], wv[2], wv[3])),
        "{v[32:63]}"(cat(wv[4], wv[5], wv[6], wv[7])), "{v[64:95]}"(cat(wo[0][0], wo[0][1], wo[1][0], wo[1][1])),
        "{v[96:127]}"(cat(wo[2][0], wo[2][1], wo[3][0], wo[3][1])), "{v[128:143]}"(k0)
      : A_SCLOB A_DIAG_CLOB);
}

#define PROBES 6
constexpr unsigned PROBER = 32, PROBER_BLOCK = 2;
DEV void lead(rsrc_t ws) {
  const unsigned l = threadIdx.x & 63;
  unsigned i, a0[4], last[4];
  const bool init = role_of(l, i) == R_V1;
#pragma unroll
  for (int k = 0; k < 4; ++k) {
    a0[k] = ld4c(ws, H_X + (k * 64u + l) * FL + 4u, 0);
  }
#pragma unroll
  for (unsigned k = 0; k < N_FLAGS / 64; ++k) st4c(0u, ws, (k * 64u + l) * FL, 0);
  drain();
  for (unsigned it = 0; it < SPIN_CAP; ++it) {
    fence();
    u2 v[4];
#pragma unroll
    for (int k = 0; k < 4; ++k) v[k] = ld8c(ws, H_X + (k * 64u + l) * FL, 0);
    const bool done = v[0][1] != a0[0] && v[1][1] != a0[1] && v[2][1] != a0[2] && v[3][1] != a0[3];
    const unsigned half = __builtin_amdgcn_readlane(v[0][1] - a0[0], PROBER_BLOCK);
    const unsigned stage = all_lanes(done || !init) ? (half == 2u ? 3u : 2u) : 1u;
#pragma unroll
    for (int k = 0; k < 4; ++k) {
      const unsigned e = (v[k][0] & ~3u) | stage;
      st4c(e, ws, it == 0 || e != last[k] ? H_Y + (k * 64u + l) * FL : OOB, 0);
      last[k] = e;
    }
    if (all_lanes(done)) break;
  }
}
DEV void role_v1(const Params& p, unsigned j, char* sm) {
  const unsigned l = threadIdx.x & 63, w = rfl(threadIdx.x >> 6), c = l & 15, g = l >> 4;
  const unsigned cg = j >> 1, rg = j & 1;
  const rsrc_t ws = mk(p.ws, WS_BYTES);
  bool alive = true;
  if (j == 0 && w == 7) {
    lead(ws);
    return;
  }
  const unsigned cb0 = crit_base(p);
  const u4 ones = {~0u, ~0u, ~0u, ~0u};
  Hs hs = hs_begin(ws);
  if (j) {
    for (unsigned o = ((j - 1u) * 512u + threadIdx.x) * 16u; o < B_END * 16384u; o += 63u * 512u * 16u)
      st16c(ones, ws, o, cb0);
    constexpr unsigned N = (NSEG - FAST) * 65536u;
    for (unsigned o = ((j - 1u) * 512u + threadIdx.x) * 16u; o < 2u * N + N / 4u; o += 63u * 512u * 16u)
      st16c(ones, ws, o + (o < N ? O_KM + FAST * 65536u : o < 2u * N ? O_PX + FAST * 65536u - N : O_PM + FAST * 16384u - 2u * N), 0);
  }
  hs_post(ws, hs, sm);
  drain();
  unsigned half = 0;
  if (j == PROBER && w == 0) {
    unsigned best[2] = {~0u, ~0u};
    for (int k = 0; k < PROBES; ++k) {
      const unsigned off = B_PROBE * 16384u + (k & 1) * 8192u + (l & 7) * 16u + (k >> 1) * 1024u;
      const unsigned long long t0 = __builtin_amdgcn_s_memtime();
      asm volatile("s_waitcnt lgkmcnt(0)" ::: "memory");
      st16c(ones, ws, off, cb0);
      drain();
      const unsigned long long t1 = __builtin_amdgcn_s_memtime();
      asm volatile("s_waitcnt lgkmcnt(0)" ::: "memory");
      const unsigned dt = (unsigned)(t1 - t0);
      if (k & 1) best[1] = min(best[1], dt); else best[0] = min(best[0], dt);
    }
    half = best[1] < best[0];
  }
  if (j) bar();
  if (w >= 4) return;
  if (w == 0) {
    hs_wait(ws, sm, hs_echo(ws), 1u, alive);
    hs_ack(ws, hs, 1u + half);
  }
  if (j == 0) bar();
  const unsigned cb = window(p, hs_wait(ws, sm, hs_echo(ws), 2u, alive));
  STAMP(15, 8 + w);

  const unsigned lb = lds_base(sm);
  const unsigned piece = rg * 4u + w;
  u16v k0 = {};
  k0[0] = (c & 4) ? 0xeeeeeeeeu : 0x44444444u;
  k0[1] = cb + (B_P + (piece >> 1)) * 16384u + (piece & 1) * 4096u + g * 128u + (c & 7) * 16u + 2048u;
  k0[2] = k0[1] - 2048u + (c >> 3) * 512u;
  k0[3] = cb + (B_M + cg) * 16384u + w * 2048u + l * 16u;
  k0[4] = O_U + (cg * 32u + 8u * w) * 1024u + l * 16u;
  k0[5] = k0[4] + 4096u;
  k0[6] = F_U + ((8u * w + (l & 7)) * 8u + (cg >> 2)) * FL;
  k0[7] = lb + ((g * 2u + ((c >> 1) & 1)) * 8u + 2u * (c >> 2) + (c & 1)) * 16u + w * 4u;
  k0[8] = lb + l * 16u;
  k0[9] = cg * 256u + rg * 128u + (l >> 3) * 16u;

  unsigned s = 0, mem = rfl(cb + B_MEM * 16384u), cap = rfl(alive ? SPIN_CAP : 0u), uo = 0, seen = 0;
  u32v t0, t1;
  A_DIAG_DECL;
#define V1_TAGS(d, a, b, c, OP) "v_bitop3_b32 v" d ", v" a ", v" b ", v" c " bitop3:" OP "\n"
#define V1_ZERO "v_mov_b32 v52, 0\nv_mov_b32 v53, 0\nv_mov_b32 v54, 0\nv_mov_b32 v55, 0\n" \
                "v_mov_b32 v56, 0\nv_mov_b32 v57, 0\nv_mov_b32 v58, 0\nv_mov_b32 v59, 0\n"
#define V1_LOAD_P \
      "buffer_load_dwordx4 v[40:43], v66, %[ws], 0 offen sc1\n" \
      "buffer_load_dwordx4 v[44:47], v66, %[ws], 0 offen offset:1024 sc1\n" \
      "buffer_load_dwordx4 v[48:51], v65, %[ws], 0 offen sc1\n"
#define V1_STEP(P, OP, MWANT, PWANT, NEG) \
      ".Ltop" P "%=:\n" \
      "v_cmp_ge_u32 vcc, %[s], v80\n" \
      "s_cbranch_vccnz .Luw" P "%=\n" \
      SLEEP(D_U) \
      "buffer_load_dwordx4 v[0:3], v68, %[ws], %[uo] offen sc1\n" \
      "buffer_load_dwordx4 v[4:7], v68, %[ws], %[uo] offen offset:1024 sc1\n" \
      "buffer_load_dwordx4 v[8:11], v68, %[ws], %[uo] offen offset:2048 sc1\n" \
      "buffer_load_dwordx4 v[12:15], v68, %[ws], %[uo] offen offset:3072 sc1\n" \
      "buffer_load_dwordx4 v[16:19], v69, %[ws], %[uo] offen sc1\n" \
      "buffer_load_dwordx4 v[20:23], v69, %[ws], %[uo] offen offset:1024 sc1\n" \
      "buffer_load_dwordx4 v[24:27], v69, %[ws], %[uo] offen offset:2048 sc1\n" \
      "buffer_load_dwordx4 v[28:31], v69, %[ws], %[uo] offen offset:3072 sc1\n" \
      "buffer_load_dword v80, v70, %[ws], 0 offen sc1\n" \
      "s_add_u32 %[uo], %[uo], 0x100000\n" \
      V1_ZERO \
      SLEEP(D_V1) \
      ".Lboth" P "%=:\n" \
      "buffer_load_dwordx4 v[32:35], v67, %[ws], 0 offen sc1\n" \
      "buffer_load_dwordx4 v[36:39], v67, %[ws], 0 offen offset:1024 sc1\n" \
      V1_LOAD_P \
      WV(3) \
      V1_TAGS("60", "32", "33", "34", OP) V1_TAGS("61", "35", "36", "37", OP) V1_TAGS("60", "60", "38", "39", OP) \
      V1_TAGS("60", "60", "61", "61", OP) \
      "v_and_b32 v60, 1, v60\n" \
      "v_cmp_ne_u32 s[80:81], " MWANT ", v60\n" \
      ".Lp" P "%=:\n" \
      WV(2) SMF("52:55", "40:43", "0:7", "v64") SMF("56:59", "40:43", "8:15", "v64") \
      V1_TAGS("60", "40", "41", "42", OP) \
      WV(1) SMF("52:55", "44:47", "16:23", "v64") SMF("56:59", "44:47", "24:31", "v64") \
      V1_TAGS("60", "60", "43", "44", OP) V1_TAGS("61", "45", "46", "47", OP) \
      WV(0) SMF("52:55", "48:51", "32:39", "v64") \
      V1_TAGS("60", "60", "48", "49", OP) V1_TAGS("61", "61", "50", "51", OP) V1_TAGS("60", "60", "61", "61", OP) \
      "v_and_b32 v60, s82, v60\n" \
      "v_cmp_ne_u32 vcc, " PWANT ", v60\n" \
      "s_or_b64 vcc, vcc, s[80:81]\n" \
      "s_cbranch_scc1 .Lre" P "%=\n" \
      A_STAMP("s[86:87]") A_CLK("s[70:71]") \
      "s_nop 0\n" \
      "v_permlane32_swap_b32 v56, v54\n" \
      "v_permlane32_swap_b32 v57, v55\n" \
      "v_add_f32 v60, v52, v54\n" \
      "v_add_f32 v62, v58, v56\n" \
      "v_add_f32 v61, v53, v55\n" \
      "v_add_f32 v63, v59, v57\n" \
      "v_cndmask_b32 v60, v60, v62, s[84:85]\n" \
      "v_cndmask_b32 v61, v61, v63, s[84:85]\n" \
      "s_cmp_lg_u32 %[w], 0\n" \
      "s_nop 0\n" \
      "v_permlane16_swap_b32 v60, v61\n" \
      "v_add_f32 v60, v60, v61\n" \
      "ds_write_b32 v71, v60 offset:" P "*1024\n" \
      A_CLK("s[72:73]") \
      "s_waitcnt lgkmcnt(0)\n" \
      "s_barrier\n" \
      "s_cbranch_scc1 .Lend" P "%=\n" \
      A_CLK("s[74:75]") \
      "ds_read_b128 v[52:55], v72 offset:" P "*1024\n" \
      "s_waitcnt lgkmcnt(0)\n" \
      A_CLK("s[76:77]") \
      "v_add_f32 v52, v52, v53\n" \
      "v_add_f32 v54, v54, v55\n" \
      "v_add_f32_e64 v52, " NEG "v52, " NEG "v54\n" \
      "s_nop 1\n" \
      "v_mov_b32_dpp v53, v52 quad_perm:[1,0,3,2]" DPP_ALL \
      "v_cvt_pk_bf16_f32 v56, v52, v53\n" \
      "s_nop 1\n" \
      "v_mov_b32_dpp v57, v56 row_shl:2" DPP_ALL \
      "v_mov_b32_dpp v58, v56 row_shl:4" DPP_ALL \
      "v_mov_b32_dpp v59, v56 row_shl:6" DPP_ALL \
      "s_mov_b64 exec, s[90:91]\n" \
      "buffer_store_dwordx4 v[56:59], v73, %[ws], %[mem] offen sc1\n" \
      "s_mov_b64 exec, -1\n" \
      A_STEP A_STAMP("s[88:89]") A_CLK("s[78:79]") A_SAVE("s86", 1) A_SAVE("s88", 2) A_SAVECLK \
      ".Lend" P "%=:\n" \
      "s_barrier\n" \
      A_NEXT \
      "s_add_u32 %[mem], %[mem], 0x4000\n" \
      "s_add_u32 %[s], %[s], 1\n"
#define V1_SIDE(P) \
      ".Luw" P "%=:\n" \
      "buffer_load_dword v80, v70, %[ws], 0 offen sc1\n" \
      "s_sub_u32 %[cap], %[cap], 1\n" \
      "s_cbranch_scc1 .Lexit%=\n" \
      "s_waitcnt vmcnt(0)\n" \
      "s_branch .Ltop" P "%=\n" \
      ".Lre" P "%=:\n" \
      "s_sub_u32 %[cap], %[cap], 1\n" \
      "s_cbranch_scc1 .Lexit%=\n" \
      "s_cmp_lg_u64 s[80:81], 0\n" \
      V1_ZERO \
      "s_cbranch_scc1 .Lboth" P "%=\n" \
      V1_LOAD_P \
      "s_branch .Lp" P "%=\n"
  asm volatile(
      "s_mov_b32 s82, 0x80008000\n"
      "s_mov_b32 s84, 0\n"
      "s_mov_b32 s85, -1\n"
      "s_mov_b32 s90, 0x01010101\n"
      "s_mov_b32 s91, 0x01010101\n"
      V1_STEP("0", "0xfe", "0", "0", "")
      "s_cmp_ge_u32 %[s], 63\n"
      "s_cbranch_scc1 .Lexit%=\n"
      V1_STEP("1", "0x80", "1", "s82", "-")
      "s_branch .Ltop0%=\n"
      V1_SIDE("0") V1_SIDE("1")
      ".Lexit%=:\n"
      : [s] "+s"(s), [mem] "+s"(mem), [cap] "+s"(cap), [uo] "+s"(uo), "=&{v[0:31]}"(t0), "=&{v[32:63]}"(t1),
        "+{v80}"(seen) A_DIAG_OUT
      : [ws] "s"(srd(p.ws, WS_BYTES)), [w] "s"(w), "{v[64:79]}"(k0)
      : A_SCLOB A_DIAG_CLOB);
}

DEV void load_x(rsrc_t xr, unsigned w, unsigned c, unsigned g, int s, u4 (&xp)[4]) {
  const unsigned voff = ((16u * (w >> 2) + c) * 512u + 128u * (w & 3) + 8u * g) * 2u;
#pragma unroll
  for (int i = 0; i < 4; ++i) xp[i] = ld16(xr, voff + i * 64u, s * 32768u);
}
DEV void put_x(char* sm, unsigned base, unsigned w, unsigned l, const u4 (&xp)[4]) {
#pragma unroll
  for (int i = 0; i < 4; ++i) lds16(sm, base + (4u * w + i) * 1024u + l * 16u) = xp[i];
}

DEV void token_softmax(rsrc_t ws, char* sm, unsigned qb, const u4 (&km)[4], unsigned h, unsigned w, unsigned l, int s) {
  const unsigned c = l & 15, g = l >> 4;
  u4 q[4], k0[4], k1[4];
#pragma unroll
  for (int m = 0; m < 4; ++m) {
    q[m] = lds16(sm, qb + (w * 4u + m) * 1024u);
    k0[m] = lds16(sm, qb + 8192u + m * 1024u);
    k1[m] = lds16(sm, qb + 12288u + m * 1024u);
  }
  sched();
  f4 s0 = {0, 0, 0, 0}, s1 = {0, 0, 0, 0}, sm_ = {0, 0, 0, 0};
#pragma unroll
  for (int m = 0; m < 4; ++m) {
    s0 = mma(k0[m], q[m], s0);
    s1 = mma(k1[m], q[m], s1);
  }
#pragma unroll
  for (int m = 0; m < 4; ++m) sm_ = mma(km[m], q[m], sm_);
  const unsigned tq = 16u * w + c;
  const float ninf = -__builtin_inff();
#pragma unroll
  for (int r = 0; r < 4; ++r) {
    s0[r] = 4u * g + r <= tq ? s0[r] : ninf;
    s1[r] = 16u + 4u * g + r <= tq ? s1[r] : ninf;
  }
  float mx = fmaxf(fmaxf(fmaxf(s0[0], s0[1]), fmaxf(s0[2], s0[3])), fmaxf(fmaxf(s1[0], s1[1]), fmaxf(s1[2], s1[3])));
  mx = group_max(fmaxf(mx, fmaxf(sm_[0], sm_[1])));
  const float mc = -mx * SCALE_LOG2E;
  float e[10];
#pragma unroll
  for (int r = 0; r < 4; ++r) {
    e[r] = __builtin_amdgcn_exp2f(fmaf(s0[r], SCALE_LOG2E, mc));
    e[4 + r] = __builtin_amdgcn_exp2f(fmaf(s1[r], SCALE_LOG2E, mc));
  }
  e[8] = __builtin_amdgcn_exp2f(fmaf(sm_[0], SCALE_LOG2E, mc));
  e[9] = __builtin_amdgcn_exp2f(fmaf(sm_[1], SCALE_LOG2E, mc));
  const float sum = group_sum(((e[0] + e[1]) + (e[2] + e[3])) + ((e[4] + e[5]) + (e[6] + e[7])) + (e[8] + e[9]));
  const float rs = __builtin_amdgcn_rcpf(sum);
  const u4 px = {pk(e[0] * rs, e[1] * rs), pk(e[2] * rs, e[3] * rs), pk(e[4] * rs, e[5] * rs), pk(e[6] * rs, e[7] * rs)};
  st16c(px, ws, O_PX + (w * 32u + h) * 1024u + l * 16u, s * 65536u);
  const unsigned dm = pk(e[8] * rs, e[9] * rs);
  auto r16 = __builtin_amdgcn_permlane16_swap(dm, dm, false, false);
  auto ra = __builtin_amdgcn_permlane32_swap(r16[0], r16[0], false, false);
  auto rb = __builtin_amdgcn_permlane32_swap(r16[1], r16[1], false, false);
  const u4 pm = {ra[0], rb[0], ra[1], rb[1]};
  st16c(pm, ws, g == 0 ? O_PM + (w * 8u + (h >> 2)) * 1024u + ((h & 3) * 16u + c) * 16u : OOB, s * 16384u);
}

DEV void role_s2(const Params& p, unsigned h, char* sm) {
#define S2_MID 5
#define S2_SW 5
  constexpr int LAG = 4, R = LAG + 1;
  constexpr unsigned L_X = 0, L_QK = 65536;
  const unsigned l = threadIdx.x & 63, w = rfl(threadIdx.x >> 6), c = l & 15, g = l >> 4;
  const rsrc_t ws = mk(p.ws, WS_BYTES), xr = mk(p.bx, 2u << 20);
  bool alive = true;

  u4 xp[4], wa[16], wb[16];
  {
    const rsrc_t wr = mk(w < 4 ? p.wq : p.wk, 4u << 20);
    Hs hs = hs_begin(ws);
    u4 d0[16], d1[16];
    stage_slice(wr, h, 64u * (w & 3), l, d0);
    fence();
    hs_post(ws, hs, sm);
    fence();
    stage_slice(wr, h, 64u * (w & 3) + 256u, l, d1);
    load_x(xr, w, c, g, 0, xp);
    unsigned echo;
    const unsigned fa = (w >> 2) * 65536u + frag_base(256u, 2u * (w & 3), l), fb = fa ^ 32u;
#pragma unroll
    for (int k = 0; k < 2; ++k) {
      put_image(sm, w, l, k ? d1 : d0);
      bar();
#pragma unroll
      for (int i = 0; i < 8; ++i) {
        wa[8 * k + i] = frag(sm, fa + i * 8192u, 256u);
        wb[8 * k + i] = frag(sm, fb + i * 8192u, 256u);
      }
      if (k == 0) echo = hs_echo(ws);
      else hs_wait(ws, sm, echo, 1u, alive);
      bar();
    }
    hs_ack(ws, hs, 1u);
  }
  put_x(sm, L_X, w, l, xp);
  load_x(xr, w, c, g, 1, xp);
  bar();
  STAMP(15, 8 + w);
  if (w >= 4) __builtin_amdgcn_s_setprio(1);

  const unsigned km_off = w < 2 ? O_KM + h * 2048u + (g * 8u + ((2u * (c >> 2) + (c & 1)) ^ swz(g))) * 16u : OOB;
  const unsigned km_flag = w < 2 ? F_KM + h * FL : OOB;
  const unsigned px_flag = w < 2 && l == 0 ? F_PX + (w * 32u + h) * FL : OOB;
  const unsigned kx_flag = w >= 4 && l == 0 ? F_KX + (h * 4u + (w & 3)) * FL : OOB;
  const unsigned kx_off = w >= 4 ? O_KX + h * 8192u + (w & 3) * 2048u + l * 16u : OOB;
  unsigned km_seen = 0;
  int slot = 0;
  for (int it = 0; it < NSEG; ++it) {
    const int s = max(it - LAG, 0);
    const bool soft = it >= LAG && w < 2;
    if (it == S2_SW && w < 4) __builtin_amdgcn_s_setprio(2);
    if (soft && !all_lanes(km_seen > (unsigned)s)) wait_flags(ws, km_flag, s + 1, alive);
    if (w == 0 || w == 4) STAMP(3 + (w >> 2) * 4, it);
    u4 km[4];
#pragma unroll
    for (int m = 0; m < 4; ++m) km[m] = ld16c(ws, km_off + m * 512u, s * 65536u);
    km_seen = ld4c(ws, km_flag, 0);
    f4 acc[2][2] = {};
    const unsigned xb = L_X + (it & 1) * 32768u + l * 16u;
    u4 x[3][2];
#pragma unroll
    for (int k = 0; k < 2; ++k)
#pragma unroll
      for (int t = 0; t < 2; ++t) x[k][t] = lds16(sm, xb + (t * 16 + k) * 1024u);
#pragma unroll
    for (int ks = 0; ks < 16; ++ks) {
      sched();
      if (ks + 2 < 16) {
#pragma unroll
        for (int t = 0; t < 2; ++t) x[(ks + 2) % 3][t] = lds16(sm, xb + (t * 16 + ks + 2) * 1024u);
      }
      if (ks == S2_MID) {
        asm volatile("s_waitcnt vmcnt(9)" ::: "memory");
        st4c(it, ws, kx_flag, 0);
      }
      sched();
#pragma unroll
      for (int t = 0; t < 2; ++t) {
        acc[0][t] = mma(wa[ks], x[ks % 3][t], acc[0][t]);
        acc[1][t] = mma(wb[ks], x[ks % 3][t], acc[1][t]);
      }
    }
    sched();
    asm volatile("s_waitcnt vmcnt(6)" ::: "memory");
    if (w == 0 || w == 4) STAMP(4 + (w >> 2) * 4, it);
    st4c(s, ws, px_flag, 0);
    put_x(sm, L_X + ((it + 1) & 1) * 32768u, w, l, xp);
#pragma unroll
    for (int t = 0; t < 2; ++t) {
      const u4 o = {pk(acc[0][t][0], acc[1][t][0]), pk(acc[0][t][1], acc[1][t][1]), pk(acc[0][t][2], acc[1][t][2]),
                    pk(acc[0][t][3], acc[1][t][3])};
      lds16(sm, L_QK + slot * 16384u + (w >> 2) * 8192u + (t * 4u + (w & 3)) * 1024u + l * 16u) = o;
      st16c(o, ws, kx_off + t * 1024u, it * 262144u);
    }
    load_x(xr, w, c, g, it + 2, xp);
    if (w == 0 || w == 4) STAMP(5 + (w >> 2) * 4, it);
    slot = slot == R - 1 ? 0 : slot + 1;
    if (soft) {
      token_softmax(ws, sm, L_QK + slot * 16384u + l * 16u, km, h, w, l, s);
      if (w == 0) STAMP(2, s);
    }
    bar();
  }
  if (w >= 2) return;
  for (int s = NSEG - LAG; s < NSEG; ++s) {
    if (s <= FAST) {
      drain();
      st4c(s, ws, px_flag, 0);
    }
    if (s < FAST) wait_flags(ws, km_flag, s + 1, alive);
    u4 km[4];
    for (unsigned it = 0; it < SPIN_CAP; ++it) {
      fence();
#pragma unroll
      for (int m = 0; m < 4; ++m) km[m] = ld16c(ws, km_off + m * 512u, s * 65536u);
      if (s < FAST || all_lanes(filled(km[0]) && filled(km[1]) && filled(km[2]) && filled(km[3]))) break;
    }
    slot = slot == R - 1 ? 0 : slot + 1;
    token_softmax(ws, sm, L_QK + slot * 16384u + l * 16u, km, h, w, l, s);
    if (w == 0) STAMP(2, s);
  }
}

DEV void role_vu(const Params& p, unsigned h, char* sm) {
  constexpr unsigned L_X = 0, L_V = 65536;
  const unsigned l = threadIdx.x & 63, w = rfl(threadIdx.x >> 6), c = l & 15, g = l >> 4;
  const rsrc_t ws = mk(p.ws, WS_BYTES), xr = mk(p.bx, 2u << 20);

  u4 xp[4], wv[16], wo[4][4];
  {
    bool alive = true;
    Hs hs = hs_begin(ws);
    u4 d0[16], d1[16];
    stage_slice(mk(p.wv, 4u << 20), h, 64u * w, l, d0);
    fence();
    hs_post(ws, hs, sm);
    fence();
    stage_wo(mk(p.wo, 4u << 20), h, w, l, d1);
    load_x(xr, w, c, g, 0, xp);
    put_image(sm, w, l, d0);
    bar();
    const unsigned fa = frag_base(256u, w, l);
#pragma unroll
    for (int f = 0; f < 16; ++f) wv[f] = frag(sm, fa + f * 8192u, 256u);
    const unsigned echo = hs_echo(ws);
    bar();
    put_image(sm, w, l, d1);
    bar();
    read_wo(sm, w, l, wo);
    hs_wait(ws, sm, echo, 1u, alive);
    hs_ack(ws, hs, 1u);
    bar();
  }
  put_x(sm, L_X, w, l, xp);
  load_x(xr, w, c, g, 1, xp);
  bar();
  STAMP(15, 8 + w);

  const unsigned v_off = L_V + ((w >> 1) * 64u + (2u * (w & 1) + (g >> 1)) * 16u + c) * 16u + (g & 1) * 8u;
  const unsigned flag = l == 0 ? F_U + (h * 8u + w) * FL : OOB;
  for (int s = 0; s < NSEG; ++s) {
    f4 acc[2] = {};
    const unsigned xb = L_X + (s & 1) * 32768u + l * 16u;
    u4 x[4][2];
#pragma unroll
    for (int k = 0; k < 3; ++k)
#pragma unroll
      for (int t = 0; t < 2; ++t) x[k][t] = lds16(sm, xb + (t * 16 + k) * 1024u);
#pragma unroll
    for (int ks = 0; ks < 16; ++ks) {
      sched();
      if (ks + 3 < 16) {
#pragma unroll
        for (int t = 0; t < 2; ++t) x[(ks + 3) & 3][t] = lds16(sm, xb + (t * 16 + ks + 3) * 1024u);
      }
      sched();
#pragma unroll
      for (int t = 0; t < 2; ++t) acc[t] = mma(wv[ks], x[ks & 3][t], acc[t]);
    }
    sched();
#pragma unroll
    for (int t = 0; t < 2; ++t)
      *reinterpret_cast<u2*>(sm + v_off + (s & 1) * 8192u + t * 4096u) = u2{pk(acc[t][0], acc[t][1]), pk(acc[t][2], acc[t][3])};
    if (w == 0) STAMP(3, s);
    drain();
    st4c(s, ws, flag, 0);
    if (w == 0) STAMP(4, s);
    put_x(sm, L_X + ((s + 1) & 1) * 32768u, w, l, xp);
    load_x(xr, w, c, g, s + 2, xp);
    bar();
    if (w == 0) STAMP(5, s);
    u4 v[8];
#pragma unroll
    for (int k = 0; k < 8; ++k) v[k] = lds16(sm, L_V + (s & 1) * 8192u + ((k & 1) * 4 + (k >> 1)) * 1024u + l * 16u);
    sched();
    f4 u[4][2] = {};
#pragma unroll
    for (int k = 0; k < 8; ++k)
#pragma unroll
      for (int i = 0; i < 4; ++i) u[i][k & 1] = mma(v[k], wo[i][k >> 1], u[i][k & 1]);
#pragma unroll
    for (int i = 0; i < 4; ++i) {
      const u4 o = {pk(u[i][0][0], u[i][0][1]), pk(u[i][0][2], u[i][0][3]), pk(u[i][1][0], u[i][1][1]),
                    pk(u[i][1][2], u[i][1][3])};
      st16c(o, ws, O_U + ((4u * w + i) * 32u + h) * 1024u + l * 16u, s * 1048576u);
    }
    if (w == 0) STAMP(2, s);
  }
  drain();
  st4c(NSEG, ws, flag, 0);
}

DEV void role_v2(const Params& p, unsigned j, char* sm) {
  constexpr unsigned ROW = 144, PART = 16 * ROW, BUF = 8 * PART;
  const unsigned l = threadIdx.x & 63, w = rfl(threadIdx.x >> 6), c = l & 15, g = l >> 4;
  const unsigned b = j >> 1, qt = j & 1;
  const rsrc_t ws = mk(p.ws, WS_BYTES), outr = mk(p.out, 8u << 20);
  bool alive = true;

  const unsigned hq = 4u * w + (l & 3);
  const unsigned uf_off = F_U + (hq * 8u + (b >> 1)) * FL;
  const unsigned pf_off = (l & 4) ? F_M + hq * FL : F_PX + (qt * 32u + hq) * FL;
  const unsigned u_off = O_U + (2u * b * 32u + 4u * w) * 1024u + l * 16u;
  const unsigned m_off = O_M2 + 2u * b * 8192u + w * 1024u + l * 16u;
  const unsigned px_off = O_PX + (qt * 32u + 4u * w) * 1024u + l * 16u;
  const unsigned pm_off = O_PM + (qt * 8u + w) * 1024u + l * 16u;
  const unsigned out_off = ((16u * qt + (threadIdx.x >> 5)) * 1024u + 512u + 32u * b + (threadIdx.x & 31)) * 4u;
  unsigned ring;
  {
    Hs hs = hs_begin(ws);
    hs_post(ws, hs, sm);
    bar();
    ring = window(p, hs_wait(ws, sm, hs_echo(ws), 2u, alive)) + (B_M + 2u * b) * 16384u + w * 1024u + l * 16u;
    hs_ack(ws, hs, 1u);
  }
  unsigned u_seen = wait_flags(ws, uf_off, 1u, alive), p_seen = 0;
  u4 ua[2][4];
#pragma unroll
  for (int k = 0; k < 8; ++k) ua[k >> 2][k & 3] = ld16c(ws, u_off + (k >> 2) * 32768u + (k & 3) * 1024u, 0);

  for (int s = 0; s < NSEG; ++s) {
    const unsigned need = s < FAST || ((l & 4) && s < LAST) ? s + 1 : 0;
    if (!all_lanes(p_seen >= need)) p_seen = wait_flags(ws, pf_off, need, alive);
    const unsigned mo = s < LAST ? m_off + s * 262144u : ring, ms = s < LAST ? 8192u : 16384u;
    u4 px[4], pm, m0, m1;
    for (unsigned it = 0; it < SPIN_CAP; ++it) {
      fence();
#pragma unroll
      for (int i = 0; i < 4; ++i) px[i] = ld16c(ws, px_off + i * 1024u, s * 65536u);
      pm = ld16c(ws, pm_off, s * 16384u);
      m0 = ld16c(ws, mo, 0);
      m1 = ld16c(ws, mo, ms);
      if (s < FAST) break;
      const bool tag = s < LAST || (m0[0] & m0[1] & m0[2] & m0[3] & m1[0] & m1[1] & m1[2] & m1[3] & 1u);
      if (all_lanes(tag && filled(px[0]) && filled(px[1]) && filled(px[2]) && filled(px[3]) && filled(pm))) break;
    }
    f4 a0 = {0, 0, 0, 0}, a1 = {0, 0, 0, 0};
#pragma unroll
    for (int i = 0; i < 4; ++i) {
      a0 = mma(ua[0][i], px[i], a0);
      a1 = mma(ua[1][i], px[i], a1);
    }
    a0 = mma(m0, pm, a0);
    a1 = mma(m1, pm, a1);
    sched();
    if (s + 1 < NSEG) {
      if (!all_lanes(u_seen > (unsigned)s + 1u)) u_seen = wait_flags(ws, uf_off, s + 2, alive);
#pragma unroll
      for (int k = 0; k < 8; ++k)
        ua[k >> 2][k & 3] = ld16c(ws, u_off + (k >> 2) * 32768u + (k & 3) * 1024u, (s + 1) * 1048576u);
    }
    sched();
    const unsigned rb = (s & 1) * BUF;
    *reinterpret_cast<f4*>(sm + rb + w * PART + c * ROW + g * 16u) = a0;
    *reinterpret_cast<f4*>(sm + rb + w * PART + c * ROW + g * 16u + 64u) = a1;
    bar();
    float v = 0.f;
#pragma unroll
    for (int k = 0; k < 8; ++k)
      v += *reinterpret_cast<const float*>(sm + rb + k * PART + (threadIdx.x >> 5) * ROW + (threadIdx.x & 31) * 4u);
    __builtin_amdgcn_raw_buffer_store_b32(__float_as_uint(rbf(v)), outr, out_off, s * 131072u, 0);
    if (w == 0) STAMP(2, s);
  }
}

DEV f2 cmul(f2 a, f2 b) {
  float re, im;
  asm("v_mul_f32 %0, %3, %5\n\tv_mul_f32 %1, %3, %4\n\tv_fma_f32 %0, %2, %4, -%0\n\tv_fma_f32 %1, %2, %5, %1"
      : "=&v"(re), "=&v"(im) : "v"(a[0]), "v"(a[1]), "v"(b[0]), "v"(b[1]));
  return f2{re, im};
}
DEV f2 cfma(f2 a, f2 b, f2 c) {
  float re, im;
  asm("v_fma_f32 %0, -%3, %5, %6\n\tv_fma_f32 %1, %3, %4, %7\n\tv_fma_f32 %0, %2, %4, %0\n\tv_fma_f32 %1, %2, %5, %1"
      : "=&v"(re), "=&v"(im) : "v"(a[0]), "v"(a[1]), "v"(b[0]), "v"(b[1]), "v"(c[0]), "v"(c[1]));
  return f2{re, im};
}
DEV f2 from_group(float re, float im, unsigned c, int k) {
  return f2{__shfl(re, (int)c + 16 * k, 64), __shfl(im, (int)c + 16 * k, 64)};
}

DEV void role_a(const Params& p, unsigned ci, char* sm) {
  constexpr unsigned GS = 272, KS = 4 * GS, TS = 32 * KS + 32, L_TAB = 4 * TS;
  const unsigned tid = threadIdx.x, l = tid & 63, w = rfl(tid >> 6), c = l & 15, g = l >> 4;
  const rsrc_t ws = mk(p.ws, WS_BYTES), xr = mk(p.ax, 2u << 20), outr = mk(p.out, 8u << 20);
  bool alive = true;
  {
    Hs hs = hs_begin(ws);
    hs_post(ws, hs, sm);
    if (w == 0) {
      hs_wait(ws, sm, hs_echo(ws), 1u, alive);
      hs_ack(ws, hs, 1u);
      wait_flags(ws, F_U + (l & 31) * 8u * FL, 1u, alive);
    }
    bar();
  }
  {
    const unsigned voff = ((64u * ci + 16u * (c >> 2) + 4u * (w >> 1) + (c & 3)) * 512u + 256u * (w & 1) + 8u * g) * 2u;
    u4 xp[8];
#pragma unroll
    for (int i = 0; i < 8; ++i) xp[i] = ld16(xr, voff + i * 64u, 0);
#pragma unroll
    for (int i = 0; i < 8; ++i) lds16(sm, (8u * w + i) * 1024u + l * 16u) = xp[i];
  }
  f2 lam64;
  {
    const float lr = bf2f(((const unsigned short*)p.lre)[tid]), li = bf2f(((const unsigned short*)p.lim)[tid]);
    const float dt = rbf(expf(bf2f(((const unsigned short*)p.ldt)[tid])));
    const float er = expf(dt * lr);
    float sn, cs;
    sincosf(dt * li, &sn, &cs);
    f2 lam = {er * cs, er * sn};
    asm volatile("s_nop 1" : "+v"(lam));
    const float nr = lam[0] - 1.0f, ni = lam[1];
    f2 coef;
    if (fabsf(lr) >= fabsf(li)) {
      const float t = li / lr, d = 1.0f / (lr + li * t);
      coef = f2{smul(nr + smul(ni, t), d), smul(ni - smul(nr, t), d)};
    } else {
      const float t = lr / li, d = 1.0f / (lr * t + li);
      coef = f2{smul(smul(nr, t) + ni, d), smul(smul(ni, t) - nr, d)};
    }
    f2 l16 = cmul(lam, lam);
    l16 = cmul(l16, l16);
    l16 = cmul(l16, l16);
    l16 = cmul(l16, l16);
    lam64 = cmul(l16, l16);
    lam64 = cmul(lam64, lam64);
    *reinterpret_cast<f4*>(sm + L_TAB + tid * 32u) = f4{lam[0], lam[1], coef[0], coef[1]};
    *reinterpret_cast<f2*>(sm + L_TAB + tid * 32u + 16u) = l16;
  }
  bar();

  f4 hr[4][4] = {}, hi[4][4] = {};
  {
    const rsrc_t br = mk(p.bre, 1u << 19), bi = mk(p.bim, 1u << 19);
    const unsigned voff = ((64u * w + c) * 512u + 8u * g) * 2u;
    u4 fr[4], fi[4];
#pragma unroll
    for (int k = 0; k < 4; ++k) {
      fr[k] = ld16(br, voff + k * 64u, 0);
      fi[k] = ld16(bi, voff + k * 64u, 0);
    }
#pragma unroll
    for (int n = 0; n < 64; ++n) {
      const int ct = n >> 4, ks = n & 15;
      const u4 r = fr[n & 3], i = fi[n & 3];
      fence();
      if (n + 4 < 64) {
        fr[n & 3] = ld16(br, voff + ((n + 4) & 15) * 64u, ((n + 4) >> 4) * 16384u);
        fi[n & 3] = ld16(bi, voff + ((n + 4) & 15) * 64u, ((n + 4) >> 4) * 16384u);
      }
#pragma unroll
      for (int t = 0; t < 4; ++t) {
        const u4 x = lds16(sm, (t * 16 + ks) * 1024u + l * 16u);
        hr[ct][t] = mma(x, r, hr[ct][t]);
        hi[ct][t] = mma(x, i, hi[ct][t]);
      }
    }
  }
  __builtin_amdgcn_sched_barrier(0);
  asm volatile("s_nop 7\n\ts_nop 7" ::: "memory");
  __builtin_amdgcn_sched_barrier(0);
#pragma unroll
  for (int ct = 0; ct < 4; ++ct) {
    const unsigned ch = 64u * w + 16u * ct + c;
    const f4 tb = *reinterpret_cast<const f4*>(sm + L_TAB + ch * 32u);
    const f2 l16 = *reinterpret_cast<const f2*>(sm + L_TAB + ch * 32u + 16u);
    const f2 lam = {tb[0], tb[1]}, coef = {tb[2], tb[3]};
    f2 hh = {0.f, 0.f};
#pragma unroll
    for (int i = 0; i < 16; ++i) {
      hh = cfma(lam, hh, cmul(coef, f2{hr[ct][i >> 2][i & 3], hi[ct][i >> 2][i & 3]}));
      hr[ct][i >> 2][i & 3] = hh[0];
      hi[ct][i >> 2][i & 3] = hh[1];
    }
    f2 e = from_group(hh[0], hh[1], c, 0);
#pragma unroll
    for (int k = 1; k < 4; ++k) e = cfma(l16, e, from_group(hh[0], hh[1], c, k));
    if (g == 0) st8c(__builtin_bit_cast(u2, e), ws, O_AE + ci * 4096u + ch * 8u, 0);
  }
  drain();
  bar();
  if (tid == 0) st4c(1u, ws, F_A + ci * FL, 0);
  wait_flags(ws, F_A + (l & 31) * FL, (l & 31) < ci ? 1u : 0u, alive);
  {
    f2 hin = {0.f, 0.f};
#pragma unroll 8
    for (unsigned k = 0; k < ci; ++k) hin = cfma(lam64, hin, __builtin_bit_cast(f2, ld8c(ws, O_AE + tid * 8u, k * 4096u)));
    *reinterpret_cast<f2*>(sm + L_TAB + tid * 32u + 24u) = hin;
  }
  bar();
#pragma unroll
  for (int ct = 0; ct < 4; ++ct) {
    const unsigned ch = 64u * w + 16u * ct + c;
    const f4 t0 = *reinterpret_cast<const f4*>(sm + L_TAB + ch * 32u);
    const f4 t1 = *reinterpret_cast<const f4*>(sm + L_TAB + ch * 32u + 16u);
    const f2 lam = {t0[0], t0[1]}, l16 = {t1[0], t1[1]};
    f2 z = {t1[2], t1[3]};
#pragma unroll
    for (int k = 0; k < 3; ++k) {
      const f2 n = cfma(l16, z, from_group(hr[ct][3][3], hi[ct][3][3], c, k));
      z = (unsigned)k < g ? n : z;
    }
    const unsigned hb = g * TS + (ch >> 5) * KS + ((ch >> 3) & 3) * GS + (ch & 7) * 2u;
#pragma unroll
    for (int i = 0; i < 16; ++i) {
      z = cmul(lam, z);
      const unsigned d = pk(2.0f * (hr[ct][i >> 2][i & 3] + z[0]), -2.0f * (hi[ct][i >> 2][i & 3] + z[1]));
      lds_put2(sm, hb + i * 16u, d);
      lds_put2(sm, hb + 16u * KS + i * 16u, d >> 16);
    }
  }
  bar();
  f4 y[4][4] = {};
  {
    const rsrc_t cr = mk(p.cre, 1u << 19), cm = mk(p.cim, 1u << 19);
    const unsigned voff = ((64u * w + c) * 512u + 8u * g) * 2u;
    u4 fa[2][4];
#pragma unroll
    for (int k = 0; k < 2; ++k)
#pragma unroll
      for (int d = 0; d < 4; ++d) fa[k][d] = ld16(cr, voff + k * 64u, d * 16384u);
#pragma unroll
    for (int ks = 0; ks < 32; ++ks) {
      u4 a[4];
      fence();
#pragma unroll
      for (int d = 0; d < 4; ++d) {
        a[d] = fa[ks & 1][d];
        if (ks + 2 < 32) fa[ks & 1][d] = ld16(ks + 2 < 16 ? cr : cm, voff + ((ks + 2) & 15) * 64u, d * 16384u);
      }
#pragma unroll
      for (int t = 0; t < 4; ++t) {
        const u4 hb = lds16(sm, t * TS + ks * KS + g * GS + c * 16u);
#pragma unroll
        for (int d = 0; d < 4; ++d) y[d][t] = mma(a[d], hb, y[d][t]);
      }
    }
  }
  const rsrc_t dr = mk(p.dsk, 1024);
#pragma unroll
  for (int d = 0; d < 4; ++d) {
    const unsigned col = 64u * w + 16u * d + 4u * g;
    const u2 dk = ld8(dr, col * 2u, 0);
#pragma unroll
    for (int t = 0; t < 4; ++t) {
      const unsigned row = 64u * ci + 16u * t + c;
      const u2 xv = ld8(xr, (row * 512u + col) * 2u, 0);
      const f4 o = {y[d][t][0] + rbf(smul(bf2f(xv[0] & 0xffffu), bf2f(dk[0] & 0xffffu))),
                    y[d][t][1] + rbf(smul(bf2f(xv[0] >> 16), bf2f(dk[0] >> 16))),
                    y[d][t][2] + rbf(smul(bf2f(xv[1] & 0xffffu), bf2f(dk[1] & 0xffffu))),
                    y[d][t][3] + rbf(smul(bf2f(xv[1] >> 16), bf2f(dk[1] >> 16)))};
      __builtin_amdgcn_raw_buffer_store_b128(__builtin_bit_cast(u4, o), outr, (row * 1024u + col) * 4u, 0, 0);
    }
  }
  if (w == 0) STAMP(2, 0);
}

__global__ void __launch_bounds__(512, 2) fused_kernel(Params p) {
  extern __shared__ __attribute__((aligned(16))) char smem[];
  unsigned i;
  const unsigned role = role_of(blockIdx.x, i);
  if (threadIdx.x < 64) TSTAMP(0);
  switch (role) {
    case R_S1: role_s1(p, i, smem); break;
    case R_VM: role_vm(p, i, smem); break;
    case R_V1: role_v1(p, i, smem); break;
    case R_S2: role_s2(p, i, smem); break;
    case R_VU: role_vu(p, i, smem); break;
    case R_V2: role_v2(p, i, smem); break;
    case R_A: role_a(p, i, smem); break;
  }
  if (threadIdx.x < 64) TSTAMP(2);
}

__global__ void prt(const unsigned* dg, int mode) {
  __shared__ unsigned m[4];
  const int b = threadIdx.x;
  if (b == 0) { m[0] = ~0u; m[1] = 0; m[2] = 0; m[3] = 0; }
  __syncthreads();
  const unsigned* q = &dg[b * 1024 + 14 * 64];
  atomicMin(&m[0], q[0]);
  atomicMax(&m[3], q[2]);
  unsigned ri; const unsigned ro = role_of(b, ri);
  if (ro == R_S1) atomicMax(&m[1], q[1]);
  if (ro == R_V1) atomicMax(&m[2], q[2]);
  __syncthreads();
  unsigned v = 0;
  if (mode == 1) v = (m[1] - m[0]) * 10;
  if (mode == 2) v = m[2] - m[1];
  if (mode == 3) v = (m[3] - m[2]) * 10;
  if (mode == 4) v = m[3] - m[0];
  v = min(v, 60000u);
  for (int i = 0; i < 2000000; ++i) {
    if ((unsigned)__builtin_amdgcn_s_memrealtime() - m[0] >= 30000u + v) break;
    __builtin_amdgcn_s_sleep(2);
  }
}
extern "C" void kernel_launch(void* const* d_in, const int* in_sizes, int n_in, void* d_out, int out_size, void* d_ws,
                              size_t ws_size, hipStream_t stream) {
  Params p;
  p.ax = d_in[0]; p.lre = d_in[1]; p.lim = d_in[2]; p.bre = d_in[3]; p.bim = d_in[4]; p.cre = d_in[5];
  p.cim = d_in[6]; p.ldt = d_in[7]; p.dsk = d_in[8]; p.bx = d_in[9]; p.mem0 = d_in[10]; p.wq = d_in[11];
  p.wk = d_in[12]; p.wv = d_in[13]; p.wo = d_in[14];
  p.out = (float*)d_out;
  p.ws = (char*)d_ws;
  (void)hipFuncSetAttribute((const void*)fused_kernel, hipFuncAttributeMaxDynamicSharedMemorySize, DYN_LDS);
  fused_kernel<<<256, 512, DYN_LDS, stream>>>(p);
  prt<<<1, 256, 0, stream>>>((const unsigned*)((char*)d_ws + O_DIAG), 0);
}
```

```cpp
#define OAUX 16
#include <hip/hip_runtime.h>
#include <hip/hip_bf16.h>
#include <stdint.h>

typedef __attribute__((ext_vector_type(2))) unsigned u2;
typedef __attribute__((ext_vector_type(4))) unsigned u4;
typedef __attribute__((ext_vector_type(8))) unsigned u8;
typedef __attribute__((ext_vector_type(16))) unsigned u16v;
typedef __attribute__((ext_vector_type(32))) unsigned u32v;
typedef __attribute__((ext_vector_type(2))) float f2;
typedef __attribute__((ext_vector_type(4))) float f4;
typedef __attribute__((ext_vector_type(2))) __bf16 bf2;
typedef __attribute__((ext_vector_type(8))) __bf16 bf8;
typedef __attribute__((ext_vector_type(16))) __bf16 bf16v;
typedef __amdgpu_buffer_rsrc_t rsrc_t;
#define DEV __device__ __forceinline__

struct Params {
  const void *ax, *lre, *lim, *bre, *bim, *cre, *cim, *ldt, *dsk, *bx, *mem0, *wq, *wk, *wv, *wo;
  float* out;
  char* ws;
};

#define WSLEEP 4
constexpr int NSEG = 64, LAST = NSEG - 1;
#define NFAST 3
constexpr int FAST = NSEG - NFAST;
constexpr unsigned SPIN_CAP = 400000u;
constexpr unsigned DYN_LDS = 156u * 1024u;

constexpr unsigned FL = 128, F_KM = 0, F_PX = 32 * FL, F_M = 96 * FL, F_A = 128 * FL, F_KX = 192 * FL, F_U = 320 * FL,
                   N_FLAGS = 576;
constexpr unsigned H_X = N_FLAGS * FL, H_Y = H_X + 256 * FL, H_END = H_Y + 256 * FL;
constexpr unsigned O_AE = 5u << 20, O_DIAG = 6u << 20;
constexpr unsigned O_KX = 8u << 20, O_KM = 24u << 20, O_PX = 28u << 20, O_PM = 32u << 20, O_M2 = 34u << 20,
                   O_U = 50u << 20, WS_BYTES = 114u << 20;
constexpr unsigned OOB = 1u << 30;
constexpr unsigned B_MEM = 0, B_P = 63, B_M = 67, B_END = 99, B_PROBE = 100;

enum { R_S1, R_VM, R_V1, R_S2, R_VU, R_V2, R_A };
__host__ __device__ inline unsigned role_of(unsigned b, unsigned& i) {
  const unsigned x = b & 7, r = b >> 3;
  if (x >= 4) {
    i = (x - 4) * 8u + (r >> 2);
    return R_S2 + (r & 3);
  }
  if (x == 1 || x == 2) {
    i = (x - 1) * 32u + r;
    return R_V1;
  }
  i = (x ? 16u : 0u) + (r >> 1);
  return r & 1;
}

DEV rsrc_t mk(const void* p, unsigned bytes) {
  return __builtin_amdgcn_make_buffer_rsrc((void*)p, 0, bytes, 0x00020000);
}
DEV unsigned rfl(unsigned v) { return __builtin_amdgcn_readfirstlane(v); }
DEV u4 ld16(rsrc_t r, unsigned v, unsigned s) { return __builtin_amdgcn_raw_buffer_load_b128(r, v, s, 0); }
#define WAUX 0
DEV u4 ld16w(rsrc_t r, unsigned v, unsigned s) { return __builtin_amdgcn_raw_buffer_load_b128(r, v, s, WAUX); }
DEV u2 ld8(rsrc_t r, unsigned v, unsigned s) { return __builtin_amdgcn_raw_buffer_load_b64(r, v, s, 0); }
DEV unsigned ld4(rsrc_t r, unsigned v, unsigned s) { return __builtin_amdgcn_raw_buffer_load_b32(r, v, s, 0); }
DEV unsigned ld2(rsrc_t r, unsigned v, unsigned s) { return __builtin_amdgcn_raw_buffer_load_b16(r, v, s, 0); }
DEV u4 ld16c(rsrc_t r, unsigned v, unsigned s) { return __builtin_amdgcn_raw_buffer_load_b128(r, v, s, 16); }
DEV u2 ld8c(rsrc_t r, unsigned v, unsigned s) { return __builtin_amdgcn_raw_buffer_load_b64(r, v, s, 16); }
DEV unsigned ld4c(rsrc_t r, unsigned v, unsigned s) { return __builtin_amdgcn_raw_buffer_load_b32(r, v, s, 16); }
DEV void st16c(u4 d, rsrc_t r, unsigned v, unsigned s) { __builtin_amdgcn_raw_buffer_store_b128(d, r, v + s, 0, 16); }
DEV void st8c(u2 d, rsrc_t r, unsigned v, unsigned s) { __builtin_amdgcn_raw_buffer_store_b64(d, r, v, s, 16); }
DEV void st4c(unsigned d, rsrc_t r, unsigned v, unsigned s) { __builtin_amdgcn_raw_buffer_store_b32(d, r, v, s, 16); }
DEV void fence() { asm volatile("" ::: "memory"); }
DEV void drain() { asm volatile("s_waitcnt vmcnt(0)" ::: "memory"); }
DEV void bar() { asm volatile("s_waitcnt lgkmcnt(0)\n\ts_barrier" ::: "memory"); }
DEV bool all_lanes(bool ok) { return __builtin_amdgcn_ballot_w64(!ok) == 0ull; }

DEV float bf2f(unsigned h) { return __uint_as_float(h << 16); }
DEV unsigned pk(float a, float b) {
  f2 v = {a, b};
  return __builtin_bit_cast(unsigned, __builtin_convertvector(v, bf2));
}
DEV float rbf(float a) { return bf2f(pk(a, a) & 0xffffu); }
DEV float smul(float a, float b) {
  float r;
  asm("v_mul_f32 %0, %1, %2" : "=v"(r) : "v"(a), "v"(b));
  return r;
}
DEV float sadd(float a, float b) {
  float r;
  asm("v_add_f32 %0, %1, %2" : "=v"(r) : "v"(a), "v"(b));
  return r;
}
DEV f4 mma(u4 a, u4 b, f4 c) {
  return __builtin_amdgcn_mfma_f32_16x16x32_bf16(__builtin_bit_cast(bf8, a), __builtin_bit_cast(bf8, b), c, 0, 0, 0);
}
DEV f4 smma(u4 a, u8 b, f4 c, unsigned idx) {
  return __builtin_amdgcn_smfmac_f32_16x16x64_bf16(__builtin_bit_cast(bf8, a), __builtin_bit_cast(bf16v, b), c,
                                                   (int)idx, 0, 0);
}
DEV float swap32_add(float a, float b) {
  auto r = __builtin_amdgcn_permlane32_swap(__float_as_uint(a), __float_as_uint(b), false, false);
  return __uint_as_float(r[0]) + __uint_as_float(r[1]);
}
DEV float swap16_add(float a, float b) {
  auto r = __builtin_amdgcn_permlane16_swap(__float_as_uint(a), __float_as_uint(b), false, false);
  return __uint_as_float(r[0]) + __uint_as_float(r[1]);
}
DEV float group_max(float m) {
  auto r = __builtin_amdgcn_permlane16_swap(__float_as_uint(m), __float_as_uint(m), false, false);
  m = fmaxf(__uint_as_float(r[0]), __uint_as_float(r[1]));
  auto q = __builtin_amdgcn_permlane32_swap(__float_as_uint(m), __float_as_uint(m), false, false);
  return fmaxf(__uint_as_float(q[0]), __uint_as_float(q[1]));
}
DEV float group_sum(float m) {
  const float t = swap16_add(m, m);
  return swap32_add(t, t);
}
DEV u4& lds16(char* sm, unsigned off) { return *reinterpret_cast<u4*>(sm + off); }
DEV void lds_put2(char* sm, unsigned off, unsigned v) { *reinterpret_cast<unsigned short*>(sm + off) = (unsigned short)v; }

#define STAMP(slot, idx) do { } while (0)

#define TSTAMP(idx) do { } while (0)

DEV unsigned wait_flags(rsrc_t ws, unsigned off, unsigned need, bool& alive) {
  unsigned v = 0;
  for (unsigned it = 0; alive; ++it) {
    fence();
    v = ld4c(ws, off, 0);
    if (all_lanes(v >= need)) break;
    if (it > SPIN_CAP) alive = false;
    __builtin_amdgcn_s_sleep(WSLEEP);
  }
  fence();
  return v;
}
DEV bool filled(u4 v) { return max(max(v[0], v[1]), max(v[2], v[3])) != ~0u; }
DEV unsigned crit_base(const Params& p) {
  const unsigned lo = (unsigned)(uintptr_t)p.ws;
  return ((lo + H_END + 0x1fffffu) & ~0x1fffffu) - lo;
}
struct Hs { unsigned x, a; };
constexpr unsigned L_HS = DYN_LDS - 16u;
DEV unsigned& lds4(char* sm, unsigned off) { return *reinterpret_cast<unsigned*>(sm + off); }
DEV Hs hs_begin(rsrc_t ws) {
  const unsigned o = threadIdx.x == 0 ? blockIdx.x * FL : OOB;
  return Hs{ld4c(ws, o + H_Y, 0), ld4c(ws, o + H_X + 4u, 0)};
}
DEV void hs_post(rsrc_t ws, Hs& h, char* sm) {
  h.x = (h.x & ~3u) + 4u;
  st4c(h.x, ws, threadIdx.x == 0 ? blockIdx.x * FL + H_X : OOB, 0);
  lds4(sm, threadIdx.x == 0 ? L_HS : L_HS + 4u) = h.x;
}
DEV unsigned hs_echo(rsrc_t ws) { return ld4c(ws, blockIdx.x * FL + H_Y, 0); }
DEV unsigned hs_wait(rsrc_t ws, char* sm, unsigned e, unsigned stage, bool& alive) {
  const unsigned x = rfl(lds4(sm, L_HS));
  unsigned v = rfl(e) - x;
  for (unsigned it = 0; alive && (v < stage || v > 3u); ++it) {
    if (it > SPIN_CAP) alive = false;
    __builtin_amdgcn_s_sleep(WSLEEP);
    fence();
    v = rfl(hs_echo(ws)) - x;
  }
  fence();
  return v;
}
DEV void hs_ack(rsrc_t ws, const Hs& h, unsigned d) {
  st4c(h.a + d, ws, threadIdx.x == 0 ? blockIdx.x * FL + H_X + 4u : OOB, 0);
}
DEV unsigned window(const Params& p, unsigned stage) { return crit_base(p) + (stage == 3u ? 8192u : 0u); }

DEV void stage_slice(rsrc_t r, unsigned h, unsigned row0, unsigned l, u4 (&d)[16]) {
  const unsigned v = (l >> 4) * 8192u + h * 256u + ((l & 15) ^ ((l >> 4) << 1)) * 16u;
#pragma unroll
  for (int i = 0; i < 16; ++i) d[i] = ld16w(r, v ^ ((i & 2) * 64u), (row0 + 4u * i) * 8192u);
}
DEV void stage_wo(rsrc_t r, unsigned h, unsigned w, unsigned l, u4 (&d)[16]) {
#pragma unroll
  for (int i = 0; i < 16; ++i)
    d[i] = ld16w(r, (l ^ (((i & 3) | ((i >> 1) & 4)) << 1)) * 16u, (h * 128u + w * 16u + i) * 1024u);
}
DEV void put_image(char* sm, unsigned w, unsigned l, const u4 (&d)[16]) {
#pragma unroll
  for (int i = 0; i < 16; ++i) lds16(sm, (16u * w + i) * 1024u + l * 16u) = d[i];
}
typedef __attribute__((ext_vector_type(4))) short s4;
DEV u2 lds_tr(char* sm, unsigned off) {
  return __builtin_bit_cast(u2, __builtin_amdgcn_ds_read_tr16_b64_v4i16((__attribute__((address_space(3))) s4*)(sm + off)));
}
DEV unsigned frag_base(unsigned pitch, unsigned cb, unsigned l) {
  const unsigned g = l >> 4, q = (l >> 2) & 3, p = l & 3;
  return (8u * g + q) * pitch + ((2u * cb + (p >> 1)) ^ ((q | ((g & 1) << 2)) << 1)) * 16u + (p & 1) * 8u;
}
DEV u4 frag(char* sm, unsigned off, unsigned pitch) {
  const u2 a = lds_tr(sm, off), b = lds_tr(sm, off + 4u * pitch);
  return u4{a[0], a[1], b[0], b[1]};
}
DEV u8 cat(u4 a, u4 b) { return __builtin_shufflevector(a, b, 0, 1, 2, 3, 4, 5, 6, 7); }
DEV u16v cat(u8 a, u8 b) { return __builtin_shufflevector(a, b, 0, 1, 2, 3, 4, 5, 6, 7, 8, 9, 10, 11, 12, 13, 14, 15); }
DEV u32v cat(u8 a, u8 b, u8 c, u8 d) {
  return __builtin_shufflevector(cat(a, b), cat(c, d), 0, 1, 2, 3, 4, 5, 6, 7, 8, 9, 10, 11, 12, 13, 14, 15, 16, 17, 18,
                                 19, 20, 21, 22, 23, 24, 25, 26, 27, 28, 29, 30, 31);
}
DEV u4 srd(const void* p, unsigned bytes) {
  return u4{(unsigned)(uintptr_t)p, (unsigned)((uintptr_t)p >> 32) & 0xffffu, bytes, 0x00020000u};
}
DEV unsigned lds_base(char* sm) { return (unsigned)(uintptr_t)(__attribute__((address_space(3))) char*)sm; }
DEV unsigned swz(unsigned g) { return (g & 1) | ((g & 2) << 1); }
DEV void sched() { __builtin_amdgcn_sched_barrier(0); }

DEV u4 load_mem0(const Params& p, unsigned w, unsigned c, unsigned g) {
  const rsrc_t r = mk(p.mem0, 8192);
  const unsigned voff = ((c >> 1) * 512u + 64u * w + 16u * g + 2u * (c & 1)) * 2u;
  u4 a;
#pragma unroll
  for (int i = 0; i < 4; ++i) a[i] = ld4(r, voff, 8 * i);
  return a;
}
constexpr float SCALE_LOG2E = 0.08838834764831845f * 1.4426950408889634f;

#define A_STEP
#define A_STAMP(r)
#define A_SAVE(r, slot)
#define A_NEXT
#define A_KXWAIT "s_waitcnt vmcnt(2)\n"
#define A_DIAG_OUT
#define A_DIAG_CLOB
#define A_CLK(r)
#define A_SAVECLK
#define A_DIAG_DECL
#define A_SCLOB "s80", "s81", "s82", "s83", "s84", "s85", "s86", "s87", "s88", "s89", "s90", "s91", "vcc", "scc", "memory"
#define SMF(d, a, b, i) "v_smfmac_f32_16x16x64_bf16 v[" d "], v[" a "], v[" b "], " i "\n"
#define MFM(d, a, b, c) "v_mfma_f32_16x16x32_bf16 v[" d "], v[" a "], v[" b "], " c "\n"
#define DPP_ALL " row_mask:0xf bank_mask:0xf\n"
#define A_POLL_MEM \
  ".Lpoll%=:\n" \
  "buffer_load_dwordx4 v[148:151], v129, %[ws], %[mem] offen sc1\n" \
  "s_sub_u32 %[cap], %[cap], 1\n" \
  "s_cbranch_scc1 .Lexit%=\n" \
  "s_waitcnt vmcnt(0)\n" \
  "ds_write_b128 v130, v[148:151]\n" \
  "v_max3_u32 v152, v148, v149, v150\n" \
  "v_max_u32 v152, v152, v151\n" \
  "v_cmp_eq_u32 vcc, -1, v152\n" \
  "s_cbranch_vccnz .Lpoll%=\n"
#define A_READ_MEM \
  "s_waitcnt lgkmcnt(0)\n" \
  "s_barrier\n" \
  A_CLK("s[72:73]") \
  "ds_read_b128 v[152:155], v131\n" \
  "ds_read_b128 v[156:159], v132\n" \
  "ds_read_b128 v[160:163], v133\n" \
  "ds_read_b128 v[164:167], v134\n" \
  "ds_read_b128 v[168:171], v135\n" \
  "ds_read_b128 v[172:175], v136\n" \
  "ds_read_b128 v[176:179], v137\n"
#define D_S1 0
#define D_S0 0
#define D_VM 0
#define D_V1 0
#define D_U 8
#define STR_(x) #x
#define SLEEP(x) "s_sleep " STR_(x) "\n"
#define WL(n) "s_waitcnt lgkmcnt(" #n ")\n"
#define WV(n) "s_waitcnt vmcnt(" #n ")\n"

DEV void role_s1(const Params& p, unsigned h, char* sm) {
  constexpr unsigned L_MEM = 131072, L_Q = L_MEM + 8192, L_K = L_MEM + 10240;
  const unsigned l = threadIdx.x & 63, w = rfl(threadIdx.x >> 6), c = l & 15, g = l >> 4;
  const rsrc_t ws = mk(p.ws, WS_BYTES);
  bool alive = true;
  STAMP(15, w);

  u8 wa[8], wb[8];
  u4 a0;
  unsigned cb, echo;
  {
    const rsrc_t wr = mk(w < 4 ? p.wq : p.wk, 4u << 20);
    Hs hs = hs_begin(ws);
    u4 d0[16], d1[16];
    stage_slice(wr, h, 64u * w, l, d0);
    fence();
    hs_post(ws, hs, sm);
    fence();
    stage_slice(wr, h, 64u * (w ^ 4u), l, d1);
    a0 = load_mem0(p, w, c, g);
    const unsigned fa = (w >> 2) * 65536u + frag_base(256u, 2u * (w & 3), l), fb = fa ^ 32u;
#pragma unroll
    for (int k = 0; k < 2; ++k) {
      put_image(sm, w, l, k ? d1 : d0);
      bar();
#pragma unroll
      for (int i = 0; i < 4; ++i) {
        const unsigned o = ((w ^ i) & 3) * 16384u;
        wa[4 * k + i] = cat(frag(sm, fa + o, 256u), frag(sm, fa + o + 8192u, 256u));
        wb[4 * k + i] = cat(frag(sm, fb + o, 256u), frag(sm, fb + o + 8192u, 256u));
      }
      if (k == 0) {
        bar();
        echo = hs_echo(ws);
      }
    }
    cb = window(p, hs_wait(ws, sm, echo, 2u, alive));
    hs_ack(ws, hs, 1u);
  }
  STAMP(15, 8 + w);

  const unsigned lb = lds_base(sm);
  u16v k0;
  u8 k1;
  k0[0] = (c & 1) ? 0xeeeeeeeeu : 0x44444444u;
  k0[1] = w * 1024u + l * 16u;
#pragma unroll
  for (int i = 0; i < 8; ++i) k0[2 + i] = lb + L_MEM + (w ^ i) * 1024u + l * 16u;
#pragma unroll
  for (int r = 0; r < 2; ++r)
    k0[10 + r] = lb + (w < 4 ? L_Q : L_K) + (((w & 3) * 4u + (c >> 2)) * 8u + ((2u * g + r) ^ swz(c >> 2))) * 16u + (c & 3) * 4u;
  k0[12] = k0[13] = 0;
  k0[14] = lb + L_Q + (g * 8u + ((c & 7) ^ swz(g))) * 16u;
  k0[15] = lb + L_K + (g * 8u + ((((c >> 2) & 1) * 4u + (c >> 3) * 2u + (c & 1)) ^ swz(g))) * 16u;
  const unsigned piece = ((c & 7) >> 2) * 4u + (h >> 3), hl = h & 7;
  const unsigned p_base = cb + (B_P + (piece >> 1)) * 16384u + (piece & 1) * 4096u;
  const unsigned tok = ((c >> 1) & 1) * 16u + (c >> 3) * 8u + ((c >> 2) & 1) * 2u + (c & 1);
  k1[0] = p_base + (hl >> 1) * 512u + (2u * (hl & 1) + (g >> 1)) * 128u + ((g & 1) * 4u + (c & 3)) * 16u;
  k1[1] = p_base + 2048u + (hl >> 1) * 128u + ((g >> 1) * 4u + (c & 3)) * 16u + (hl & 1) * 8u;
  k1[2] = O_KX + h * 8192u + ((tok >> 4) * 64u + g * 16u + (tok & 15)) * 16u;
  k1[3] = k1[2] + 4096u;
  k1[4] = w == 0 ? F_KX + (h * 4u + (l & 3)) * FL : OOB;
  k1[5] = lb + L_K + l * 16u;
  k1[6] = O_KM + h * 2048u + l * 16u;
  k1[7] = F_KM + h * FL;

  u32v kx = {};
  unsigned seen;
  if (w == 0) {
    wait_flags(ws, k1[4], 1u, alive);
    u4 f[8];
#pragma unroll
    for (int i = 0; i < 8; ++i) f[i] = ld16c(ws, k1[2] + (i & 3) * 2048u + (i >> 2) * 64u, 0);
    kx = cat(cat(f[0], f[1]), cat(f[2], f[3]), cat(f[4], f[5]), cat(f[6], f[7]));
  }
  STAMP(15, 16 + w);
  if (w == 0) TSTAMP(1);

  unsigned s = 0, mem = rfl(cb + B_MEM * 16384u - 16384u), cap = rfl(alive ? SPIN_CAP : 0u), kxo = 262144u, kmo = 0;
  u32v t0;
  u8 t1;
  u16v t2;
  A_DIAG_DECL;
  asm volatile(
      "s_mov_b32 s80, 0xff00ff00\n"
      "s_mov_b32 s81, 0xff00ff00\n"
      "s_mov_b32 s82, 0x3e0293ee\n"
      "s_mov_b32 s83, 0\n"
      "s_cmp_eq_u32 %[w], 0\n"
      "s_cselect_b32 s90, 63, 0\n"
      "v_mov_b32 v184, 0\nv_mov_b32 v185, 0\nv_mov_b32 v186, 0\nv_mov_b32 v187, 0\n"
      "v_mov_b32 v188, 0\nv_mov_b32 v189, 0\nv_mov_b32 v190, 0\nv_mov_b32 v191, 0\n"
      "ds_write_b128 v130, v[148:151]\n"
      "s_branch .Lhave%=\n"
      A_POLL_MEM
      ".Lhave%=:\n"
      A_STAMP("s[84:85]") A_CLK("s[70:71]")
      SMF("184:187", "148:151", "0:7", "v128") SMF("188:191", "148:151", "8:15", "v128")
      "buffer_load_dword v248, v244, %[ws], 0 offen sc1\n"
      A_READ_MEM
      WL(6) SMF("184:187", "152:155", "16:23", "v128") SMF("188:191", "152:155", "24:31", "v128")
      WL(5) SMF("184:187", "156:159", "32:39", "v128") SMF("188:191", "156:159", "40:47", "v128")
      WL(4) SMF("184:187", "160:163", "48:55", "v128") SMF("188:191", "160:163", "56:63", "v128")
      WL(3) SMF("184:187", "164:167", "64:71", "v128") SMF("188:191", "164:167", "72:79", "v128")
      WL(2) SMF("184:187", "168:171", "80:87", "v128") SMF("188:191", "168:171", "88:95", "v128")
      WL(1) SMF("184:187", "172:175", "96:103", "v128") SMF("188:191", "172:175", "104:111", "v128")
      WL(0) SMF("184:187", "176:179", "112:119", "v128") SMF("188:191", "176:179", "120:127", "v128")
      A_CLK("s[74:75]")
      "s_cmp_lt_u32 %[s], s90\n"
      "s_nop 5\n"
      "v_add_f32 v152, v184, v185\n"
      "v_add_f32 v154, v186, v187\n"
      "v_add_f32 v153, v188, v189\n"
      "v_add_f32 v155, v190, v191\n"
      "v_cvt_pk_bf16_f32 v152, v152, v153\n"
      "v_cvt_pk_bf16_f32 v154, v154, v155\n"
      "ds_write_b32 v138, v152\n"
      "ds_write_b32 v139, v154\n"
      A_STAMP("s[86:87]") A_CLK("s[76:77]")
      "v_mov_b32 v184, 0\nv_mov_b32 v185, 0\nv_mov_b32 v186, 0\nv_mov_b32 v187, 0\n"
      "v_mov_b32 v188, 0\nv_mov_b32 v189, 0\nv_mov_b32 v190, 0\nv_mov_b32 v191, 0\n"
      "s_waitcnt lgkmcnt(0)\n"
      "s_barrier\n"
      "s_cbranch_scc0 .Lother%=\n"
      A_CLK("s[78:79]")
      "ds_read_b128 v[152:155], v142\n"
      "ds_read_b128 v[168:171], v143\n"
      "ds_read_b128 v[156:159], v142 offset:512\n"
      "ds_read_b128 v[172:175], v143 offset:512\n"
      "ds_read_b128 v[160:163], v142 offset:1024\n"
      "ds_read_b128 v[176:179], v143 offset:1024\n"
      "ds_read_b128 v[164:167], v142 offset:1536\n"
      "ds_read_b128 v[180:183], v143 offset:1536\n"
      WL(7) MFM("224:227", "192:195", "152:155", "0") MFM("228:231", "208:211", "152:155", "0")
      WL(6) MFM("232:235", "168:171", "152:155", "0")
      WL(5) MFM("224:227", "196:199", "156:159", "v[224:227]") MFM("228:231", "212:215", "156:159", "v[228:231]")
      WL(4) MFM("232:235", "172:175", "156:159", "v[232:235]")
      WL(3) MFM("224:227", "200:203", "160:163", "v[224:227]") MFM("228:231", "216:219", "160:163", "v[228:231]")
      WL(2) MFM("232:235", "176:179", "160:163", "v[232:235]")
      WL(1) MFM("224:227", "204:207", "164:167", "v[224:227]") MFM("228:231", "220:223", "164:167", "v[228:231]")
      WL(0) MFM("232:235", "180:183", "164:167", "v[232:235]")
      A_CLK("s[92:93]")
      "s_nop 5\n"
      "v_max3_f32 v152, v224, v225, v226\n"
      "v_max3_f32 v153, v227, v228, v229\n"
      "v_max3_f32 v154, v230, v231, v232\n"
      "v_max3_f32 v152, v152, v153, v233\n"
      "v_max_f32 v153, v152, v154\n"
      "v_max_f32 v152, v152, v154\n"
      "v_cndmask_b32 v156, v224, v228, s[80:81]\n"
      "v_cndmask_b32 v157, v225, v229, s[80:81]\n"
      "v_permlane16_swap_b32 v152, v153\n"
      "v_max_f32 v154, v152, v153\n"
      "v_max_f32 v155, v152, v153\n"
      "v_cndmask_b32 v158, v226, v230, s[80:81]\n"
      "v_cndmask_b32 v159, v227, v231, s[80:81]\n"
      "v_permlane32_swap_b32 v154, v155\n"
      "v_cndmask_b32 v160, v232, v233, s[80:81]\n"
      "v_max_f32 v152, v154, v155\n"
      "v_mul_f32 v152, 0xbe0293ee, v152\n"
      "v_fma_f32 v156, v156, s82, v152\n"
      "v_fma_f32 v157, v157, s82, v152\n"
      "v_fma_f32 v158, v158, s82, v152\n"
      "v_fma_f32 v159, v159, s82, v152\n"
      "v_fma_f32 v160, v160, s82, v152\n"
      "v_exp_f32 v156, v156\n"
      "v_exp_f32 v157, v157\n"
      "v_exp_f32 v158, v158\n"
      "v_exp_f32 v159, v159\n"
      "v_exp_f32 v160, v160\n"
      "v_add_f32 v152, v156, v157\n"
      "v_add_f32 v153, v158, v159\n"
      "v_add_f32 v152, v152, v160\n"
      "v_add_f32 v152, v152, v153\n"
      "v_xor_b32 v156, s83, v156\n"
      "v_xor_b32 v157, s83, v157\n"
      "v_add_f32_dpp v154, v152, v152 row_ror:8" DPP_ALL
      "v_add_f32_dpp v155, v152, v152 row_ror:8" DPP_ALL
      "v_xor_b32 v158, s83, v158\n"
      "v_xor_b32 v159, s83, v159\n"
      "v_permlane16_swap_b32 v154, v155\n"
      "v_add_f32 v152, v154, v155\n"
      "v_add_f32 v153, v154, v155\n"
      "v_xor_b32 v160, s83, v160\n"
      "s_xor_b32 s83, s83, 0x80000000\n"
      "v_permlane32_swap_b32 v152, v153\n"
      "v_add_f32 v152, v152, v153\n"
      "v_rcp_f32 v152, v152\n"
      A_CLK("s[94:95]")
      "s_add_u32 s91, %[s], 2\n"
      "v_mul_f32 v156, v156, v152\n"
      "v_mul_f32 v157, v157, v152\n"
      "v_mul_f32 v158, v158, v152\n"
      "v_mul_f32 v159, v159, v152\n"
      "v_mul_f32 v160, v160, v152\n"
      "v_cvt_pk_bf16_f32 v164, v156, v157\n"
      "v_cvt_pk_bf16_f32 v165, v158, v159\n"
      "v_mov_b32_dpp v161, v160 row_ror:8" DPP_ALL
      "v_mov_b32_dpp v166, v164 row_ror:8" DPP_ALL
      "v_mov_b32_dpp v167, v165 row_ror:8" DPP_ALL
      "v_cvt_pk_bf16_f32 v162, v160, v161\n"
      "v_cvt_pk_bf16_f32 v163, v160, v161\n"
      "s_not_b64 exec, s[80:81]\n"
      "buffer_store_dwordx4 v[164:167], v240, %[ws], 0 offen sc1\n"
      "s_mov_b64 exec, -1\n"
      "v_permlane16_swap_b32 v162, v163\n"
      "s_mov_b32 exec_lo, 0xff\n"
      "s_mov_b32 exec_hi, 0xff\n"
      "buffer_store_dwordx2 v[162:163], v241, %[ws], 0 offen sc1\n"
      "s_mov_b64 exec, -1\n"
      A_STAMP("s[88:89]") A_CLK("s[96:97]") A_SAVE("s84", 0) A_SAVE("s86", 1) A_SAVE("s88", 2) A_SAVECLK
      "s_cmp_ge_u32 %[s], 62\n"
      "s_cbranch_scc1 .Lnext%=\n"
      A_KXWAIT
      ".Lkx%=:\n"
      "v_cmp_gt_u32 vcc, s91, v248\n"
      "s_cbranch_vccz .Lkxok%=\n"
      "buffer_load_dword v248, v244, %[ws], 0 offen sc1\n"
      "s_sub_u32 %[cap], %[cap], 1\n"
      "s_cbranch_scc1 .Lexit%=\n"
      "s_waitcnt vmcnt(0)\n"
      "s_branch .Lkx%=\n"
      ".Lkxok%=:\n"
      "buffer_load_dwordx4 v[192:195], v242, %[ws], %[kxo] offen sc1\n"
      "buffer_load_dwordx4 v[208:211], v242, %[ws], %[kxo] offen offset:64 sc1\n"
      "buffer_load_dwordx4 v[196:199], v242, %[ws], %[kxo] offen offset:2048 sc1\n"
      "buffer_load_dwordx4 v[212:215], v242, %[ws], %[kxo] offen offset:2112 sc1\n"
      "buffer_load_dwordx4 v[200:203], v243, %[ws], %[kxo] offen sc1\n"
      "buffer_load_dwordx4 v[216:219], v243, %[ws], %[kxo] offen offset:64 sc1\n"
      "buffer_load_dwordx4 v[204:207], v243, %[ws], %[kxo] offen offset:2048 sc1\n"
      "buffer_load_dwordx4 v[220:223], v243, %[ws], %[kxo] offen offset:2112 sc1\n"
      "s_add_u32 %[kxo], %[kxo], 0x40000\n"
      SLEEP(D_S0)
      "s_branch .Lnext%=\n"
      ".Lidle%=:\n"
      SLEEP(D_S1)
      "s_branch .Lnext%=\n"
      ".Lother%=:\n"
      "s_cmp_lg_u32 %[w], 6\n"
      "s_cbranch_scc1 .Lidle%=\n"
      "ds_read_b128 v[152:155], v245\n"
      "ds_read_b128 v[156:159], v245 offset:1024\n"
      "s_add_u32 s91, %[s], 1\n"
      "v_mov_b32 v160, s91\n"
      WL(1) "buffer_store_dwordx4 v[152:155], v246, %[ws], %[kmo] offen sc1\n"
      WL(0) "buffer_store_dwordx4 v[156:159], v246, %[ws], %[kmo] offen offset:1024 sc1\n"
      "s_add_u32 %[kmo], %[kmo], 0x10000\n"
      "s_waitcnt vmcnt(0)\n"
      "s_mov_b64 exec, 1\n"
      "buffer_store_dword v160, v247, %[ws], 0 offen sc1\n"
      "s_mov_b64 exec, -1\n"
      SLEEP(D_S0)
      ".Lnext%=:\n"
      A_NEXT
      "s_add_u32 %[mem], %[mem], 0x4000\n"
      "s_add_u32 %[s], %[s], 1\n"
      "s_cmp_lt_u32 %[s], 64\n"
      "s_cbranch_scc1 .Lpoll%=\n"
      ".Lexit%=:\n"
      : [s] "+s"(s), [mem] "+s"(mem), [cap] "+s"(cap), [kxo] "+s"(kxo), [kmo] "+s"(kmo), "+{v[148:151]}"(a0),
        "=&{v[152:183]}"(t0), "=&{v[184:191]}"(t1), "+{v[192:223]}"(kx), "=&{v[224:239]}"(t2), "=&{v248}"(seen) A_DIAG_OUT
      : [ws] "s"(srd(p.ws, WS_BYTES)), [w] "s"(w), "{v[0:31]}"(cat(wa[0], wb[0], wa[1], wb[1])),
        "{v[32:63]}"(cat(wa[2], wb[2], wa[3], wb[3])), "{v[64:95]}"(cat(wa[4], wb[4], wa[5], wb[5])),
        "{v[96:127]}"(cat(wa[6], wb[6], wa[7], wb[7])), "{v[128:143]}"(k0), "{v[240:247]}"(k1)
      : A_SCLOB A_DIAG_CLOB);
}

DEV void read_wo(char* sm, unsigned w, unsigned l, u4 (&wo)[4][4]) {
#pragma unroll
  for (int i = 0; i < 4; ++i) {
    const unsigned fa = frag_base(1024u, 4u * w + i, l);
#pragma unroll
    for (int f = 0; f < 4; ++f) wo[i][f] = frag(sm, fa + f * 32768u, 1024u);
  }
}

DEV void role_vm(const Params& p, unsigned h, char* sm) {
  constexpr unsigned L_MEM = 131072, L_V = L_MEM + 8192;
  const unsigned l = threadIdx.x & 63, w = rfl(threadIdx.x >> 6), c = l & 15, g = l >> 4;
  const rsrc_t ws = mk(p.ws, WS_BYTES);
  bool alive = true;

  u8 wv[8], wo[4][2];
  u4 a0;
  unsigned cb;
  {
    Hs hs = hs_begin(ws);
    u4 d0[16], d1[16], t[4][4];
    stage_slice(mk(p.wv, 4u << 20), h, 64u * w, l, d0);
    fence();
    hs_post(ws, hs, sm);
    fence();
    stage_wo(mk(p.wo, 4u << 20), h, w, l, d1);
    a0 = load_mem0(p, w, c, g);
    put_image(sm, w, l, d0);
    bar();
    const unsigned fa = frag_base(256u, w, l);
#pragma unroll
    for (int i = 0; i < 8; ++i) {
      const unsigned o = fa + (w ^ i) * 16384u;
      wv[i] = cat(frag(sm, o, 256u), frag(sm, o + 8192u, 256u));
    }
    bar();
    const unsigned echo = hs_echo(ws);
    put_image(sm, w, l, d1);
    bar();
    read_wo(sm, w, l, t);
    cb = window(p, hs_wait(ws, sm, echo, 2u, alive));
    hs_ack(ws, hs, 1u);
#pragma unroll
    for (int i = 0; i < 4; ++i) {
      wo[i][0] = cat(t[i][0], t[i][1]);
      wo[i][1] = cat(t[i][2], t[i][3]);
    }
  }
  STAMP(15, 8 + w);

  const unsigned lb = lds_base(sm);
  u16v k0;
  k0[0] = (c & 1) ? 0xeeeeeeeeu : 0x44444444u;
  k0[1] = w * 1024u + l * 16u;
#pragma unroll
  for (int i = 0; i < 8; ++i) k0[2 + i] = lb + L_MEM + (w ^ i) * 1024u + l * 16u;
  k0[10] = lb + L_V + (w * 16u + 4u * g + ((c >> 1) & 1)) * 16u + (2u * (c >> 2) + (c & 1)) * 2u;
  k0[11] = lb + L_V + l * 16u;
  const unsigned cg = 4u * w + g;
  k0[12] = cb + (B_M + cg) * 16384u + h * 256u + c * 16u;
  k0[13] = O_M2 + cg * 8192u + h * 256u + c * 16u;
  k0[14] = threadIdx.x == 448 ? F_M + h * FL : OOB;
  k0[15] = 0;

  unsigned s = 0, mem = rfl(cb + B_MEM * 16384u - 16384u), cap = rfl(alive ? SPIN_CAP : 0u), m2o = 0;
  u32v t0;
  u16v t1, t2;
  A_DIAG_DECL;
#define VM_ZERO(a) "v_mov_b32 v" #a ", 0\n"
#define VM_ZERO_M \
      VM_ZERO(192) VM_ZERO(193) VM_ZERO(194) VM_ZERO(195) VM_ZERO(196) VM_ZERO(197) VM_ZERO(198) VM_ZERO(199) \
      VM_ZERO(200) VM_ZERO(201) VM_ZERO(202) VM_ZERO(203) VM_ZERO(204) VM_ZERO(205) VM_ZERO(206) VM_ZERO(207)
  asm volatile(
      "s_mov_b32 s83, 0\n"
      VM_ZERO(180) VM_ZERO(184) VM_ZERO(185) VM_ZERO(186) VM_ZERO(187) VM_ZERO_M
      "ds_write_b128 v130, v[148:151]\n"
      "s_branch .Lhave%=\n"
      A_POLL_MEM
      ".Lhave%=:\n"
      A_STAMP("s[84:85]") A_CLK("s[70:71]")
      SMF("184:187", "148:151", "0:7", "v128")
      A_READ_MEM
      WL(6) SMF("184:187", "152:155", "8:15", "v128")
      WL(5) SMF("184:187", "156:159", "16:23", "v128")
      WL(4) SMF("184:187", "160:163", "24:31", "v128")
      WL(3) SMF("184:187", "164:167", "32:39", "v128")
      WL(2) SMF("184:187", "168:171", "40:47", "v128")
      WL(1) SMF("184:187", "172:175", "48:55", "v128")
      WL(0) SMF("184:187", "176:179", "56:63", "v128")
      A_CLK("s[74:75]")
      "s_nop 7\n"
      "v_add_f32 v152, v184, v185\n"
      "v_add_f32 v153, v186, v187\n"
      "v_cvt_pk_bf16_f32 v152, v152, v153\n"
      "ds_write_b16 v138, v152\n"
      "ds_write_b16_d16_hi v138, v152 offset:32\n"
      A_STAMP("s[86:87]") A_CLK("s[76:77]")
      VM_ZERO(184) VM_ZERO(185) VM_ZERO(186) VM_ZERO(187)
      "s_waitcnt lgkmcnt(0)\n"
      "s_barrier\n"
      A_CLK("s[78:79]")
      "ds_read_b128 v[152:155], v139\n"
      "ds_read_b128 v[156:159], v139 offset:1024\n"
      "buffer_store_dword v180, v142, %[ws], 0 offen sc1\n"
      WL(1) SMF("192:195", "152:155", "64:71", "v128") SMF("200:203", "152:155", "96:103", "v128")
      WL(0) SMF("192:195", "156:159", "72:79", "v128") SMF("200:203", "156:159", "104:111", "v128")
      SMF("196:199", "152:155", "80:87", "v128") SMF("204:207", "152:155", "112:119", "v128")
      SMF("196:199", "156:159", "88:95", "v128") SMF("204:207", "156:159", "120:127", "v128")
      A_CLK("s[92:93]")
      "s_nop 2\n"
      "v_add_f32 v192, v192, v193\n"
      "v_add_f32 v200, v200, v201\n"
      "v_add_f32 v194, v194, v195\n"
      "v_add_f32 v202, v202, v203\n"
      "v_cvt_pk_bf16_f32 v160, v192, v194\n"
      "v_cvt_pk_bf16_f32 v162, v200, v202\n"
      "v_bfi_b32 v160, -2, v160, s83\n"
      "v_bfi_b32 v162, -2, v162, s83\n"
      "v_add_f32 v196, v196, v197\n"
      "v_add_f32 v204, v204, v205\n"
      "v_add_f32 v198, v198, v199\n"
      "v_add_f32 v206, v206, v207\n"
      "v_cvt_pk_bf16_f32 v161, v196, v198\n"
      "v_cvt_pk_bf16_f32 v163, v204, v206\n"
      "v_bfi_b32 v161, -2, v161, s83\n"
      "v_bfi_b32 v163, -2, v163, s83\n"
      "v_permlane32_swap_b32 v160, v162\n"
      "s_add_u32 %[s], %[s], 1\n"
      "v_permlane32_swap_b32 v161, v163\n"
      "s_nop 1\n"
      "v_permlane16_swap_b32 v160, v161\n"
      "v_permlane16_swap_b32 v162, v163\n"
      "buffer_store_dwordx4 v[160:163], v140, %[ws], 0 offen sc1\n"
      A_STAMP("s[88:89]") A_CLK("s[94:95]") A_CLK("s[96:97]")
      "buffer_store_dwordx4 v[160:163], v141, %[ws], %[m2o] offen sc1\n"
      "v_mov_b32 v180, %[s]\n"
      VM_ZERO_M
      "s_xor_b32 s83, s83, 1\n"
      "s_add_u32 %[m2o], %[m2o], 0x40000\n"
      "s_add_u32 %[mem], %[mem], 0x4000\n"
      A_NEXT
      SLEEP(D_VM)
      "s_cmp_lt_u32 %[s], 64\n"
      "s_cbranch_scc1 .Lpoll%=\n"
      "s_waitcnt vmcnt(0)\n"
      "s_barrier\n"
      "buffer_store_dword v180, v142, %[ws], 0 offen sc1\n"
      ".Lexit%=:\n"
      : [s] "+s"(s), [mem] "+s"(mem), [cap] "+s"(cap), [m2o] "+s"(m2o), "+{v[148:151]}"(a0), "=&{v[152:183]}"(t0),
        "=&{v[184:199]}"(t1), "=&{v[200:215]}"(t2) A_DIAG_OUT
      : [ws] "s"(srd(p.ws, WS_BYTES)), [w] "s"(w), "{v[0:31]}"(cat(wv[0], wv[1], wv[2], wv[3])),
        "{v[32:63]}"(cat(wv[4], wv[5], wv[6], wv[7])), "{v[64:95]}"(cat(wo[0][0], wo[0][1], wo[1][0], wo[1][1])),
        "{v[96:127]}"(cat(wo[2][0], wo[2][1], wo[3][0], wo[3][1])), "{v[128:143]}"(k0)
      : A_SCLOB A_DIAG_CLOB);
}

#define PROBES 6
constexpr unsigned PROBER = 32, PROBER_BLOCK = 2;
DEV void lead(rsrc_t ws) {
  const unsigned l = threadIdx.x & 63;
  unsigned i, a0[4], last[4];
  const bool init = role_of(l, i) == R_V1;
#pragma unroll
  for (int k = 0; k < 4; ++k) {
    a0[k] = ld4c(ws, H_X + (k * 64u + l) * FL + 4u, 0);
  }
#pragma unroll
  for (unsigned k = 0; k < N_FLAGS / 64; ++k) st4c(0u, ws, (k * 64u + l) * FL, 0);
  drain();
  for (unsigned it = 0; it < SPIN_CAP; ++it) {
    fence();
    u2 v[4];
#pragma unroll
    for (int k = 0; k < 4; ++k) v[k] = ld8c(ws, H_X + (k * 64u + l) * FL, 0);
    const bool done = v[0][1] != a0[0] && v[1][1] != a0[1] && v[2][1] != a0[2] && v[3][1] != a0[3];
    const unsigned half = __builtin_amdgcn_readlane(v[0][1] - a0[0], PROBER_BLOCK);
    const unsigned stage = all_lanes(done || !init) ? (half == 2u ? 3u : 2u) : 1u;
#pragma unroll
    for (int k = 0; k < 4; ++k) {
      const unsigned e = (v[k][0] & ~3u) | stage;
      st4c(e, ws, it == 0 || e != last[k] ? H_Y + (k * 64u + l) * FL : OOB, 0);
      last[k] = e;
    }
    if (all_lanes(done)) break;
  }
}
DEV void role_v1(const Params& p, unsigned j, char* sm) {
  const unsigned l = threadIdx.x & 63, w = rfl(threadIdx.x >> 6), c = l & 15, g = l >> 4;
  const unsigned cg = j >> 1, rg = j & 1;
  const rsrc_t ws = mk(p.ws, WS_BYTES);
  bool alive = true;
  if (j == 0 && w == 7) {
    lead(ws);
    return;
  }
  const unsigned cb0 = crit_base(p);
  const u4 ones = {~0u, ~0u, ~0u, ~0u};
  Hs hs = hs_begin(ws);
  if (j) {
    for (unsigned o = ((j - 1u) * 512u + threadIdx.x) * 16u; o < B_END * 16384u; o += 63u * 512u * 16u)
      st16c(ones, ws, o, cb0);
    constexpr unsigned N = (NSEG - FAST) * 65536u;
    for (unsigned o = ((j - 1u) * 512u + threadIdx.x) * 16u; o < 2u * N + N / 4u; o += 63u * 512u * 16u)
      st16c(ones, ws, o + (o < N ? O_KM + FAST * 65536u : o < 2u * N ? O_PX + FAST * 65536u - N : O_PM + FAST * 16384u - 2u * N), 0);
  }
  hs_post(ws, hs, sm);
  drain();
  unsigned half = 0;
  if (j == PROBER && w == 0) {
    unsigned best[2] = {~0u, ~0u};
    for (int k = 0; k < PROBES; ++k) {
      const unsigned off = B_PROBE * 16384u + (k & 1) * 8192u + (l & 7) * 16u + (k >> 1) * 1024u;
      const unsigned long long t0 = __builtin_amdgcn_s_memtime();
      asm volatile("s_waitcnt lgkmcnt(0)" ::: "memory");
      st16c(ones, ws, off, cb0);
      drain();
      const unsigned long long t1 = __builtin_amdgcn_s_memtime();
      asm volatile("s_waitcnt lgkmcnt(0)" ::: "memory");
      const unsigned dt = (unsigned)(t1 - t0);
      if (k & 1) best[1] = min(best[1], dt); else best[0] = min(best[0], dt);
    }
    half = best[1] < best[0];
  }
  if (j) bar();
  if (w >= 4) return;
  if (w == 0) {
    hs_wait(ws, sm, hs_echo(ws), 1u, alive);
    hs_ack(ws, hs, 1u + half);
  }
  if (j == 0) bar();
  const unsigned cb = window(p, hs_wait(ws, sm, hs_echo(ws), 2u, alive));
  STAMP(15, 8 + w);

  const unsigned lb = lds_base(sm);
  const unsigned piece = rg * 4u + w;
  u16v k0 = {};
  k0[0] = (c & 4) ? 0xeeeeeeeeu : 0x44444444u;
  k0[1] = cb + (B_P + (piece >> 1)) * 16384u + (piece & 1) * 4096u + g * 128u + (c & 7) * 16u + 2048u;
  k0[2] = k0[1] - 2048u + (c >> 3) * 512u;
  k0[3] = cb + (B_M + cg) * 16384u + w * 2048u + l * 16u;
  k0[4] = O_U + (cg * 32u + 8u * w) * 1024u + l * 16u;
  k0[5] = k0[4] + 4096u;
  k0[6] = F_U + ((8u * w + (l & 7)) * 8u + (cg >> 2)) * FL;
  k0[7] = lb + ((g * 2u + ((c >> 1) & 1)) * 8u + 2u * (c >> 2) + (c & 1)) * 16u + w * 4u;
  k0[8] = lb + l * 16u;
  k0[9] = cg * 256u + rg * 128u + (l >> 3) * 16u;

  unsigned s = 0, mem = rfl(cb + B_MEM * 16384u), cap = rfl(alive ? SPIN_CAP : 0u), uo = 0, seen = 0;
  u32v t0, t1;
  A_DIAG_DECL;
#define V1_TAGS(d, a, b, c, OP) "v_bitop3_b32 v" d ", v" a ", v" b ", v" c " bitop3:" OP "\n"
#define V1_ZERO "v_mov_b32 v52, 0\nv_mov_b32 v53, 0\nv_mov_b32 v54, 0\nv_mov_b32 v55, 0\n" \
                "v_mov_b32 v56, 0\nv_mov_b32 v57, 0\nv_mov_b32 v58, 0\nv_mov_b32 v59, 0\n"
#define V1_LOAD_P \
      "buffer_load_dwordx4 v[40:43], v66, %[ws], 0 offen sc1\n" \
      "buffer_load_dwordx4 v[44:47], v66, %[ws], 0 offen offset:1024 sc1\n" \
      "buffer_load_dwordx4 v[48:51], v65, %[ws], 0 offen sc1\n"
#define V1_STEP(P, OP, MWANT, PWANT, NEG) \
      ".Ltop" P "%=:\n" \
      "v_cmp_ge_u32 vcc, %[s], v80\n" \
      "s_cbranch_vccnz .Luw" P "%=\n" \
      SLEEP(D_U) \
      "buffer_load_dwordx4 v[0:3], v68, %[ws], %[uo] offen sc1\n" \
      "buffer_load_dwordx4 v[4:7], v68, %[ws], %[uo] offen offset:1024 sc1\n" \
      "buffer_load_dwordx4 v[8:11], v68, %[ws], %[uo] offen offset:2048 sc1\n" \
      "buffer_load_dwordx4 v[12:15], v68, %[ws], %[uo] offen offset:3072 sc1\n" \
      "buffer_load_dwordx4 v[16:19], v69, %[ws], %[uo] offen sc1\n" \
      "buffer_load_dwordx4 v[20:23], v69, %[ws], %[uo] offen offset:1024 sc1\n" \
      "buffer_load_dwordx4 v[24:27], v69, %[ws], %[uo] offen offset:2048 sc1\n" \
      "buffer_load_dwordx4 v[28:31], v69, %[ws], %[uo] offen offset:3072 sc1\n" \
      "buffer_load_dword v80, v70, %[ws], 0 offen sc1\n" \
      "s_add_u32 %[uo], %[uo], 0x100000\n" \
      V1_ZERO \
      SLEEP(D_V1) \
      ".Lboth" P "%=:\n" \
      "buffer_load_dwordx4 v[32:35], v67, %[ws], 0 offen sc1\n" \
      "buffer_load_dwordx4 v[36:39], v67, %[ws], 0 offen offset:1024 sc1\n" \
      V1_LOAD_P \
      WV(3) \
      V1_TAGS("60", "32", "33", "34", OP) V1_TAGS("61", "35", "36", "37", OP) V1_TAGS("60", "60", "38", "39", OP) \
      V1_TAGS("60", "60", "61", "61", OP) \
      "v_and_b32 v60, 1, v60\n" \
      "v_cmp_ne_u32 s[80:81], " MWANT ", v60\n" \
      ".Lp" P "%=:\n" \
      WV(2) SMF("52:55", "40:43", "0:7", "v64") SMF("56:59", "40:43", "8:15", "v64") \
      V1_TAGS("60", "40", "41", "42", OP) \
      WV(1) SMF("52:55", "44:47", "16:23", "v64") SMF("56:59", "44:47", "24:31", "v64") \
      V1_TAGS("60", "60", "43", "44", OP) V1_TAGS("61", "45", "46", "47", OP) \
      WV(0) SMF("52:55", "48:51", "32:39", "v64") \
      V1_TAGS("60", "60", "48", "49", OP) V1_TAGS("61", "61", "50", "51", OP) V1_TAGS("60", "60", "61", "61", OP) \
      "v_and_b32 v60, s82, v60\n" \
      "v_cmp_ne_u32 vcc, " PWANT ", v60\n" \
      "s_or_b64 vcc, vcc, s[80:81]\n" \
      "s_cbranch_scc1 .Lre" P "%=\n" \
      A_STAMP("s[86:87]") A_CLK("s[70:71]") \
      "s_nop 0\n" \
      "v_permlane32_swap_b32 v56, v54\n" \
      "v_permlane32_swap_b32 v57, v55\n" \
      "v_add_f32 v60, v52, v54\n" \
      "v_add_f32 v62, v58, v56\n" \
      "v_add_f32 v61, v53, v55\n" \
      "v_add_f32 v63, v59, v57\n" \
      "v_cndmask_b32 v60, v60, v62, s[84:85]\n" \
      "v_cndmask_b32 v61, v61, v63, s[84:85]\n" \
      "s_cmp_lg_u32 %[w], 0\n" \
      "s_nop 0\n" \
      "v_permlane16_swap_b32 v60, v61\n" \
      "v_add_f32 v60, v60, v61\n" \
      "ds_write_b32 v71, v60 offset:" P "*1024\n" \
      A_CLK("s[72:73]") \
      "s_waitcnt lgkmcnt(0)\n" \
      "s_barrier\n" \
      "s_cbranch_scc1 .Lend" P "%=\n" \
      A_CLK("s[74:75]") \
      "ds_read_b128 v[52:55], v72 offset:" P "*1024\n" \
      "s_waitcnt lgkmcnt(0)\n" \
      A_CLK("s[76:77]") \
      "v_add_f32 v52, v52, v53\n" \
      "v_add_f32 v54, v54, v55\n" \
      "v_add_f32_e64 v52, " NEG "v52, " NEG "v54\n" \
      "s_nop 1\n" \
      "v_mov_b32_dpp v53, v52 quad_perm:[1,0,3,2]" DPP_ALL \
      "v_cvt_pk_bf16_f32 v56, v52, v53\n" \
      "s_nop 1\n" \
      "v_mov_b32_dpp v57, v56 row_shl:2" DPP_ALL \
      "v_mov_b32_dpp v58, v56 row_shl:4" DPP_ALL \
      "v_mov_b32_dpp v59, v56 row_shl:6" DPP_ALL \
      "s_mov_b64 exec, s[90:91]\n" \
      "buffer_store_dwordx4 v[56:59], v73, %[ws], %[mem] offen sc1\n" \
      "s_mov_b64 exec, -1\n" \
      A_STEP A_STAMP("s[88:89]") A_CLK("s[78:79]") A_SAVE("s86", 1) A_SAVE("s88", 2) A_SAVECLK \
      ".Lend" P "%=:\n" \
      "s_barrier\n" \
      A_NEXT \
      "s_add_u32 %[mem], %[mem], 0x4000\n" \
      "s_add_u32 %[s], %[s], 1\n"
#define V1_SIDE(P) \
      ".Luw" P "%=:\n" \
      "buffer_load_dword v80, v70, %[ws], 0 offen sc1\n" \
      "s_sub_u32 %[cap], %[cap], 1\n" \
      "s_cbranch_scc1 .Lexit%=\n" \
      "s_waitcnt vmcnt(0)\n" \
      "s_branch .Ltop" P "%=\n" \
      ".Lre" P "%=:\n" \
      "s_sub_u32 %[cap], %[cap], 1\n" \
      "s_cbranch_scc1 .Lexit%=\n" \
      "s_cmp_lg_u64 s[80:81], 0\n" \
      V1_ZERO \
      "s_cbranch_scc1 .Lboth" P "%=\n" \
      V1_LOAD_P \
      "s_branch .Lp" P "%=\n"
  asm volatile(
      "s_mov_b32 s82, 0x80008000\n"
      "s_mov_b32 s84, 0\n"
      "s_mov_b32 s85, -1\n"
      "s_mov_b32 s90, 0x01010101\n"
      "s_mov_b32 s91, 0x01010101\n"
      V1_STEP("0", "0xfe", "0", "0", "")
      "s_cmp_ge_u32 %[s], 63\n"
      "s_cbranch_scc1 .Lexit%=\n"
      V1_STEP("1", "0x80", "1", "s82", "-")
      "s_branch .Ltop0%=\n"
      V1_SIDE("0") V1_SIDE("1")
      ".Lexit%=:\n"
      : [s] "+s"(s), [mem] "+s"(mem), [cap] "+s"(cap), [uo] "+s"(uo), "=&{v[0:31]}"(t0), "=&{v[32:63]}"(t1),
        "+{v80}"(seen) A_DIAG_OUT
      : [ws] "s"(srd(p.ws, WS_BYTES)), [w] "s"(w), "{v[64:79]}"(k0)
      : A_SCLOB A_DIAG_CLOB);
}

DEV void load_x(rsrc_t xr, unsigned w, unsigned c, unsigned g, int s, u4 (&xp)[4]) {
  const unsigned voff = ((16u * (w >> 2) + c) * 512u + 128u * (w & 3) + 8u * g) * 2u;
#pragma unroll
  for (int i = 0; i < 4; ++i) xp[i] = ld16(xr, voff + i * 64u, s * 32768u);
}
DEV void put_x(char* sm, unsigned base, unsigned w, unsigned l, const u4 (&xp)[4]) {
#pragma unroll
  for (int i = 0; i < 4; ++i) lds16(sm, base + (4u * w + i) * 1024u + l * 16u) = xp[i];
}

DEV void token_softmax(rsrc_t ws, char* sm, unsigned qb, const u4 (&km)[4], unsigned h, unsigned w, unsigned l, int s) {
  const unsigned c = l & 15, g = l >> 4;
  u4 q[4], k0[4], k1[4];
#pragma unroll
  for (int m = 0; m < 4; ++m) {
    q[m] = lds16(sm, qb + (w * 4u + m) * 1024u);
    k0[m] = lds16(sm, qb + 8192u + m * 1024u);
    k1[m] = lds16(sm, qb + 12288u + m * 1024u);
  }
  sched();
  f4 s0 = {0, 0, 0, 0}, s1 = {0, 0, 0, 0}, sm_ = {0, 0, 0, 0};
#pragma unroll
  for (int m = 0; m < 4; ++m) {
    s0 = mma(k0[m], q[m], s0);
    s1 = mma(k1[m], q[m], s1);
  }
#pragma unroll
  for (int m = 0; m < 4; ++m) sm_ = mma(km[m], q[m], sm_);
  const unsigned tq = 16u * w + c;
  const float ninf = -__builtin_inff();
#pragma unroll
  for (int r = 0; r < 4; ++r) {
    s0[r] = 4u * g + r <= tq ? s0[r] : ninf;
    s1[r] = 16u + 4u * g + r <= tq ? s1[r] : ninf;
  }
  float mx = fmaxf(fmaxf(fmaxf(s0[0], s0[1]), fmaxf(s0[2], s0[3])), fmaxf(fmaxf(s1[0], s1[1]), fmaxf(s1[2], s1[3])));
  mx = group_max(fmaxf(mx, fmaxf(sm_[0], sm_[1])));
  const float mc = -mx * SCALE_LOG2E;
  float e[10];
#pragma unroll
  for (int r = 0; r < 4; ++r) {
    e[r] = __builtin_amdgcn_exp2f(fmaf(s0[r], SCALE_LOG2E, mc));
    e[4 + r] = __builtin_amdgcn_exp2f(fmaf(s1[r], SCALE_LOG2E, mc));
  }
  e[8] = __builtin_amdgcn_exp2f(fmaf(sm_[0], SCALE_LOG2E, mc));
  e[9] = __builtin_amdgcn_exp2f(fmaf(sm_[1], SCALE_LOG2E, mc));
  const float sum = group_sum(((e[0] + e[1]) + (e[2] + e[3])) + ((e[4] + e[5]) + (e[6] + e[7])) + (e[8] + e[9]));
  const float rs = __builtin_amdgcn_rcpf(sum);
  const u4 px = {pk(e[0] * rs, e[1] * rs), pk(e[2] * rs, e[3] * rs), pk(e[4] * rs, e[5] * rs), pk(e[6] * rs, e[7] * rs)};
  st16c(px, ws, O_PX + (w * 32u + h) * 1024u + l * 16u, s * 65536u);
  const unsigned dm = pk(e[8] * rs, e[9] * rs);
  auto r16 = __builtin_amdgcn_permlane16_swap(dm, dm, false, false);
  auto ra = __builtin_amdgcn_permlane32_swap(r16[0], r16[0], false, false);
  auto rb = __builtin_amdgcn_permlane32_swap(r16[1], r16[1], false, false);
  const u4 pm = {ra[0], rb[0], ra[1], rb[1]};
  st16c(pm, ws, g == 0 ? O_PM + (w * 8u + (h >> 2)) * 1024u + ((h & 3) * 16u + c) * 16u : OOB, s * 16384u);
}

DEV void role_s2(const Params& p, unsigned h, char* sm) {
#define S2_MID 5
#define S2_SW 5
  constexpr int LAG = 4, R = LAG + 1;
  constexpr unsigned L_X = 0, L_QK = 65536;
  const unsigned l = threadIdx.x & 63, w = rfl(threadIdx.x >> 6), c = l & 15, g = l >> 4;
  const rsrc_t ws = mk(p.ws, WS_BYTES), xr = mk(p.bx, 2u << 20);
  bool alive = true;

  u4 xp[4], wa[16], wb[16];
  {
    const rsrc_t wr = mk(w < 4 ? p.wq : p.wk, 4u << 20);
    Hs hs = hs_begin(ws);
    u4 d0[16], d1[16];
    stage_slice(wr, h, 64u * (w & 3), l, d0);
    fence();
    hs_post(ws, hs, sm);
    fence();
    stage_slice(wr, h, 64u * (w & 3) + 256u, l, d1);
    load_x(xr, w, c, g, 0, xp);
    unsigned echo;
    const unsigned fa = (w >> 2) * 65536u + frag_base(256u, 2u * (w & 3), l), fb = fa ^ 32u;
#pragma unroll
    for (int k = 0; k < 2; ++k) {
      put_image(sm, w, l, k ? d1 : d0);
      bar();
#pragma unroll
      for (int i = 0; i < 8; ++i) {
        wa[8 * k + i] = frag(sm, fa + i * 8192u, 256u);
        wb[8 * k + i] = frag(sm, fb + i * 8192u, 256u);
      }
      if (k == 0) echo = hs_echo(ws);
      else hs_wait(ws, sm, echo, 1u, alive);
      bar();
    }
    hs_ack(ws, hs, 1u);
  }
  put_x(sm, L_X, w, l, xp);
  load_x(xr, w, c, g, 1, xp);
  bar();
  STAMP(15, 8 + w);
  if (w >= 4) __builtin_amdgcn_s_setprio(1);

  const unsigned km_off = w < 2 ? O_KM + h * 2048u + (g * 8u + ((2u * (c >> 2) + (c & 1)) ^ swz(g))) * 16u : OOB;
  const unsigned km_flag = w < 2 ? F_KM + h * FL : OOB;
  const unsigned px_flag = w < 2 && l == 0 ? F_PX + (w * 32u + h) * FL : OOB;
  const unsigned kx_flag = w >= 4 && l == 0 ? F_KX + (h * 4u + (w & 3)) * FL : OOB;
  const unsigned kx_off = w >= 4 ? O_KX + h * 8192u + (w & 3) * 2048u + l * 16u : OOB;
  unsigned km_seen = 0;
  int slot = 0;
  for (int it = 0; it < NSEG; ++it) {
    const int s = max(it - LAG, 0);
    const bool soft = it >= LAG && w < 2;
    if (it == S2_SW && w < 4) __builtin_amdgcn_s_setprio(2);
    if (soft && !all_lanes(km_seen > (unsigned)s)) wait_flags(ws, km_flag, s + 1, alive);
    if (w == 0 || w == 4) STAMP(3 + (w >> 2) * 4, it);
    u4 km[4];
#pragma unroll
    for (int m = 0; m < 4; ++m) km[m] = ld16c(ws, km_off + m * 512u, s * 65536u);
    km_seen = ld4c(ws, km_flag, 0);
    f4 acc[2][2] = {};
    const unsigned xb = L_X + (it & 1) * 32768u + l * 16u;
    u4 x[3][2];
#pragma unroll
    for (int k = 0; k < 2; ++k)
#pragma unroll
      for (int t = 0; t < 2; ++t) x[k][t] = lds16(sm, xb + (t * 16 + k) * 1024u);
#pragma unroll
    for (int ks = 0; ks < 16; ++ks) {
      sched();
      if (ks + 2 < 16) {
#pragma unroll
        for (int t = 0; t < 2; ++t) x[(ks + 2) % 3][t] = lds16(sm, xb + (t * 16 + ks + 2) * 1024u);
      }
      if (ks == S2_MID) {
        asm volatile("s_waitcnt vmcnt(9)" ::: "memory");
        st4c(it, ws, kx_flag, 0);
      }
      sched();
#pragma unroll
      for (int t = 0; t < 2; ++t) {
        acc[0][t] = mma(wa[ks], x[ks % 3][t], acc[0][t]);
        acc[1][t] = mma(wb[ks], x[ks % 3][t], acc[1][t]);
      }
    }
    sched();
    asm volatile("s_waitcnt vmcnt(6)" ::: "memory");
    if (w == 0 || w == 4) STAMP(4 + (w >> 2) * 4, it);
    st4c(s, ws, px_flag, 0);
    put_x(sm, L_X + ((it + 1) & 1) * 32768u, w, l, xp);
#pragma unroll
    for (int t = 0; t < 2; ++t) {
      const u4 o = {pk(acc[0][t][0], acc[1][t][0]), pk(acc[0][t][1], acc[1][t][1]), pk(acc[0][t][2], acc[1][t][2]),
                    pk(acc[0][t][3], acc[1][t][3])};
      lds16(sm, L_QK + slot * 16384u + (w >> 2) * 8192u + (t * 4u + (w & 3)) * 1024u + l * 16u) = o;
      st16c(o, ws, kx_off + t * 1024u, it * 262144u);
    }
    load_x(xr, w, c, g, it + 2, xp);
    if (w == 0 || w == 4) STAMP(5 + (w >> 2) * 4, it);
    slot = slot == R - 1 ? 0 : slot + 1;
    if (soft) {
      token_softmax(ws, sm, L_QK + slot * 16384u + l * 16u, km, h, w, l, s);
      if (w == 0) STAMP(2, s);
    }
    bar();
  }
  if (w >= 2) return;
  for (int s = NSEG - LAG; s < NSEG; ++s) {
    if (s <= FAST) {
      drain();
      st4c(s, ws, px_flag, 0);
    }
    if (s < FAST) wait_flags(ws, km_flag, s + 1, alive);
    u4 km[4];
    for (unsigned it = 0; it < SPIN_CAP; ++it) {
      fence();
#pragma unroll
      for (int m = 0; m < 4; ++m) km[m] = ld16c(ws, km_off + m * 512u, s * 65536u);
      if (s < FAST || all_lanes(filled(km[0]) && filled(km[1]) && filled(km[2]) && filled(km[3]))) break;
    }
    slot = slot == R - 1 ? 0 : slot + 1;
    token_softmax(ws, sm, L_QK + slot * 16384u + l * 16u, km, h, w, l, s);
    if (w == 0) STAMP(2, s);
  }
}

DEV void role_vu(const Params& p, unsigned h, char* sm) {
  constexpr unsigned L_X = 0, L_V = 65536;
  const unsigned l = threadIdx.x & 63, w = rfl(threadIdx.x >> 6), c = l & 15, g = l >> 4;
  const rsrc_t ws = mk(p.ws, WS_BYTES), xr = mk(p.bx, 2u << 20);

  u4 xp[4], wv[16], wo[4][4];
  {
    bool alive = true;
    Hs hs = hs_begin(ws);
    u4 d0[16], d1[16];
    stage_slice(mk(p.wv, 4u << 20), h, 64u * w, l, d0);
    fence();
    hs_post(ws, hs, sm);
    fence();
    stage_wo(mk(p.wo, 4u << 20), h, w, l, d1);
    load_x(xr, w, c, g, 0, xp);
    put_image(sm, w, l, d0);
    bar();
    const unsigned fa = frag_base(256u, w, l);
#pragma unroll
    for (int f = 0; f < 16; ++f) wv[f] = frag(sm, fa + f * 8192u, 256u);
    const unsigned echo = hs_echo(ws);
    bar();
    put_image(sm, w, l, d1);
    bar();
    read_wo(sm, w, l, wo);
    hs_wait(ws, sm, echo, 1u, alive);
    hs_ack(ws, hs, 1u);
    bar();
  }
  put_x(sm, L_X, w, l, xp);
  load_x(xr, w, c, g, 1, xp);
  bar();
  STAMP(15, 8 + w);

  const unsigned v_off = L_V + ((w >> 1) * 64u + (2u * (w & 1) + (g >> 1)) * 16u + c) * 16u + (g & 1) * 8u;
  const unsigned flag = l == 0 ? F_U + (h * 8u + w) * FL : OOB;
  for (int s = 0; s < NSEG; ++s) {
    f4 acc[2] = {};
    const unsigned xb = L_X + (s & 1) * 32768u + l * 16u;
    u4 x[4][2];
#pragma unroll
    for (int k = 0; k < 3; ++k)
#pragma unroll
      for (int t = 0; t < 2; ++t) x[k][t] = lds16(sm, xb + (t * 16 + k) * 1024u);
#pragma unroll
    for (int ks = 0; ks < 16; ++ks) {
      sched();
      if (ks + 3 < 16) {
#pragma unroll
        for (int t = 0; t < 2; ++t) x[(ks + 3) & 3][t] = lds16(sm, xb + (t * 16 + ks + 3) * 1024u);
      }
      sched();
#pragma unroll
      for (int t = 0; t < 2; ++t) acc[t] = mma(wv[ks], x[ks & 3][t], acc[t]);
    }
    sched();
#pragma unroll
    for (int t = 0; t < 2; ++t)
      *reinterpret_cast<u2*>(sm + v_off + (s & 1) * 8192u + t * 4096u) = u2{pk(acc[t][0], acc[t][1]), pk(acc[t][2], acc[t][3])};
    if (w == 0) STAMP(3, s);
    drain();
    st4c(s, ws, flag, 0);
    if (w == 0) STAMP(4, s);
    put_x(sm, L_X + ((s + 1) & 1) * 32768u, w, l, xp);
    load_x(xr, w, c, g, s + 2, xp);
    bar();
    if (w == 0) STAMP(5, s);
    u4 v[8];
#pragma unroll
    for (int k = 0; k < 8; ++k) v[k] = lds16(sm, L_V + (s & 1) * 8192u + ((k & 1) * 4 + (k >> 1)) * 1024u + l * 16u);
    sched();
    f4 u[4][2] = {};
#pragma unroll
    for (int k = 0; k < 8; ++k)
#pragma unroll
      for (int i = 0; i < 4; ++i) u[i][k & 1] = mma(v[k], wo[i][k >> 1], u[i][k & 1]);
#pragma unroll
    for (int i = 0; i < 4; ++i) {
      const u4 o = {pk(u[i][0][0], u[i][0][1]), pk(u[i][0][2], u[i][0][3]), pk(u[i][1][0], u[i][1][1]),
                    pk(u[i][1][2], u[i][1][3])};
      st16c(o, ws, O_U + ((4u * w + i) * 32u + h) * 1024u + l * 16u, s * 1048576u);
    }
    if (w == 0) STAMP(2, s);
  }
  drain();
  st4c(NSEG, ws, flag, 0);
}

DEV void role_v2(const Params& p, unsigned j, char* sm) {
  constexpr unsigned ROW = 144, PART = 16 * ROW, BUF = 8 * PART;
  const unsigned l = threadIdx.x & 63, w = rfl(threadIdx.x >> 6), c = l & 15, g = l >> 4;
  const unsigned b = j >> 1, qt = j & 1;
  const rsrc_t ws = mk(p.ws, WS_BYTES), outr = mk(p.out, 8u << 20);
  bool alive = true;

  const unsigned hq = 4u * w + (l & 3);
  const unsigned uf_off = F_U + (hq * 8u + (b >> 1)) * FL;
  const unsigned pf_off = (l & 4) ? F_M + hq * FL : F_PX + (qt * 32u + hq) * FL;
  const unsigned u_off = O_U + (2u * b * 32u + 4u * w) * 1024u + l * 16u;
  const unsigned m_off = O_M2 + 2u * b * 8192u + w * 1024u + l * 16u;
  const unsigned px_off = O_PX + (qt * 32u + 4u * w) * 1024u + l * 16u;
  const unsigned pm_off = O_PM + (qt * 8u + w) * 1024u + l * 16u;
  const unsigned out_off = ((16u * qt + (threadIdx.x >> 5)) * 1024u + 512u + 32u * b + (threadIdx.x & 31)) * 4u;
  unsigned ring;
  {
    Hs hs = hs_begin(ws);
    hs_post(ws, hs, sm);
    bar();
    ring = window(p, hs_wait(ws, sm, hs_echo(ws), 2u, alive)) + (B_M + 2u * b) * 16384u + w * 1024u + l * 16u;
    hs_ack(ws, hs, 1u);
  }
  unsigned u_seen = wait_flags(ws, uf_off, 1u, alive), p_seen = 0;
  u4 ua[2][4];
#pragma unroll
  for (int k = 0; k < 8; ++k) ua[k >> 2][k & 3] = ld16c(ws, u_off + (k >> 2) * 32768u + (k & 3) * 1024u, 0);

  for (int s = 0; s < NSEG; ++s) {
    const unsigned need = s < FAST || ((l & 4) && s < LAST) ? s + 1 : 0;
    if (!all_lanes(p_seen >= need)) p_seen = wait_flags(ws, pf_off, need, alive);
    const unsigned mo = s < LAST ? m_off + s * 262144u : ring, ms = s < LAST ? 8192u : 16384u;
    u4 px[4], pm, m0, m1;
    for (unsigned it = 0; it < SPIN_CAP; ++it) {
      fence();
#pragma unroll
      for (int i = 0; i < 4; ++i) px[i] = ld16c(ws, px_off + i * 1024u, s * 65536u);
      pm = ld16c(ws, pm_off, s * 16384u);
      m0 = ld16c(ws, mo, 0);
      m1 = ld16c(ws, mo, ms);
      if (s < FAST) break;
      const bool tag = s < LAST || (m0[0] & m0[1] & m0[2] & m0[3] & m1[0] & m1[1] & m1[2] & m1[3] & 1u);
      if (all_lanes(tag && filled(px[0]) && filled(px[1]) && filled(px[2]) && filled(px[3]) && filled(pm))) break;
    }
    f4 a0 = {0, 0, 0, 0}, a1 = {0, 0, 0, 0};
#pragma unroll
    for (int i = 0; i < 4; ++i) {
      a0 = mma(ua[0][i], px[i], a0);
      a1 = mma(ua[1][i], px[i], a1);
    }
    a0 = mma(m0, pm, a0);
    a1 = mma(m1, pm, a1);
    sched();
    if (s + 1 < NSEG) {
      if (!all_lanes(u_seen > (unsigned)s + 1u)) u_seen = wait_flags(ws, uf_off, s + 2, alive);
#pragma unroll
      for (int k = 0; k < 8; ++k)
        ua[k >> 2][k & 3] = ld16c(ws, u_off + (k >> 2) * 32768u + (k & 3) * 1024u, (s + 1) * 1048576u);
    }
    sched();
    const unsigned rb = (s & 1) * BUF;
    *reinterpret_cast<f4*>(sm + rb + w * PART + c * ROW + g * 16u) = a0;
    *reinterpret_cast<f4*>(sm + rb + w * PART + c * ROW + g * 16u + 64u) = a1;
    bar();
    float v = 0.f;
#pragma unroll
    for (int k = 0; k < 8; ++k)
      v += *reinterpret_cast<const float*>(sm + rb + k * PART + (threadIdx.x >> 5) * ROW + (threadIdx.x & 31) * 4u);
    __builtin_amdgcn_raw_buffer_store_b32(__float_as_uint(rbf(v)), outr, out_off, s * 131072u, OAUX);
    if (w == 0) STAMP(2, s);
  }
}

DEV f2 cmul(f2 a, f2 b) {
  float re, im;
  asm("v_mul_f32 %0, %3, %5\n\tv_mul_f32 %1, %3, %4\n\tv_fma_f32 %0, %2, %4, -%0\n\tv_fma_f32 %1, %2, %5, %1"
      : "=&v"(re), "=&v"(im) : "v"(a[0]), "v"(a[1]), "v"(b[0]), "v"(b[1]));
  return f2{re, im};
}
DEV f2 cfma(f2 a, f2 b, f2 c) {
  float re, im;
  asm("v_fma_f32 %0, -%3, %5, %6\n\tv_fma_f32 %1, %3, %4, %7\n\tv_fma_f32 %0, %2, %4, %0\n\tv_fma_f32 %1, %2, %5, %1"
      : "=&v"(re), "=&v"(im) : "v"(a[0]), "v"(a[1]), "v"(b[0]), "v"(b[1]), "v"(c[0]), "v"(c[1]));
  return f2{re, im};
}
DEV f2 from_group(float re, float im, unsigned c, int k) {
  return f2{__shfl(re, (int)c + 16 * k, 64), __shfl(im, (int)c + 16 * k, 64)};
}

DEV void role_a(const Params& p, unsigned ci, char* sm) {
  constexpr unsigned GS = 272, KS = 4 * GS, TS = 32 * KS + 32, L_TAB = 4 * TS;
  const unsigned tid = threadIdx.x, l = tid & 63, w = rfl(tid >> 6), c = l & 15, g = l >> 4;
  const rsrc_t ws = mk(p.ws, WS_BYTES), xr = mk(p.ax, 2u << 20), outr = mk(p.out, 8u << 20);
  bool alive = true;
  {
    Hs hs = hs_begin(ws);
    hs_post(ws, hs, sm);
    if (w == 0) {
      hs_wait(ws, sm, hs_echo(ws), 1u, alive);
      hs_ack(ws, hs, 1u);
      wait_flags(ws, F_U + (l & 31) * 8u * FL, 1u, alive);
    }
    bar();
  }
  {
    const unsigned voff = ((64u * ci + 16u * (c >> 2) + 4u * (w >> 1) + (c & 3)) * 512u + 256u * (w & 1) + 8u * g) * 2u;
    u4 xp[8];
#pragma unroll
    for (int i = 0; i < 8; ++i) xp[i] = ld16(xr, voff + i * 64u, 0);
#pragma unroll
    for (int i = 0; i < 8; ++i) lds16(sm, (8u * w + i) * 1024u + l * 16u) = xp[i];
  }
  f2 lam64;
  {
    const float lr = bf2f(((const unsigned short*)p.lre)[tid]), li = bf2f(((const unsigned short*)p.lim)[tid]);
    const float dt = rbf(expf(bf2f(((const unsigned short*)p.ldt)[tid])));
    const float er = expf(dt * lr);
    float sn, cs;
    sincosf(dt * li, &sn, &cs);
    f2 lam = {er * cs, er * sn};
    asm volatile("s_nop 1" : "+v"(lam));
    const float nr = lam[0] - 1.0f, ni = lam[1];
    f2 coef;
    if (fabsf(lr) >= fabsf(li)) {
      const float t = li / lr, d = 1.0f / (lr + li * t);
      coef = f2{smul(nr + smul(ni, t), d), smul(ni - smul(nr, t), d)};
    } else {
      const float t = lr / li, d = 1.0f / (lr * t + li);
      coef = f2{smul(smul(nr, t) + ni, d), smul(smul(ni, t) - nr, d)};
    }
    f2 l16 = cmul(lam, lam);
    l16 = cmul(l16, l16);
    l16 = cmul(l16, l16);
    l16 = cmul(l16, l16);
    lam64 = cmul(l16, l16);
    lam64 = cmul(lam64, lam64);
    *reinterpret_cast<f4*>(sm + L_TAB + tid * 32u) = f4{lam[0], lam[1], coef[0], coef[1]};
    *reinterpret_cast<f2*>(sm + L_TAB + tid * 32u + 16u) = l16;
  }
  bar();

  f4 hr[4][4] = {}, hi[4][4] = {};
  {
    const rsrc_t br = mk(p.bre, 1u << 19), bi = mk(p.bim, 1u << 19);
    const unsigned voff = ((64u * w + c) * 512u + 8u * g) * 2u;
    u4 fr[4], fi[4];
#pragma unroll
    for (int k = 0; k < 4; ++k) {
      fr[k] = ld16(br, voff + k * 64u, 0);
      fi[k] = ld16(bi, voff + k * 64u, 0);
    }
#pragma unroll
    for (int n = 0; n < 64; ++n) {
      const int ct = n >> 4, ks = n & 15;
      const u4 r = fr[n & 3], i = fi[n & 3];
      fence();
      if (n + 4 < 64) {
        fr[n & 3] = ld16(br, voff + ((n + 4) & 15) * 64u, ((n + 4) >> 4) * 16384u);
        fi[n & 3] = ld16(bi, voff + ((n + 4) & 15) * 64u, ((n + 4) >> 4) * 16384u);
      }
#pragma unroll
      for (int t = 0; t < 4; ++t) {
        const u4 x = lds16(sm, (t * 16 + ks) * 1024u + l * 16u);
        hr[ct][t] = mma(x, r, hr[ct][t]);
        hi[ct][t] = mma(x, i, hi[ct][t]);
      }
    }
  }
  __builtin_amdgcn_sched_barrier(0);
  asm volatile("s_nop 7\n\ts_nop 7" ::: "memory");
  __builtin_amdgcn_sched_barrier(0);
#pragma unroll
  for (int ct = 0; ct < 4; ++ct) {
    const unsigned ch = 64u * w + 16u * ct + c;
    const f4 tb = *reinterpret_cast<const f4*>(sm + L_TAB + ch * 32u);
    const f2 l16 = *reinterpret_cast<const f2*>(sm + L_TAB + ch * 32u + 16u);
    const f2 lam = {tb[0], tb[1]}, coef = {tb[2], tb[3]};
    f2 hh = {0.f, 0.f};
#pragma unroll
    for (int i = 0; i < 16; ++i) {
      hh = cfma(lam, hh, cmul(coef, f2{hr[ct][i >> 2][i & 3], hi[ct][i >> 2][i & 3]}));
      hr[ct][i >> 2][i & 3] = hh[0];
      hi[ct][i >> 2][i & 3] = hh[1];
    }
    f2 e = from_group(hh[0], hh[1], c, 0);
#pragma unroll
    for (int k = 1; k < 4; ++k) e = cfma(l16, e, from_group(hh[0], hh[1], c, k));
    if (g == 0) st8c(__builtin_bit_cast(u2, e), ws, O_AE + ci * 4096u + ch * 8u, 0);
  }
  drain();
  bar();
  if (tid == 0) st4c(1u, ws, F_A + ci * FL, 0);
  wait_flags(ws, F_A + (l & 31) * FL, (l & 31) < ci ? 1u : 0u, alive);
  {
    f2 hin = {0.f, 0.f};
#pragma unroll 8
    for (unsigned k = 0; k < ci; ++k) hin = cfma(lam64, hin, __builtin_bit_cast(f2, ld8c(ws, O_AE + tid * 8u, k * 4096u)));
    *reinterpret_cast<f2*>(sm + L_TAB + tid * 32u + 24u) = hin;
  }
  bar();
#pragma unroll
  for (int ct = 0; ct < 4; ++ct) {
    const unsigned ch = 64u * w + 16u * ct + c;
    const f4 t0 = *reinterpret_cast<const f4*>(sm + L_TAB + ch * 32u);
    const f4 t1 = *reinterpret_cast<const f4*>(sm + L_TAB + ch * 32u + 16u);
    const f2 lam = {t0[0], t0[1]}, l16 = {t1[0], t1[1]};
    f2 z = {t1[2], t1[3]};
#pragma unroll
    for (int k = 0; k < 3; ++k) {
      const f2 n = cfma(l16, z, from_group(hr[ct][3][3], hi[ct][3][3], c, k));
      z = (unsigned)k < g ? n : z;
    }
    const unsigned hb = g * TS + (ch >> 5) * KS + ((ch >> 3) & 3) * GS + (ch & 7) * 2u;
#pragma unroll
    for (int i = 0; i < 16; ++i) {
      z = cmul(lam, z);
      const unsigned d = pk(2.0f * (hr[ct][i >> 2][i & 3] + z[0]), -2.0f * (hi[ct][i >> 2][i & 3] + z[1]));
      lds_put2(sm, hb + i * 16u, d);
      lds_put2(sm, hb + 16u * KS + i * 16u, d >> 16);
    }
  }
  bar();
  f4 y[4][4] = {};
  {
    const rsrc_t cr = mk(p.cre, 1u << 19), cm = mk(p.cim, 1u << 19);
    const unsigned voff = ((64u * w + c) * 512u + 8u * g) * 2u;
    u4 fa[2][4];
#pragma unroll
    for (int k = 0; k < 2; ++k)
#pragma unroll
      for (int d = 0; d < 4; ++d) fa[k][d] = ld16(cr, voff + k * 64u, d * 16384u);
#pragma unroll
    for (int ks = 0; ks < 32; ++ks) {
      u4 a[4];
      fence();
#pragma unroll
      for (int d = 0; d < 4; ++d) {
        a[d] = fa[ks & 1][d];
        if (ks + 2 < 32) fa[ks & 1][d] = ld16(ks + 2 < 16 ? cr : cm, voff + ((ks + 2) & 15) * 64u, d * 16384u);
      }
#pragma unroll
      for (int t = 0; t < 4; ++t) {
        const u4 hb = lds16(sm, t * TS + ks * KS + g * GS + c * 16u);
#pragma unroll
        for (int d = 0; d < 4; ++d) y[d][t] = mma(a[d], hb, y[d][t]);
      }
    }
  }
  const rsrc_t dr = mk(p.dsk, 1024);
#pragma unroll
  for (int d = 0; d < 4; ++d) {
    const unsigned col = 64u * w + 16u * d + 4u * g;
    const u2 dk = ld8(dr, col * 2u, 0);
#pragma unroll
    for (int t = 0; t < 4; ++t) {
      const unsigned row = 64u * ci + 16u * t + c;
      const u2 xv = ld8(xr, (row * 512u + col) * 2u, 0);
      const f4 o = {y[d][t][0] + rbf(smul(bf2f(xv[0] & 0xffffu), bf2f(dk[0] & 0xffffu))),
                    y[d][t][1] + rbf(smul(bf2f(xv[0] >> 16), bf2f(dk[0] >> 16))),
                    y[d][t][2] + rbf(smul(bf2f(xv[1] & 0xffffu), bf2f(dk[1] & 0xffffu))),
                    y[d][t][3] + rbf(smul(bf2f(xv[1] >> 16), bf2f(dk[1] >> 16)))};
      __builtin_amdgcn_raw_buffer_store_b128(__builtin_bit_cast(u4, o), outr, (row * 1024u + col) * 4u, 0, OAUX);
    }
  }
  if (w == 0) STAMP(2, 0);
}

__global__ void __launch_bounds__(512, 2) fused_kernel(Params p) {
  extern __shared__ __attribute__((aligned(16))) char smem[];
  unsigned i;
  const unsigned role = role_of(blockIdx.x, i);
  if (threadIdx.x < 64) TSTAMP(0);
  switch (role) {
    case R_S1: role_s1(p, i, smem); break;
    case R_VM: role_vm(p, i, smem); break;
    case R_V1: role_v1(p, i, smem); break;
    case R_S2: role_s2(p, i, smem); break;
    case R_VU: role_vu(p, i, smem); break;
    case R_V2: role_v2(p, i, smem); break;
    case R_A: role_a(p, i, smem); break;
  }
  if (threadIdx.x < 64) TSTAMP(2);
}

extern "C" void kernel_launch(void* const* d_in, const int* in_sizes, int n_in, void* d_out, int out_size, void* d_ws,
                              size_t ws_size, hipStream_t stream) {
  Params p;
  p.ax = d_in[0]; p.lre = d_in[1]; p.lim = d_in[2]; p.bre = d_in[3]; p.bim = d_in[4]; p.cre = d_in[5];
  p.cim = d_in[6]; p.ldt = d_in[7]; p.dsk = d_in[8]; p.bx = d_in[9]; p.mem0 = d_in[10]; p.wq = d_in[11];
  p.wk = d_in[12]; p.wv = d_in[13]; p.wo = d_in[14];
  p.out = (float*)d_out;
  p.ws = (char*)d_ws;
  (void)hipFuncSetAttribute((const void*)fused_kernel, hipFuncAttributeMaxDynamicSharedMemorySize, DYN_LDS);
  fused_kernel<<<256, 512, DYN_LDS, stream>>>(p);
}
```

```cpp
#include <hip/hip_runtime.h>
#include <hip/hip_bf16.h>
#include <stdint.h>

typedef __attribute__((ext_vector_type(2))) unsigned u2;
typedef __attribute__((ext_vector_type(4))) unsigned u4;
typedef __attribute__((ext_vector_type(8))) unsigned u8;
typedef __attribute__((ext_vector_type(16))) unsigned u16v;
typedef __attribute__((ext_vector_type(32))) unsigned u32v;
typedef __attribute__((ext_vector_type(2))) float f2;
typedef __attribute__((ext_vector_type(4))) float f4;
typedef __attribute__((ext_vector_type(2))) __bf16 bf2;
typedef __attribute__((ext_vector_type(8))) __bf16 bf16x8;
typedef __attribute__((ext_vector_type(16))) __bf16 bf16x16;
typedef __amdgpu_buffer_rsrc_t rsrc_t;
#define DEV __device__ __forceinline__

struct Params {
  const void *ax, *lre, *lim, *bre, *bim, *cre, *cim, *ldt, *dsk, *bx, *mem0, *wq, *wk, *wv, *wo;
  float* out;
  char* ws;
};

constexpr int NSEG = 64, LAST = NSEG - 1, WSLEEP = 4;
constexpr int FAST = NSEG - 3;
constexpr unsigned SPIN_CAP = 400000u;
constexpr unsigned DYN_LDS = 156u * 1024u;

constexpr unsigned FL = 128, F_KM = 0, F_PX = 32 * FL, F_M = 96 * FL, F_A = 128 * FL, F_KX = 192 * FL, F_U = 320 * FL,
                   F_PH = 576 * FL, N_FLAGS = 640;
constexpr unsigned H_X = N_FLAGS * FL, H_Y = H_X + 256 * FL;
constexpr unsigned O_AE = 5u << 20;
constexpr unsigned O_KX = 8u << 20, O_KM = 24u << 20, O_PX = 28u << 20, O_PM = 32u << 20, O_W = 34u << 20,
                   WS_BYTES = 208u << 20;
constexpr unsigned OOB = 1u << 30;
constexpr unsigned W_PROBE = 100u << 14, W_P = 2u << 20, P_SLOT = 4u << 14, W_M = 8u << 20, M_SLOT = 32u << 14;
constexpr unsigned UH = 8192u, W_U = 42u << 20, U_SLOT = 2u << 20;

enum { R_S1, R_VM, R_V1, R_S2, R_VU, R_V2, R_A };
__host__ __device__ inline unsigned role_of(unsigned b, unsigned& i) {
  const unsigned x = b & 7, r = b >> 3;
  if (x >= 4) {
    i = (x - 4) * 8u + (r >> 2);
    return R_S2 + (r & 3);
  }
  if (x == 1 || x == 2) {
    i = (x - 1) * 32u + r;
    return R_V1;
  }
  i = (x ? 16u : 0u) + (r >> 1);
  return r & 1;
}

DEV rsrc_t mk(const void* p, unsigned bytes) {
  return __builtin_amdgcn_make_buffer_rsrc((void*)p, 0, bytes, 0x00020000);
}
DEV unsigned rfl(unsigned v) { return __builtin_amdgcn_readfirstlane(v); }
DEV u4 ld16(rsrc_t r, unsigned v, unsigned s) { return __builtin_amdgcn_raw_buffer_load_b128(r, v, s, 0); }
DEV u2 ld8(rsrc_t r, unsigned v, unsigned s) { return __builtin_amdgcn_raw_buffer_load_b64(r, v, s, 0); }
DEV unsigned ld4(rsrc_t r, unsigned v, unsigned s) { return __builtin_amdgcn_raw_buffer_load_b32(r, v, s, 0); }
DEV unsigned ld2(rsrc_t r, unsigned v, unsigned s) { return __builtin_amdgcn_raw_buffer_load_b16(r, v, s, 0); }
DEV u4 ld16c(rsrc_t r, unsigned v, unsigned s) { return __builtin_amdgcn_raw_buffer_load_b128(r, v, s, 16); }
DEV u2 ld8c(rsrc_t r, unsigned v, unsigned s) { return __builtin_amdgcn_raw_buffer_load_b64(r, v, s, 16); }
DEV unsigned ld4c(rsrc_t r, unsigned v, unsigned s) { return __builtin_amdgcn_raw_buffer_load_b32(r, v, s, 16); }
DEV void st16c(u4 d, rsrc_t r, unsigned v, unsigned s) { __builtin_amdgcn_raw_buffer_store_b128(d, r, v + s, 0, 16); }
DEV void st8c(u2 d, rsrc_t r, unsigned v, unsigned s) { __builtin_amdgcn_raw_buffer_store_b64(d, r, v, s, 16); }
DEV void st4c(unsigned d, rsrc_t r, unsigned v, unsigned s) { __builtin_amdgcn_raw_buffer_store_b32(d, r, v, s, 16); }
DEV void fence() { asm volatile("" ::: "memory"); }
DEV void drain() { asm volatile("s_waitcnt vmcnt(0)" ::: "memory"); }
DEV void bar() { asm volatile("s_waitcnt lgkmcnt(0)\n\ts_barrier" ::: "memory"); }
DEV bool all_lanes(bool ok) { return __builtin_amdgcn_ballot_w64(!ok) == 0ull; }

DEV float bf2f(unsigned h) { return __uint_as_float(h << 16); }
DEV unsigned pk(float a, float b) {
  f2 v = {a, b};
  return __builtin_bit_cast(unsigned, __builtin_convertvector(v, bf2));
}
DEV float rbf(float a) { return bf2f(pk(a, a) & 0xffffu); }
DEV float smul(float a, float b) {
  float r;
  asm("v_mul_f32 %0, %1, %2" : "=v"(r) : "v"(a), "v"(b));
  return r;
}
DEV float sadd(float a, float b) {
  float r;
  asm("v_add_f32 %0, %1, %2" : "=v"(r) : "v"(a), "v"(b));
  return r;
}
DEV f4 mma(u4 a, u4 b, f4 c) {
  return __builtin_amdgcn_mfma_f32_16x16x32_bf16(__builtin_bit_cast(bf16x8, a), __builtin_bit_cast(bf16x8, b), c, 0, 0, 0);
}
DEV f4 smma(u4 a, u8 b, f4 c, unsigned idx) {
  return __builtin_amdgcn_smfmac_f32_16x16x64_bf16(__builtin_bit_cast(bf16x8, a), __builtin_bit_cast(bf16x16, b), c,
                                                   (int)idx, 0, 0);
}
DEV float swap32_add(float a, float b) {
  auto r = __builtin_amdgcn_permlane32_swap(__float_as_uint(a), __float_as_uint(b), false, false);
  return __uint_as_float(r[0]) + __uint_as_float(r[1]);
}
DEV float swap16_add(float a, float b) {
  auto r = __builtin_amdgcn_permlane16_swap(__float_as_uint(a), __float_as_uint(b), false, false);
  return __uint_as_float(r[0]) + __uint_as_float(r[1]);
}
DEV float group_max(float m) {
  auto r = __builtin_amdgcn_permlane16_swap(__float_as_uint(m), __float_as_uint(m), false, false);
  m = fmaxf(__uint_as_float(r[0]), __uint_as_float(r[1]));
  auto q = __builtin_amdgcn_permlane32_swap(__float_as_uint(m), __float_as_uint(m), false, false);
  return fmaxf(__uint_as_float(q[0]), __uint_as_float(q[1]));
}
DEV float group_sum(float m) {
  const float t = swap16_add(m, m);
  return swap32_add(t, t);
}
DEV u4& lds16(char* sm, unsigned off) { return *reinterpret_cast<u4*>(sm + off); }
DEV void lds_put2(char* sm, unsigned off, unsigned v) { *reinterpret_cast<unsigned short*>(sm + off) = (unsigned short)v; }

DEV unsigned wait_flags(rsrc_t ws, unsigned off, unsigned need, bool& alive) {
  unsigned v = 0;
  for (unsigned it = 0; alive; ++it) {
    fence();
    v = ld4c(ws, off, 0);
    if (all_lanes(v >= need)) break;
    if (it > SPIN_CAP) alive = false;
    __builtin_amdgcn_s_sleep(WSLEEP);
  }
  fence();
  return v;
}
DEV bool filled(u4 v) { return max(max(v[0], v[1]), max(v[2], v[3])) != ~0u; }
DEV unsigned crit_base(const Params& p) {
  const unsigned lo = (unsigned)(uintptr_t)p.ws;
  return ((lo + O_W + 0x1fffffu) & ~0x1fffffu) - lo;
}
struct Hs { unsigned x, a; };
constexpr unsigned L_HS = DYN_LDS - 16u;
DEV unsigned& lds4(char* sm, unsigned off) { return *reinterpret_cast<unsigned*>(sm + off); }
DEV Hs hs_begin(rsrc_t ws) {
  const unsigned o = threadIdx.x == 0 ? blockIdx.x * FL : OOB;
  return Hs{ld4c(ws, o + H_Y, 0), ld4c(ws, o + H_X + 4u, 0)};
}
DEV void hs_post(rsrc_t ws, Hs& h, char* sm) {
  h.x = (h.x & ~3u) + 4u;
  st4c(h.x, ws, threadIdx.x == 0 ? blockIdx.x * FL + H_X : OOB, 0);
  lds4(sm, threadIdx.x == 0 ? L_HS : L_HS + 4u) = h.x;
}
DEV unsigned hs_echo(rsrc_t ws) { return ld4c(ws, blockIdx.x * FL + H_Y, 0); }
DEV unsigned hs_wait(rsrc_t ws, char* sm, unsigned e, unsigned stage, bool& alive) {
  const unsigned x = rfl(lds4(sm, L_HS));
  unsigned v = rfl(e) - x;
  for (unsigned it = 0; alive && (v < stage || v > 3u); ++it) {
    if (it > SPIN_CAP) alive = false;
    __builtin_amdgcn_s_sleep(WSLEEP);
    fence();
    v = rfl(hs_echo(ws)) - x;
  }
  fence();
  return v;
}
DEV void hs_ack(rsrc_t ws, const Hs& h, unsigned d) {
  st4c(h.a + d, ws, threadIdx.x == 0 ? blockIdx.x * FL + H_X + 4u : OOB, 0);
}
DEV unsigned slot_at(unsigned o, unsigned stage) { return rfl(o | (((o >> 8) ^ (stage == 3u ? 8192u : 0u)) & 8192u)); }

DEV void stage_slice(rsrc_t r, unsigned h, unsigned row0, unsigned l, u4 (&d)[16]) {
  const unsigned v = (l >> 4) * 8192u + h * 256u + ((l & 15) ^ ((l >> 4) << 1)) * 16u;
#pragma unroll
  for (int i = 0; i < 16; ++i) d[i] = ld16(r, v ^ ((i & 2) * 64u), (row0 + 4u * i) * 8192u);
}
DEV void stage_wo(rsrc_t r, unsigned h, unsigned w, unsigned l, u4 (&d)[16]) {
#pragma unroll
  for (int i = 0; i < 16; ++i)
    d[i] = ld16(r, (l ^ (((i & 3) | ((i >> 1) & 4)) << 1)) * 16u, (h * 128u + w * 16u + i) * 1024u);
}
DEV void put_image(char* sm, unsigned w, unsigned l, const u4 (&d)[16]) {
#pragma unroll
  for (int i = 0; i < 16; ++i) lds16(sm, (16u * w + i) * 1024u + l * 16u) = d[i];
}
typedef __attribute__((ext_vector_type(4))) short s4;
DEV u2 lds_tr(char* sm, unsigned off) {
  return __builtin_bit_cast(u2, __builtin_amdgcn_ds_read_tr16_b64_v4i16((__attribute__((address_space(3))) s4*)(sm + off)));
}
DEV unsigned frag_base(unsigned pitch, unsigned cb, unsigned l) {
  const unsigned g = l >> 4, q = (l >> 2) & 3, p = l & 3;
  return (8u * g + q) * pitch + ((2u * cb + (p >> 1)) ^ ((q | ((g & 1) << 2)) << 1)) * 16u + (p & 1) * 8u;
}
DEV u4 frag(char* sm, unsigned off, unsigned pitch) {
  const u2 a = lds_tr(sm, off), b = lds_tr(sm, off + 4u * pitch);
  return u4{a[0], a[1], b[0], b[1]};
}
DEV u8 cat(u4 a, u4 b) { return __builtin_shufflevector(a, b, 0, 1, 2, 3, 4, 5, 6, 7); }
DEV u16v cat(u8 a, u8 b) { return __builtin_shufflevector(a, b, 0, 1, 2, 3, 4, 5, 6, 7, 8, 9, 10, 11, 12, 13, 14, 15); }
DEV u32v cat(u8 a, u8 b, u8 c, u8 d) {
  return __builtin_shufflevector(cat(a, b), cat(c, d), 0, 1, 2, 3, 4, 5, 6, 7, 8, 9, 10, 11, 12, 13, 14, 15, 16, 17, 18,
                                 19, 20, 21, 22, 23, 24, 25, 26, 27, 28, 29, 30, 31);
}
DEV u4 srd(const void* p, unsigned bytes) {
  return u4{(unsigned)(uintptr_t)p, (unsigned)((uintptr_t)p >> 32) & 0xffffu, bytes, 0x00020000u};
}
DEV unsigned lds_base(char* sm) { return (unsigned)(uintptr_t)(__attribute__((address_space(3))) char*)sm; }
DEV unsigned swz(unsigned g) { return (g & 1) | ((g & 2) << 1); }
DEV void sched() { __builtin_amdgcn_sched_barrier(0); }

DEV u4 load_mem0(const Params& p, unsigned w, unsigned c, unsigned g) {
  const rsrc_t r = mk(p.mem0, 8192);
  const unsigned voff = ((c >> 1) * 512u + 64u * w + 16u * g + 2u * (c & 1)) * 2u;
  u4 a;
#pragma unroll
  for (int i = 0; i < 4; ++i) a[i] = ld4(r, voff, 8 * i);
  return a;
}
constexpr float SCALE_LOG2E = 0.08838834764831845f * 1.4426950408889634f;

#define A_SCLOB "s80", "s81", "s82", "s83", "s84", "s85", "s86", "s87", "s88", "s89", "s90", "s91", "s98", "s99", "vcc", "scc", "memory"
#define SMF(d, a, b, i) "v_smfmac_f32_16x16x64_bf16 v[" d "], v[" a "], v[" b "], " i "\n"
#define MFM(d, a, b, c) "v_mfma_f32_16x16x32_bf16 v[" d "], v[" a "], v[" b "], " c "\n"
#define A_ADVANCE(r, step, mask) "s_add_u32 " r ", " r ", " step "\ns_and_b32 s91, " r ", " mask "\n" \
  "s_cselect_b32 s91, 0, 0x2000\ns_xor_b32 " r ", " r ", s91\n"
#define A_NEXT_P(r) A_ADVANCE(r, "0x10000", "0x1f0000")
#define A_NEXT_M(r) A_ADVANCE(r, "0x80000", "0x180000")
#define DPP_ALL " row_mask:0xf bank_mask:0xf\n"
#define A_POLL_MEM \
  ".Lpoll%=:\n" \
  "buffer_load_dwordx4 v[148:151], v129, %[ws], %[mem] offen sc1\n" \
  "s_sub_u32 %[cap], %[cap], 1\n" \
  "s_cbranch_scc1 .Lexit%=\n" \
  "s_waitcnt vmcnt(0)\n" \
  "ds_write_b128 v130, v[148:151]\n" \
  "v_max3_u32 v152, v148, v149, v150\n" \
  "v_max_u32 v152, v152, v151\n" \
  "v_cmp_eq_u32 vcc, -1, v152\n" \
  "s_cbranch_vccnz .Lpoll%=\n"
#define A_READ_MEM \
  "s_waitcnt lgkmcnt(0)\n" \
  "s_barrier\n" \
  "ds_read_b128 v[152:155], v131\n" \
  "ds_read_b128 v[156:159], v132\n" \
  "ds_read_b128 v[160:163], v133\n" \
  "ds_read_b128 v[164:167], v134\n" \
  "ds_read_b128 v[168:171], v135\n" \
  "ds_read_b128 v[172:175], v136\n" \
  "ds_read_b128 v[176:179], v137\n"
#define D_KM 20
#define D_M2 22
#define D_P 4
#define D_U 2
#define STR_(x) #x
#define SLEEP(x) "s_sleep " STR_(x) "\n"
#define G_T 166
#define G_S1 80
#define G_S0 (G_S1 - 20)
#define G_S6 (G_S1 - 4)
#define G_VM (G_S1 + 2)
#define G_V1 88
#define XSTR_(x) STR_(x)
#define G_XS 30
#define G_XM G_XS
#define G_XV 36
#define G_OS 72
#define G_OM (G_OS + 11)
#define A_MARK \
  "buffer_load_dword %[pg], %[pa], %[ws], 0 offen sc1\n" \
  "s_memrealtime s[98:99]\n" \
  "s_add_u32 %[due], %[due], " XSTR_(G_T) "\n" \
  "s_add_u32 %[ofs], %[ofs], " XSTR_(G_T) "\n" \
  "s_waitcnt lgkmcnt(0)\n"
#define A_PLAN(P, d, x, POST) \
  "v_readfirstlane_b32 s99, %[pg]\n" \
  "s_sub_u32 %[tgt], %[due], s98\n" \
  "s_add_u32 s99, s99, %[ofs]\n" \
  "s_sub_u32 s99, s99, s98\n" \
  "s_cmp_lt_u32 s99, 2 * " XSTR_(G_T) "\n" \
  "s_cselect_b32 s99, s99, %[tgt]\n" \
  "s_max_i32 s99, %[tgt], s99\n" \
  "s_add_u32 %[due], s99, s98\n" \
  "s_mov_b32 %[tgt], %[due]\n" \
  "s_cmp_ge_i32 s99, " XSTR_(d) "\n" \
  "s_cbranch_scc1 .Lplan" P "%=\n" \
  "s_add_u32 %[tgt], s98, " XSTR_(d + x) "\n" \
  POST(P) \
  ".Lplan" P "%=:\n"
#define A_KEEP(P)
#define A_POST(P) \
  "s_cmp_lg_u32 %[mute], 0\n" \
  "s_cbranch_scc1 .Lplan" P "%=\n" \
  "s_mov_b32 %[due], %[tgt]\n" \
  "s_sub_u32 s99, %[due], %[ofs]\n" \
  "v_mov_b32 %[pg], s99\n" \
  "buffer_atomic_umax %[pg], %[pb], %[ws], 0 offen sc1\n"
#define A_PACE_DECL(o) \
  unsigned due = (unsigned)__builtin_amdgcn_s_memrealtime() - 1000u, ofs = o - G_T, tgt, pg; \
  const unsigned pa = F_PH + ((blockIdx.x + 37u * w) & 63u) * FL, pb = F_PH + l * FL
#define A_PACE_OUT [due] "+s"(due), [ofs] "+s"(ofs), [tgt] "=&s"(tgt), [pg] "=&v"(pg)
#define A_PACE_IN [pa] "v"(pa), [pb] "v"(pb), [mute] "s"(w | ((blockIdx.x >> 3) & 7))
#define A_WAIT(P) \
  "s_branch .Lclk" P "%=\n" \
  ".Lnap" P "%=:\n" \
  "s_sleep 2\n" \
  ".Lclk" P "%=:\n" \
  "s_memrealtime s[98:99]\n" \
  "s_waitcnt lgkmcnt(0)\n" \
  "s_sub_u32 s98, %[tgt], s98\n" \
  "s_cmp_gt_i32 s98, 12\n" \
  "s_cbranch_scc1 .Lnap" P "%=\n" \
  "s_cmp_gt_i32 s98, 0\n" \
  "s_cbranch_scc1 .Lclk" P "%=\n"
#define WL(n) "s_waitcnt lgkmcnt(" #n ")\n"
#define WV(n) "s_waitcnt vmcnt(" #n ")\n"

DEV void role_s1(const Params& p, unsigned h, char* sm) {
  constexpr unsigned L_MEM = 131072, L_Q = L_MEM + 8192, L_K = L_MEM + 10240;
  const unsigned l = threadIdx.x & 63, w = rfl(threadIdx.x >> 6), c = l & 15, g = l >> 4;
  const rsrc_t ws = mk(p.ws, WS_BYTES);
  bool alive = true;

  u8 wa[8], wb[8];
  u4 a0;
  unsigned stage, echo;
  {
    const rsrc_t wr = mk(w < 4 ? p.wq : p.wk, 4u << 20);
    Hs hs = hs_begin(ws);
    u4 d0[16], d1[16];
    stage_slice(wr, h, 64u * w, l, d0);
    fence();
    hs_post(ws, hs, sm);
    fence();
    stage_slice(wr, h, 64u * (w ^ 4u), l, d1);
    a0 = load_mem0(p, w, c, g);
    const unsigned fa = (w >> 2) * 65536u + frag_base(256u, 2u * (w & 3), l), fb = fa ^ 32u;
#pragma unroll
    for (int k = 0; k < 2; ++k) {
      put_image(sm, w, l, k ? d1 : d0);
      bar();
#pragma unroll
      for (int i = 0; i < 4; ++i) {
        const unsigned o = ((w ^ i) & 3) * 16384u;
        wa[4 * k + i] = cat(frag(sm, fa + o, 256u), frag(sm, fa + o + 8192u, 256u));
        wb[4 * k + i] = cat(frag(sm, fb + o, 256u), frag(sm, fb + o + 8192u, 256u));
      }
      if (k == 0) {
        bar();
        echo = hs_echo(ws);
      }
    }
    stage = hs_wait(ws, sm, echo, 2u, alive);
    hs_ack(ws, hs, 1u);
  }

  const unsigned lb = lds_base(sm), cb = crit_base(p);
  u16v k0;
  u8 k1;
  k0[0] = (c & 1) ? 0xeeeeeeeeu : 0x44444444u;
  k0[1] = w * 1024u + l * 16u;
#pragma unroll
  for (int i = 0; i < 8; ++i) k0[2 + i] = lb + L_MEM + (w ^ i) * 1024u + l * 16u;
#pragma unroll
  for (int r = 0; r < 2; ++r)
    k0[10 + r] = lb + (w < 4 ? L_Q : L_K) + (((w & 3) * 4u + (c >> 2)) * 8u + ((2u * g + r) ^ swz(c >> 2))) * 16u + (c & 3) * 4u;
  k0[12] = k0[13] = 0;
  k0[14] = lb + L_Q + (g * 8u + ((c & 7) ^ swz(g))) * 16u;
  k0[15] = lb + L_K + (g * 8u + ((((c >> 2) & 1) * 4u + (c >> 3) * 2u + (c & 1)) ^ swz(g))) * 16u;
  const unsigned piece = ((c & 7) >> 2) * 4u + (h >> 3), hl = h & 7;
  const unsigned p_base = cb + (piece >> 1) * 16384u + (piece & 1) * 4096u;
  const unsigned tok = ((c >> 1) & 1) * 16u + (c >> 3) * 8u + ((c >> 2) & 1) * 2u + (c & 1);
  k1[0] = p_base + (hl >> 1) * 512u + (2u * (hl & 1) + (g >> 1)) * 128u + ((g & 1) * 4u + (c & 3)) * 16u;
  k1[1] = p_base + 2048u + (hl >> 1) * 128u + ((g >> 1) * 4u + (c & 3)) * 16u + (hl & 1) * 8u;
  k1[2] = O_KX + h * 8192u + ((tok >> 4) * 64u + g * 16u + (tok & 15)) * 16u;
  k1[3] = k1[2] + 4096u;
  k1[4] = w == 0 ? F_KX + (h * 4u + (l & 3)) * FL : OOB;
  k1[5] = lb + L_K + l * 16u;
  k1[6] = O_KM + h * 2048u + l * 16u;
  k1[7] = F_KM + h * FL;

  u32v kx = ~u32v{};
  unsigned seen;
  if (w == 0) {
    wait_flags(ws, k1[4], 1u, alive);
    u4 f[8];
#pragma unroll
    for (int i = 0; i < 8; ++i) f[i] = ld16c(ws, k1[2] + (i & 3) * 2048u + (i >> 2) * 64u, 0);
    kx = cat(cat(f[0], f[1]), cat(f[2], f[3]), cat(f[4], f[5]), cat(f[6], f[7]));
  }

  unsigned s = 0, mem = rfl(cb + slot_at(0, stage) - 16384u), cap = rfl(alive ? SPIN_CAP : 0u), kxo = 262144u, kmo = 0;
  A_PACE_DECL(G_OS);
  unsigned po = slot_at(W_P + (w ? 2u * P_SLOT : 0u), stage);
  u32v t0;
  u8 t1;
  u16v t2;
  asm volatile(
      "s_mov_b32 s80, 0xff00ff00\n"
      "s_mov_b32 s81, 0xff00ff00\n"
      "s_mov_b32 s82, 0x3e0293ee\n"
      "s_cmp_eq_u32 %[w], 0\n"
      "s_cselect_b32 s90, 63, 0\n"
      "v_mov_b32 v184, 0\nv_mov_b32 v185, 0\nv_mov_b32 v186, 0\nv_mov_b32 v187, 0\n"
      "v_mov_b32 v188, 0\nv_mov_b32 v189, 0\nv_mov_b32 v190, 0\nv_mov_b32 v191, 0\n"
      "ds_write_b128 v130, v[148:151]\n"
      "s_branch .Lhave%=\n"
      A_POLL_MEM
      ".Lhave%=:\n"
      SMF("184:187", "148:151", "0:7", "v128") SMF("188:191", "148:151", "8:15", "v128")
      A_READ_MEM
      WL(6) SMF("184:187", "152:155", "16:23", "v128") SMF("188:191", "152:155", "24:31", "v128")
      WL(5) SMF("184:187", "156:159", "32:39", "v128") SMF("188:191", "156:159", "40:47", "v128")
      WL(4) SMF("184:187", "160:163", "48:55", "v128") SMF("188:191", "160:163", "56:63", "v128")
      WL(3) SMF("184:187", "164:167", "64:71", "v128") SMF("188:191", "164:167", "72:79", "v128")
      WL(2) SMF("184:187", "168:171", "80:87", "v128") SMF("188:191", "168:171", "88:95", "v128")
      WL(1) SMF("184:187", "172:175", "96:103", "v128") SMF("188:191", "172:175", "104:111", "v128")
      WL(0) SMF("184:187", "176:179", "112:119", "v128") SMF("188:191", "176:179", "120:127", "v128")
      "s_cmp_lt_u32 %[s], s90\n"
      "s_nop 5\n"
      "v_add_f32 v152, v184, v185\n"
      "v_add_f32 v154, v186, v187\n"
      "v_add_f32 v153, v188, v189\n"
      "v_add_f32 v155, v190, v191\n"
      "v_cvt_pk_bf16_f32 v152, v152, v153\n"
      "v_cvt_pk_bf16_f32 v154, v154, v155\n"
      "ds_write_b32 v138, v152\n"
      "ds_write_b32 v139, v154\n"
      "v_mov_b32 v184, 0\nv_mov_b32 v185, 0\nv_mov_b32 v186, 0\nv_mov_b32 v187, 0\n"
      "v_mov_b32 v188, 0\nv_mov_b32 v189, 0\nv_mov_b32 v190, 0\nv_mov_b32 v191, 0\n"
      "s_waitcnt lgkmcnt(0)\n"
      "s_barrier\n"
      "s_cbranch_scc0 .Lother%=\n"
      "ds_read_b128 v[152:155], v142\n"
      "ds_read_b128 v[168:171], v143\n"
      "ds_read_b128 v[156:159], v142 offset:512\n"
      "ds_read_b128 v[172:175], v143 offset:512\n"
      "ds_read_b128 v[160:163], v142 offset:1024\n"
      "ds_read_b128 v[176:179], v143 offset:1024\n"
      "ds_read_b128 v[164:167], v142 offset:1536\n"
      "ds_read_b128 v[180:183], v143 offset:1536\n"
      WL(7) MFM("224:227", "192:195", "152:155", "0") MFM("228:231", "208:211", "152:155", "0")
      WL(6) MFM("232:235", "168:171", "152:155", "0")
      WL(5) MFM("224:227", "196:199", "156:159", "v[224:227]") MFM("228:231", "212:215", "156:159", "v[228:231]")
      WL(4) MFM("232:235", "172:175", "156:159", "v[232:235]")
      WL(3) MFM("224:227", "200:203", "160:163", "v[224:227]") MFM("228:231", "216:219", "160:163", "v[228:231]")
      WL(2) MFM("232:235", "176:179", "160:163", "v[232:235]")
      WL(1) MFM("224:227", "204:207", "164:167", "v[224:227]") MFM("228:231", "220:223", "164:167", "v[228:231]")
      WL(0) MFM("232:235", "180:183", "164:167", "v[232:235]")
      "s_nop 5\n"
      "v_max3_f32 v152, v224, v225, v226\n"
      "v_max3_f32 v153, v227, v228, v229\n"
      "v_max3_f32 v154, v230, v231, v232\n"
      "v_max3_f32 v152, v152, v153, v233\n"
      "v_max_f32 v153, v152, v154\n"
      "v_max_f32 v152, v152, v154\n"
      "v_cndmask_b32 v156, v224, v228, s[80:81]\n"
      "v_cndmask_b32 v157, v225, v229, s[80:81]\n"
      "v_permlane16_swap_b32 v152, v153\n"
      "v_max_f32 v154, v152, v153\n"
      "v_max_f32 v155, v152, v153\n"
      "v_cndmask_b32 v158, v226, v230, s[80:81]\n"
      "v_cndmask_b32 v159, v227, v231, s[80:81]\n"
      "v_permlane32_swap_b32 v154, v155\n"
      "v_cndmask_b32 v160, v232, v233, s[80:81]\n"
      "v_max_f32 v152, v154, v155\n"
      "v_mul_f32 v152, 0xbe0293ee, v152\n"
      "v_fma_f32 v156, v156, s82, v152\n"
      "v_fma_f32 v157, v157, s82, v152\n"
      "v_fma_f32 v158, v158, s82, v152\n"
      "v_fma_f32 v159, v159, s82, v152\n"
      "v_fma_f32 v160, v160, s82, v152\n"
      "v_exp_f32 v156, v156\n"
      "v_exp_f32 v157, v157\n"
      "v_exp_f32 v158, v158\n"
      "v_exp_f32 v159, v159\n"
      "v_exp_f32 v160, v160\n"
      "v_add_f32 v152, v156, v157\n"
      "v_add_f32 v153, v158, v159\n"
      "v_add_f32 v152, v152, v160\n"
      "v_add_f32 v152, v152, v153\n"
      "s_nop 1\n"
      "v_add_f32_dpp v154, v152, v152 row_ror:8" DPP_ALL
      "v_add_f32_dpp v155, v152, v152 row_ror:8" DPP_ALL
      "s_nop 1\n"
      "v_permlane16_swap_b32 v154, v155\n"
      "v_add_f32 v152, v154, v155\n"
      "v_add_f32 v153, v154, v155\n"
      "s_nop 1\n"
      "v_permlane32_swap_b32 v152, v153\n"
      "v_add_f32 v152, v152, v153\n"
      "v_rcp_f32 v152, v152\n"
      "s_add_u32 s91, %[s], 2\n"
      "v_mul_f32 v156, v156, v152\n"
      "v_mul_f32 v157, v157, v152\n"
      "v_mul_f32 v158, v158, v152\n"
      "v_mul_f32 v159, v159, v152\n"
      "v_mul_f32 v160, v160, v152\n"
      "v_cvt_pk_bf16_f32 v164, v156, v157\n"
      "v_cvt_pk_bf16_f32 v165, v158, v159\n"
      "v_mov_b32_dpp v161, v160 row_ror:8" DPP_ALL
      "v_mov_b32_dpp v166, v164 row_ror:8" DPP_ALL
      "v_mov_b32_dpp v167, v165 row_ror:8" DPP_ALL
      "v_cvt_pk_bf16_f32 v162, v160, v161\n"
      "v_cvt_pk_bf16_f32 v163, v160, v161\n"
      "s_not_b64 exec, s[80:81]\n"
      "buffer_store_dwordx4 v[164:167], v240, %[ws], %[po] offen sc1\n"
      "s_mov_b64 exec, -1\n"
      "v_permlane16_swap_b32 v162, v163\n"
      "s_mov_b32 exec_lo, 0xff\n"
      "s_mov_b32 exec_hi, 0xff\n"
      "buffer_store_dwordx2 v[162:163], v241, %[ws], %[po] offen sc1\n"
      "s_mov_b64 exec, -1\n"
      "buffer_load_dword v248, v244, %[ws], 0 offen sc1\n"
      A_MARK
      WV(0)
      A_PLAN("0", G_S0, G_XS, A_KEEP)
      "s_cmp_ge_u32 %[s], 62\n"
      "s_cbranch_scc1 .Lnext%=\n"
      ".Lkx%=:\n"
      "v_cmp_gt_u32 vcc, s91, v248\n"
      "s_cbranch_vccz .Lkxok%=\n"
      "buffer_load_dword v248, v244, %[ws], 0 offen sc1\n"
      "s_sub_u32 %[cap], %[cap], 1\n"
      "s_cbranch_scc1 .Lexit%=\n"
      "s_waitcnt vmcnt(0)\n"
      "s_branch .Lkx%=\n"
      ".Lkxok%=:\n"
      "buffer_load_dwordx4 v[192:195], v242, %[ws], %[kxo] offen sc1\n"
      "buffer_load_dwordx4 v[208:211], v242, %[ws], %[kxo] offen offset:64 sc1\n"
      "buffer_load_dwordx4 v[196:199], v242, %[ws], %[kxo] offen offset:2048 sc1\n"
      "buffer_load_dwordx4 v[212:215], v242, %[ws], %[kxo] offen offset:2112 sc1\n"
      "buffer_load_dwordx4 v[200:203], v243, %[ws], %[kxo] offen sc1\n"
      "buffer_load_dwordx4 v[216:219], v243, %[ws], %[kxo] offen offset:64 sc1\n"
      "buffer_load_dwordx4 v[204:207], v243, %[ws], %[kxo] offen offset:2048 sc1\n"
      "buffer_load_dwordx4 v[220:223], v243, %[ws], %[kxo] offen offset:2112 sc1\n"
      "s_add_u32 %[kxo], %[kxo], 0x40000\n"
      "s_branch .Lnext%=\n"
      ".Lidle%=:\n"
      A_MARK WV(0) A_PLAN("1", G_S1, G_XS, A_KEEP)
      "s_branch .Lnext%=\n"
      ".Lother%=:\n"
      "s_cmp_lg_u32 %[w], 6\n"
      "s_cbranch_scc1 .Lidle%=\n"
      "ds_read_b128 v[152:155], v245\n"
      "ds_read_b128 v[156:159], v245 offset:1024\n"
      "s_add_u32 s91, %[s], 1\n"
      "v_mov_b32 v160, s91\n"
      WL(1) "buffer_store_dwordx4 v[152:155], v246, %[ws], %[kmo] offen sc1\n"
      WL(0) "buffer_store_dwordx4 v[156:159], v246, %[ws], %[kmo] offen offset:1024 sc1\n"
      "s_add_u32 %[kmo], %[kmo], 0x10000\n"
      A_MARK
      SLEEP(D_KM)
      WV(0)
      A_PLAN("6", G_S6, G_XS, A_KEEP)
      "s_mov_b64 exec, 1\n"
      "buffer_store_dword v160, v247, %[ws], 0 offen sc1\n"
      "s_not_b64 exec, s[80:81]\n"
      "buffer_store_dwordx4 v[192:195], v240, %[ws], %[po] offen sc1\n"
      "s_mov_b32 exec_lo, 0xff\n"
      "s_mov_b32 exec_hi, 0xff\n"
      "buffer_store_dwordx2 v[192:193], v241, %[ws], %[po] offen sc1\n"
      "s_mov_b64 exec, -1\n"
      ".Lnext%=:\n"
      A_NEXT_P("%[po]")
      "s_add_u32 %[mem], %[mem], 0x4000\n"
      "s_add_u32 %[s], %[s], 1\n"
      A_WAIT("")
      "s_cmp_lt_u32 %[s], 64\n"
      "s_cbranch_scc1 .Lpoll%=\n"
      ".Lexit%=:\n"
      : [s] "+s"(s), [mem] "+s"(mem), [cap] "+s"(cap), [kxo] "+s"(kxo), [kmo] "+s"(kmo), A_PACE_OUT, [po] "+s"(po),
        "+{v[148:151]}"(a0),
        "=&{v[152:183]}"(t0), "=&{v[184:191]}"(t1), "+{v[192:223]}"(kx), "=&{v[224:239]}"(t2), "=&{v248}"(seen)
      : [ws] "s"(srd(p.ws, WS_BYTES)), [w] "s"(w), A_PACE_IN, "{v[0:31]}"(cat(wa[0], wb[0], wa[1], wb[1])),
        "{v[32:63]}"(cat(wa[2], wb[2], wa[3], wb[3])), "{v[64:95]}"(cat(wa[4], wb[4], wa[5], wb[5])),
        "{v[96:127]}"(cat(wa[6], wb[6], wa[7], wb[7])), "{v[128:143]}"(k0), "{v[240:247]}"(k1)
      : A_SCLOB);
}

DEV void read_wo(char* sm, unsigned w, unsigned l, u4 (&wo)[4][4]) {
#pragma unroll
  for (int i = 0; i < 4; ++i) {
    const unsigned fa = frag_base(1024u, 4u * w + i, l);
#pragma unroll
    for (int f = 0; f < 4; ++f) wo[i][f] = frag(sm, fa + f * 32768u, 1024u);
  }
}

DEV void role_vm(const Params& p, unsigned h, char* sm) {
  constexpr unsigned L_MEM = 131072, L_V = L_MEM + 8192;
  const unsigned l = threadIdx.x & 63, w = rfl(threadIdx.x >> 6), c = l & 15, g = l >> 4;
  const rsrc_t ws = mk(p.ws, WS_BYTES);
  bool alive = true;

  u8 wv[8], wo[4][2];
  u4 a0;
  unsigned stage;
  {
    Hs hs = hs_begin(ws);
    u4 d0[16], d1[16], t[4][4];
    stage_slice(mk(p.wv, 4u << 20), h, 64u * w, l, d0);
    fence();
    hs_post(ws, hs, sm);
    fence();
    stage_wo(mk(p.wo, 4u << 20), h, w, l, d1);
    a0 = load_mem0(p, w, c, g);
    put_image(sm, w, l, d0);
    bar();
    const unsigned fa = frag_base(256u, w, l);
#pragma unroll
    for (int i = 0; i < 8; ++i) {
      const unsigned o = fa + (w ^ i) * 16384u;
      wv[i] = cat(frag(sm, o, 256u), frag(sm, o + 8192u, 256u));
    }
    bar();
    const unsigned echo = hs_echo(ws);
    put_image(sm, w, l, d1);
    bar();
    read_wo(sm, w, l, t);
    stage = hs_wait(ws, sm, echo, 2u, alive);
    hs_ack(ws, hs, 1u);
#pragma unroll
    for (int i = 0; i < 4; ++i) {
      wo[i][0] = cat(t[i][0], t[i][1]);
      wo[i][1] = cat(t[i][2], t[i][3]);
    }
  }

  const unsigned lb = lds_base(sm);
  u16v k0;
  k0[0] = (c & 1) ? 0xeeeeeeeeu : 0x44444444u;
  k0[1] = w * 1024u + l * 16u;
#pragma unroll
  for (int i = 0; i < 8; ++i) k0[2 + i] = lb + L_MEM + (w ^ i) * 1024u + l * 16u;
  k0[10] = lb + L_V + (w * 16u + 4u * g + ((c >> 1) & 1)) * 16u + (2u * (c >> 2) + (c & 1)) * 2u;
  k0[11] = lb + L_V + l * 16u;
  const unsigned cb = crit_base(p);
  k0[12] = cb + (4u * w + g) * 16384u + h * 256u + c * 16u;
  k0[13] = 0;
  k0[14] = threadIdx.x == 448 ? F_M + h * FL : OOB;
  k0[15] = 0;

  unsigned s = 0, mem = rfl(cb + slot_at(0, stage) - 16384u), cap = rfl(alive ? SPIN_CAP : 0u);
  unsigned mo = slot_at(W_M, stage), mi = slot_at(W_M + 2u * M_SLOT, stage);
  A_PACE_DECL(G_OM);
  u32v t0;
  u16v t1, t2;
#define VM_ZERO(a) "v_mov_b32 v" #a ", 0\n"
#define VM_ZERO_M \
      VM_ZERO(192) VM_ZERO(193) VM_ZERO(194) VM_ZERO(195) VM_ZERO(196) VM_ZERO(197) VM_ZERO(198) VM_ZERO(199) \
      VM_ZERO(200) VM_ZERO(201) VM_ZERO(202) VM_ZERO(203) VM_ZERO(204) VM_ZERO(205) VM_ZERO(206) VM_ZERO(207)
  asm volatile(
      "v_mov_b32 v208, -1\nv_mov_b32 v209, -1\nv_mov_b32 v210, -1\nv_mov_b32 v211, -1\n"
      VM_ZERO(180) VM_ZERO(184) VM_ZERO(185) VM_ZERO(186) VM_ZERO(187) VM_ZERO_M
      "ds_write_b128 v130, v[148:151]\n"
      "s_branch .Lhave%=\n"
      A_POLL_MEM
      ".Lhave%=:\n"
      SMF("184:187", "148:151", "0:7", "v128")
      A_READ_MEM
      WL(6) SMF("184:187", "152:155", "8:15", "v128")
      WL(5) SMF("184:187", "156:159", "16:23", "v128")
      WL(4) SMF("184:187", "160:163", "24:31", "v128")
      WL(3) SMF("184:187", "164:167", "32:39", "v128")
      WL(2) SMF("184:187", "168:171", "40:47", "v128")
      WL(1) SMF("184:187", "172:175", "48:55", "v128")
      WL(0) SMF("184:187", "176:179", "56:63", "v128")
      "s_nop 7\n"
      "v_add_f32 v152, v184, v185\n"
      "v_add_f32 v153, v186, v187\n"
      "v_cvt_pk_bf16_f32 v152, v152, v153\n"
      "ds_write_b16 v138, v152\n"
      "ds_write_b16_d16_hi v138, v152 offset:32\n"
      VM_ZERO(184) VM_ZERO(185) VM_ZERO(186) VM_ZERO(187)
      "s_waitcnt lgkmcnt(0)\n"
      "s_barrier\n"
      "ds_read_b128 v[152:155], v139\n"
      "ds_read_b128 v[156:159], v139 offset:1024\n"
      WL(1) SMF("192:195", "152:155", "64:71", "v128") SMF("200:203", "152:155", "96:103", "v128")
      WL(0) SMF("192:195", "156:159", "72:79", "v128") SMF("200:203", "156:159", "104:111", "v128")
      SMF("196:199", "152:155", "80:87", "v128") SMF("204:207", "152:155", "112:119", "v128")
      SMF("196:199", "156:159", "88:95", "v128") SMF("204:207", "156:159", "120:127", "v128")
      "s_nop 2\n"
      "v_add_f32 v192, v192, v193\n"
      "v_add_f32 v200, v200, v201\n"
      "v_add_f32 v194, v194, v195\n"
      "v_add_f32 v202, v202, v203\n"
      "v_cvt_pk_bf16_f32 v160, v192, v194\n"
      "v_cvt_pk_bf16_f32 v162, v200, v202\n"
      "v_add_f32 v196, v196, v197\n"
      "v_add_f32 v204, v204, v205\n"
      "v_add_f32 v198, v198, v199\n"
      "v_add_f32 v206, v206, v207\n"
      "v_cvt_pk_bf16_f32 v161, v196, v198\n"
      "v_cvt_pk_bf16_f32 v163, v204, v206\n"
      "v_permlane32_swap_b32 v160, v162\n"
      "s_add_u32 %[s], %[s], 1\n"
      "v_permlane32_swap_b32 v161, v163\n"
      "s_nop 1\n"
      "v_permlane16_swap_b32 v160, v161\n"
      "v_permlane16_swap_b32 v162, v163\n"
      "buffer_store_dwordx4 v[160:163], v140, %[ws], %[mo] offen sc1\n"
      VM_ZERO_M
      A_MARK
      SLEEP(D_M2)
      WV(0)
      A_PLAN("", G_VM, G_XM, A_KEEP)
      "buffer_store_dwordx4 v[208:211], v140, %[ws], %[mi] offen sc1\n"
      "buffer_store_dword v180, v142, %[ws], 0 offen sc1\n"
      "v_mov_b32 v180, %[s]\n"
      A_NEXT_M("%[mo]") A_NEXT_M("%[mi]")
      "s_add_u32 %[mem], %[mem], 0x4000\n"
      A_WAIT("")
      "s_cmp_lt_u32 %[s], 64\n"
      "s_cbranch_scc1 .Lpoll%=\n"
      ".Lexit%=:\n"
      : [s] "+s"(s), [mem] "+s"(mem), [cap] "+s"(cap), [mo] "+s"(mo), [mi] "+s"(mi), A_PACE_OUT, "+{v[148:151]}"(a0), "=&{v[152:183]}"(t0),
        "=&{v[184:199]}"(t1), "=&{v[200:215]}"(t2)
      : [ws] "s"(srd(p.ws, WS_BYTES)), [w] "s"(w), A_PACE_IN, "{v[0:31]}"(cat(wv[0], wv[1], wv[2], wv[3])),
        "{v[32:63]}"(cat(wv[4], wv[5], wv[6], wv[7])), "{v[64:95]}"(cat(wo[0][0], wo[0][1], wo[1][0], wo[1][1])),
        "{v[96:127]}"(cat(wo[2][0], wo[2][1], wo[3][0], wo[3][1])), "{v[128:143]}"(k0)
      : A_SCLOB);
}

constexpr int PROBES = 6;
constexpr unsigned PROBER = 32, PROBER_BLOCK = 2;
DEV void lead(rsrc_t ws) {
  const unsigned l = threadIdx.x & 63;
  unsigned i, a0[4], last[4];
  const bool init = role_of(l, i) == R_V1;
#pragma unroll
  for (int k = 0; k < 4; ++k) {
    a0[k] = ld4c(ws, H_X + (k * 64u + l) * FL + 4u, 0);
  }
#pragma unroll
  for (unsigned k = 0; k < N_FLAGS / 64; ++k) st4c(0u, ws, (k * 64u + l) * FL, 0);
  drain();
  for (unsigned it = 0; it < SPIN_CAP; ++it) {
    fence();
    u2 v[4];
#pragma unroll
    for (int k = 0; k < 4; ++k) v[k] = ld8c(ws, H_X + (k * 64u + l) * FL, 0);
    const bool done = v[0][1] != a0[0] && v[1][1] != a0[1] && v[2][1] != a0[2] && v[3][1] != a0[3];
    const unsigned half = __builtin_amdgcn_readlane(v[0][1] - a0[0], PROBER_BLOCK);
    const unsigned stage = all_lanes(done || !init) ? (half == 2u ? 3u : 2u) : 1u;
#pragma unroll
    for (int k = 0; k < 4; ++k) {
      const unsigned e = (v[k][0] & ~3u) | stage;
      st4c(e, ws, it == 0 || e != last[k] ? H_Y + (k * 64u + l) * FL : OOB, 0);
      last[k] = e;
    }
    if (all_lanes(done)) break;
  }
}
DEV void role_v1(const Params& p, unsigned j, char* sm) {
  const unsigned l = threadIdx.x & 63, w = rfl(threadIdx.x >> 6), c = l & 15, g = l >> 4;
  const unsigned cg = j >> 1, rg = j & 1;
  const rsrc_t ws = mk(p.ws, WS_BYTES);
  bool alive = true;
  if (j == 0 && w == 7) {
    lead(ws);
    return;
  }
  const unsigned cb0 = crit_base(p);
  const u4 ones = {~0u, ~0u, ~0u, ~0u};
  Hs hs = hs_begin(ws);
  if (j) {
    constexpr unsigned E0 = 63u << 14, E1 = E0 + 2u * P_SLOT, E2 = E1 + 2u * M_SLOT;
    for (unsigned o = ((j - 1u) * 512u + threadIdx.x) * 16u; o < E2; o += 63u * 512u * 16u)
      st16c(ones, ws, o + (o < E0 ? 0u : o < E1 ? W_P - E0 : W_M - E1), cb0);
    constexpr unsigned N = (NSEG - FAST) * 65536u;
    for (unsigned o = ((j - 1u) * 512u + threadIdx.x) * 16u; o < 2u * N + N / 4u; o += 63u * 512u * 16u)
      st16c(ones, ws, o + (o < N ? O_KM + FAST * 65536u : o < 2u * N ? O_PX + FAST * 65536u - N : O_PM + FAST * 16384u - 2u * N), 0);
  }
  hs_post(ws, hs, sm);
  drain();
  unsigned half = 0;
  if (j == PROBER && w == 0) {
    unsigned best[2] = {~0u, ~0u};
    for (int k = 0; k < PROBES; ++k) {
      const unsigned off = W_PROBE + (k & 1) * 8192u + (l & 7) * 16u + (k >> 1) * 1024u;
      const unsigned long long t0 = __builtin_amdgcn_s_memtime();
      asm volatile("s_waitcnt lgkmcnt(0)" ::: "memory");
      st16c(ones, ws, off, cb0);
      drain();
      const unsigned long long t1 = __builtin_amdgcn_s_memtime();
      asm volatile("s_waitcnt lgkmcnt(0)" ::: "memory");
      const unsigned dt = (unsigned)(t1 - t0);
      if (k & 1) best[1] = min(best[1], dt); else best[0] = min(best[0], dt);
    }
    half = best[1] < best[0];
  }
  if (j) bar();
  if (w >= 4) return;
  if (w == 0) {
    hs_wait(ws, sm, hs_echo(ws), 1u, alive);
    hs_ack(ws, hs, 1u + half);
  }
  if (j == 0) bar();
  const unsigned stage = hs_wait(ws, sm, hs_echo(ws), 2u, alive);

  const unsigned lb = lds_base(sm);
  const unsigned piece = rg * 4u + w;
  u16v k0 = {};
  k0[0] = (c & 4) ? 0xeeeeeeeeu : 0x44444444u;
  k0[1] = cb0 + (piece >> 1) * 16384u + (piece & 1) * 4096u + g * 128u + (c & 7) * 16u + 2048u;
  k0[2] = k0[1] - 2048u + (c >> 3) * 512u;
  k0[3] = cb0 + cg * 16384u + w * 2048u + l * 16u;
  k0[4] = cb0 + (cg * 4u + w) * 16384u + l * 16u;
  k0[5] = k0[4] + 4096u;
  k0[6] = F_U + ((8u * w + (l & 7)) * 8u + (cg >> 2)) * FL;
  k0[7] = lb + ((g * 2u + ((c >> 1) & 1)) * 8u + 2u * (c >> 2) + (c & 1)) * 16u + w * 4u;
  k0[8] = lb + l * 16u;
  k0[9] = cg * 256u + rg * 128u + (l >> 3) * 16u;

  unsigned s = 0, mem = rfl(cb0 + slot_at(0, stage)), cap = rfl(alive ? SPIN_CAP : 0u), seen = 0;
  unsigned uo = slot_at(W_U, stage) ^ UH;
  unsigned po = slot_at(W_P, stage), mo = slot_at(W_M, stage);
  A_PACE_DECL(0);
  u32v t0, t1;
#define V1_MAX(d, a, b, c) "v_max3_u32 v" d ", v" a ", v" b ", v" c "\n"
#define V1_ZERO "v_mov_b32 v52, 0\nv_mov_b32 v53, 0\nv_mov_b32 v54, 0\nv_mov_b32 v55, 0\n" \
                "v_mov_b32 v56, 0\nv_mov_b32 v57, 0\nv_mov_b32 v58, 0\nv_mov_b32 v59, 0\n"
#define V1_LOAD_M \
      "buffer_load_dwordx4 v[32:35], v67, %[ws], %[mo] offen sc1\n" \
      "buffer_load_dwordx4 v[36:39], v67, %[ws], %[mo] offen offset:1024 sc1\n"
#define V1_LOAD_P \
      "buffer_load_dwordx4 v[40:43], v66, %[ws], %[po] offen sc1\n" \
      "buffer_load_dwordx4 v[44:47], v66, %[ws], %[po] offen offset:1024 sc1\n" \
      "buffer_load_dwordx4 v[48:51], v65, %[ws], %[po] offen sc1\n"
#define V1_STEP(P) \
      ".Ltop" P "%=:\n" \
      A_MARK \
      "v_cmp_ge_u32 vcc, %[s], v80\n" \
      "s_cbranch_vccnz .Luw" P "%=\n" \
      SLEEP(D_U) \
      ".Lu" P "%=:\n" \
      "buffer_load_dwordx4 v[0:3], v68, %[ws], %[uo] offen sc1\n" \
      "buffer_load_dwordx4 v[4:7], v68, %[ws], %[uo] offen offset:1024 sc1\n" \
      "buffer_load_dwordx4 v[8:11], v68, %[ws], %[uo] offen offset:2048 sc1\n" \
      "buffer_load_dwordx4 v[12:15], v68, %[ws], %[uo] offen offset:3072 sc1\n" \
      "buffer_load_dwordx4 v[16:19], v69, %[ws], %[uo] offen sc1\n" \
      "buffer_load_dwordx4 v[20:23], v69, %[ws], %[uo] offen offset:1024 sc1\n" \
      "buffer_load_dwordx4 v[24:27], v69, %[ws], %[uo] offen offset:2048 sc1\n" \
      "buffer_load_dwordx4 v[28:31], v69, %[ws], %[uo] offen offset:3072 sc1\n" \
      "buffer_load_dword v80, v70, %[ws], 0 offen sc1\n" \
      "s_add_u32 %[uo], %[uo], 0x200000\n" \
      "s_xor_b32 %[uo], %[uo], 0x2000\n" \
      V1_ZERO \
      WV(9) \
      A_PLAN(P, G_V1, G_XV, A_POST) \
      A_WAIT(P) \
      V1_LOAD_M \
      SLEEP(D_P) \
      ".Lpm" P "%=:\n" \
      V1_LOAD_P \
      WV(3) \
      V1_MAX("60", "32", "33", "34") V1_MAX("61", "35", "36", "37") V1_MAX("60", "60", "38", "39") \
      V1_MAX("60", "60", "61", "61") \
      "v_cmp_eq_u32 s[80:81], -1, v60\n" \
      ".Lp" P "%=:\n" \
      WV(2) SMF("52:55", "40:43", "0:7", "v64") SMF("56:59", "40:43", "8:15", "v64") \
      V1_MAX("60", "40", "41", "42") \
      WV(1) SMF("52:55", "44:47", "16:23", "v64") SMF("56:59", "44:47", "24:31", "v64") \
      V1_MAX("60", "60", "43", "44") V1_MAX("61", "45", "46", "47") \
      WV(0) SMF("52:55", "48:51", "32:39", "v64") \
      V1_MAX("60", "60", "48", "49") V1_MAX("61", "61", "50", "51") V1_MAX("60", "60", "61", "61") \
      "v_cmp_eq_u32 vcc, -1, v60\n" \
      "s_or_b64 vcc, vcc, s[80:81]\n" \
      "s_cbranch_scc1 .Lre" P "%=\n" \
      "s_nop 1\n" \
      "v_permlane32_swap_b32 v56, v54\n" \
      "v_permlane32_swap_b32 v57, v55\n" \
      "v_add_f32 v60, v52, v54\n" \
      "v_add_f32 v62, v58, v56\n" \
      "v_add_f32 v61, v53, v55\n" \
      "v_add_f32 v63, v59, v57\n" \
      "v_cndmask_b32 v60, v60, v62, s[84:85]\n" \
      "v_cndmask_b32 v61, v61, v63, s[84:85]\n" \
      "s_cmp_lg_u32 %[w], 0\n" \
      "s_nop 0\n" \
      "v_permlane16_swap_b32 v60, v61\n" \
      "v_add_f32 v60, v60, v61\n" \
      "ds_write_b32 v71, v60 offset:" P "*1024\n" \
      "s_waitcnt lgkmcnt(0)\n" \
      "s_barrier\n" \
      "s_cbranch_scc1 .Lend" P "%=\n" \
      "ds_read_b128 v[52:55], v72 offset:" P "*1024\n" \
      "s_waitcnt lgkmcnt(0)\n" \
      "v_add_f32 v52, v52, v53\n" \
      "v_add_f32 v54, v54, v55\n" \
      "v_add_f32 v52, v52, v54\n" \
      "s_nop 1\n" \
      "v_mov_b32_dpp v53, v52 quad_perm:[1,0,3,2]" DPP_ALL \
      "v_cvt_pk_bf16_f32 v56, v52, v53\n" \
      "s_nop 1\n" \
      "v_mov_b32_dpp v57, v56 row_shl:2" DPP_ALL \
      "v_mov_b32_dpp v58, v56 row_shl:4" DPP_ALL \
      "v_mov_b32_dpp v59, v56 row_shl:6" DPP_ALL \
      "s_mov_b64 exec, s[92:93]\n" \
      "buffer_store_dwordx4 v[56:59], v73, %[ws], %[mem] offen sc1\n" \
      "s_mov_b64 exec, -1\n" \
      ".Lend" P "%=:\n" \
      "s_barrier\n" \
      A_NEXT_P("%[po]") A_NEXT_M("%[mo]") \
      "s_add_u32 %[mem], %[mem], 0x4000\n" \
      "s_add_u32 %[s], %[s], 1\n"
#define V1_SIDE(P) \
      ".Luw" P "%=:\n" \
      "buffer_load_dword v80, v70, %[ws], 0 offen sc1\n" \
      "s_sub_u32 %[cap], %[cap], 1\n" \
      "s_cbranch_scc1 .Lexit%=\n" \
      "s_waitcnt vmcnt(0)\n" \
      "v_cmp_ge_u32 vcc, %[s], v80\n" \
      "s_cbranch_vccnz .Luw" P "%=\n" \
      "s_branch .Lu" P "%=\n" \
      ".Lre" P "%=:\n" \
      "s_sub_u32 %[cap], %[cap], 1\n" \
      "s_cbranch_scc1 .Lexit%=\n" \
      "s_cmp_lg_u64 s[80:81], 0\n" \
      V1_ZERO \
      "s_cbranch_scc1 .Lboth" P "%=\n" \
      V1_LOAD_P \
      "s_branch .Lp" P "%=\n" \
      ".Lboth" P "%=:\n" \
      V1_LOAD_M \
      "s_branch .Lpm" P "%=\n"
  asm volatile(
      "s_mov_b32 s84, 0\n"
      "s_mov_b32 s85, -1\n"
      "s_mov_b32 s92, 0x01010101\n"
      "s_mov_b32 s93, 0x01010101\n"
      V1_STEP("0")
      "s_cmp_ge_u32 %[s], 63\n"
      "s_cbranch_scc1 .Lexit%=\n"
      V1_STEP("1")
      "s_branch .Ltop0%=\n"
      V1_SIDE("0") V1_SIDE("1")
      ".Lexit%=:\n"
      : [s] "+s"(s), [mem] "+s"(mem), [cap] "+s"(cap), [uo] "+s"(uo), [po] "+s"(po), [mo] "+s"(mo), A_PACE_OUT, "=&{v[0:31]}"(t0), "=&{v[32:63]}"(t1),
        "+{v80}"(seen)
      : [ws] "s"(srd(p.ws, WS_BYTES)), [w] "s"(w), A_PACE_IN, "{v[64:79]}"(k0)
      : "s92", "s93", A_SCLOB);
}

DEV void load_x(rsrc_t xr, unsigned w, unsigned c, unsigned g, int s, u4 (&xp)[4]) {
  const unsigned voff = ((16u * (w >> 2) + c) * 512u + 128u * (w & 3) + 8u * g) * 2u;
#pragma unroll
  for (int i = 0; i < 4; ++i) xp[i] = ld16(xr, voff + i * 64u, s * 32768u);
}
DEV void swap_x(rsrc_t xr, char* sm, unsigned base, unsigned w, unsigned l, int i, int s, u4 (&xp)[4]) {
  const unsigned voff = ((16u * (w >> 2) + (l & 15)) * 512u + 128u * (w & 3) + 8u * (l >> 4)) * 2u;
  lds16(sm, base + (4u * w + i) * 1024u + l * 16u) = xp[i];
  xp[i] = ld16(xr, voff + i * 64u, min(s, LAST) * 32768u);
}
DEV void fetch_x(u4 xr, unsigned base, unsigned w, unsigned c, unsigned g, int s) {
  const unsigned voff = ((16u * (w >> 2) + c) * 512u + 128u * (w & 3) + 8u * g) * 2u;
#define FETCH_X(i) \
  asm volatile("s_mov_b32 m0, %0\n\ts_nop 0\n\tbuffer_load_dwordx4 %1, %2, %3 offen offset:%4 lds" \
               : \
               : "s"(base + 4096u * w + 960u * i), "v"(voff), "s"(xr), "s"(s * 32768u), "n"(64 * i) \
               : "memory")
  FETCH_X(0);
  FETCH_X(1);
  FETCH_X(2);
  FETCH_X(3);
}
DEV void put_x(char* sm, unsigned base, unsigned w, unsigned l, const u4 (&xp)[4]) {
#pragma unroll
  for (int i = 0; i < 4; ++i) lds16(sm, base + (4u * w + i) * 1024u + l * 16u) = xp[i];
}

DEV void token_softmax(rsrc_t ws, char* sm, unsigned qb, const u4 (&km)[4], unsigned h, unsigned w, unsigned l, int s) {
  const unsigned c = l & 15, g = l >> 4;
  u4 q[4], k0[4], k1[4];
#pragma unroll
  for (int m = 0; m < 4; ++m) {
    q[m] = lds16(sm, qb + (w * 4u + m) * 1024u);
    k0[m] = lds16(sm, qb + 8192u + m * 1024u);
    k1[m] = lds16(sm, qb + 12288u + m * 1024u);
  }
  sched();
  f4 s0 = {0, 0, 0, 0}, s1 = {0, 0, 0, 0}, sm_ = {0, 0, 0, 0};
#pragma unroll
  for (int m = 0; m < 4; ++m) {
    s0 = mma(k0[m], q[m], s0);
    s1 = mma(k1[m], q[m], s1);
  }
#pragma unroll
  for (int m = 0; m < 4; ++m) sm_ = mma(km[m], q[m], sm_);
  const unsigned tq = 16u * w + c;
  const float ninf = -__builtin_inff();
#pragma unroll
  for (int r = 0; r < 4; ++r) {
    s0[r] = 4u * g + r <= tq ? s0[r] : ninf;
    s1[r] = 16u + 4u * g + r <= tq ? s1[r] : ninf;
  }
  float mx = fmaxf(fmaxf(fmaxf(s0[0], s0[1]), fmaxf(s0[2], s0[3])), fmaxf(fmaxf(s1[0], s1[1]), fmaxf(s1[2], s1[3])));
  mx = group_max(fmaxf(mx, fmaxf(sm_[0], sm_[1])));
  const float mc = -mx * SCALE_LOG2E;
  float e[10];
#pragma unroll
  for (int r = 0; r < 4; ++r) {
    e[r] = __builtin_amdgcn_exp2f(fmaf(s0[r], SCALE_LOG2E, mc));
    e[4 + r] = __builtin_amdgcn_exp2f(fmaf(s1[r], SCALE_LOG2E, mc));
  }
  e[8] = __builtin_amdgcn_exp2f(fmaf(sm_[0], SCALE_LOG2E, mc));
  e[9] = __builtin_amdgcn_exp2f(fmaf(sm_[1], SCALE_LOG2E, mc));
  const float sum = group_sum(((e[0] + e[1]) + (e[2] + e[3])) + ((e[4] + e[5]) + (e[6] + e[7])) + (e[8] + e[9]));
  const float rs = __builtin_amdgcn_rcpf(sum);
  const u4 px = {pk(e[0] * rs, e[1] * rs), pk(e[2] * rs, e[3] * rs), pk(e[4] * rs, e[5] * rs), pk(e[6] * rs, e[7] * rs)};
  st16c(px, ws, O_PX + (w * 32u + h) * 1024u + l * 16u, s * 65536u);
  const unsigned dm = pk(e[8] * rs, e[9] * rs);
  auto r16 = __builtin_amdgcn_permlane16_swap(dm, dm, false, false);
  auto ra = __builtin_amdgcn_permlane32_swap(r16[0], r16[0], false, false);
  auto rb = __builtin_amdgcn_permlane32_swap(r16[1], r16[1], false, false);
  const u4 pm = {ra[0], rb[0], ra[1], rb[1]};
  st16c(pm, ws, g == 0 ? O_PM + (w * 8u + (h >> 2)) * 1024u + ((h & 3) * 16u + c) * 16u : OOB, s * 16384u);
}

DEV void role_s2(const Params& p, unsigned h, char* sm) {
  constexpr int LAG = 4, R = LAG + 1, PF = 2;
  constexpr int S2_MID = 5, S2_SW = 5, S2_X0 = 8, S2_XD = 2, S2_KM = 5;
  constexpr unsigned L_X = 0, L_QK = 65536;
  const unsigned l = threadIdx.x & 63, w = rfl(threadIdx.x >> 6), c = l & 15, g = l >> 4;
  const rsrc_t ws = mk(p.ws, WS_BYTES), xr = mk(p.bx, 2u << 20);
  bool alive = true;

  u4 xp[4], wa[16], wb[16];
  {
    const rsrc_t wr = mk(w < 4 ? p.wq : p.wk, 4u << 20);
    Hs hs = hs_begin(ws);
    u4 d0[16], d1[16];
    stage_slice(wr, h, 64u * (w & 3), l, d0);
    fence();
    hs_post(ws, hs, sm);
    fence();
    stage_slice(wr, h, 64u * (w & 3) + 256u, l, d1);
    load_x(xr, w, c, g, 0, xp);
    unsigned echo;
    const unsigned fa = (w >> 2) * 65536u + frag_base(256u, 2u * (w & 3), l), fb = fa ^ 32u;
#pragma unroll
    for (int k = 0; k < 2; ++k) {
      put_image(sm, w, l, k ? d1 : d0);
      bar();
#pragma unroll
      for (int i = 0; i < 8; ++i) {
        wa[8 * k + i] = frag(sm, fa + i * 8192u, 256u);
        wb[8 * k + i] = frag(sm, fb + i * 8192u, 256u);
      }
      if (k == 0) echo = hs_echo(ws);
      else hs_wait(ws, sm, echo, 1u, alive);
      bar();
    }
    hs_ack(ws, hs, 1u);
  }
  put_x(sm, L_X, w, l, xp);
  load_x(xr, w, c, g, 1, xp);
  bar();
  if (w >= 4) __builtin_amdgcn_s_setprio(1);

  const unsigned km_all = O_KM + h * 2048u + (g * 8u + ((2u * (c >> 2) + (c & 1)) ^ swz(g))) * 16u;
  const unsigned km_off = w < 2 ? km_all : OOB;
  const unsigned km_flag = w < 2 ? F_KM + h * FL : OOB;
  const unsigned px_flag = w < 2 && l == 0 ? F_PX + (w * 32u + h) * FL : OOB;
  const unsigned kx_flag = w >= 4 && l == 0 ? F_KX + (h * 4u + (w & 3)) * FL : OOB;
  const unsigned kx_off = w >= 4 ? O_KX + h * 8192u + (w & 3) * 2048u + l * 16u : OOB;
  unsigned km_seen = 0;
  int slot = 0;
#pragma unroll
  for (int i = 0; i < 3; ++i) st4c(0u, ws, OOB, 4u * i);
  for (int it = 0; it < NSEG; ++it) {
    const int s = max(it - LAG, 0);
    const bool soft = it >= LAG && w < 2;
    if (it == S2_SW && w < 4) __builtin_amdgcn_s_setprio(2);
    u4 km[4];
    f4 acc[2][2] = {};
    const unsigned xb = L_X + (it & 1) * 32768u + l * 16u;
    const unsigned xn = L_X + (~it & 1) * 32768u;
    u4 x[PF + 1][2];
#pragma unroll
    for (int k = 0; k < PF; ++k)
#pragma unroll
      for (int t = 0; t < 2; ++t) x[k][t] = lds16(sm, xb + (t * 16 + k) * 1024u);
#pragma unroll
    for (int ks = 0; ks < 16; ++ks) {
      sched();
      if (ks + PF < 16) {
#pragma unroll
        for (int t = 0; t < 2; ++t) x[(ks + PF) % (PF + 1)][t] = lds16(sm, xb + (t * 16 + ks + PF) * 1024u);
      }
      if (ks == S2_KM) {
        if (soft && !all_lanes(km_seen > (unsigned)s)) wait_flags(ws, km_flag, s + 1, alive);
#pragma unroll
        for (int m = 0; m < 4; ++m) km[m] = ld16c(ws, km_off + m * 512u, s * 65536u);
        km_seen = ld4c(ws, km_flag, 0);
      }
#pragma unroll
      for (int i = 0; i < 4; ++i)
        if (ks == S2_X0 + i * S2_XD) swap_x(xr, sm, xn, w, l, i, it + 2, xp);
      if (ks == S2_MID) {
        if (w >= 4) asm volatile("s_waitcnt vmcnt(%0)" ::"n"(S2_KM <= S2_MID ? 5 : 0) : "memory");
        st4c(it, ws, kx_flag, 0);
      }
      if (ks == 15) {
        if (w < 4) asm volatile("s_waitcnt vmcnt(10)" ::: "memory");
        st4c(s, ws, px_flag, 0);
      }
      sched();
#pragma unroll
      for (int t = 0; t < 2; ++t) {
        acc[0][t] = mma(wa[ks], x[ks % (PF + 1)][t], acc[0][t]);
        acc[1][t] = mma(wb[ks], x[ks % (PF + 1)][t], acc[1][t]);
      }
    }
    sched();
#pragma unroll
    for (int t = 0; t < 2; ++t) {
      const u4 o = {pk(acc[0][t][0], acc[1][t][0]), pk(acc[0][t][1], acc[1][t][1]), pk(acc[0][t][2], acc[1][t][2]),
                    pk(acc[0][t][3], acc[1][t][3])};
      lds16(sm, L_QK + slot * 16384u + (w >> 2) * 8192u + (t * 4u + (w & 3)) * 1024u + l * 16u) = o;
      st16c(o, ws, kx_off + t * 1024u, it * 262144u);
    }
    slot = slot == R - 1 ? 0 : slot + 1;
    if (soft) {
      token_softmax(ws, sm, L_QK + slot * 16384u + l * 16u, km, h, w, l, s);
    }
    bar();
  }
  static_assert(LAG == 4 && FAST == NSEG - 3, "one segment per pair of waves");
  const int s = NSEG - LAG + (w >> 1);
  if (w < 2) {
    drain();
    st4c(s, ws, px_flag, 0);
    wait_flags(ws, km_flag, s + 1, alive);
  }
  u4 km[4];
  for (unsigned it = 0; it < SPIN_CAP; ++it) {
    fence();
#pragma unroll
    for (int m = 0; m < 4; ++m) km[m] = ld16c(ws, km_all + m * 512u, s * 65536u);
    if (w < 2 || all_lanes(filled(km[0]) && filled(km[1]) && filled(km[2]) && filled(km[3]))) break;
  }
  token_softmax(ws, sm, L_QK + (s % R) * 16384u + l * 16u, km, h, w & 1, l, s);
  if (w < 2) {
    drain();
    st4c(s + 1, ws, px_flag, 0);
  }
}

DEV void role_vu(const Params& p, unsigned h, char* sm) {
  constexpr int VU_X0 = 0, VU_XD = 2;
  constexpr unsigned L_X = 0, L_V = 65536;
  const unsigned l = threadIdx.x & 63, w = rfl(threadIdx.x >> 6), c = l & 15, g = l >> 4;
  const rsrc_t ws = mk(p.ws, WS_BYTES), xr = mk(p.bx, 2u << 20);

  u4 xp[4], wv[16], wo[4][4];
  unsigned stage = 0;
  Hs hs = hs_begin(ws);
  {
    u4 d0[16], d1[16];
    stage_slice(mk(p.wv, 4u << 20), h, 64u * w, l, d0);
    fence();
    hs_post(ws, hs, sm);
    fence();
    stage_wo(mk(p.wo, 4u << 20), h, w, l, d1);
    load_x(xr, w, c, g, 0, xp);
    put_image(sm, w, l, d0);
    bar();
    const unsigned fa = frag_base(256u, w, l);
#pragma unroll
    for (int f = 0; f < 16; ++f) wv[f] = frag(sm, fa + f * 8192u, 256u);
    bar();
    put_image(sm, w, l, d1);
    bar();
    read_wo(sm, w, l, wo);
    bool alive = true;
    stage = hs_wait(ws, sm, hs_echo(ws), 2u, alive);
    hs_ack(ws, hs, 1u);
    bar();
  }
  put_x(sm, L_X, w, l, xp);
  fetch_x(srd(p.bx, 2u << 20), lds_base(sm) + L_X + 32768u, w, c, g, 1);
  load_x(xr, w, c, g, 2, xp);
  bar();

  const unsigned v_off = L_V + ((w >> 1) * 64u + (2u * (w & 1) + (g >> 1)) * 16u + c) * 16u + (g & 1) * 8u;
  const unsigned u_off = crit_base(p) + (16u * w + (h >> 3)) * 16384u + (h & 7) * 1024u + l * 16u;
  const unsigned flag = l == 0 ? F_U + (h * 8u + w) * FL : OOB;
#pragma unroll
  for (int i = 0; i < 5; ++i) st4c(0u, ws, OOB, 4u * i);
  for (int s = 0; s < NSEG; ++s) {
    f4 acc[2] = {};
    const unsigned xb = L_X + (s & 1) * 32768u + l * 16u;
    u4 x[4][2];
#pragma unroll
    for (int k = 0; k < 3; ++k)
#pragma unroll
      for (int t = 0; t < 2; ++t) x[k][t] = lds16(sm, xb + (t * 16 + k) * 1024u);
#pragma unroll
    for (int ks = 0; ks < 16; ++ks) {
      sched();
      if (ks + 3 < 16) {
#pragma unroll
        for (int t = 0; t < 2; ++t) x[(ks + 3) & 3][t] = lds16(sm, xb + (t * 16 + ks + 3) * 1024u);
      }
      sched();
#pragma unroll
      for (int t = 0; t < 2; ++t) acc[t] = mma(wv[ks], x[ks & 3][t], acc[t]);
    }
    sched();
#pragma unroll
    for (int t = 0; t < 2; ++t)
      *reinterpret_cast<u2*>(sm + v_off + (s & 1) * 8192u + t * 4096u) = u2{pk(acc[t][0], acc[t][1]), pk(acc[t][2], acc[t][3])};
    asm volatile("s_waitcnt vmcnt(9)" ::: "memory");
    bar();
    u4 v[8];
#pragma unroll
    for (int k = 0; k < 8; ++k) v[k] = lds16(sm, L_V + (s & 1) * 8192u + ((k & 1) * 4 + (k >> 1)) * 1024u + l * 16u);
    sched();
    const unsigned xn = L_X + (s & 1) * 32768u;
    f4 u[4][2] = {};
#pragma unroll
    for (int k = 0; k < 8; ++k) {
#pragma unroll
      for (int i = 0; i < 4; ++i)
        if (k == VU_X0 + i * VU_XD) swap_x(xr, sm, xn, w, l, i, s + 3, xp);
      sched();
#pragma unroll
      for (int i = 0; i < 4; ++i) u[i][k & 1] = mma(v[k], wo[i][k >> 1], u[i][k & 1]);
      sched();
    }
    asm volatile("s_waitcnt vmcnt(4)" ::: "memory");
    st4c(s, ws, flag, 0);
#pragma unroll
    for (int i = 0; i < 4; ++i) {
      const u4 o = {pk(u[i][0][0], u[i][0][1]), pk(u[i][0][2], u[i][0][3]), pk(u[i][1][0], u[i][1][1]),
                    pk(u[i][1][2], u[i][1][3])};
      st16c(o, ws, u_off + i * 65536u, slot_at(W_U + s * U_SLOT, stage) ^ UH);
    }
  }
  drain();
  st4c(NSEG, ws, flag, 0);
}

constexpr int SPLIT = 54;
DEV void role_v2(const Params& p, unsigned j, char* sm, bool helper) {
  constexpr unsigned ROW = 144, PART = 16 * ROW, BUF = 8 * PART;
  const unsigned l = threadIdx.x & 63, w = rfl(threadIdx.x >> 6), c = l & 15, g = l >> 4;
  const unsigned b = j >> 1, qt = j & 1;
  const rsrc_t ws = mk(p.ws, WS_BYTES), outr = mk(p.out, 8u << 20);
  bool alive = true;

  const unsigned hq = 4u * w + (l & 3);
  const unsigned uf_off = F_U + (hq * 8u + (b >> 1)) * FL;
  const unsigned pf_off = (l & 4) ? F_M + hq * FL : F_PX + (qt * 32u + hq) * FL;
  const unsigned u_off = crit_base(p) + (8u * b + (w >> 1)) * 16384u + (w & 1) * 4096u + l * 16u;
  const unsigned m_off = crit_base(p) + 2u * b * 16384u + w * 1024u + l * 16u;
  const unsigned px_off = O_PX + (qt * 32u + 4u * w) * 1024u + l * 16u;
  const unsigned pm_off = O_PM + (qt * 8u + w) * 1024u + l * 16u;
  const unsigned out_off = ((16u * qt + (threadIdx.x >> 5)) * 1024u + 512u + 32u * b + (threadIdx.x & 31)) * 4u;
  constexpr int X_AHEAD = 14, X_FIRST = 6;
  const rsrc_t xr = mk(p.bx, 2u << 20);
  auto touch = [&](int s, int n) {
    const unsigned k = threadIdx.x >> 5;
    return ld4(xr, !helper && k < n && s + k < NSEG ? (s + k) * 32768u + (j & 7) * 4096u + (l & 31) * 128u : OOB, 0);
  };
  Hs hs;
  if (!helper) {
    hs = hs_begin(ws);
    hs_post(ws, hs, sm);
  }
  unsigned touched = touch(0, X_FIRST);
  bar();
  const unsigned stage = hs_wait(ws, sm, hs_echo(ws), 2u, alive);
  if (!helper) hs_ack(ws, hs, 1u);
  const unsigned touched1 = touch(X_FIRST, X_AHEAD - X_FIRST);
  const int first = helper ? SPLIT + 1 : 0;
  unsigned u_seen = wait_flags(ws, uf_off, first + 1, alive), p_seen = 0, rb = 0;
  asm volatile("" ::"v"(touched1));
  u4 ua[2][4];
#pragma unroll
  for (int k = 0; k < 8; ++k) ua[k >> 2][k & 3] = ld16c(ws, u_off + (k >> 2) * 65536u + (k & 3) * 1024u, slot_at(W_U + first * U_SLOT, stage) ^ UH);

  for (int s = first, n; s < NSEG; s = n) {
    n = s + (s < SPLIT ? 1 : 2);
    const unsigned need = s < FAST ? s + 1 : (l & 4) ? s - 1 : 0;
    if (!all_lanes(p_seen >= need)) p_seen = wait_flags(ws, pf_off, need, alive);
    const unsigned mo = slot_at(W_M + s * M_SLOT, stage);
    u4 px[4], pm, m0, m1;
    for (unsigned it = 0; it < SPIN_CAP; ++it) {
      fence();
      m0 = ld16c(ws, m_off, mo);
      m1 = ld16c(ws, m_off + 16384u, mo);
      if (s < FAST || all_lanes(filled(m0) && filled(m1))) break;
    }
    for (unsigned it = 0; it < SPIN_CAP; ++it) {
      fence();
#pragma unroll
      for (int i = 0; i < 4; ++i) px[i] = ld16c(ws, px_off + i * 1024u, s * 65536u);
      pm = ld16c(ws, pm_off, s * 16384u);
      if (s < FAST || all_lanes(filled(px[0]) && filled(px[1]) && filled(px[2]) && filled(px[3]) && filled(pm))) break;
    }
    f4 a0 = {0, 0, 0, 0}, a1 = {0, 0, 0, 0};
#pragma unroll
    for (int i = 0; i < 4; ++i) {
      a0 = mma(ua[0][i], px[i], a0);
      a1 = mma(ua[1][i], px[i], a1);
    }
    a0 = mma(m0, pm, a0);
    a1 = mma(m1, pm, a1);
    sched();
    if (n < NSEG) {
      if (!all_lanes(u_seen > (unsigned)n)) u_seen = wait_flags(ws, uf_off, n + 1, alive);
#pragma unroll
      for (int k = 0; k < 8; ++k)
        ua[k >> 2][k & 3] = ld16c(ws, u_off + (k >> 2) * 65536u + (k & 3) * 1024u, slot_at(W_U + n * U_SLOT, stage) ^ UH);
    }
    asm volatile("" ::"v"(touched));
    touched = touch(s + X_AHEAD, 1);
    sched();
    rb ^= BUF;
    *reinterpret_cast<f4*>(sm + rb + w * PART + c * ROW + g * 16u) = a0;
    *reinterpret_cast<f4*>(sm + rb + w * PART + c * ROW + g * 16u + 64u) = a1;
    bar();
    float v = 0.f;
#pragma unroll
    for (int k = 0; k < 8; ++k)
      v += *reinterpret_cast<const float*>(sm + rb + k * PART + (threadIdx.x >> 5) * ROW + (threadIdx.x & 31) * 4u);
    __builtin_amdgcn_raw_buffer_store_b32(__float_as_uint(rbf(v)), outr, out_off, s * 131072u, 0);
  }
}

DEV f2 cmul(f2 a, f2 b) {
  float re, im;
  asm("v_mul_f32 %0, %3, %5\n\tv_mul_f32 %1, %3, %4\n\tv_fma_f32 %0, %2, %4, -%0\n\tv_fma_f32 %1, %2, %5, %1"
      : "=&v"(re), "=&v"(im) : "v"(a[0]), "v"(a[1]), "v"(b[0]), "v"(b[1]));
  return f2{re, im};
}
DEV f2 cfma(f2 a, f2 b, f2 c) {
  float re, im;
  asm("v_fma_f32 %0, -%3, %5, %6\n\tv_fma_f32 %1, %3, %4, %7\n\tv_fma_f32 %0, %2, %4, %0\n\tv_fma_f32 %1, %2, %5, %1"
      : "=&v"(re), "=&v"(im) : "v"(a[0]), "v"(a[1]), "v"(b[0]), "v"(b[1]), "v"(c[0]), "v"(c[1]));
  return f2{re, im};
}
DEV f2 from_group(float re, float im, unsigned c, int k) {
  return f2{__shfl(re, (int)c + 16 * k, 64), __shfl(im, (int)c + 16 * k, 64)};
}

DEV void role_a(const Params& p, unsigned ci, char* sm) {
  constexpr unsigned GS = 272, KS = 4 * GS, TS = 32 * KS + 32, L_TAB = 4 * TS;
  const unsigned tid = threadIdx.x, l = tid & 63, w = rfl(tid >> 6), c = l & 15, g = l >> 4;
  const rsrc_t ws = mk(p.ws, WS_BYTES), xr = mk(p.ax, 2u << 20), outr = mk(p.out, 8u << 20);
  bool alive = true;
  {
    Hs hs = hs_begin(ws);
    hs_post(ws, hs, sm);
    if (w == 0) {
      hs_wait(ws, sm, hs_echo(ws), 1u, alive);
      hs_ack(ws, hs, 1u);
    }
    bar();
  }
  {
    const unsigned voff = ((64u * ci + 16u * (c >> 2) + 4u * (w >> 1) + (c & 3)) * 512u + 256u * (w & 1) + 8u * g) * 2u;
    u4 xp[8];
#pragma unroll
    for (int i = 0; i < 8; ++i) xp[i] = ld16(xr, voff + i * 64u, 0);
#pragma unroll
    for (int i = 0; i < 8; ++i) lds16(sm, (8u * w + i) * 1024u + l * 16u) = xp[i];
  }
  f2 lam64;
  {
    const float lr = bf2f(((const unsigned short*)p.lre)[tid]), li = bf2f(((const unsigned short*)p.lim)[tid]);
    const float dt = rbf(expf(bf2f(((const unsigned short*)p.ldt)[tid])));
    const float er = expf(dt * lr);
    float sn, cs;
    sincosf(dt * li, &sn, &cs);
    f2 lam = {er * cs, er * sn};
    asm volatile("s_nop 1" : "+v"(lam));
    const float nr = lam[0] - 1.0f, ni = lam[1];
    f2 coef;
    if (fabsf(lr) >= fabsf(li)) {
      const float t = li / lr, d = 1.0f / (lr + li * t);
      coef = f2{smul(nr + smul(ni, t), d), smul(ni - smul(nr, t), d)};
    } else {
      const float t = lr / li, d = 1.0f / (lr * t + li);
      coef = f2{smul(smul(nr, t) + ni, d), smul(smul(ni, t) - nr, d)};
    }
    f2 l16 = cmul(lam, lam);
    l16 = cmul(l16, l16);
    l16 = cmul(l16, l16);
    l16 = cmul(l16, l16);
    lam64 = cmul(l16, l16);
    lam64 = cmul(lam64, lam64);
    *reinterpret_cast<f4*>(sm + L_TAB + tid * 32u) = f4{lam[0], lam[1], coef[0], coef[1]};
    *reinterpret_cast<f2*>(sm + L_TAB + tid * 32u + 16u) = l16;
  }
  bar();

  f4 hr[4][4] = {}, hi[4][4] = {};
  {
    const rsrc_t br = mk(p.bre, 1u << 19), bi = mk(p.bim, 1u << 19);
    const unsigned voff = ((64u * w + c) * 512u + 8u * g) * 2u;
    u4 fr[4], fi[4];
#pragma unroll
    for (int k = 0; k < 4; ++k) {
      fr[k] = ld16(br, voff + k * 64u, 0);
      fi[k] = ld16(bi, voff + k * 64u, 0);
    }
#pragma unroll
    for (int n = 0; n < 64; ++n) {
      const int ct = n >> 4, ks = n & 15;
      const u4 r = fr[n & 3], i = fi[n & 3];
      fence();
      if (n + 4 < 64) {
        fr[n & 3] = ld16(br, voff + ((n + 4) & 15) * 64u, ((n + 4) >> 4) * 16384u);
        fi[n & 3] = ld16(bi, voff + ((n + 4) & 15) * 64u, ((n + 4) >> 4) * 16384u);
      }
#pragma unroll
      for (int t = 0; t < 4; ++t) {
        const u4 x = lds16(sm, (t * 16 + ks) * 1024u + l * 16u);
        hr[ct][t] = mma(x, r, hr[ct][t]);
        hi[ct][t] = mma(x, i, hi[ct][t]);
      }
    }
  }
  __builtin_amdgcn_sched_barrier(0);
  asm volatile("s_nop 7\n\ts_nop 7" ::: "memory");
  __builtin_amdgcn_sched_barrier(0);
#pragma unroll
  for (int ct = 0; ct < 4; ++ct) {
    const unsigned ch = 64u * w + 16u * ct + c;
    const f4 tb = *reinterpret_cast<const f4*>(sm + L_TAB + ch * 32u);
    const f2 l16 = *reinterpret_cast<const f2*>(sm + L_TAB + ch * 32u + 16u);
    const f2 lam = {tb[0], tb[1]}, coef = {tb[2], tb[3]};
    f2 hh = {0.f, 0.f};
#pragma unroll
    for (int i = 0; i < 16; ++i) {
      hh = cfma(lam, hh, cmul(coef, f2{hr[ct][i >> 2][i & 3], hi[ct][i >> 2][i & 3]}));
      hr[ct][i >> 2][i & 3] = hh[0];
      hi[ct][i >> 2][i & 3] = hh[1];
    }
    f2 e = from_group(hh[0], hh[1], c, 0);
#pragma unroll
    for (int k = 1; k < 4; ++k) e = cfma(l16, e, from_group(hh[0], hh[1], c, k));
    if (g == 0) st8c(__builtin_bit_cast(u2, e), ws, O_AE + ci * 4096u + ch * 8u, 0);
  }
  drain();
  bar();
  if (tid == 0) st4c(1u, ws, F_A + ci * FL, 0);
  wait_flags(ws, F_A + (l & 31) * FL, (l & 31) < ci ? 1u : 0u, alive);
  {
    f2 hin = {0.f, 0.f};
#pragma unroll 8
    for (unsigned k = 0; k < ci; ++k) hin = cfma(lam64, hin, __builtin_bit_cast(f2, ld8c(ws, O_AE + tid * 8u, k * 4096u)));
    *reinterpret_cast<f2*>(sm + L_TAB + tid * 32u + 24u) = hin;
  }
  bar();
#pragma unroll
  for (int ct = 0; ct < 4; ++ct) {
    const unsigned ch = 64u * w + 16u * ct + c;
    const f4 t0 = *reinterpret_cast<const f4*>(sm + L_TAB + ch * 32u);
    const f4 t1 = *reinterpret_cast<const f4*>(sm + L_TAB + ch * 32u + 16u);
    const f2 lam = {t0[0], t0[1]}, l16 = {t1[0], t1[1]};
    f2 z = {t1[2], t1[3]};
#pragma unroll
    for (int k = 0; k < 3; ++k) {
      const f2 n = cfma(l16, z, from_group(hr[ct][3][3], hi[ct][3][3], c, k));
      z = (unsigned)k < g ? n : z;
    }
    const unsigned hb = g * TS + (ch >> 5) * KS + ((ch >> 3) & 3) * GS + (ch & 7) * 2u;
#pragma unroll
    for (int i = 0; i < 16; ++i) {
      z = cmul(lam, z);
      const unsigned d = pk(2.0f * (hr[ct][i >> 2][i & 3] + z[0]), -2.0f * (hi[ct][i >> 2][i & 3] + z[1]));
      lds_put2(sm, hb + i * 16u, d);
      lds_put2(sm, hb + 16u * KS + i * 16u, d >> 16);
    }
  }
  bar();
  f4 y[4][4] = {};
  {
    const rsrc_t cr = mk(p.cre, 1u << 19), cm = mk(p.cim, 1u << 19);
    const unsigned voff = ((64u * w + c) * 512u + 8u * g) * 2u;
    u4 fa[2][4];
#pragma unroll
    for (int k = 0; k < 2; ++k)
#pragma unroll
      for (int d = 0; d < 4; ++d) fa[k][d] = ld16(cr, voff + k * 64u, d * 16384u);
#pragma unroll
    for (int ks = 0; ks < 32; ++ks) {
      u4 a[4];
      fence();
#pragma unroll
      for (int d = 0; d < 4; ++d) {
        a[d] = fa[ks & 1][d];
        if (ks + 2 < 32) fa[ks & 1][d] = ld16(ks + 2 < 16 ? cr : cm, voff + ((ks + 2) & 15) * 64u, d * 16384u);
      }
#pragma unroll
      for (int t = 0; t < 4; ++t) {
        const u4 hb = lds16(sm, t * TS + ks * KS + g * GS + c * 16u);
#pragma unroll
        for (int d = 0; d < 4; ++d) y[d][t] = mma(a[d], hb, y[d][t]);
      }
    }
  }
  const rsrc_t dr = mk(p.dsk, 1024);
#pragma unroll
  for (int d = 0; d < 4; ++d) {
    const unsigned col = 64u * w + 16u * d + 4u * g;
    const u2 dk = ld8(dr, col * 2u, 0);
#pragma unroll
    for (int t = 0; t < 4; ++t) {
      const unsigned row = 64u * ci + 16u * t + c;
      const u2 xv = ld8(xr, (row * 512u + col) * 2u, 0);
      const f4 o = {y[d][t][0] + rbf(smul(bf2f(xv[0] & 0xffffu), bf2f(dk[0] & 0xffffu))),
                    y[d][t][1] + rbf(smul(bf2f(xv[0] >> 16), bf2f(dk[0] >> 16))),
                    y[d][t][2] + rbf(smul(bf2f(xv[1] & 0xffffu), bf2f(dk[1] & 0xffffu))),
                    y[d][t][3] + rbf(smul(bf2f(xv[1] >> 16), bf2f(dk[1] >> 16)))};
      __builtin_amdgcn_raw_buffer_store_b128(__builtin_bit_cast(u4, o), outr, (row * 1024u + col) * 4u, 0, 0);
    }
  }
}

__global__ void __launch_bounds__(512, 2) fused_kernel(Params p) {
  extern __shared__ __attribute__((aligned(16))) char smem[];
  unsigned i;
  const unsigned role = role_of(blockIdx.x, i);
  switch (role) {
    case R_S1: role_s1(p, i, smem); break;
    case R_VM: role_vm(p, i, smem); break;
    case R_V1: role_v1(p, i, smem); break;
    case R_S2: role_s2(p, i, smem); break;
    case R_VU: role_vu(p, i, smem); break;
    case R_A: role_a(p, i, smem); [[fallthrough]];
    default: role_v2(p, i, smem, role == R_A); break;
  }
}

extern "C" void kernel_launch(void* const* d_in, const int* in_sizes, int n_in, void* d_out, int out_size, void* d_ws,
                              size_t ws_size, hipStream_t stream) {
  Params p;
  p.ax = d_in[0]; p.lre = d_in[1]; p.lim = d_in[2]; p.bre = d_in[3]; p.bim = d_in[4]; p.cre = d_in[5];
  p.cim = d_in[6]; p.ldt = d_in[7]; p.dsk = d_in[8]; p.bx = d_in[9]; p.mem0 = d_in[10]; p.wq = d_in[11];
  p.wk = d_in[12]; p.wv = d_in[13]; p.wo = d_in[14];
  p.out = (float*)d_out;
  p.ws = (char*)d_ws;
  (void)hipFuncSetAttribute((const void*)fused_kernel, hipFuncAttributeMaxDynamicSharedMemorySize, DYN_LDS);
  fused_kernel<<<256, 512, DYN_LDS, stream>>>(p);
}
```
